```python
import torch

def _softmax(x, axis=-1):
    return torch.softmax(x, dim=axis)

def _gelu(x):
    return torch.nn.functional.gelu(x, approximate='tanh')

def _silu(x):
    return torch.nn.functional.silu(x)

def _sigmoid(x):
    return torch.sigmoid(x)

def _layernorm(x, axis=-1, eps=1e-05):
    m = x.mean(dim=axis, keepdim=True)
    v = x.var(dim=axis, keepdim=True, unbiased=False)
    return (x - m) / torch.sqrt(v + eps)

def _scatter_add(tgt, idx, src, axis=0):
    return tgt.clone().index_add_(axis, idx, src)

def _segment_sum(src, seg, n):
    out = torch.zeros((n,) + src.shape[1:], dtype=src.dtype)
    return out.index_add_(0, seg, src)

def _pad(x, pw):
    flat = tuple((v for pair in reversed(pw) for v in pair))
    return torch.nn.functional.pad(x, flat)
kernel_name = 'adaLN_cond__ssm_conv_h3__glu_gated_reglu__prompt'

def setup_inputs():

    _w = lambda a: (lambda t: t.to(torch.bfloat16) if t.is_floating_point() else t)(torch.as_tensor(a))

    def _A_setup():
        B, S, D = (1, 2048, 640)
        ks = 4
        return {'q': _w((lambda *s: torch.randn(*s, dtype=torch.bfloat16) * 0.05)(B, S, D)), 'k': _w((lambda *s: torch.randn(*s, dtype=torch.bfloat16) * 0.8)(B, S, D)), 'v': _w((lambda *s: torch.randn(*s, dtype=torch.bfloat16) * 0.8)(B, S, D)), 'shift_k': _w((lambda *s: torch.randn(*s, dtype=torch.bfloat16) * 0.8)(D, ks) * 0.5), 'ssm_k': _w((lambda *s: torch.randn(*s, dtype=torch.bfloat16) * 0.8)(D, S) * (1.0 / S ** 0.5)), 'ssm_D': _w((lambda *s: torch.randn(*s, dtype=torch.bfloat16) * 0.8)(D))}
    _A_in = _A_setup()

    def _B_setup():
        B, S, D, F = (1, 2048, 640, 1536)
        return {'x': _w((lambda *s: torch.randn(*s, dtype=torch.bfloat16) * 0.05)(B, S, D)), 'W_gate': _w((lambda *s: torch.randn(*s, dtype=torch.bfloat16) * 0.8)(D, F) * (2.0 / D) ** 0.5), 'W_up': _w((lambda *s: torch.randn(*s, dtype=torch.bfloat16) * 0.8)(D, F) * (2.0 / D) ** 0.5), 'W_down': _w((lambda *s: torch.randn(*s, dtype=torch.bfloat16) * 0.8)(F, D) * (1.0 / F) ** 0.5)}
    _B_in = _B_setup()
    return {'A_q': _A_in['q'], 'A_k': _A_in['k'], 'A_v': _A_in['v'], 'A_shift_k': _A_in['shift_k'], 'A_ssm_k': _A_in['ssm_k'], 'A_ssm_D': _A_in['ssm_D'], 'B_x': _B_in['x'], 'B_W_gate': _B_in['W_gate'], 'B_W_up': _B_in['W_up'], 'B_W_down': _B_in['W_down'], 'c': _w((lambda *s: torch.randn(*s, dtype=torch.bfloat16) * 0.2)(1, 640) * 8.0), 'w_mod': _w((lambda *s: torch.randn(*s, dtype=torch.bfloat16) * 0.2)(640, 6 * 640) * 640 ** (-0.5) * 8.0)}

def reference(A_q, A_k, A_v, A_shift_k, A_ssm_k, A_ssm_D, B_x, B_W_gate, B_W_up, B_W_down, c, w_mod):

    def _A_ref(q, k, v, shift_k, ssm_k, ssm_D):
        B, S, D = q.shape
        ks = shift_k.shape[-1]
        k_pad = _pad(k, ((0, 0), (ks - 1, 0), (0, 0)))
        k_shift = torch.zeros_like(k)
        for t in range(ks):
            k_shift = k_shift + k_pad[:, t:t + S, :] * shift_k[:, ks - 1 - t][None, None, :]
        kv = k_shift * v
        L = 2 * S
        kv_pad = _pad(kv, ((0, 0), (0, S), (0, 0)))
        kf_pad = _pad(ssm_k, ((0, 0), (0, S)))
        kv_f = (lambda x, axes=(-2, -1): torch.fft.rfft2(x.float(), dim=axes))(kv_pad[:, None, :, :], axes=(1, 2))
        kf_f = (lambda x, axes=(-2, -1): torch.fft.rfft2(x.float(), dim=axes))(kf_pad[:, None, :], axes=(1, 2))
        kf_f = (lambda x, *d: x.permute(*d))(kf_f, 1, 2, 0)
        y_f = kv_f * kf_f[None, :, :, :]
        y = (lambda x, s, axes=(-2, -1): torch.fft.irfft2(x, s=s, dim=axes).to(torch.bfloat16))(y_f, (1, L), axes=(1, 2))[:, 0, :S, :]
        y = y + kv * ssm_D[None, None, :]
        return q * y

    def _B_ref(x, W_gate, W_up, W_down):
        gate = torch.einsum('bsd,df->bsf', x, W_gate)
        up = torch.einsum('bsd,df->bsf', x, W_up)
        gate = gate * (lambda x, dt: x.to(dt))(gate > 0, gate.dtype)
        h = gate * up
        return torch.einsum('bsf,fd->bsd', h, W_down)
    x = A_q
    mod = (_silu(c) @ w_mod).reshape(c.shape[0], 6, 1, -1)
    sa, ca, ga, sb, cb, gb = (mod[:, 0].clone(), mod[:, 1].clone(), mod[:, 2].clone(), mod[:, 3].clone(), mod[:, 4].clone(), mod[:, 5].clone())
    h = _layernorm(x, axis=-1) * (1 + ca) + sa
    x = x + ga * _A_ref(h, A_k, A_v, A_shift_k, A_ssm_k, A_ssm_D)
    h = _layernorm(x, axis=-1) * (1 + cb) + sb
    x = x + gb * _B_ref(h, B_W_gate, B_W_up, B_W_down)
    return x
```

```cpp
#include <hip/hip_runtime.h>
#include <stdint.h>

typedef unsigned short u16;
typedef unsigned int u32;
typedef __attribute__((ext_vector_type(2))) __bf16 bf16x2;
typedef __attribute__((ext_vector_type(4))) float f32x4;
typedef __attribute__((ext_vector_type(2))) float f32x2;
typedef __attribute__((ext_vector_type(4))) u32 u32x4;
typedef __attribute__((ext_vector_type(2))) u32 u32x2;

constexpr int S_ = 2048, D_ = 640, F_ = 1536;
constexpr int TP_ = S_ + 64;
constexpr int NMOD = 6 * D_;
constexpr int MODP = 8;
constexpr int CWG = 160;

__device__ __forceinline__ float bflo(u32 w) { return __uint_as_float(w << 16); }
__device__ __forceinline__ float bfhi(u32 w) { return __uint_as_float(w & 0xffff0000u); }
__device__ __forceinline__ u32 pack_bf16(float lo, float hi) {
  bf16x2 v; v[0] = (__bf16)lo; v[1] = (__bf16)hi; return __builtin_bit_cast(u32, v);
}
__device__ __forceinline__ float rbf(float x) { __bf16 b = (__bf16)x; return (float)b; }
#define SCHED() __builtin_amdgcn_sched_barrier(0)
template <int V> struct IC { static constexpr int value = V; };
template <int B, int E, class Fn> __device__ __forceinline__ void static_for(Fn&& f) {
  if constexpr (B < E) { f(IC<B>{}); static_for<B + 1, E>(f); }
}
__device__ __forceinline__ void mfma(f32x4& c, u32x4 a, u32x4 b) {
  asm volatile("v_mfma_f32_16x16x32_bf16 %0, %1, %2, %0" : "+v"(c) : "v"(a), "v"(b));
}
__device__ __forceinline__ void mfma0(f32x4& c, u32x4 a, u32x4 b) {
  asm volatile("v_mfma_f32_16x16x32_bf16 %0, %1, %2, 0" : "=&v"(c) : "v"(a), "v"(b));
}
template <bool FIRST> __device__ __forceinline__ void mfma_(f32x4& c, u32x4 a, u32x4 b) { if constexpr (FIRST) mfma0(c, a, b); else mfma(c, a, b); }
__device__ __forceinline__ u32 alignbit(u32 hi, u32 lo, u32 sh) {
  u32 d; asm volatile("v_alignbit_b32 %0, %1, %2, %3" : "=v"(d) : "v"(hi), "v"(lo), "v"(sh)); return d;
}
template <int OFF> __device__ __forceinline__ void ds_tr(u32x2& dst, unsigned addr) {
  asm volatile("ds_read_b64_tr_b16 %0, %1 offset:%2" : "=v"(dst) : "v"(addr), "i"(OFF) : "memory");
}
template <int OFF> __device__ __forceinline__ void ds_b32(u32& dst, unsigned addr) {
  asm volatile("ds_read_b32 %0, %1 offset:%2" : "=v"(dst) : "v"(addr), "i"(OFF) : "memory");
}
template <int OFF> __device__ __forceinline__ void ds_b64(u32x2& dst, unsigned addr) {
  asm volatile("ds_read_b64 %0, %1 offset:%2" : "=v"(dst) : "v"(addr), "i"(OFF) : "memory");
}
template <int OFF> __device__ __forceinline__ void ds_b128(u32x4& dst, unsigned addr) {
  asm volatile("ds_read_b128 %0, %1 offset:%2" : "=v"(dst) : "v"(addr), "i"(OFF) : "memory");
}
template <int OFF> __device__ __forceinline__ void ds_w128(unsigned addr, f32x4 v) {
  asm volatile("ds_write_b128 %0, %1 offset:%2" :: "v"(addr), "v"(v), "i"(OFF) : "memory");
}
template <int OFF> __device__ __forceinline__ void gld128(u32x4& dst, unsigned voff, const char* sb) {
  asm volatile("global_load_dwordx4 %0, %1, %2 offset:%3" : "=v"(dst) : "v"(voff), "s"(sb), "i"(OFF) : "memory");
}
__device__ __forceinline__ void gld32(u32& dst, unsigned voff, const char* sb) {
  asm volatile("global_load_dword %0, %1, %2" : "=v"(dst) : "v"(voff), "s"(sb) : "memory");
}
template <int LOFF> __device__ __forceinline__ void dma16(unsigned ldsb, unsigned voff, const char* sb) {
  asm volatile("s_add_u32 m0, %0, %3\n\ts_nop 0\n\tglobal_load_lds_dwordx4 %1, %2" :: "s"(ldsb), "v"(voff), "s"(sb), "i"(LOFF) : "memory", "scc");
}
template <int LOFF> __device__ __forceinline__ void dma16x4(unsigned ldsb, unsigned v0, unsigned v1, const char* sb) {
  asm volatile("s_add_u32 m0, %0, %4\n\ts_nop 0\n\tglobal_load_lds_dwordx4 %1, %3\n\tglobal_load_lds_dwordx4 %2, %3 offset:1024\n\t"
               "global_load_lds_dwordx4 %1, %3 offset:2048\n\tglobal_load_lds_dwordx4 %2, %3 offset:3072"
               :: "s"(ldsb), "v"(v0), "v"(v1), "s"(sb), "i"(LOFF) : "memory", "scc");
}
__device__ __forceinline__ void sld32(u32& dst, const void* p) { asm volatile("s_load_dword %0, %1, 0x0" : "=s"(dst) : "s"(p) : "memory"); }
template <int N> __device__ __forceinline__ void wait_vm() { asm volatile("s_waitcnt vmcnt(%0)" :: "i"(N) : "memory"); }
template <int N> __device__ __forceinline__ void wait_lgkm() { asm volatile("s_waitcnt lgkmcnt(%0)" :: "i"(N) : "memory"); }
__device__ __forceinline__ void bar() { asm volatile("s_barrier" ::: "memory"); }
template <class T> __device__ __forceinline__ void pin(T& a) { asm volatile("" : "+v"(a)); }
__device__ __forceinline__ u32x4 cat(u32x2 a, u32x2 b) { return u32x4{a[0], a[1], b[0], b[1]}; }

typedef __amdgpu_buffer_rsrc_t rsrc_t;
__device__ __forceinline__ rsrc_t mk_rsrc(const void* p, int bytes) { return __builtin_amdgcn_make_buffer_rsrc((void*)p, 0, bytes, 0x00020000); }
template <int CTRL> __device__ __forceinline__ float dpp_add(float x) {
  return x + __builtin_bit_cast(float, __builtin_amdgcn_mov_dpp(__builtin_bit_cast(int, x), CTRL, 0xf, 0xf, true));
}
constexpr int DPP_XOR1 = 0xB1, DPP_XOR2 = 0x4E, DPP_HMIRROR = 0x141, DPP_ROR8 = 0x128;
__device__ __forceinline__ float sum8(float x) { return dpp_add<DPP_HMIRROR>(dpp_add<DPP_XOR2>(dpp_add<DPP_XOR1>(x))); }
__device__ __forceinline__ float tred32(float a, float b) {
  auto r = __builtin_amdgcn_permlane32_swap(__float_as_uint(a), __float_as_uint(b), false, false);
  return __uint_as_float(r[0]) + __uint_as_float(r[1]);
}
__device__ __forceinline__ float tred16(float a, float b) {
  auto r = __builtin_amdgcn_permlane16_swap(__float_as_uint(a), __float_as_uint(b), false, false);
  return __uint_as_float(r[0]) + __uint_as_float(r[1]);
}
__device__ __forceinline__ float rdl(float x, int l) { return __int_as_float(__builtin_amdgcn_readlane(__float_as_int(x), l)); }
#define WAUX 16

#define STAMP_GET(V)
#define STAMP_DECL()
#define STAMP_END(K)

__device__ __forceinline__ float modv(const float* __restrict__ modp, int i) {
  float s = modp[i];
#pragma unroll
  for (int p = 1; p < MODP; ++p) s += modp[p * NMOD + i];
  return rbf(s);
}

__global__ __launch_bounds__(512) void k_prep(const u16* __restrict__ xIn, const u16* __restrict__ kIn, const u16* __restrict__ vIn,
                                              const u16* __restrict__ shk, const u16* __restrict__ cIn,
                                              const u16* __restrict__ wmod,
                                              u32x4* __restrict__ kvG, u32x4* __restrict__ xG,
                                              float* __restrict__ modp, float* __restrict__ st1) {
  STAMP_DECL();
  asm volatile("" :: "s"(xIn), "s"(kIn), "s"(vIn), "s"(shk), "s"(cIn), "s"(wmod), "s"(kvG), "s"(xG), "s"(modp), "s"(st1));
  const int tid = threadIdx.x, lane = tid & 63;
  const int wv = __builtin_amdgcn_readfirstlane(tid >> 6);
  const int b = blockIdx.x;
  const int r8 = lane >> 3, cp = lane & 7;
  if (wv < 5) {
    const rsrc_t rk = mk_rsrc(kIn, S_ * D_ * 2), rv = mk_rsrc(vIn, S_ * D_ * 2), rx = mk_rsrc(xIn, S_ * D_ * 2);
    const int vo = tid * 4, so = b * 8 * 1280;
    const int sh0 = b ? so - 3 * 1280 : 0;
    u32 kk[11], vv[8], xx[8];
#pragma unroll
    for (int j = 0; j < 3; ++j) kk[j] = __builtin_amdgcn_raw_buffer_load_b32(rk, vo + j * 1280, sh0, 0);
#pragma unroll
    for (int j = 0; j < 8; ++j) kk[3 + j] = __builtin_amdgcn_raw_buffer_load_b32(rk, vo + (j & 3) * 1280, so + (j >> 2) * 5120, 0);
    const u32x4 taps = *(const u32x4*)(shk + tid * 8);
#pragma unroll
    for (int j = 0; j < 8; ++j) vv[j] = __builtin_amdgcn_raw_buffer_load_b32(rv, vo + (j & 3) * 1280, so + (j >> 2) * 5120, 0);
#pragma unroll
    for (int j = 0; j < 8; ++j) xx[j] = __builtin_amdgcn_raw_buffer_load_b32(rx, vo + (j & 3) * 1280, so + (j >> 2) * 5120, 0);
    SCHED();
    if (b == 0) { kk[0] = 0; kk[1] = 0; kk[2] = 0; }
    const f32x2 w0 = {bflo(taps[0]), bflo(taps[2])}, w1 = {bfhi(taps[0]), bfhi(taps[2])};
    const f32x2 w2 = {bflo(taps[1]), bflo(taps[3])}, w3 = {bfhi(taps[1]), bfhi(taps[3])};
    const int ob = ((tid >> 2) * 512 + b) * 4 + (tid & 3);
    f32x2 kv[8];
#pragma unroll
    for (int j = 0; j < 8; ++j) {
      f32x2 a = f32x2{bflo(kk[j]), bfhi(kk[j])} * w3;
      a = __builtin_elementwise_fma(f32x2{bflo(kk[j + 1]), bfhi(kk[j + 1])}, w2, a);
      a = __builtin_elementwise_fma(f32x2{bflo(kk[j + 2]), bfhi(kk[j + 2])}, w1, a);
      a = __builtin_elementwise_fma(f32x2{bflo(kk[j + 3]), bfhi(kk[j + 3])}, w0, a);
      kv[j] = a * f32x2{bflo(vv[j]), bfhi(vv[j])};
    }
    u32x4 o0, o1, p0, p1;
#pragma unroll
    for (int t = 0; t < 4; ++t) {
      o0[t] = pack_bf16(kv[7 - 2 * t][0], kv[6 - 2 * t][0]);
      o1[t] = pack_bf16(kv[7 - 2 * t][1], kv[6 - 2 * t][1]);
      p0[t] = __builtin_amdgcn_perm(xx[2 * t + 1], xx[2 * t], 0x05040100u);
      p1[t] = __builtin_amdgcn_perm(xx[2 * t + 1], xx[2 * t], 0x07060302u);
    }
    kvG[ob] = o0; kvG[ob + 1024] = o1;
    xG[ob] = p0; xG[ob + 1024] = p1;
  } else if (wv == 5) {
    u32x4 xv[10];
#pragma unroll
    for (int i = 0; i < 10; ++i) xv[i] = *(const u32x4*)(xIn + (size_t)(b * 8 + r8) * D_ + i * 64 + cp * 8);
    SCHED();
    const bf16x2 ones = {(__bf16)1.0f, (__bf16)1.0f};
    float s1[2] = {0.f, 0.f}, s2[2] = {0.f, 0.f};
#pragma unroll
    for (int i = 0; i < 10; ++i)
#pragma unroll
      for (int q = 0; q < 4; ++q) {
        const u32 raw = xv[i][q];
        const bf16x2 v = __builtin_bit_cast(bf16x2, raw);
        s1[q & 1] = __builtin_amdgcn_fdot2_f32_bf16(v, ones, s1[q & 1], false);
        s2[q & 1] = __builtin_amdgcn_fdot2_f32_bf16(v, v, s2[q & 1], false);
      }
    const float mean = sum8(s1[0] + s1[1]) * (1.f / D_);
    const float var = fmaxf(sum8(s2[0] + s2[1]) * (1.f / D_) - mean * mean, 0.f);
    const float sd = rbf(__builtin_sqrtf(rbf(rbf(var) + 1e-5f)));
    const float rs = 1.f / sd;
    const int s = b * 8 + r8;
    const int nt = s >> 8, col = (s >> 4) & 15, kg = (s >> 2) & 3, r = s & 3;
    if (cp == 0) *(f32x2*)(st1 + ((nt * 2 + (r >> 1)) * 64 + kg * 16 + col) * 4 + (r & 1) * 2) = f32x2{rs, -rbf(mean) * rs};
  } else if (b < 240) {
    const int gcol = b >> 2, p = (b & 3) * 2 + (wv - 6);
    const rsrc_t rw = mk_rsrc(wmod, D_ * NMOD * 2);
    u32 cv[10]; u32x4 wm[10];
#pragma unroll
    for (int it = 0; it < 10; ++it) cv[it] = cIn[p * 80 + it * 8 + r8];
#pragma unroll
    for (int it = 0; it < 10; ++it) wm[it] = __builtin_amdgcn_raw_buffer_load_b128(rw, r8 * (NMOD * 2) + cp * 16, (p * 80 + it * 8) * (NMOD * 2) + gcol * 128, 0);
    SCHED();
    f32x2 acc[4];
#pragma unroll
    for (int it = 0; it < 10; ++it) {
      const float x = bflo(cv[it]);
      const float s = rbf(x * __builtin_amdgcn_rcpf(1.f + __expf(-x)));
      const f32x2 s2 = {s, s};
#pragma unroll
      for (int q = 0; q < 4; ++q) {
        const f32x2 w2 = {bflo(wm[it][q]), bfhi(wm[it][q])};
        acc[q] = it ? __builtin_elementwise_fma(s2, w2, acc[q]) : s2 * w2;
      }
    }
    const float a0 = tred16(tred32(acc[0][0], acc[2][0]), tred32(acc[1][0], acc[3][0]));
    const float a1 = tred16(tred32(acc[0][1], acc[2][1]), tred32(acc[1][1], acc[3][1]));
    const float e0 = dpp_add<DPP_ROR8>(a0), e1 = dpp_add<DPP_ROR8>(a1);
    modp[p * NMOD + gcol * 64 + cp * 8 + r8] = (lane & 8) ? e1 : e0;
  }
  STAMP_END(0);
}

constexpr int FC_BYTES = 2080 * 2;
constexpr int CH_BYTES = 2 * FC_BYTES;
constexpr int CKV_OFF = 33280;
constexpr int CX_OFF = CKV_OFF + 2048 + 16384, CS_OFF = CX_OFF + 16384;
constexpr int K1_LDS = CS_OFF + 16384;
constexpr int W1_STEPS = 21;
constexpr int conv_gap(int a, int b) { int n = 0; for (int i = 0; i < a; ++i) n += 8 - i; return n + b; }
__global__ __launch_bounds__(256, 1) void k_conv(const u32x4* __restrict__ kvG, const u32x4* __restrict__ xG, const u16* __restrict__ ssmk,
                                                 const u16* __restrict__ ssmD, const float* __restrict__ modp,
                                                 const float* __restrict__ st1, u16* __restrict__ x2T,
                                                 const u16* __restrict__ wg, const u16* __restrict__ wu, const u16* __restrict__ wd,
                                                 u32x4* __restrict__ w1f, u32x4* __restrict__ w2f) {
  __shared__ __attribute__((aligned(16))) char smem[K1_LDS];
  STAMP_DECL();
  asm volatile("" :: "s"(kvG), "s"(xG), "s"(ssmk), "s"(ssmD), "s"(modp), "s"(st1), "s"(x2T), "s"(wg), "s"(wu), "s"(wd), "s"(w1f), "s"(w2f));
  const int tid = threadIdx.x, lane = tid & 63;
  const int wv = __builtin_amdgcn_readfirstlane(tid >> 6);
  const unsigned sbase = (unsigned)(size_t)smem;
  if (__builtin_expect(blockIdx.x < CWG, 1)) {
    const int jb = blockIdx.x;
    const int d = (jb >> 1) * 8 + (jb & 1) + 2 * wv;
    const int row = lane & 15, kg = lane >> 4;
    const unsigned l16 = lane * 16;
    {
      const unsigned ko0 = (lane ^ (lane >> 4)) * 16;
      dma16x4<CKV_OFF + 2048>(sbase + wv * 4096, ko0, ko0 ^ 64, (const char*)(kvG + (size_t)jb * 1024) + wv * 4096);
      const char* fs = (const char*)(ssmk + (size_t)d * S_);
      dma16x4<32>(sbase + wv * CH_BYTES, l16, l16, fs);
      dma16x4<FC_BYTES + 28>(sbase + wv * CH_BYTES, l16, l16, fs);
      dma16x4<CX_OFF>(sbase + wv * 4096, l16, l16, (const char*)(xG + (size_t)jb * 1024) + wv * 4096);
      dma16x4<CS_OFF>(sbase + wv * 4096, l16, l16, (const char*)st1 + wv * 4096);
    }
    u32 mraw, draw;
    gld32(mraw, ((lane & 7) * NMOD + ((lane >> 3) & 3) * D_) * 4, (const char*)(modp + d));
    sld32(draw, (const u32*)ssmD + (d >> 1));
    *(u32x2*)(smem + CKV_OFF + tid * 8) = u32x2{0, 0};
    if (lane < 15) *(u32*)(smem + wv * CH_BYTES + (lane >> 3) * FC_BYTES + (lane & 7) * 4) = 0;
    const int rho = row + 1;
    u32 sh = (rho & 1) * 16;
    unsigned aAddr = sbase + wv * CH_BYTES + ((rho >> 1) & 1) * FC_BYTES + 2 * (64 * kg + (rho & ~3));
    pin(sh); pin(aAddr);
    unsigned bA[8];
#pragma unroll
    for (int e = 0; e < 8; ++e) { const int g = 32 + 2 * row + 1 - 8 * kg - e; bA[e] = sbase + CKV_OFF + 16 * ((4 * g + wv) ^ ((g >> 2) & 7)); pin(bA[e]); }
    wait_vm<9>();
    wait_lgkm<0>();
    bar();
    STAMP_GET(t1);
    f32x4 acc[8];
    u32x2 ra[2][8][2]; u32x4 rb[2][8]; u32 carry[8]; u32x4 A[8];
    auto load_n = [&](auto E, auto N) {
      constexpr int e = E.value, n = N.value, q = n / 3, bf = e & 1;
      if constexpr (n % 3 == 0) ds_b64<512 * q + 16 * e>(ra[bf][q][0], aAddr);
      else if constexpr (n % 3 == 1) ds_b64<512 * q + 16 * e + 8>(ra[bf][q][1], aAddr);
      else ds_b128<2048 * q>(rb[bf][q], bA[e]);
    };
    auto build = [&](auto E, auto AA, auto I) {
      constexpr int e = E.value, a = AA.value, i = I.value, bf = e & 1;
      if constexpr (i == 0) A[a][0] = alignbit(ra[bf][a][0][1], ra[bf][a][0][0], sh);
      else if constexpr (i == 1) A[a][1] = alignbit(ra[bf][a][1][0], ra[bf][a][0][1], sh);
      else if constexpr (i == 2) A[a][2] = alignbit(ra[bf][a][1][1], ra[bf][a][1][0], sh);
      else { A[a][3] = alignbit(carry[a], ra[bf][a][1][1], sh); carry[a] = ra[bf][a][0][0]; }
    };
    static_for<0, 8>([&](auto I) { ds_b32<512 * I.value + 128>(carry[I.value], aAddr); });
    static_for<0, 24>([&](auto N) { load_n(IC<7>{}, N); });
    wait_lgkm<0>();
    static_for<0, 4>([&](auto I) { build(IC<7>{}, IC<0>{}, I); });
    asm volatile("s_nop 1");
    static_for<0, 8>([&](auto EE) {
      constexpr int e = 7 - EE.value, bf = e & 1;
      const u32x4 A0 = A[0];
      static_for<0, 8>([&](auto AA) {
        constexpr int a = AA.value;
        static_for<0, 8 - a>([&](auto BB) {
          constexpr int bb = BB.value, n = conv_gap(a, bb);
          mfma_<(e == 7 && a == 0)>(acc[a + bb], a == 0 ? A0 : A[a], rb[bf][bb]);
          if constexpr (e > 0 && n < 24) load_n(IC<(e > 0 ? e - 1 : 0)>{}, IC<n>{});
          if constexpr (n < 28) build(IC<e>{}, IC<1 + n / 4>{}, IC<n % 4>{});
          if constexpr (e > 0 && n == 30) wait_lgkm<0>();
          if constexpr (e > 0 && (n == 31 || n == 32)) { build(IC<(e > 0 ? e - 1 : 0)>{}, IC<0>{}, IC<2 * (n - 31)>{}); build(IC<(e > 0 ? e - 1 : 0)>{}, IC<0>{}, IC<2 * (n - 31) + 1>{}); }
        });
      });
    });
    STAMP_GET(t2);
    asm volatile("s_waitcnt vmcnt(0)\n\ts_nop 7\n\ts_barrier" : "+v"(acc[0]), "+v"(acc[1]), "+v"(acc[2]), "+v"(acc[3]), "+v"(acc[4]), "+v"(acc[5]), "+v"(acc[6]), "+v"(acc[7]), "+v"(mraw), "+s"(draw) :: "memory");
    const float mv = rbf(sum8(__uint_as_float(mraw)));
    const float m_ga = rdl(mv, 16), c0 = rdl(mv, 0) * m_ga, c1 = rbf(1.f + rdl(mv, 8)) * m_ga;
    const float Dv = (d & 1) ? bfhi(draw) : bflo(draw);
#pragma unroll
    for (int np = 0; np < 4; ++np) {
      u32x4 o;
#pragma unroll
      for (int u = 0; u < 2; ++u) {
        const int nt = 2 * np + u;
        const int tg = 32 * nt + 2 * row + (kg >> 1), g = 32 + tg;
        const u32x2 kvw = *(const u32x2*)(smem + CKV_OFF + 16 * ((4 * g + wv) ^ ((g >> 2) & 7)) + ((kg & 1) ? 0 : 8));
        const u32x2 xw = *(const u32x2*)(smem + CX_OFF + tg * 64 + wv * 16 + (kg & 1) * 8);
        const f32x4 sA = *(const f32x4*)(smem + CS_OFF + (nt * 2) * 1024 + lane * 16);
        const f32x4 sB = *(const f32x4*)(smem + CS_OFF + (nt * 2 + 1) * 1024 + lane * 16);
        const float kv4[4] = {bfhi(kvw[1]), bflo(kvw[1]), bfhi(kvw[0]), bflo(kvw[0])};
        const float x4[4] = {bflo(xw[0]), bfhi(xw[0]), bflo(xw[1]), bfhi(xw[1])};
        const float rs4[4] = {sA[0], sA[2], sB[0], sB[2]};
        const float nb4[4] = {sA[1], sA[3], sB[1], sB[3]};
        float r4[4];
#pragma unroll
        for (int r = 0; r < 4; ++r) {
          const float yb = fmaf(kv4[r], Dv, acc[nt][r]);
          const float n1 = fmaf(x4[r], rs4[r], nb4[r]);
          const float h = fmaf(n1, c1, c0);
          r4[r] = fmaf(h, yb, x4[r]);
        }
        o[2 * u] = pack_bf16(r4[0], r4[1]); o[2 * u + 1] = pack_bf16(r4[2], r4[3]);
      }
      *(u32x4*)(x2T + (size_t)d * TP_ + 512 * np + 8 * lane) = o;
    }
    STAMP_END(1);
    return;
  }
  const int j = blockIdx.x - CWG;
  const int ug = j % 24, kq = j / 24;
  const int r = tid >> 3, cc = tid & 7;
  u32x4 gw[5], uw[5], dw[5];
  float* LM = (float*)(smem + 160 * 256);
  float* LR = LM + 320;
  char* L2 = smem + 49152;
  const int ks2 = j >> 1, hf = j & 1;
  const rsrc_t r1 = mk_rsrc(w1f, 64 * W1_STEPS * 3 * 1024), r2 = mk_rsrc(w2f, 8 * 48 * 5 * 1024);
  float mc = 0.f, ms = 0.f;
  if (tid < 160) { ms = modv(modp, 3 * D_ + kq * 160 + tid); mc = rbf(1.f + modv(modp, 4 * D_ + kq * 160 + tid)); }
#pragma unroll
  for (int it = 0; it < 5; ++it) {
    const int k = kq * 160 + it * 32 + r;
    gw[it] = *(const u32x4*)(wg + (size_t)k * F_ + ug * 64 + cc * 8);
    uw[it] = *(const u32x4*)(wu + (size_t)k * F_ + ug * 64 + cc * 8);
  }
#pragma unroll
  for (int it = 0; it < 5; ++it) dw[it] = *(const u32x4*)(wd + (size_t)(ks2 * 32 + r) * D_ + hf * 320 + (it * 8 + cc) * 8);
  if (tid < 160) *(f32x2*)(LM + 2 * tid) = f32x2{mc, ms};
  __syncthreads();
  float su[16], sw[16];
#pragma unroll
  for (int i = 0; i < 16; ++i) { su[i] = 0.f; sw[i] = 0.f; }
#pragma unroll
  for (int it = 0; it < 5; ++it) {
    const f32x2 m2 = *(const f32x2*)(LM + 2 * (it * 32 + r));
    u32 o[8];
#pragma unroll
    for (int q = 0; q < 4; ++q) {
      const float g0 = bflo(gw[it][q]), g1 = bfhi(gw[it][q]), u0 = bflo(uw[it][q]), u1 = bfhi(uw[it][q]);
      o[2 * q] = pack_bf16(g0 * m2[0], u0 * m2[0]);
      o[2 * q + 1] = pack_bf16(g1 * m2[0], u1 * m2[0]);
      su[4 * q + 0] += bflo(o[2 * q]); su[4 * q + 1] += bfhi(o[2 * q]);
      su[4 * q + 2] += bflo(o[2 * q + 1]); su[4 * q + 3] += bfhi(o[2 * q + 1]);
      sw[4 * q + 0] = fmaf(g0, m2[1], sw[4 * q + 0]); sw[4 * q + 1] = fmaf(u0, m2[1], sw[4 * q + 1]);
      sw[4 * q + 2] = fmaf(g1, m2[1], sw[4 * q + 2]); sw[4 * q + 3] = fmaf(u1, m2[1], sw[4 * q + 3]);
    }
    char* dst = smem + (it * 32 + r) * 256 + ((cc ^ (r & 7)) * 32);
    *(u32x4*)(dst) = u32x4{o[0], o[1], o[2], o[3]};
    *(u32x4*)(dst + 16) = u32x4{o[4], o[5], o[6], o[7]};
  }
#pragma unroll
  for (int it = 0; it < 5; ++it) *(u32x4*)(L2 + r * 672 + (it * 8 + cc) * 16) = dw[it];
  float v8[8];
#pragma unroll
  for (int i = 0; i < 8; ++i) v8[i] = dpp_add<DPP_ROR8>(tred16(tred32(su[i], sw[i]), tred32(su[i + 8], sw[i + 8])));
  if (!(lane & 8)) {
    *(f32x4*)(LR + (wv * 8 + cc) * 32 + (lane >> 4) * 8) = f32x4{v8[0], v8[1], v8[2], v8[3]};
    *(f32x4*)(LR + (wv * 8 + cc) * 32 + (lane >> 4) * 8 + 4) = f32x4{v8[4], v8[5], v8[6], v8[7]};
  }
  STAMP_GET(t1);
  __syncthreads();
  STAMP_GET(t2);
  {
    const int rr = 4 * (lane >> 4) + ((lane & 15) >> 2);
    u32x2 lo[10], hi[10];
    unsigned ad[2];
#pragma unroll
    for (int t = 0; t < 2; ++t) ad[t] = sbase + rr * 256 + (((wv * 2 + t) ^ (rr & 7)) * 32) + (lane & 3) * 8;
    static_for<0, 10>([&](auto I) { constexpr int i = I.value; ds_tr<(i >> 1) * 8192>(lo[i], ad[i & 1]); ds_tr<(i >> 1) * 8192 + 4096>(hi[i], ad[i & 1]); });
    wait_lgkm<0>();
#pragma unroll
    for (int i = 0; i < 10; ++i) { pin(lo[i]); pin(hi[i]); }
#pragma unroll
    for (int i = 0; i < 10; ++i) {
      const int T = ug * 8 + wv * 2 + (i & 1);
      const int sw3 = T / 3, ti = T - sw3 * 3;
      __builtin_amdgcn_raw_buffer_store_b128(cat(lo[i], hi[i]), r1, lane * 16, ((sw3 * W1_STEPS + kq * 5 + (i >> 1)) * 3 + ti) * 1024, WAUX);
    }
  }
  {
    const unsigned ad = sbase + 49152 + (8 * (lane >> 4) + ((lane & 15) >> 2)) * 672 + wv * 160 + (lane & 3) * 8;
    u32x2 lo[5], hi[5];
    static_for<0, 5>([&](auto I) { ds_tr<I.value * 32>(lo[I.value], ad); ds_tr<I.value * 32 + 4 * 672>(hi[I.value], ad); });
    wait_lgkm<0>();
#pragma unroll
    for (int i = 0; i < 5; ++i) { pin(lo[i]); pin(hi[i]); }
#pragma unroll
    for (int i = 0; i < 5; ++i) __builtin_amdgcn_raw_buffer_store_b128(cat(lo[i], hi[i]), r2, lane * 16, (((hf * 4 + wv) * 48 + ks2) * 5 + i) * 1024, WAUX);
  }
  if (tid < 128) {
    const int tl = tid >> 4, rho = tid & 15;
    float U = 0.f, W = 0.f;
#pragma unroll
    for (int w4 = 0; w4 < 4; ++w4) { U += LR[(w4 * 8 + tl) * 32 + rho]; W += LR[(w4 * 8 + tl) * 32 + 16 + rho]; }
    const float u1 = rbf(U), u2 = rbf(U - u1), u3 = U - u1 - u2;
    const float w1 = rbf(W), w2 = rbf(W - w1), w3 = W - w1 - w2;
    const int T = ug * 8 + tl;
    const int sw3 = T / 3, ti = T - sw3 * 3;
    w1f[((size_t)(sw3 * W1_STEPS + 20) * 3 + ti) * 64 + kq * 16 + rho] = u32x4{pack_bf16(u1, u2), pack_bf16(u3, w1), pack_bf16(w2, w3), 0u};
  }
  STAMP_END(1);
}

constexpr int G1_R = 6;
constexpr int G1_PRE = 3;
constexpr int G1_SLOTS = 8;
constexpr int G1_LA = 4;
constexpr int G1_STAT = G1_SLOTS * 8192;
constexpr int G1_LDS = 84 * 1024;
constexpr int g1_issued(int t) {
  int n = 5 * G1_PRE;
  for (int i = 0; i < t; ++i) {
    const int ks = i >> 3;
    const int lim = (ks + G1_R - 1 < 20) ? 5 * (ks + G1_R) : 103;
    if (n < lim && n < 103) ++n;
  }
  return n;
}
__global__ __launch_bounds__(256, 1) void k_gemm1(const u16* __restrict__ x2T, const u32x4* __restrict__ w1f, u32x4* __restrict__ h2f) {
  __shared__ __attribute__((aligned(16))) char smem[G1_LDS];
  STAMP_DECL();
  const int tid = threadIdx.x, lane = tid & 63;
  const int wv = __builtin_amdgcn_readfirstlane(tid >> 6);
  const int xcd = blockIdx.x & 7, idx = blockIdx.x >> 3;
  const int mt = (xcd & 1) * 8 + (idx & 7), nc = (xcd >> 1) * 4 + (idx >> 3);
  const unsigned sbase = (unsigned)(size_t)smem;
  unsigned xoff[2];
#pragma unroll
  for (int p = 0; p < 2; ++p) {
    const int k = 4 * p + (lane >> 4);
    xoff[p] = (k * TP_ + (((lane & 15) ^ (2 * k)) * 8)) * 2;
  }
  const char* xsrc = (const char*)(x2T + (size_t)(8 * wv) * TP_ + mt * 128);
  const char* wsrc = (const char*)(w1f + (size_t)(nc * 4 + wv) * W1_STEPS * 3 * 64);
  const unsigned voff = lane * 16;
  const unsigned ldsw = sbase + wv * 2048;
  u32x4 W[G1_R][3];
  auto issue = [&](auto O) {
    constexpr int o = O.value, ks = o < 100 ? o / 5 : 20, p = o < 100 ? o % 5 : o - 98;
    if constexpr (p < 2) dma16<(ks % G1_SLOTS) * 8192 + p * 1024>(ldsw, xoff[p], xsrc + (size_t)ks * 32 * TP_ * 2);
    else gld128<(p - 2) * 1024>(W[ks % G1_R][p - 2], voff, wsrc + ks * 3072);
  };
  static_for<0, 5 * G1_PRE>(issue);
  const int kg = lane >> 4;
  unsigned xa[8];
#pragma unroll
  for (int jj = 0; jj < 8; ++jj) {
    const int rr = 4 * kg + ((lane & 15) >> 2);
    xa[jj] = sbase + rr * 256 + ((((jj + 2 * wv) & 7) ^ (rr & 7)) * 32) + (lane & 3) * 8;
    pin(xa[jj]);
  }
  u32x4 ones = {0x3f803f80u, 0x3f803f80u, 0x3f803f80u, 0x3f803f80u};
  pin(ones);
  f32x4 acc[8][3], gram[2], sum1[2];
  u32x2 X[G1_LA + 1][2];
  auto xread = [&](auto T, auto H) {
    constexpr int t = T.value, h = H.value;
    ds_tr<((t >> 3) % G1_SLOTS) * 8192 + h * 4096>(X[t % (G1_LA + 1)][h], xa[t & 7]);
  };
  wait_vm<5 * G1_PRE - 5>();
  bar();
  STAMP_GET(t1);
  static_for<0, G1_LA>([&](auto T) { xread(T, IC<0>{}); xread(T, IC<1>{}); });
  static_for<0, 160>([&](auto T) {
    constexpr int t = T.value, ks = t >> 3, jj = t & 7, tn = t + G1_LA;
    wait_lgkm<2 * ((tn <= 160 ? tn : 160) - t - 1)>();
    if constexpr (tn < 160 && (tn & 7) == 0) {
      wait_vm<g1_issued(t) - 5 * (tn / 8 + 1)>();
      bar();
    }
    const u32x4 xb = cat(X[t % (G1_LA + 1)][0], X[t % (G1_LA + 1)][1]);
    mfma_<ks == 0>(acc[jj][0], W[ks % G1_R][0], xb);
    if constexpr (tn < 160) xread(IC<tn>{}, IC<0>{});
    mfma_<ks == 0>(acc[jj][1], W[ks % G1_R][1], xb);
    if constexpr (tn < 160) xread(IC<tn>{}, IC<1>{});
    mfma_<ks == 0>(acc[jj][2], W[ks % G1_R][2], xb);
    static_for<g1_issued(t), g1_issued(t + 1)>(issue);
    if constexpr (jj < 2) { mfma_<ks == 0>(gram[jj], xb, xb); mfma_<ks == 0>(sum1[jj], ones, xb); }
  });
  STAMP_GET(t2);
  asm volatile("s_waitcnt vmcnt(0)\n\ts_nop 7" : "+v"(gram[0]), "+v"(gram[1]), "+v"(sum1[0]), "+v"(sum1[1]) :: "memory");
  f32x2* stat = (f32x2*)(smem + G1_STAT);
#pragma unroll
  for (int sj = 0; sj < 2; ++sj) {
    const int c3 = lane & 3;
    const float q = c3 == 0 ? gram[sj][0] : c3 == 1 ? gram[sj][1] : c3 == 2 ? gram[sj][2] : gram[sj][3];
    const float mean = sum1[sj][0] * (1.f / D_);
    const float var = fmaxf(q * (1.f / D_) - mean * mean, 0.f);
    const float sd = rbf(__builtin_sqrtf(rbf(rbf(var) + 1e-5f)));
    const float rs = 1.f / sd;
    if (((lane & 15) >> 2) == kg) stat[((sj + 2 * wv) & 7) * 16 + (lane & 15)] = f32x2{__uint_as_float(pack_bf16(-mean, sd)), rs * rs};
  }
  __syncthreads();
  u32* stage = (u32*)smem;
  f32x2 st[8];
#pragma unroll
  for (int jj = 0; jj < 8; ++jj) st[jj] = stat[((jj + 2 * wv) & 7) * 16 + (lane & 15)];
  u32x4 xc[8];
#pragma unroll
  for (int jj = 0; jj < 8; ++jj) {
    const u32 p = __float_as_uint(st[jj][0]);
    xc[jj] = u32x4{__builtin_amdgcn_perm(p, p, 0x01000100u), p, __builtin_amdgcn_perm(p, p, 0x03020302u), 0u};
    pin(xc[jj]);
  }
  asm volatile("s_nop 1");
#pragma unroll
  for (int jj = 0; jj < 8; ++jj)
#pragma unroll
    for (int i = 0; i < 3; ++i) mfma(acc[jj][i], W[20 % G1_R][i], xc[jj]);
  asm volatile("s_nop 7");
#pragma unroll
  for (int jj = 0; jj < 8; ++jj) {
    const int m16 = (jj + 2 * wv) & 7;
#pragma unroll
    for (int i = 0; i < 3; ++i) {
      pin(acc[jj][i]);
      const float g0 = __int_as_float(max(__float_as_int(acc[jj][i][0]), 0)), g1 = __int_as_float(max(__float_as_int(acc[jj][i][2]), 0));
      const int cI = 3 * wv + i;
      stage[(m16 * 3 + (cI >> 2)) * 256 + ((cI & 3) * 16 + (lane & 15)) * 4 + kg] =
          pack_bf16(g0 * st[jj][1] * acc[jj][i][1], g1 * st[jj][1] * acc[jj][i][3]);
    }
  }
  __syncthreads();
#pragma unroll
  for (int i = 0; i < 6; ++i) {
    const int m16 = wv * 2 + i / 3, f3 = i % 3;
    const int g16 = mt * 8 + m16;
    h2f[(size_t)(((g16 >> 2) * 48 + 3 * nc + f3) * 4 + (g16 & 3)) * 64 + lane] = *(const u32x4*)(stage + (m16 * 3 + f3) * 256 + lane * 4);
  }
  STAMP_END(2);
}

constexpr int G2_R = 4;
constexpr int G2_LDS = 84 * 1024;
__global__ __launch_bounds__(256, 1) void k_gemm2(const u32x4* __restrict__ h2f, const u32x4* __restrict__ w2f,
                                                  const u16* __restrict__ x2T, const float* __restrict__ modp,
                                                  u16* __restrict__ out) {
  __shared__ __attribute__((aligned(16))) char smem[G2_LDS];
  STAMP_DECL();
  const int tid = threadIdx.x, lane = tid & 63;
  const int wv = __builtin_amdgcn_readfirstlane(tid >> 6);
  const int xcd = blockIdx.x & 7, idx = blockIdx.x >> 3;
  const int M = (xcd >> 1) * 8 + (idx & 7), N = (xcd & 1) * 4 + (idx >> 3);
  const unsigned sbase = (unsigned)(size_t)smem;
  const char* hp = (const char*)(h2f + (size_t)(M * 48 + wv * 12) * 4 * 64);
  const char* wp = (const char*)(w2f + (size_t)(N * 48 + wv * 12) * 5 * 64) + 2048;
  unsigned hoff[4];
#pragma unroll
  for (int jj = 0; jj < 4; ++jj) hoff[jj] = ((jj + wv + 1) & 3) * 1024 + lane * 16;
  const unsigned voff = lane * 16;
  u32x4 H[G2_R][4], W[G2_R][5];
  auto issue = [&](auto O) {
    constexpr int o = O.value, ks = o / 9, p = o % 9;
    if constexpr (p < 5) gld128<(p - 2) * 1024>(W[ks % G2_R][p], voff, wp + ks * 5120);
    else gld128<0>(H[ks % G2_R][p - 5], hoff[p - 5], hp + ks * 4096);
  };
  static_for<0, 9 * (G2_R - 1)>(issue);
  constexpr int XR_OFF = 61440;
  {
    const char* xs = (const char*)(x2T + (size_t)(N * 80) * TP_ + M * 64);
    const unsigned xo = ((lane >> 3) * TP_ + (lane & 7) * 8) * 2;
    dma16<XR_OFF>(sbase + wv * 1024, xo, xs + (size_t)wv * 8 * TP_ * 2);
    dma16<XR_OFF + 4096>(sbase + wv * 1024, xo, xs + (size_t)(wv + 4) * 8 * TP_ * 2);
    dma16<XR_OFF + 8192>(sbase + (wv & 1) * 1024, xo, xs + (size_t)((wv & 1) + 8) * 8 * TP_ * 2);
  }
  u32 gp[MODP];
  {
    const unsigned go = (5 * D_ + N * 80 + (tid < 80 ? tid : 0)) * 4;
#pragma unroll
    for (int p = 0; p < MODP; ++p) gld32(gp[p], go, (const char*)(modp + p * NMOD));
  }
  f32x4 acc[4][5];
  static_for<0, 12>([&](auto KS) {
    constexpr int ks = KS.value;
    constexpr int issued = 9 * (ks + G2_R - 1 < 12 ? ks + G2_R - 1 : 12);
    wait_vm<issued - 9 * (ks + 1) + (ks < G2_R - 1 ? 3 + MODP : 0)>();
    if constexpr (ks == 0) STAMP_GET(t1);
    static_for<0, 20>([&](auto G) {
      constexpr int g = G.value, jj = g / 5, i = g % 5;
      mfma_<ks == 0>(acc[jj][i], W[ks % G2_R][i], H[ks % G2_R][jj]);
      if constexpr (ks + G2_R - 1 < 12 && (g & 1) == 0 && g / 2 < 9) issue(IC<9 * (ks + G2_R - 1) + g / 2>{});
    });
  });
  STAMP_GET(t2);
  const int kg = lane >> 4;
  float* gbl = (float*)(smem + XR_OFF + 10240);
  {
    float g = 0.f;
#pragma unroll
    for (int p = 0; p < MODP; ++p) { pin(gp[p]); g += __uint_as_float(gp[p]); }
    if (tid < 80) gbl[tid] = rbf(g);
  }
  asm volatile("s_nop 7");
  static_for<0, 15>([&](auto G) {
    constexpr int jj = G.value / 5, i = G.value % 5;
    ds_w128<(jj * 5 + i) * 1024>(sbase + ((jj + wv + 1) & 3) * 15360 + voff, acc[jj][i]);
  });
  wait_lgkm<0>();
  __syncthreads();
  const f32x4* part = (const f32x4*)(smem + wv * 15360);
  const unsigned xad = sbase + XR_OFF + (4 * kg + ((lane & 15) >> 2)) * 128 + wv * 32 + (lane & 3) * 8;
  u32x2 xw[5];
  static_for<0, 5>([&](auto I) { ds_tr<I.value * 2048>(xw[I.value], xad); });
  wait_lgkm<0>();
  const int pos = M * 64 + wv * 16 + (lane & 15);
  const int l8 = (pos >> 3) & 63;
  const int tok = 256 * (2 * (pos >> 9) + ((pos >> 2) & 1)) + 16 * (l8 & 15) + 4 * (l8 >> 4) + (pos & 3);
#pragma unroll
  for (int i = 0; i < 5; ++i) {
    pin(xw[i]); pin(acc[3][i]);
    f32x4 o = acc[3][i];
#pragma unroll
    for (int sl = 0; sl < 3; ++sl) o += part[(sl * 5 + i) * 64 + lane];
    const float x4[4] = {bflo(xw[i][0]), bfhi(xw[i][0]), bflo(xw[i][1]), bfhi(xw[i][1])};
    const f32x4 gb = *(const f32x4*)(gbl + i * 16 + 4 * kg);
    float r[4];
#pragma unroll
    for (int q = 0; q < 4; ++q) r[q] = fmaf(gb[q], o[q], x4[q]);
    *(u32x2*)(out + (size_t)tok * D_ + N * 80 + i * 16 + 4 * kg) = u32x2{pack_bf16(r[0], r[1]), pack_bf16(r[2], r[3])};
  }
  STAMP_END(3);
}

extern "C" void kernel_launch(void* const* d_in, const int* in_sizes, int n_in, void* d_out, int out_size,
                              void* d_ws, size_t ws_size, hipStream_t stream) {
  const u16* q = (const u16*)d_in[0];
  const u16* k = (const u16*)d_in[1];
  const u16* v = (const u16*)d_in[2];
  const u16* shk = (const u16*)d_in[3];
  const u16* ssmk = (const u16*)d_in[4];
  const u16* ssmD = (const u16*)d_in[5];
  const u16* wg = (const u16*)d_in[7];
  const u16* wu = (const u16*)d_in[8];
  const u16* wd = (const u16*)d_in[9];
  const u16* c = (const u16*)d_in[10];
  const u16* wmod = (const u16*)d_in[11];
  char* ws = (char*)d_ws;
  float* modp = (float*)ws; ws += MODP * NMOD * 4;
  float* st1 = (float*)ws; ws += 16384;
  u32x4* kvG = (u32x4*)ws; ws += (size_t)S_ * D_ * 2;
  u32x4* xG = (u32x4*)ws; ws += (size_t)S_ * D_ * 2;
  u16* x2T = (u16*)ws; ws += (size_t)TP_ * D_ * 2;
  u32x4* w1f = (u32x4*)ws; ws += (size_t)64 * W1_STEPS * 3 * 1024;
  u32x4* w2f = (u32x4*)ws; ws += (size_t)8 * 48 * 5 * 1024;
  u32x4* h2f = (u32x4*)ws; ws += (size_t)S_ * F_ * 2;
  k_prep<<<256, 512, 0, stream>>>(q, k, v, shk, c, wmod, kvG, xG, modp, st1);
  k_conv<<<256, 256, 0, stream>>>(kvG, xG, ssmk, ssmD, modp, st1, x2T, wg, wu, wd, w1f, w2f);
  k_gemm1<<<256, 256, 0, stream>>>(x2T, w1f, h2f);
  k_gemm2<<<256, 256, 0, stream>>>(h2f, w2f, x2T, modp, (u16*)d_out);
}
```

```cpp
#define PF_CODE
#include <hip/hip_runtime.h>
#include <stdint.h>

typedef unsigned short u16;
typedef unsigned int u32;
typedef __attribute__((ext_vector_type(2))) __bf16 bf16x2;
typedef __attribute__((ext_vector_type(4))) float f32x4;
typedef __attribute__((ext_vector_type(2))) float f32x2;
typedef __attribute__((ext_vector_type(4))) u32 u32x4;
typedef __attribute__((ext_vector_type(2))) u32 u32x2;

constexpr int S_ = 2048, D_ = 640, F_ = 1536;
constexpr int TP_ = S_ + 64;
constexpr int NMOD = 6 * D_;
constexpr int MODP = 8;
constexpr int CWG = 160;

__device__ __forceinline__ float bflo(u32 w) { return __uint_as_float(w << 16); }
__device__ __forceinline__ float bfhi(u32 w) { return __uint_as_float(w & 0xffff0000u); }
__device__ __forceinline__ u32 pack_bf16(float lo, float hi) {
  bf16x2 v; v[0] = (__bf16)lo; v[1] = (__bf16)hi; return __builtin_bit_cast(u32, v);
}
__device__ __forceinline__ float rbf(float x) { __bf16 b = (__bf16)x; return (float)b; }
#define SCHED() __builtin_amdgcn_sched_barrier(0)
template <int V> struct IC { static constexpr int value = V; };
template <int B, int E, class Fn> __device__ __forceinline__ void static_for(Fn&& f) {
  if constexpr (B < E) { f(IC<B>{}); static_for<B + 1, E>(f); }
}
__device__ __forceinline__ void mfma(f32x4& c, u32x4 a, u32x4 b) {
  asm volatile("v_mfma_f32_16x16x32_bf16 %0, %1, %2, %0" : "+v"(c) : "v"(a), "v"(b));
}
__device__ __forceinline__ void mfma0(f32x4& c, u32x4 a, u32x4 b) {
  asm volatile("v_mfma_f32_16x16x32_bf16 %0, %1, %2, 0" : "=&v"(c) : "v"(a), "v"(b));
}
template <bool FIRST> __device__ __forceinline__ void mfma_(f32x4& c, u32x4 a, u32x4 b) { if constexpr (FIRST) mfma0(c, a, b); else mfma(c, a, b); }
__device__ __forceinline__ u32 alignbit(u32 hi, u32 lo, u32 sh) {
  u32 d; asm volatile("v_alignbit_b32 %0, %1, %2, %3" : "=v"(d) : "v"(hi), "v"(lo), "v"(sh)); return d;
}
template <int OFF> __device__ __forceinline__ void ds_tr(u32x2& dst, unsigned addr) {
  asm volatile("ds_read_b64_tr_b16 %0, %1 offset:%2" : "=v"(dst) : "v"(addr), "i"(OFF) : "memory");
}
template <int OFF> __device__ __forceinline__ void ds_b32(u32& dst, unsigned addr) {
  asm volatile("ds_read_b32 %0, %1 offset:%2" : "=v"(dst) : "v"(addr), "i"(OFF) : "memory");
}
template <int OFF> __device__ __forceinline__ void ds_b64(u32x2& dst, unsigned addr) {
  asm volatile("ds_read_b64 %0, %1 offset:%2" : "=v"(dst) : "v"(addr), "i"(OFF) : "memory");
}
template <int OFF> __device__ __forceinline__ void ds_b128(u32x4& dst, unsigned addr) {
  asm volatile("ds_read_b128 %0, %1 offset:%2" : "=v"(dst) : "v"(addr), "i"(OFF) : "memory");
}
template <int OFF> __device__ __forceinline__ void ds_w128(unsigned addr, f32x4 v) {
  asm volatile("ds_write_b128 %0, %1 offset:%2" :: "v"(addr), "v"(v), "i"(OFF) : "memory");
}
template <int OFF> __device__ __forceinline__ void gld128(u32x4& dst, unsigned voff, const char* sb) {
  asm volatile("global_load_dwordx4 %0, %1, %2 offset:%3" : "=v"(dst) : "v"(voff), "s"(sb), "i"(OFF) : "memory");
}
__device__ __forceinline__ void gld32(u32& dst, unsigned voff, const char* sb) {
  asm volatile("global_load_dword %0, %1, %2" : "=v"(dst) : "v"(voff), "s"(sb) : "memory");
}
template <int LOFF> __device__ __forceinline__ void dma16(unsigned ldsb, unsigned voff, const char* sb) {
  asm volatile("s_add_u32 m0, %0, %3\n\ts_nop 0\n\tglobal_load_lds_dwordx4 %1, %2" :: "s"(ldsb), "v"(voff), "s"(sb), "i"(LOFF) : "memory", "scc");
}
template <int LOFF> __device__ __forceinline__ void dma16x4(unsigned ldsb, unsigned v0, unsigned v1, const char* sb) {
  asm volatile("s_add_u32 m0, %0, %4\n\ts_nop 0\n\tglobal_load_lds_dwordx4 %1, %3\n\tglobal_load_lds_dwordx4 %2, %3 offset:1024\n\t"
               "global_load_lds_dwordx4 %1, %3 offset:2048\n\tglobal_load_lds_dwordx4 %2, %3 offset:3072"
               :: "s"(ldsb), "v"(v0), "v"(v1), "s"(sb), "i"(LOFF) : "memory", "scc");
}
__device__ __forceinline__ void sld32(u32& dst, const void* p) { asm volatile("s_load_dword %0, %1, 0x0" : "=s"(dst) : "s"(p) : "memory"); }
template <int N> __device__ __forceinline__ void wait_vm() { asm volatile("s_waitcnt vmcnt(%0)" :: "i"(N) : "memory"); }
template <int N> __device__ __forceinline__ void wait_lgkm() { asm volatile("s_waitcnt lgkmcnt(%0)" :: "i"(N) : "memory"); }
__device__ __forceinline__ void bar() { asm volatile("s_barrier" ::: "memory"); }
template <class T> __device__ __forceinline__ void pin(T& a) { asm volatile("" : "+v"(a)); }
__device__ __forceinline__ u32x4 cat(u32x2 a, u32x2 b) { return u32x4{a[0], a[1], b[0], b[1]}; }

typedef __amdgpu_buffer_rsrc_t rsrc_t;
__device__ __forceinline__ rsrc_t mk_rsrc(const void* p, int bytes) { return __builtin_amdgcn_make_buffer_rsrc((void*)p, 0, bytes, 0x00020000); }
template <int CTRL> __device__ __forceinline__ float dpp_add(float x) {
  return x + __builtin_bit_cast(float, __builtin_amdgcn_mov_dpp(__builtin_bit_cast(int, x), CTRL, 0xf, 0xf, true));
}
constexpr int DPP_XOR1 = 0xB1, DPP_XOR2 = 0x4E, DPP_HMIRROR = 0x141, DPP_ROR8 = 0x128;
__device__ __forceinline__ float sum8(float x) { return dpp_add<DPP_HMIRROR>(dpp_add<DPP_XOR2>(dpp_add<DPP_XOR1>(x))); }
__device__ __forceinline__ float tred32(float a, float b) {
  auto r = __builtin_amdgcn_permlane32_swap(__float_as_uint(a), __float_as_uint(b), false, false);
  return __uint_as_float(r[0]) + __uint_as_float(r[1]);
}
__device__ __forceinline__ float tred16(float a, float b) {
  auto r = __builtin_amdgcn_permlane16_swap(__float_as_uint(a), __float_as_uint(b), false, false);
  return __uint_as_float(r[0]) + __uint_as_float(r[1]);
}
__device__ __forceinline__ float rdl(float x, int l) { return __int_as_float(__builtin_amdgcn_readlane(__float_as_int(x), l)); }
#define WAUX 16

#define STAMP_GET(V)
#define STAMP_DECL()
#define STAMP_END(K)

__device__ __forceinline__ float modv(const float* __restrict__ modp, int i) {
  float s = modp[i];
#pragma unroll
  for (int p = 1; p < MODP; ++p) s += modp[p * NMOD + i];
  return rbf(s);
}

__global__ __launch_bounds__(512) void k_prep(const u16* __restrict__ xIn, const u16* __restrict__ kIn, const u16* __restrict__ vIn,
                                              const u16* __restrict__ shk, const u16* __restrict__ cIn,
                                              const u16* __restrict__ wmod,
                                              u32x4* __restrict__ kvG, u32x4* __restrict__ xG,
                                              float* __restrict__ modp, float* __restrict__ st1,
                                              const char* t0p, const char* t1p, const char* t2p, const char* t3p, const char* t4p) {
  STAMP_DECL();
  asm volatile("" :: "s"(xIn), "s"(kIn), "s"(vIn), "s"(shk), "s"(cIn), "s"(wmod), "s"(kvG), "s"(xG), "s"(modp), "s"(st1));
  const int tid = threadIdx.x, lane = tid & 63;
  const int wv = __builtin_amdgcn_readfirstlane(tid >> 6);
  const int b = blockIdx.x;
  const int r8 = lane >> 3, cp = lane & 7;
  if (wv < 5) {
    const rsrc_t rk = mk_rsrc(kIn, S_ * D_ * 2), rv = mk_rsrc(vIn, S_ * D_ * 2), rx = mk_rsrc(xIn, S_ * D_ * 2);
    const int vo = tid * 4, so = b * 8 * 1280;
    const int sh0 = b ? so - 3 * 1280 : 0;
    u32 kk[11], vv[8], xx[8];
#pragma unroll
    for (int j = 0; j < 3; ++j) kk[j] = __builtin_amdgcn_raw_buffer_load_b32(rk, vo + j * 1280, sh0, 0);
#pragma unroll
    for (int j = 0; j < 8; ++j) kk[3 + j] = __builtin_amdgcn_raw_buffer_load_b32(rk, vo + (j & 3) * 1280, so + (j >> 2) * 5120, 0);
    const u32x4 taps = *(const u32x4*)(shk + tid * 8);
#pragma unroll
    for (int j = 0; j < 8; ++j) vv[j] = __builtin_amdgcn_raw_buffer_load_b32(rv, vo + (j & 3) * 1280, so + (j >> 2) * 5120, 0);
#pragma unroll
    for (int j = 0; j < 8; ++j) xx[j] = __builtin_amdgcn_raw_buffer_load_b32(rx, vo + (j & 3) * 1280, so + (j >> 2) * 5120, 0);
    SCHED();
    if (b == 0) { kk[0] = 0; kk[1] = 0; kk[2] = 0; }
    const f32x2 w0 = {bflo(taps[0]), bflo(taps[2])}, w1 = {bfhi(taps[0]), bfhi(taps[2])};
    const f32x2 w2 = {bflo(taps[1]), bflo(taps[3])}, w3 = {bfhi(taps[1]), bfhi(taps[3])};
    const int ob = ((tid >> 2) * 512 + b) * 4 + (tid & 3);
    f32x2 kv[8];
#pragma unroll
    for (int j = 0; j < 8; ++j) {
      f32x2 a = f32x2{bflo(kk[j]), bfhi(kk[j])} * w3;
      a = __builtin_elementwise_fma(f32x2{bflo(kk[j + 1]), bfhi(kk[j + 1])}, w2, a);
      a = __builtin_elementwise_fma(f32x2{bflo(kk[j + 2]), bfhi(kk[j + 2])}, w1, a);
      a = __builtin_elementwise_fma(f32x2{bflo(kk[j + 3]), bfhi(kk[j + 3])}, w0, a);
      kv[j] = a * f32x2{bflo(vv[j]), bfhi(vv[j])};
    }
    u32x4 o0, o1, p0, p1;
#pragma unroll
    for (int t = 0; t < 4; ++t) {
      o0[t] = pack_bf16(kv[7 - 2 * t][0], kv[6 - 2 * t][0]);
      o1[t] = pack_bf16(kv[7 - 2 * t][1], kv[6 - 2 * t][1]);
      p0[t] = __builtin_amdgcn_perm(xx[2 * t + 1], xx[2 * t], 0x05040100u);
      p1[t] = __builtin_amdgcn_perm(xx[2 * t + 1], xx[2 * t], 0x07060302u);
    }
    kvG[ob] = o0; kvG[ob + 1024] = o1;
    xG[ob] = p0; xG[ob + 1024] = p1;
  } else if (wv == 5) {
    u32x4 xv[10];
#pragma unroll
    for (int i = 0; i < 10; ++i) xv[i] = *(const u32x4*)(xIn + (size_t)(b * 8 + r8) * D_ + i * 64 + cp * 8);
    SCHED();
    const bf16x2 ones = {(__bf16)1.0f, (__bf16)1.0f};
    float s1[2] = {0.f, 0.f}, s2[2] = {0.f, 0.f};
#pragma unroll
    for (int i = 0; i < 10; ++i)
#pragma unroll
      for (int q = 0; q < 4; ++q) {
        const u32 raw = xv[i][q];
        const bf16x2 v = __builtin_bit_cast(bf16x2, raw);
        s1[q & 1] = __builtin_amdgcn_fdot2_f32_bf16(v, ones, s1[q & 1], false);
        s2[q & 1] = __builtin_amdgcn_fdot2_f32_bf16(v, v, s2[q & 1], false);
      }
    const float mean = sum8(s1[0] + s1[1]) * (1.f / D_);
    const float var = fmaxf(sum8(s2[0] + s2[1]) * (1.f / D_) - mean * mean, 0.f);
    const float sd = rbf(__builtin_sqrtf(rbf(rbf(var) + 1e-5f)));
    const float rs = 1.f / sd;
    const int s = b * 8 + r8;
    const int nt = s >> 8, col = (s >> 4) & 15, kg = (s >> 2) & 3, r = s & 3;
    if (cp == 0) *(f32x2*)(st1 + ((nt * 2 + (r >> 1)) * 64 + kg * 16 + col) * 4 + (r & 1) * 2) = f32x2{rs, -rbf(mean) * rs};
  } else if (b < 240) {
    const int gcol = b >> 2, p = (b & 3) * 2 + (wv - 6);
    const rsrc_t rw = mk_rsrc(wmod, D_ * NMOD * 2);
    u32 cv[10]; u32x4 wm[10];
#pragma unroll
    for (int it = 0; it < 10; ++it) cv[it] = cIn[p * 80 + it * 8 + r8];
#pragma unroll
    for (int it = 0; it < 10; ++it) wm[it] = __builtin_amdgcn_raw_buffer_load_b128(rw, r8 * (NMOD * 2) + cp * 16, (p * 80 + it * 8) * (NMOD * 2) + gcol * 128, 0);
    SCHED();
    f32x2 acc[4];
#pragma unroll
    for (int it = 0; it < 10; ++it) {
      const float x = bflo(cv[it]);
      const float s = rbf(x * __builtin_amdgcn_rcpf(1.f + __expf(-x)));
      const f32x2 s2 = {s, s};
#pragma unroll
      for (int q = 0; q < 4; ++q) {
        const f32x2 w2 = {bflo(wm[it][q]), bfhi(wm[it][q])};
        acc[q] = it ? __builtin_elementwise_fma(s2, w2, acc[q]) : s2 * w2;
      }
    }
    const float a0 = tred16(tred32(acc[0][0], acc[2][0]), tred32(acc[1][0], acc[3][0]));
    const float a1 = tred16(tred32(acc[0][1], acc[2][1]), tred32(acc[1][1], acc[3][1]));
    const float e0 = dpp_add<DPP_ROR8>(a0), e1 = dpp_add<DPP_ROR8>(a1);
    modp[p * NMOD + gcol * 64 + cp * 8 + r8] = (lane & 8) ? e1 : e0;
  } else {
    u32 acc = 0;
    if (wv == 6) {
      uint64_t pc; asm volatile("s_getpc_b64 %0" : "=s"(pc));
      const char* cb = (const char*)(pc & ~(uint64_t)255) + ((b >> 3) & 1) * 32768;
#pragma unroll
      for (int i = 0; i < 4; ++i) acc += *(const u32*)(cb + (i * 64 + lane) * 128);
    }
    asm volatile("" :: "v"(acc));
  }
  STAMP_END(0);
}

constexpr int FC_BYTES = 2080 * 2;
constexpr int CH_BYTES = 2 * FC_BYTES;
constexpr int CKV_OFF = 33280;
constexpr int CX_OFF = CKV_OFF + 2048 + 16384, CS_OFF = CX_OFF + 16384;
constexpr int K1_LDS = CS_OFF + 16384;
constexpr int W1_STEPS = 21;
constexpr int conv_gap(int a, int b) { int n = 0; for (int i = 0; i < a; ++i) n += 8 - i; return n + b; }
__global__ __launch_bounds__(256, 1) void k_conv(const u32x4* __restrict__ kvG, const u32x4* __restrict__ xG, const u16* __restrict__ ssmk,
                                                 const u16* __restrict__ ssmD, const float* __restrict__ modp,
                                                 const float* __restrict__ st1, u16* __restrict__ x2T,
                                                 const u16* __restrict__ wg, const u16* __restrict__ wu, const u16* __restrict__ wd,
                                                 u32x4* __restrict__ w1f, u32x4* __restrict__ w2f) {
  __shared__ __attribute__((aligned(16))) char smem[K1_LDS];
  STAMP_DECL();
  asm volatile("" :: "s"(kvG), "s"(xG), "s"(ssmk), "s"(ssmD), "s"(modp), "s"(st1), "s"(x2T), "s"(wg), "s"(wu), "s"(wd), "s"(w1f), "s"(w2f));
  const int tid = threadIdx.x, lane = tid & 63;
  const int wv = __builtin_amdgcn_readfirstlane(tid >> 6);
  const unsigned sbase = (unsigned)(size_t)smem;
  if (__builtin_expect(blockIdx.x < CWG, 1)) {
    const int jb = blockIdx.x;
    const int d = (jb >> 1) * 8 + (jb & 1) + 2 * wv;
    const int row = lane & 15, kg = lane >> 4;
    const unsigned l16 = lane * 16;
    {
      const unsigned ko0 = (lane ^ (lane >> 4)) * 16;
      dma16x4<CKV_OFF + 2048>(sbase + wv * 4096, ko0, ko0 ^ 64, (const char*)(kvG + (size_t)jb * 1024) + wv * 4096);
      const char* fs = (const char*)(ssmk + (size_t)d * S_);
      dma16x4<32>(sbase + wv * CH_BYTES, l16, l16, fs);
      dma16x4<FC_BYTES + 28>(sbase + wv * CH_BYTES, l16, l16, fs);
      dma16x4<CX_OFF>(sbase + wv * 4096, l16, l16, (const char*)(xG + (size_t)jb * 1024) + wv * 4096);
      dma16x4<CS_OFF>(sbase + wv * 4096, l16, l16, (const char*)st1 + wv * 4096);
    }
    u32 mraw, draw;
    gld32(mraw, ((lane & 7) * NMOD + ((lane >> 3) & 3) * D_) * 4, (const char*)(modp + d));
    sld32(draw, (const u32*)ssmD + (d >> 1));
    *(u32x2*)(smem + CKV_OFF + tid * 8) = u32x2{0, 0};
    if (lane < 15) *(u32*)(smem + wv * CH_BYTES + (lane >> 3) * FC_BYTES + (lane & 7) * 4) = 0;
    const int rho = row + 1;
    u32 sh = (rho & 1) * 16;
    unsigned aAddr = sbase + wv * CH_BYTES + ((rho >> 1) & 1) * FC_BYTES + 2 * (64 * kg + (rho & ~3));
    pin(sh); pin(aAddr);
    unsigned bA[8];
#pragma unroll
    for (int e = 0; e < 8; ++e) { const int g = 32 + 2 * row + 1 - 8 * kg - e; bA[e] = sbase + CKV_OFF + 16 * ((4 * g + wv) ^ ((g >> 2) & 7)); pin(bA[e]); }
    wait_vm<9>();
    wait_lgkm<0>();
    bar();
    STAMP_GET(t1);
    f32x4 acc[8];
    u32x2 ra[2][8][2]; u32x4 rb[2][8]; u32 carry[8]; u32x4 A[8];
    auto load_n = [&](auto E, auto N) {
      constexpr int e = E.value, n = N.value, q = n / 3, bf = e & 1;
      if constexpr (n % 3 == 0) ds_b64<512 * q + 16 * e>(ra[bf][q][0], aAddr);
      else if constexpr (n % 3 == 1) ds_b64<512 * q + 16 * e + 8>(ra[bf][q][1], aAddr);
      else ds_b128<2048 * q>(rb[bf][q], bA[e]);
    };
    auto build = [&](auto E, auto AA, auto I) {
      constexpr int e = E.value, a = AA.value, i = I.value, bf = e & 1;
      if constexpr (i == 0) A[a][0] = alignbit(ra[bf][a][0][1], ra[bf][a][0][0], sh);
      else if constexpr (i == 1) A[a][1] = alignbit(ra[bf][a][1][0], ra[bf][a][0][1], sh);
      else if constexpr (i == 2) A[a][2] = alignbit(ra[bf][a][1][1], ra[bf][a][1][0], sh);
      else { A[a][3] = alignbit(carry[a], ra[bf][a][1][1], sh); carry[a] = ra[bf][a][0][0]; }
    };
    static_for<0, 8>([&](auto I) { ds_b32<512 * I.value + 128>(carry[I.value], aAddr); });
    static_for<0, 24>([&](auto N) { load_n(IC<7>{}, N); });
    wait_lgkm<0>();
    static_for<0, 4>([&](auto I) { build(IC<7>{}, IC<0>{}, I); });
    asm volatile("s_nop 1");
    static_for<0, 8>([&](auto EE) {
      constexpr int e = 7 - EE.value, bf = e & 1;
      const u32x4 A0 = A[0];
      static_for<0, 8>([&](auto AA) {
        constexpr int a = AA.value;
        static_for<0, 8 - a>([&](auto BB) {
          constexpr int bb = BB.value, n = conv_gap(a, bb);
          mfma_<(e == 7 && a == 0)>(acc[a + bb], a == 0 ? A0 : A[a], rb[bf][bb]);
          if constexpr (e > 0 && n < 24) load_n(IC<(e > 0 ? e - 1 : 0)>{}, IC<n>{});
          if constexpr (n < 28) build(IC<e>{}, IC<1 + n / 4>{}, IC<n % 4>{});
          if constexpr (e > 0 && n == 30) wait_lgkm<0>();
          if constexpr (e > 0 && (n == 31 || n == 32)) { build(IC<(e > 0 ? e - 1 : 0)>{}, IC<0>{}, IC<2 * (n - 31)>{}); build(IC<(e > 0 ? e - 1 : 0)>{}, IC<0>{}, IC<2 * (n - 31) + 1>{}); }
        });
      });
    });
    STAMP_GET(t2);
    asm volatile("s_waitcnt vmcnt(0)\n\ts_nop 7\n\ts_barrier" : "+v"(acc[0]), "+v"(acc[1]), "+v"(acc[2]), "+v"(acc[3]), "+v"(acc[4]), "+v"(acc[5]), "+v"(acc[6]), "+v"(acc[7]), "+v"(mraw), "+s"(draw) :: "memory");
    const float mv = rbf(sum8(__uint_as_float(mraw)));
    const float m_ga = rdl(mv, 16), c0 = rdl(mv, 0) * m_ga, c1 = rbf(1.f + rdl(mv, 8)) * m_ga;
    const float Dv = (d & 1) ? bfhi(draw) : bflo(draw);
#pragma unroll
    for (int np = 0; np < 4; ++np) {
      u32x4 o;
#pragma unroll
      for (int u = 0; u < 2; ++u) {
        const int nt = 2 * np + u;
        const int tg = 32 * nt + 2 * row + (kg >> 1), g = 32 + tg;
        const u32x2 kvw = *(const u32x2*)(smem + CKV_OFF + 16 * ((4 * g + wv) ^ ((g >> 2) & 7)) + ((kg & 1) ? 0 : 8));
        const u32x2 xw = *(const u32x2*)(smem + CX_OFF + tg * 64 + wv * 16 + (kg & 1) * 8);
        const f32x4 sA = *(const f32x4*)(smem + CS_OFF + (nt * 2) * 1024 + lane * 16);
        const f32x4 sB = *(const f32x4*)(smem + CS_OFF + (nt * 2 + 1) * 1024 + lane * 16);
        const float kv4[4] = {bfhi(kvw[1]), bflo(kvw[1]), bfhi(kvw[0]), bflo(kvw[0])};
        const float x4[4] = {bflo(xw[0]), bfhi(xw[0]), bflo(xw[1]), bfhi(xw[1])};
        const float rs4[4] = {sA[0], sA[2], sB[0], sB[2]};
        const float nb4[4] = {sA[1], sA[3], sB[1], sB[3]};
        float r4[4];
#pragma unroll
        for (int r = 0; r < 4; ++r) {
          const float yb = fmaf(kv4[r], Dv, acc[nt][r]);
          const float n1 = fmaf(x4[r], rs4[r], nb4[r]);
          const float h = fmaf(n1, c1, c0);
          r4[r] = fmaf(h, yb, x4[r]);
        }
        o[2 * u] = pack_bf16(r4[0], r4[1]); o[2 * u + 1] = pack_bf16(r4[2], r4[3]);
      }
      *(u32x4*)(x2T + (size_t)d * TP_ + 512 * np + 8 * lane) = o;
    }
    STAMP_END(1);
    return;
  }
  const int j = blockIdx.x - CWG;
  const int ug = j % 24, kq = j / 24;
  const int r = tid >> 3, cc = tid & 7;
  u32x4 gw[5], uw[5], dw[5];
  float* LM = (float*)(smem + 160 * 256);
  float* LR = LM + 320;
  char* L2 = smem + 49152;
  const int ks2 = j >> 1, hf = j & 1;
  const rsrc_t r1 = mk_rsrc(w1f, 64 * W1_STEPS * 3 * 1024), r2 = mk_rsrc(w2f, 8 * 48 * 5 * 1024);
  float mc = 0.f, ms = 0.f;
  if (tid < 160) { ms = modv(modp, 3 * D_ + kq * 160 + tid); mc = rbf(1.f + modv(modp, 4 * D_ + kq * 160 + tid)); }
#pragma unroll
  for (int it = 0; it < 5; ++it) {
    const int k = kq * 160 + it * 32 + r;
    gw[it] = *(const u32x4*)(wg + (size_t)k * F_ + ug * 64 + cc * 8);
    uw[it] = *(const u32x4*)(wu + (size_t)k * F_ + ug * 64 + cc * 8);
  }
#pragma unroll
  for (int it = 0; it < 5; ++it) dw[it] = *(const u32x4*)(wd + (size_t)(ks2 * 32 + r) * D_ + hf * 320 + (it * 8 + cc) * 8);
  if (tid < 160) *(f32x2*)(LM + 2 * tid) = f32x2{mc, ms};
  __syncthreads();
  float su[16], sw[16];
#pragma unroll
  for (int i = 0; i < 16; ++i) { su[i] = 0.f; sw[i] = 0.f; }
#pragma unroll
  for (int it = 0; it < 5; ++it) {
    const f32x2 m2 = *(const f32x2*)(LM + 2 * (it * 32 + r));
    u32 o[8];
#pragma unroll
    for (int q = 0; q < 4; ++q) {
      const float g0 = bflo(gw[it][q]), g1 = bfhi(gw[it][q]), u0 = bflo(uw[it][q]), u1 = bfhi(uw[it][q]);
      o[2 * q] = pack_bf16(g0 * m2[0], u0 * m2[0]);
      o[2 * q + 1] = pack_bf16(g1 * m2[0], u1 * m2[0]);
      su[4 * q + 0] += bflo(o[2 * q]); su[4 * q + 1] += bfhi(o[2 * q]);
      su[4 * q + 2] += bflo(o[2 * q + 1]); su[4 * q + 3] += bfhi(o[2 * q + 1]);
      sw[4 * q + 0] = fmaf(g0, m2[1], sw[4 * q + 0]); sw[4 * q + 1] = fmaf(u0, m2[1], sw[4 * q + 1]);
      sw[4 * q + 2] = fmaf(g1, m2[1], sw[4 * q + 2]); sw[4 * q + 3] = fmaf(u1, m2[1], sw[4 * q + 3]);
    }
    char* dst = smem + (it * 32 + r) * 256 + ((cc ^ (r & 7)) * 32);
    *(u32x4*)(dst) = u32x4{o[0], o[1], o[2], o[3]};
    *(u32x4*)(dst + 16) = u32x4{o[4], o[5], o[6], o[7]};
  }
#pragma unroll
  for (int it = 0; it < 5; ++it) *(u32x4*)(L2 + r * 672 + (it * 8 + cc) * 16) = dw[it];
  float v8[8];
#pragma unroll
  for (int i = 0; i < 8; ++i) v8[i] = dpp_add<DPP_ROR8>(tred16(tred32(su[i], sw[i]), tred32(su[i + 8], sw[i + 8])));
  if (!(lane & 8)) {
    *(f32x4*)(LR + (wv * 8 + cc) * 32 + (lane >> 4) * 8) = f32x4{v8[0], v8[1], v8[2], v8[3]};
    *(f32x4*)(LR + (wv * 8 + cc) * 32 + (lane >> 4) * 8 + 4) = f32x4{v8[4], v8[5], v8[6], v8[7]};
  }
  STAMP_GET(t1);
  __syncthreads();
  STAMP_GET(t2);
  {
    const int rr = 4 * (lane >> 4) + ((lane & 15) >> 2);
    u32x2 lo[10], hi[10];
    unsigned ad[2];
#pragma unroll
    for (int t = 0; t < 2; ++t) ad[t] = sbase + rr * 256 + (((wv * 2 + t) ^ (rr & 7)) * 32) + (lane & 3) * 8;
    static_for<0, 10>([&](auto I) { constexpr int i = I.value; ds_tr<(i >> 1) * 8192>(lo[i], ad[i & 1]); ds_tr<(i >> 1) * 8192 + 4096>(hi[i], ad[i & 1]); });
    wait_lgkm<0>();
#pragma unroll
    for (int i = 0; i < 10; ++i) { pin(lo[i]); pin(hi[i]); }
#pragma unroll
    for (int i = 0; i < 10; ++i) {
      const int T = ug * 8 + wv * 2 + (i & 1);
      const int sw3 = T / 3, ti = T - sw3 * 3;
      __builtin_amdgcn_raw_buffer_store_b128(cat(lo[i], hi[i]), r1, lane * 16, ((sw3 * W1_STEPS + kq * 5 + (i >> 1)) * 3 + ti) * 1024, WAUX);
    }
  }
  {
    const unsigned ad = sbase + 49152 + (8 * (lane >> 4) + ((lane & 15) >> 2)) * 672 + wv * 160 + (lane & 3) * 8;
    u32x2 lo[5], hi[5];
    static_for<0, 5>([&](auto I) { ds_tr<I.value * 32>(lo[I.value], ad); ds_tr<I.value * 32 + 4 * 672>(hi[I.value], ad); });
    wait_lgkm<0>();
#pragma unroll
    for (int i = 0; i < 5; ++i) { pin(lo[i]); pin(hi[i]); }
#pragma unroll
    for (int i = 0; i < 5; ++i) __builtin_amdgcn_raw_buffer_store_b128(cat(lo[i], hi[i]), r2, lane * 16, (((hf * 4 + wv) * 48 + ks2) * 5 + i) * 1024, WAUX);
  }
  if (tid < 128) {
    const int tl = tid >> 4, rho = tid & 15;
    float U = 0.f, W = 0.f;
#pragma unroll
    for (int w4 = 0; w4 < 4; ++w4) { U += LR[(w4 * 8 + tl) * 32 + rho]; W += LR[(w4 * 8 + tl) * 32 + 16 + rho]; }
    const float u1 = rbf(U), u2 = rbf(U - u1), u3 = U - u1 - u2;
    const float w1 = rbf(W), w2 = rbf(W - w1), w3 = W - w1 - w2;
    const int T = ug * 8 + tl;
    const int sw3 = T / 3, ti = T - sw3 * 3;
    w1f[((size_t)(sw3 * W1_STEPS + 20) * 3 + ti) * 64 + kq * 16 + rho] = u32x4{pack_bf16(u1, u2), pack_bf16(u3, w1), pack_bf16(w2, w3), 0u};
  }
  STAMP_END(1);
}

constexpr int G1_R = 6;
constexpr int G1_PRE = 3;
constexpr int G1_SLOTS = 8;
constexpr int G1_LA = 4;
constexpr int G1_STAT = G1_SLOTS * 8192;
constexpr int G1_LDS = 84 * 1024;
constexpr int g1_issued(int t) {
  int n = 5 * G1_PRE;
  for (int i = 0; i < t; ++i) {
    const int ks = i >> 3;
    const int lim = (ks + G1_R - 1 < 20) ? 5 * (ks + G1_R) : 103;
    if (n < lim && n < 103) ++n;
  }
  return n;
}
__global__ __launch_bounds__(256, 1) void k_gemm1(const u16* __restrict__ x2T, const u32x4* __restrict__ w1f, u32x4* __restrict__ h2f) {
  __shared__ __attribute__((aligned(16))) char smem[G1_LDS];
  STAMP_DECL();
  const int tid = threadIdx.x, lane = tid & 63;
  const int wv = __builtin_amdgcn_readfirstlane(tid >> 6);
  const int xcd = blockIdx.x & 7, idx = blockIdx.x >> 3;
  const int mt = (xcd & 1) * 8 + (idx & 7), nc = (xcd >> 1) * 4 + (idx >> 3);
  const unsigned sbase = (unsigned)(size_t)smem;
  unsigned xoff[2];
#pragma unroll
  for (int p = 0; p < 2; ++p) {
    const int k = 4 * p + (lane >> 4);
    xoff[p] = (k * TP_ + (((lane & 15) ^ (2 * k)) * 8)) * 2;
  }
  const char* xsrc = (const char*)(x2T + (size_t)(8 * wv) * TP_ + mt * 128);
  const char* wsrc = (const char*)(w1f + (size_t)(nc * 4 + wv) * W1_STEPS * 3 * 64);
  const unsigned voff = lane * 16;
  const unsigned ldsw = sbase + wv * 2048;
  u32x4 W[G1_R][3];
  auto issue = [&](auto O) {
    constexpr int o = O.value, ks = o < 100 ? o / 5 : 20, p = o < 100 ? o % 5 : o - 98;
    if constexpr (p < 2) dma16<(ks % G1_SLOTS) * 8192 + p * 1024>(ldsw, xoff[p], xsrc + (size_t)ks * 32 * TP_ * 2);
    else gld128<(p - 2) * 1024>(W[ks % G1_R][p - 2], voff, wsrc + ks * 3072);
  };
  static_for<0, 5 * G1_PRE>(issue);
  const int kg = lane >> 4;
  unsigned xa[8];
#pragma unroll
  for (int jj = 0; jj < 8; ++jj) {
    const int rr = 4 * kg + ((lane & 15) >> 2);
    xa[jj] = sbase + rr * 256 + ((((jj + 2 * wv) & 7) ^ (rr & 7)) * 32) + (lane & 3) * 8;
    pin(xa[jj]);
  }
  u32x4 ones = {0x3f803f80u, 0x3f803f80u, 0x3f803f80u, 0x3f803f80u};
  pin(ones);
  f32x4 acc[8][3], gram[2], sum1[2];
  u32x2 X[G1_LA + 1][2];
  auto xread = [&](auto T, auto H) {
    constexpr int t = T.value, h = H.value;
    ds_tr<((t >> 3) % G1_SLOTS) * 8192 + h * 4096>(X[t % (G1_LA + 1)][h], xa[t & 7]);
  };
  wait_vm<5 * G1_PRE - 5>();
  bar();
  STAMP_GET(t1);
  static_for<0, G1_LA>([&](auto T) { xread(T, IC<0>{}); xread(T, IC<1>{}); });
  static_for<0, 160>([&](auto T) {
    constexpr int t = T.value, ks = t >> 3, jj = t & 7, tn = t + G1_LA;
    wait_lgkm<2 * ((tn <= 160 ? tn : 160) - t - 1)>();
    if constexpr (tn < 160 && (tn & 7) == 0) {
      wait_vm<g1_issued(t) - 5 * (tn / 8 + 1)>();
      bar();
    }
    const u32x4 xb = cat(X[t % (G1_LA + 1)][0], X[t % (G1_LA + 1)][1]);
    mfma_<ks == 0>(acc[jj][0], W[ks % G1_R][0], xb);
    if constexpr (tn < 160) xread(IC<tn>{}, IC<0>{});
    mfma_<ks == 0>(acc[jj][1], W[ks % G1_R][1], xb);
    if constexpr (tn < 160) xread(IC<tn>{}, IC<1>{});
    mfma_<ks == 0>(acc[jj][2], W[ks % G1_R][2], xb);
    static_for<g1_issued(t), g1_issued(t + 1)>(issue);
    if constexpr (jj < 2) { mfma_<ks == 0>(gram[jj], xb, xb); mfma_<ks == 0>(sum1[jj], ones, xb); }
  });
  STAMP_GET(t2);
  asm volatile("s_waitcnt vmcnt(0)\n\ts_nop 7" : "+v"(gram[0]), "+v"(gram[1]), "+v"(sum1[0]), "+v"(sum1[1]) :: "memory");
  f32x2* stat = (f32x2*)(smem + G1_STAT);
#pragma unroll
  for (int sj = 0; sj < 2; ++sj) {
    const int c3 = lane & 3;
    const float q = c3 == 0 ? gram[sj][0] : c3 == 1 ? gram[sj][1] : c3 == 2 ? gram[sj][2] : gram[sj][3];
    const float mean = sum1[sj][0] * (1.f / D_);
    const float var = fmaxf(q * (1.f / D_) - mean * mean, 0.f);
    const float sd = rbf(__builtin_sqrtf(rbf(rbf(var) + 1e-5f)));
    const float rs = 1.f / sd;
    if (((lane & 15) >> 2) == kg) stat[((sj + 2 * wv) & 7) * 16 + (lane & 15)] = f32x2{__uint_as_float(pack_bf16(-mean, sd)), rs * rs};
  }
  __syncthreads();
  u32* stage = (u32*)smem;
  f32x2 st[8];
#pragma unroll
  for (int jj = 0; jj < 8; ++jj) st[jj] = stat[((jj + 2 * wv) & 7) * 16 + (lane & 15)];
  u32x4 xc[8];
#pragma unroll
  for (int jj = 0; jj < 8; ++jj) {
    const u32 p = __float_as_uint(st[jj][0]);
    xc[jj] = u32x4{__builtin_amdgcn_perm(p, p, 0x01000100u), p, __builtin_amdgcn_perm(p, p, 0x03020302u), 0u};
    pin(xc[jj]);
  }
  asm volatile("s_nop 1");
#pragma unroll
  for (int jj = 0; jj < 8; ++jj)
#pragma unroll
    for (int i = 0; i < 3; ++i) mfma(acc[jj][i], W[20 % G1_R][i], xc[jj]);
  asm volatile("s_nop 7");
#pragma unroll
  for (int jj = 0; jj < 8; ++jj) {
    const int m16 = (jj + 2 * wv) & 7;
#pragma unroll
    for (int i = 0; i < 3; ++i) {
      pin(acc[jj][i]);
      const float g0 = __int_as_float(max(__float_as_int(acc[jj][i][0]), 0)), g1 = __int_as_float(max(__float_as_int(acc[jj][i][2]), 0));
      const int cI = 3 * wv + i;
      stage[(m16 * 3 + (cI >> 2)) * 256 + ((cI & 3) * 16 + (lane & 15)) * 4 + kg] =
          pack_bf16(g0 * st[jj][1] * acc[jj][i][1], g1 * st[jj][1] * acc[jj][i][3]);
    }
  }
  __syncthreads();
#pragma unroll
  for (int i = 0; i < 6; ++i) {
    const int m16 = wv * 2 + i / 3, f3 = i % 3;
    const int g16 = mt * 8 + m16;
    h2f[(size_t)(((g16 >> 2) * 48 + 3 * nc + f3) * 4 + (g16 & 3)) * 64 + lane] = *(const u32x4*)(stage + (m16 * 3 + f3) * 256 + lane * 4);
  }
  STAMP_END(2);
}

constexpr int G2_R = 4;
constexpr int G2_LDS = 84 * 1024;
__global__ __launch_bounds__(256, 1) void k_gemm2(const u32x4* __restrict__ h2f, const u32x4* __restrict__ w2f,
                                                  const u16* __restrict__ x2T, const float* __restrict__ modp,
                                                  u16* __restrict__ out) {
  __shared__ __attribute__((aligned(16))) char smem[G2_LDS];
  STAMP_DECL();
  const int tid = threadIdx.x, lane = tid & 63;
  const int wv = __builtin_amdgcn_readfirstlane(tid >> 6);
  const int xcd = blockIdx.x & 7, idx = blockIdx.x >> 3;
  const int M = (xcd >> 1) * 8 + (idx & 7), N = (xcd & 1) * 4 + (idx >> 3);
  const unsigned sbase = (unsigned)(size_t)smem;
  const char* hp = (const char*)(h2f + (size_t)(M * 48 + wv * 12) * 4 * 64);
  const char* wp = (const char*)(w2f + (size_t)(N * 48 + wv * 12) * 5 * 64) + 2048;
  unsigned hoff[4];
#pragma unroll
  for (int jj = 0; jj < 4; ++jj) hoff[jj] = ((jj + wv + 1) & 3) * 1024 + lane * 16;
  const unsigned voff = lane * 16;
  u32x4 H[G2_R][4], W[G2_R][5];
  auto issue = [&](auto O) {
    constexpr int o = O.value, ks = o / 9, p = o % 9;
    if constexpr (p < 5) gld128<(p - 2) * 1024>(W[ks % G2_R][p], voff, wp + ks * 5120);
    else gld128<0>(H[ks % G2_R][p - 5], hoff[p - 5], hp + ks * 4096);
  };
  static_for<0, 9 * (G2_R - 1)>(issue);
  constexpr int XR_OFF = 61440;
  {
    const char* xs = (const char*)(x2T + (size_t)(N * 80) * TP_ + M * 64);
    const unsigned xo = ((lane >> 3) * TP_ + (lane & 7) * 8) * 2;
    dma16<XR_OFF>(sbase + wv * 1024, xo, xs + (size_t)wv * 8 * TP_ * 2);
    dma16<XR_OFF + 4096>(sbase + wv * 1024, xo, xs + (size_t)(wv + 4) * 8 * TP_ * 2);
    dma16<XR_OFF + 8192>(sbase + (wv & 1) * 1024, xo, xs + (size_t)((wv & 1) + 8) * 8 * TP_ * 2);
  }
  u32 gp[MODP];
  {
    const unsigned go = (5 * D_ + N * 80 + (tid < 80 ? tid : 0)) * 4;
#pragma unroll
    for (int p = 0; p < MODP; ++p) gld32(gp[p], go, (const char*)(modp + p * NMOD));
  }
  f32x4 acc[4][5];
  static_for<0, 12>([&](auto KS) {
    constexpr int ks = KS.value;
    constexpr int issued = 9 * (ks + G2_R - 1 < 12 ? ks + G2_R - 1 : 12);
    wait_vm<issued - 9 * (ks + 1) + (ks < G2_R - 1 ? 3 + MODP : 0)>();
    if constexpr (ks == 0) STAMP_GET(t1);
    static_for<0, 20>([&](auto G) {
      constexpr int g = G.value, jj = g / 5, i = g % 5;
      mfma_<ks == 0>(acc[jj][i], W[ks % G2_R][i], H[ks % G2_R][jj]);
      if constexpr (ks + G2_R - 1 < 12 && (g & 1) == 0 && g / 2 < 9) issue(IC<9 * (ks + G2_R - 1) + g / 2>{});
    });
  });
  STAMP_GET(t2);
  const int kg = lane >> 4;
  float* gbl = (float*)(smem + XR_OFF + 10240);
  {
    float g = 0.f;
#pragma unroll
    for (int p = 0; p < MODP; ++p) { pin(gp[p]); g += __uint_as_float(gp[p]); }
    if (tid < 80) gbl[tid] = rbf(g);
  }
  asm volatile("s_nop 7");
  static_for<0, 15>([&](auto G) {
    constexpr int jj = G.value / 5, i = G.value % 5;
    ds_w128<(jj * 5 + i) * 1024>(sbase + ((jj + wv + 1) & 3) * 15360 + voff, acc[jj][i]);
  });
  wait_lgkm<0>();
  __syncthreads();
  const f32x4* part = (const f32x4*)(smem + wv * 15360);
  const unsigned xad = sbase + XR_OFF + (4 * kg + ((lane & 15) >> 2)) * 128 + wv * 32 + (lane & 3) * 8;
  u32x2 xw[5];
  static_for<0, 5>([&](auto I) { ds_tr<I.value * 2048>(xw[I.value], xad); });
  wait_lgkm<0>();
  const int pos = M * 64 + wv * 16 + (lane & 15);
  const int l8 = (pos >> 3) & 63;
  const int tok = 256 * (2 * (pos >> 9) + ((pos >> 2) & 1)) + 16 * (l8 & 15) + 4 * (l8 >> 4) + (pos & 3);
#pragma unroll
  for (int i = 0; i < 5; ++i) {
    pin(xw[i]); pin(acc[3][i]);
    f32x4 o = acc[3][i];
#pragma unroll
    for (int sl = 0; sl < 3; ++sl) o += part[(sl * 5 + i) * 64 + lane];
    const float x4[4] = {bflo(xw[i][0]), bfhi(xw[i][0]), bflo(xw[i][1]), bfhi(xw[i][1])};
    const f32x4 gb = *(const f32x4*)(gbl + i * 16 + 4 * kg);
    float r[4];
#pragma unroll
    for (int q = 0; q < 4; ++q) r[q] = fmaf(gb[q], o[q], x4[q]);
    *(u32x2*)(out + (size_t)tok * D_ + N * 80 + i * 16 + 4 * kg) = u32x2{pack_bf16(r[0], r[1]), pack_bf16(r[2], r[3])};
  }
  STAMP_END(3);
}

extern "C" void kernel_launch(void* const* d_in, const int* in_sizes, int n_in, void* d_out, int out_size,
                              void* d_ws, size_t ws_size, hipStream_t stream) {
  const u16* q = (const u16*)d_in[0];
  const u16* k = (const u16*)d_in[1];
  const u16* v = (const u16*)d_in[2];
  const u16* shk = (const u16*)d_in[3];
  const u16* ssmk = (const u16*)d_in[4];
  const u16* ssmD = (const u16*)d_in[5];
  const u16* wg = (const u16*)d_in[7];
  const u16* wu = (const u16*)d_in[8];
  const u16* wd = (const u16*)d_in[9];
  const u16* c = (const u16*)d_in[10];
  const u16* wmod = (const u16*)d_in[11];
  char* ws = (char*)d_ws;
  float* modp = (float*)ws; ws += MODP * NMOD * 4;
  float* st1 = (float*)ws; ws += 16384;
  u32x4* kvG = (u32x4*)ws; ws += (size_t)S_ * D_ * 2;
  u32x4* xG = (u32x4*)ws; ws += (size_t)S_ * D_ * 2;
  u16* x2T = (u16*)ws; ws += (size_t)TP_ * D_ * 2;
  u32x4* w1f = (u32x4*)ws; ws += (size_t)64 * W1_STEPS * 3 * 1024;
  u32x4* w2f = (u32x4*)ws; ws += (size_t)8 * 48 * 5 * 1024;
  u32x4* h2f = (u32x4*)ws; ws += (size_t)S_ * F_ * 2;
  k_prep<<<256, 512, 0, stream>>>(q, k, v, shk, c, wmod, kvG, xG, modp, st1, (const char*)ssmk, (const char*)wg, (const char*)wu, (const char*)wd, (const char*)d_out);
  k_conv<<<256, 256, 0, stream>>>(kvG, xG, ssmk, ssmD, modp, st1, x2T, wg, wu, wd, w1f, w2f);
  k_gemm1<<<256, 256, 0, stream>>>(x2T, w1f, h2f);
  k_gemm2<<<256, 256, 0, stream>>>(h2f, w2f, x2T, modp, (u16*)d_out);
}
```

```cpp
#include <hip/hip_runtime.h>
#include <stdint.h>

typedef unsigned short u16;
typedef unsigned int u32;
typedef __attribute__((ext_vector_type(2))) __bf16 bf16x2;
typedef __attribute__((ext_vector_type(4))) float f32x4;
typedef __attribute__((ext_vector_type(2))) float f32x2;
typedef __attribute__((ext_vector_type(4))) u32 u32x4;
typedef __attribute__((ext_vector_type(2))) u32 u32x2;

constexpr int S_ = 2048, D_ = 640, F_ = 1536;
constexpr int TP_ = S_ + 64;
constexpr int NMOD = 6 * D_;
constexpr int MODP = 4;
constexpr int CWG = 160;

__device__ __forceinline__ float bflo(u32 w) { return __uint_as_float(w << 16); }
__device__ __forceinline__ float bfhi(u32 w) { return __uint_as_float(w & 0xffff0000u); }
__device__ __forceinline__ u32 pack_bf16(float lo, float hi) {
  bf16x2 v; v[0] = (__bf16)lo; v[1] = (__bf16)hi; return __builtin_bit_cast(u32, v);
}
__device__ __forceinline__ float rbf(float x) { __bf16 b = (__bf16)x; return (float)b; }
#define SCHED() __builtin_amdgcn_sched_barrier(0)
template <int V> struct IC { static constexpr int value = V; };
template <int B, int E, class Fn> __device__ __forceinline__ void static_for(Fn&& f) {
  if constexpr (B < E) { f(IC<B>{}); static_for<B + 1, E>(f); }
}
__device__ __forceinline__ void mfma(f32x4& c, u32x4 a, u32x4 b) {
  asm volatile("v_mfma_f32_16x16x32_bf16 %0, %1, %2, %0" : "+v"(c) : "v"(a), "v"(b));
}
__device__ __forceinline__ void mfma0(f32x4& c, u32x4 a, u32x4 b) {
  asm volatile("v_mfma_f32_16x16x32_bf16 %0, %1, %2, 0" : "=&v"(c) : "v"(a), "v"(b));
}
template <bool FIRST> __device__ __forceinline__ void mfma_(f32x4& c, u32x4 a, u32x4 b) { if constexpr (FIRST) mfma0(c, a, b); else mfma(c, a, b); }
__device__ __forceinline__ u32 alignbit(u32 hi, u32 lo, u32 sh) {
  u32 d; asm volatile("v_alignbit_b32 %0, %1, %2, %3" : "=v"(d) : "v"(hi), "v"(lo), "v"(sh)); return d;
}
template <int OFF> __device__ __forceinline__ void ds_tr(u32x2& dst, unsigned addr) {
  asm volatile("ds_read_b64_tr_b16 %0, %1 offset:%2" : "=v"(dst) : "v"(addr), "i"(OFF) : "memory");
}
template <int OFF> __device__ __forceinline__ void ds_b32(u32& dst, unsigned addr) {
  asm volatile("ds_read_b32 %0, %1 offset:%2" : "=v"(dst) : "v"(addr), "i"(OFF) : "memory");
}
template <int OFF> __device__ __forceinline__ void ds_b64(u32x2& dst, unsigned addr) {
  asm volatile("ds_read_b64 %0, %1 offset:%2" : "=v"(dst) : "v"(addr), "i"(OFF) : "memory");
}
template <int OFF> __device__ __forceinline__ void ds_b128(u32x4& dst, unsigned addr) {
  asm volatile("ds_read_b128 %0, %1 offset:%2" : "=v"(dst) : "v"(addr), "i"(OFF) : "memory");
}
template <int OFF> __device__ __forceinline__ void ds_w128(unsigned addr, f32x4 v) {
  asm volatile("ds_write_b128 %0, %1 offset:%2" :: "v"(addr), "v"(v), "i"(OFF) : "memory");
}
template <int OFF> __device__ __forceinline__ void gld128(u32x4& dst, unsigned voff, const char* sb) {
  asm volatile("global_load_dwordx4 %0, %1, %2 offset:%3 sc1" : "=v"(dst) : "v"(voff), "s"(sb), "i"(OFF) : "memory");
}
__device__ __forceinline__ void gld32(u32& dst, unsigned voff, const char* sb) {
  asm volatile("global_load_dword %0, %1, %2 sc1" : "=v"(dst) : "v"(voff), "s"(sb) : "memory");
}
template <int LOFF> __device__ __forceinline__ void dma16(unsigned ldsb, unsigned voff, const char* sb) {
  asm volatile("s_add_u32 m0, %0, %3\n\ts_nop 0\n\tglobal_load_lds_dwordx4 %1, %2 sc1" :: "s"(ldsb), "v"(voff), "s"(sb), "i"(LOFF) : "memory", "scc");
}
template <int LOFF> __device__ __forceinline__ void dma16x4(unsigned ldsb, unsigned v0, unsigned v1, const char* sb) {
  asm volatile("s_add_u32 m0, %0, %4\n\ts_nop 0\n\tglobal_load_lds_dwordx4 %1, %3 sc1\n\tglobal_load_lds_dwordx4 %2, %3 offset:1024 sc1\n\t"
               "global_load_lds_dwordx4 %1, %3 offset:2048 sc1\n\tglobal_load_lds_dwordx4 %2, %3 offset:3072 sc1"
               :: "s"(ldsb), "v"(v0), "v"(v1), "s"(sb), "i"(LOFF) : "memory", "scc");
}
__device__ __forceinline__ void sld32(u32& dst, const void* p) { asm volatile("s_load_dword %0, %1, 0x0" : "=s"(dst) : "s"(p) : "memory"); }
template <int N> __device__ __forceinline__ void wait_vm() { asm volatile("s_waitcnt vmcnt(%0)" :: "i"(N) : "memory"); }
template <int N> __device__ __forceinline__ void wait_lgkm() { asm volatile("s_waitcnt lgkmcnt(%0)" :: "i"(N) : "memory"); }
__device__ __forceinline__ void bar() { asm volatile("s_barrier" ::: "memory"); }
template <class T> __device__ __forceinline__ void pin(T& a) { asm volatile("" : "+v"(a)); }
__device__ __forceinline__ u32x4 cat(u32x2 a, u32x2 b) { return u32x4{a[0], a[1], b[0], b[1]}; }

typedef __amdgpu_buffer_rsrc_t rsrc_t;
__device__ __forceinline__ rsrc_t mk_rsrc(const void* p, int bytes) { return __builtin_amdgcn_make_buffer_rsrc((void*)p, 0, bytes, 0x00020000); }
template <int CTRL> __device__ __forceinline__ float dpp_add(float x) {
  return x + __builtin_bit_cast(float, __builtin_amdgcn_mov_dpp(__builtin_bit_cast(int, x), CTRL, 0xf, 0xf, true));
}
constexpr int DPP_XOR1 = 0xB1, DPP_XOR2 = 0x4E, DPP_HMIRROR = 0x141, DPP_ROR8 = 0x128;
__device__ __forceinline__ float sum8(float x) { return dpp_add<DPP_HMIRROR>(dpp_add<DPP_XOR2>(dpp_add<DPP_XOR1>(x))); }
__device__ __forceinline__ float tred32(float a, float b) {
  auto r = __builtin_amdgcn_permlane32_swap(__float_as_uint(a), __float_as_uint(b), false, false);
  return __uint_as_float(r[0]) + __uint_as_float(r[1]);
}
__device__ __forceinline__ float tred16(float a, float b) {
  auto r = __builtin_amdgcn_permlane16_swap(__float_as_uint(a), __float_as_uint(b), false, false);
  return __uint_as_float(r[0]) + __uint_as_float(r[1]);
}
__device__ __forceinline__ float rdl(float x, int l) { return __int_as_float(__builtin_amdgcn_readlane(__float_as_int(x), l)); }

typedef unsigned long long u64;
#define GAS __attribute__((address_space(1)))
constexpr int SC1 = 16;
template <int OFF> __device__ __forceinline__ void gld128a(u32x4& dst, unsigned voff, const char* sb) {
  asm volatile("global_load_dwordx4 %0, %1, %2 offset:%3 sc1" : "=a"(dst) : "v"(voff), "s"(sb), "i"(OFF) : "memory");
}
template <bool FIRST> __device__ __forceinline__ void mfma_a(f32x4& c, u32x4 a, u32x4 b) {
  if constexpr (FIRST) asm volatile("v_mfma_f32_16x16x32_bf16 %0, %1, %2, 0" : "=&v"(c) : "a"(a), "v"(b));
  else asm volatile("v_mfma_f32_16x16x32_bf16 %0, %1, %2, %0" : "+v"(c) : "a"(a), "v"(b));
}
typedef __attribute__((ext_vector_type(8))) __bf16 bf16x8;
__device__ __forceinline__ f32x4 mfma_b(u32x4 a, u32x4 b, f32x4 c) {
  return __builtin_amdgcn_mfma_f32_16x16x32_bf16(__builtin_bit_cast(bf16x8, a), __builtin_bit_cast(bf16x8, b), c, 0, 0, 0);
}
template <int LOFF> __device__ __forceinline__ void dma16p(unsigned ldsb, unsigned voff, const char* sb) {
  asm volatile("s_add_u32 m0, %0, %3\n\ts_nop 0\n\tglobal_load_lds_dwordx4 %1, %2" :: "s"(ldsb), "v"(voff), "s"(sb), "i"(LOFF) : "memory", "scc");
}
template <int LOFF> __device__ __forceinline__ void dma16x4p(unsigned ldsb, unsigned v0, const char* sb) {
  asm volatile("s_add_u32 m0, %0, %3\n\ts_nop 0\n\tglobal_load_lds_dwordx4 %1, %2\n\tglobal_load_lds_dwordx4 %1, %2 offset:1024\n\t"
               "global_load_lds_dwordx4 %1, %2 offset:2048\n\tglobal_load_lds_dwordx4 %1, %2 offset:3072"
               :: "s"(ldsb), "v"(v0), "s"(sb), "i"(LOFF) : "memory", "scc");
}

#define STAMP(I)
#define STAMP_DECL()
#define STAMP_END()

constexpr int W1_STEPS = 21;
constexpr int O_EPOCH = 0;
constexpr int O_FK = 4096;
constexpr int O_FA0 = O_FK + 160 * 256;
constexpr int O_FA1 = O_FA0 + 8 * 12 * 128;
constexpr int O_FX = O_FA1 + 8 * 12 * 128;
constexpr int O_F2 = O_FX + 8 * 20 * 128;
constexpr int O_F3 = O_F2 + 8 * 32 * 128;
constexpr int O_MODP = O_F3 + 256 * 128;
constexpr int O_ST1 = O_MODP + MODP * NMOD * 4;
constexpr int O_KVG = O_ST1 + 16384;
constexpr int O_XG = O_KVG + S_ * D_ * 2;
constexpr int O_X2T = O_XG + S_ * D_ * 2;
constexpr int O_W1F = O_X2T + TP_ * D_ * 2;
constexpr int O_W2F = O_W1F + 64 * W1_STEPS * 3 * 1024;
constexpr int O_H2F = O_W2F + 8 * 48 * 5 * 1024;
constexpr int WS_BYTES = O_H2F + S_ * F_ * 2;
static_assert(O_MODP % 256 == 0 && O_ST1 % 256 == 0 && O_KVG % 256 == 0 && O_X2T % 256 == 0 && O_W1F % 256 == 0, "alignment");

constexpr int SPIN_CAP = 1 << 16;
__device__ __forceinline__ void st_flag(char* ws, int off, u64 tag) {
  __hip_atomic_store((GAS u64*)(ws + off), tag, __ATOMIC_RELAXED, __HIP_MEMORY_SCOPE_AGENT);
}
__device__ __forceinline__ bool tag_ok(u32x4 v, u64 tag) {
  const u32 lo = (u32)tag, hi = (u32)(tag >> 32);
  return ((v[0] ^ lo) | (v[1] ^ hi) | (v[2] ^ lo) | (v[3] ^ hi)) == 0;
}
template <int NL, int SLEEP> __device__ __forceinline__ void poll(rsrc_t rw, const int (&off)[NL], u64 tag) {
#pragma unroll 1
  for (int sp = 0; sp < SPIN_CAP; ++sp) {
    bool ok = true;
#pragma unroll
    for (int i = 0; i < NL; ++i) ok &= tag_ok(__builtin_amdgcn_raw_buffer_load_b128(rw, off[i], 0, SC1), tag);
    if (__all(ok)) break;
    if (SLEEP) __builtin_amdgcn_s_sleep(SLEEP);
    asm volatile("" ::: "memory");
  }
  asm volatile("" ::: "memory");
}
__device__ __forceinline__ u64 next_tag(u32x2 ep) {
  u32 lo, hi;
  asm volatile("s_waitcnt lgkmcnt(0)\n\ts_add_u32 %0, %2, 1\n\ts_addc_u32 %1, %3, 0" : "=&s"(lo), "=&s"(hi) : "s"(ep[0]), "s"(ep[1]) : "memory", "scc");
  return ((u64)hi << 32) | lo;
}
__device__ __forceinline__ void drain_bar() { asm volatile("s_waitcnt vmcnt(0) lgkmcnt(0)\n\ts_barrier" ::: "memory"); }

constexpr int FC_BYTES = 2080 * 2;
constexpr int CH_BYTES = 2 * FC_BYTES;
constexpr int CKV_OFF = 33280;
constexpr int CX_OFF = CKV_OFF + 2048 + 16384, CS_OFF = CX_OFF + 16384;
constexpr int conv_gap(int a, int b) { int n = 0; for (int i = 0; i < a; ++i) n += 8 - i; return n + b; }
constexpr int G1_R = 6;
constexpr int G1_SLOTS = 8;
constexpr int G1_LA = 4;
constexpr int G1_STAT = G1_SLOTS * 8192;
constexpr int G1_XPRE = 3;
constexpr int G1_WD0 = 20;
constexpr int G1_WD = 40;
struct G1Sched { int kind[192]; int idx[192]; int first[162]; int n; };
constexpr G1Sched g1_make() {
  G1Sched S{};
  int mk[128] = {}, mi[128] = {}, ms[128] = {}, nm = 0;
  for (int st = 0; st <= 20; ++st) {
    if (st >= G1_R - 1) for (int i = 0; i < 3; ++i) { mk[nm] = 1; mi[nm] = 3 * st + i; ms[nm] = st; ++nm; }
    if (st < 20) for (int p = 0; p < 2; ++p) { mk[nm] = 0; mi[nm] = 2 * st + p; ms[nm] = st; ++nm; }
  }
  int n = 0, m = 0, wd = G1_WD0;
  for (; m < 2 * G1_XPRE; ++m) { S.kind[n] = mk[m]; S.idx[n] = mi[m]; ++n; }
  for (int t = 0; t < 160; ++t) {
    S.first[t] = n;
    const int ks = t >> 3, jj = t & 7;
    if (m < nm && ms[m] <= ks + G1_R - 1) { S.kind[n] = mk[m]; S.idx[n] = mi[m]; ++n; ++m; }
    else if (wd < G1_WD && t < 150 && m >= nm && jj < 6) { S.kind[n] = 2; S.idx[n] = wd; ++n; ++wd; }
  }
  S.first[160] = n; S.n = n;
  return S;
}
constexpr G1Sched G1S = g1_make();
constexpr int g1_pos(int kind, int idx) { for (int i = 0; i < G1S.n; ++i) if (G1S.kind[i] == kind && G1S.idx[i] == idx) return i; return -1; }
constexpr int g1_wd_count() { int c = G1_WD0; for (int i = 0; i < G1S.n; ++i) c += G1S.kind[i] == 2; return c; }
constexpr int G1_WDN = g1_wd_count();
static_assert(g1_pos(1, 62) >= 0 && g1_pos(0, 39) >= 0, "schedule incomplete");
constexpr int G2_R = 8, G2_PRE = 4;
constexpr int XR_OFF = 69632;
constexpr int LDS_BYTES = 96 * 1024;
constexpr int AG_OFF = 0, AU_OFF = 20480, AD_OFF = 40960, AM_OFF = 61440;

__global__ __launch_bounds__(256, 1) void k_fused(const u16* __restrict__ xIn, const u16* __restrict__ kIn, const u16* __restrict__ vIn,
                                                  const u16* __restrict__ shk, const u16* __restrict__ ssmk, const u16* __restrict__ ssmD,
                                                  const u16* __restrict__ wg, const u16* __restrict__ wu, const u16* __restrict__ wd,
                                                  const u16* __restrict__ cIn, const u16* __restrict__ wmod,
                                                  u16* __restrict__ out, char* __restrict__ ws) {
  __shared__ __attribute__((aligned(16))) char smem[LDS_BYTES];
  STAMP_DECL();
  STAMP(0);
  asm volatile("" :: "s"(xIn), "s"(kIn), "s"(vIn), "s"(shk), "s"(ssmk), "s"(ssmD), "s"(wg), "s"(wu), "s"(wd), "s"(cIn), "s"(wmod), "s"(out), "s"(ws));
  u32x2 ep;
  asm volatile("s_load_dwordx2 %0, %1, 0x0" : "=s"(ep) : "s"(ws) : "memory");
  u64 tag;
  const int tid = threadIdx.x, lane = tid & 63;
  const int wv = __builtin_amdgcn_readfirstlane(tid >> 6);
  const int b = blockIdx.x;
  const unsigned sbase = (unsigned)(size_t)smem;
  const unsigned l16 = lane * 16;
  const rsrc_t rw = mk_rsrc(ws, WS_BYTES);
  if (b >= CWG) {
    const int j = b - CWG;
    const int r8 = lane >> 3, cp = lane & 7;
    const int ks2 = j >> 1, hf = j & 1;
    const int ug = j % 24, kq = j / 24;
    float* LC = (float*)(smem + AM_OFF);
    u16* LS = (u16*)(smem + AM_OFF + 640);
    float* LP = (float*)(smem + AM_OFF + 1280);
    const rsrc_t rm = mk_rsrc(wmod, D_ * NMOD * 2);
    const unsigned vm = r8 * (NMOD * 2) + cp * 16;
    auto silu = [](u32 c) { const float x = bflo(c); return rbf(x * __builtin_amdgcn_rcpf(1.f + __expf(-x))); };
    auto colsum = [&](f32x2 (&acc)[4]) {
      const float a0 = tred16(tred32(acc[0][0], acc[2][0]), tred32(acc[1][0], acc[3][0]));
      const float a1 = tred16(tred32(acc[0][1], acc[2][1]), tred32(acc[1][1], acc[3][1]));
      const float e0 = dpp_add<DPP_ROR8>(a0), e1 = dpp_add<DPP_ROR8>(a1);
      return (lane & 8) ? e1 : e0;
    };
    const int uk = j & 3, ucol = 30 + (j < 80 ? j >> 2 : 0);
    u32x4 um[5]; u32 uc[5];
#pragma unroll
    for (int it = 0; it < 5; ++it) uc[it] = cIn[uk * 160 + wv * 40 + it * 8 + r8];
#pragma unroll
    for (int it = 0; it < 5; ++it) um[it] = __builtin_amdgcn_raw_buffer_load_b128(rm, vm, (uk * 160 + wv * 40 + it * 8) * (NMOD * 2) + ucol * 128, 0);
    SCHED();
    tag = next_tag(ep);
    {
      f32x2 acc[4];
#pragma unroll
      for (int it = 0; it < 5; ++it) {
        const float sv = silu(uc[it]);
        const f32x2 s2 = {sv, sv};
#pragma unroll
        for (int q = 0; q < 4; ++q) {
          const f32x2 w2 = {bflo(um[it][q]), bfhi(um[it][q])};
          acc[q] = it ? __builtin_elementwise_fma(s2, w2, acc[q]) : s2 * w2;
        }
      }
      LP[wv * 64 + cp * 8 + r8] = colsum(acc);
    }
    asm volatile("s_waitcnt lgkmcnt(0)\n\ts_barrier" ::: "memory");
    STAMP(1);
    {
      const unsigned key = (r8 >> 1) * 2;
      const unsigned vg = r8 * (F_ * 2) + ((cp ^ key) * 16), vu = r8 * (F_ * 2) + ((cp ^ key ^ 1) * 16), vd = r8 * (D_ * 2) + cp * 16;
      const char* gs = (const char*)(wg + (size_t)kq * 160 * F_ + ug * 64);
      const char* us = (const char*)(wu + (size_t)kq * 160 * F_ + ug * 64);
      const char* ds = (const char*)(wd + (size_t)ks2 * 32 * D_ + hf * 320);
      if (wv == 0) {
        const float m = (LP[lane] + LP[64 + lane]) + (LP[128 + lane] + LP[192 + lane]);
        if (j < 80) __builtin_amdgcn_raw_buffer_store_b32(__float_as_uint(m), rw, O_MODP + (uk * NMOD + ucol * 64 + lane) * 4, 0, SC1);
        wait_vm<0>();
        if (lane < 8 && j < 80) st_flag(ws, O_FA0 + (lane * 12 + (j >> 3)) * 128 + (j & 7) * 8, tag);
        STAMP(2);
        static_for<0, 20>([&](auto I) { dma16p<AD_OFF + I.value * 1024>(sbase, vd, ds + (I.value & 3) * (8 * D_ * 2) + (I.value >> 2) * 128); });
      } else if (wv == 3) {
        static_for<8, 20>([&](auto I) {
          dma16p<AG_OFF + I.value * 1024>(sbase, vg, gs + I.value * (8 * F_ * 2));
          dma16p<AU_OFF + I.value * 1024>(sbase, vu, us + I.value * (8 * F_ * 2));
        });
      } else {
        const int o = (wv - 1) * 4;
        static_for<0, 4>([&](auto I) {
          dma16p<AG_OFF + I.value * 1024>(sbase + o * 1024, vg, gs + (o + I.value) * (8 * F_ * 2));
          dma16p<AU_OFF + I.value * 1024>(sbase + o * 1024, vu, us + (o + I.value) * (8 * F_ * 2));
        });
        const int gt = (wv - 1) * 96 + (95 - j);
        if (gt < 160) {
          const int gk = gt & 3, gq = gt >> 2, gcol = gq < 30 ? gq : gq + 20;
          u32x4 wm[20]; u32 cv[3];
#pragma unroll
          for (int i = 0; i < 3; ++i) cv[i] = cIn[gk * 160 + (i * 64 + lane < 160 ? i * 64 + lane : 0)];
#pragma unroll
          for (int it = 0; it < 20; ++it) wm[it] = __builtin_amdgcn_raw_buffer_load_b128(rm, vm, (gk * 160 + it * 8) * (NMOD * 2) + gcol * 128, 0);
          SCHED();
          float sl[3], sv[20];
#pragma unroll
          for (int i = 0; i < 3; ++i) sl[i] = silu(cv[i]);
#pragma unroll
          for (int it = 0; it < 20; ++it) sv[it] = __shfl(sl[it >> 3], (it & 7) * 8 + r8);
          f32x2 acc[4];
#pragma unroll
          for (int it = 0; it < 20; ++it) {
            const f32x2 s2 = {sv[it], sv[it]};
#pragma unroll
            for (int q = 0; q < 4; ++q) {
              const f32x2 w2 = {bflo(wm[it][q]), bfhi(wm[it][q])};
              acc[q] = it ? __builtin_elementwise_fma(s2, w2, acc[q]) : s2 * w2;
            }
          }
          __builtin_amdgcn_raw_buffer_store_b32(__float_as_uint(colsum(acc)), rw, O_MODP + (gk * NMOD + gcol * 64 + cp * 8 + r8) * 4, 0, SC1);
        }
      }
    }
    if (wv == 0 || wv == 3) {
      const int po[1] = {O_FA0 + ((b & 7) * 12 + (lane < 40 ? lane >> 2 : 0)) * 128 + (lane & 3) * 16};
      poll<1, 1>(rw, po, tag);
      STAMP(3);
      float mm[3];
#pragma unroll
      for (int i = 0; i < 3; ++i) {
        mm[i] = 0.f;
#pragma unroll
        for (int p = 0; p < MODP; ++p)
          mm[i] += __uint_as_float(__builtin_amdgcn_raw_buffer_load_b32(rw, lane * 4 + i * 256, O_MODP + (p * NMOD + (wv ? 4 : 3) * D_ + kq * 160) * 4, SC1));
      }
#pragma unroll
      for (int i = 0; i < 3; ++i)
        if (i * 64 + lane < 160) { if (wv) LC[i * 64 + lane] = rbf(1.f + rbf(mm[i])); else LS[i * 64 + lane] = (u16)pack_bf16(mm[i], 0.f); }
    }
    drain_bar();
    if (tid < 8) st_flag(ws, O_FA1 + (tid * 12 + (j >> 3)) * 128 + (j & 7) * 8, tag);
    STAMP(4);
    {
      const int kg = lane >> 4, q4 = (lane & 15) >> 2, p4 = lane & 3;
      const int rr = 4 * kg + q4;
      u32x2 lo[10], hi[10], sbl[5], sbh[5]; u32x4 cl[5], ch[5];
      unsigned ad[2];
#pragma unroll
      for (int t = 0; t < 2; ++t) ad[t] = sbase + (p4 >> 1) * AU_OFF + rr * 128 + (((wv * 2 + t) ^ (((rr >> 1) & 3) * 2 + (p4 >> 1))) * 16) + (p4 & 1) * 8;
      static_for<0, 10>([&](auto I) { constexpr int i = I.value; ds_tr<(i >> 1) * 4096>(lo[i], ad[i & 1]); ds_tr<(i >> 1) * 4096 + 2048>(hi[i], ad[i & 1]); });
      const unsigned ac = sbase + AM_OFF + kg * 16, as = sbase + AM_OFF + 640 + kg * 8;
      static_for<0, 5>([&](auto I) { constexpr int i = I.value; ds_b128<i * 128>(cl[i], ac); ds_b128<i * 128 + 64>(ch[i], ac); ds_b64<i * 64>(sbl[i], as); ds_b64<i * 64 + 32>(sbh[i], as); });
      u32x2 dl[5], dh[5];
      const unsigned add = sbase + AD_OFF + (8 * kg + q4) * 128 + p4 * 8;
#pragma unroll
      for (int i = 0; i < 5; ++i) { const int tl = wv * 5 + i; const unsigned a2 = add + (tl >> 2) * 4096 + (tl & 3) * 32; ds_tr<0>(dl[i], a2); ds_tr<512>(dh[i], a2); }
      wait_lgkm<0>();
#pragma unroll
      for (int i = 0; i < 10; ++i) { pin(lo[i]); pin(hi[i]); }
#pragma unroll
      for (int i = 0; i < 5; ++i) { pin(cl[i]); pin(ch[i]); pin(sbl[i]); pin(sbh[i]); pin(dl[i]); pin(dh[i]); }
      const int frow = 2 * (4 * (q4 & 1) + p4) + (q4 >> 1);
      const u32x4 ones = {0x3f803f80u, 0x3f803f80u, 0x3f803f80u, 0x3f803f80u};
      f32x4 aU[2] = {}, aW[2] = {};
#pragma unroll
      for (int i = 0; i < 10; ++i) {
        const int ks = i >> 1, t = i & 1;
        const u32x4 raw = cat(lo[i], hi[i]);
        u32x4 sc;
#pragma unroll
        for (int q = 0; q < 2; ++q) {
          sc[q] = pack_bf16(bflo(raw[q]) * __uint_as_float(cl[ks][2 * q]), bfhi(raw[q]) * __uint_as_float(cl[ks][2 * q + 1]));
          sc[2 + q] = pack_bf16(bflo(raw[2 + q]) * __uint_as_float(ch[ks][2 * q]), bfhi(raw[2 + q]) * __uint_as_float(ch[ks][2 * q + 1]));
        }
        aW[t] = mfma_b(raw, cat(sbl[ks], sbh[ks]), aW[t]);
        aU[t] = mfma_b(sc, ones, aU[t]);
        const int T = ug * 8 + wv * 2 + t;
        const int sw3 = T / 3, ti = T - sw3 * 3;
        __builtin_amdgcn_raw_buffer_store_b128(sc, rw, (kg * 16 + frow) * 16, O_W1F + ((sw3 * W1_STEPS + kq * 5 + ks) * 3 + ti) * 1024, SC1);
      }
#pragma unroll
      for (int i = 0; i < 5; ++i) __builtin_amdgcn_raw_buffer_store_b128(cat(dl[i], dh[i]), rw, l16, O_W2F + (((hf * 4 + wv) * 48 + ks2) * 5 + i) * 1024, SC1);
      const f32x4 su = (lane & 4) ? aU[1] : aU[0], sw = (lane & 4) ? aW[1] : aW[0];
      const float U = p4 == 0 ? su[0] : p4 == 1 ? su[1] : p4 == 2 ? su[2] : su[3];
      const float W = p4 == 0 ? sw[0] : p4 == 1 ? sw[1] : p4 == 2 ? sw[2] : sw[3];
      const float u1 = rbf(U), u2 = rbf(U - u1), u3 = U - u1 - u2;
      const float w1 = rbf(W), w2 = rbf(W - w1), w3 = W - w1 - w2;
      const int T = ug * 8 + wv * 2 + ((lane >> 2) & 1);
      const int sw3 = T / 3, ti = T - sw3 * 3;
      const int crow = 2 * (4 * (kg & 1) + p4) + (kg >> 1);
      if (!(lane & 8))
        __builtin_amdgcn_raw_buffer_store_b128(u32x4{pack_bf16(u1, u2), pack_bf16(u3, w1), pack_bf16(w2, w3), 0u}, rw,
                                               O_W1F + (((sw3 * W1_STEPS + 20) * 3 + ti) * 64 + kq * 16 + crow) * 16, 0, SC1);
    }
    STAMP(5);
  } else {
    const int d = (b >> 1) * 8 + (b & 1) + 2 * wv;
    const int row = lane & 15, kg = lane >> 4;
    u32 xx[16]; u32x4 xs[5];
    const int tt = b & 31, ct = b >> 5;
    const int ov = ((lane >> 2) * 2048 + (lane & 3)) * 16, os = (ct * 16 * 2048 + (tt * 8 + wv * 2) * 4) * 16;
    const int srow = (b * 4 + wv) * 4 + kg;
    {
      const rsrc_t rk = mk_rsrc(kIn, S_ * D_ * 2), rv = mk_rsrc(vIn, S_ * D_ * 2);
      const int vo = ct * 256 + lane * 4, so = (tt * 64 + wv * 16) * 1280;
      const int sh0 = so ? so - 3 * 1280 : 0;
      u32 kk[19], vv[16];
      const u32x4 taps = *(const u32x4*)(shk + (ct * 64 + lane) * 8);
#pragma unroll
      for (int q = 0; q < 3; ++q) kk[q] = __builtin_amdgcn_raw_buffer_load_b32(rk, vo + q * 1280, sh0, 0);
#pragma unroll
      for (int q = 0; q < 8; ++q) kk[3 + q] = __builtin_amdgcn_raw_buffer_load_b32(rk, vo + (q & 1) * 1280, so + (q & 14) * 1280, 0);
#pragma unroll
      for (int q = 0; q < 8; ++q) vv[q] = __builtin_amdgcn_raw_buffer_load_b32(rv, vo + (q & 1) * 1280, so + (q & 14) * 1280, 0);
#pragma unroll
      for (int q = 8; q < 16; ++q) kk[3 + q] = __builtin_amdgcn_raw_buffer_load_b32(rk, vo + (q & 1) * 1280, so + (q & 14) * 1280, 0);
#pragma unroll
      for (int q = 8; q < 16; ++q) vv[q] = __builtin_amdgcn_raw_buffer_load_b32(rv, vo + (q & 1) * 1280, so + (q & 14) * 1280, 0);
      SCHED();
      const rsrc_t rx = mk_rsrc(xIn, S_ * D_ * 2);
#pragma unroll
      for (int q = 0; q < 16; ++q) xx[q] = __builtin_amdgcn_raw_buffer_load_b32(rx, vo + (q & 1) * 1280, so + (q & 14) * 1280, 0);
#pragma unroll
      for (int q = 0; q < 5; ++q) xs[q] = __builtin_amdgcn_raw_buffer_load_b128(rx, srow * 1280 + row * 16 + q * 256, 0, 0);
      SCHED();
      if (so == 0) { kk[0] = 0; kk[1] = 0; kk[2] = 0; }
      const f32x2 w0 = {bflo(taps[0]), bflo(taps[2])}, w1 = {bfhi(taps[0]), bfhi(taps[2])};
      const f32x2 w2 = {bflo(taps[1]), bflo(taps[3])}, w3 = {bfhi(taps[1]), bfhi(taps[3])};
#pragma unroll
      for (int r = 0; r < 2; ++r) {
        f32x2 kv[8];
#pragma unroll
        for (int q = 0; q < 8; ++q) {
          const int i = 8 * r + q;
          f32x2 a = f32x2{bflo(kk[i]), bfhi(kk[i])} * w3;
          a = __builtin_elementwise_fma(f32x2{bflo(kk[i + 1]), bfhi(kk[i + 1])}, w2, a);
          a = __builtin_elementwise_fma(f32x2{bflo(kk[i + 2]), bfhi(kk[i + 2])}, w1, a);
          a = __builtin_elementwise_fma(f32x2{bflo(kk[i + 3]), bfhi(kk[i + 3])}, w0, a);
          kv[q] = a * f32x2{bflo(vv[i]), bfhi(vv[i])};
        }
        u32x4 o0, o1;
#pragma unroll
        for (int t = 0; t < 4; ++t) {
          o0[t] = pack_bf16(kv[7 - 2 * t][0], kv[6 - 2 * t][0]);
          o1[t] = pack_bf16(kv[7 - 2 * t][1], kv[6 - 2 * t][1]);
        }
        __builtin_amdgcn_raw_buffer_store_b128(o0, rw, ov + r * 64, O_KVG + os, SC1);
        __builtin_amdgcn_raw_buffer_store_b128(o1, rw, ov + r * 64, O_KVG + os + 16384, SC1);
      }
    }
    STAMP(1);
    tag = next_tag(ep);
    drain_bar();
    if (tid < 32) st_flag(ws, O_FK + ((b & ~31) + tid) * 256 + (b & 31) * 8, tag);
    STAMP(2);
    {
      const char* fs = (const char*)(ssmk + (size_t)d * S_);
      dma16x4p<32>(sbase + wv * CH_BYTES, l16, fs);
      dma16x4p<FC_BYTES + 28>(sbase + wv * CH_BYTES, l16, fs);
    }
#pragma unroll
    for (int r = 0; r < 2; ++r) {
      u32x4 p0, p1;
#pragma unroll
      for (int t = 0; t < 4; ++t) {
        p0[t] = __builtin_amdgcn_perm(xx[8 * r + 2 * t + 1], xx[8 * r + 2 * t], 0x05040100u);
        p1[t] = __builtin_amdgcn_perm(xx[8 * r + 2 * t + 1], xx[8 * r + 2 * t], 0x07060302u);
      }
      __builtin_amdgcn_raw_buffer_store_b128(p0, rw, ov + r * 64, O_XG + os, SC1);
      __builtin_amdgcn_raw_buffer_store_b128(p1, rw, ov + r * 64, O_XG + os + 16384, SC1);
    }
    {
      const bf16x2 ones = {(__bf16)1.0f, (__bf16)1.0f};
      float s1[2] = {0.f, 0.f}, s2[2] = {0.f, 0.f};
#pragma unroll
      for (int i = 0; i < 5; ++i)
#pragma unroll
        for (int q = 0; q < 4; ++q) {
          const u32 raw = xs[i][q];
          const bf16x2 v = __builtin_bit_cast(bf16x2, raw);
          s1[q & 1] = __builtin_amdgcn_fdot2_f32_bf16(v, ones, s1[q & 1], false);
          s2[q & 1] = __builtin_amdgcn_fdot2_f32_bf16(v, v, s2[q & 1], false);
        }
      const float mean = dpp_add<DPP_ROR8>(sum8(s1[0] + s1[1])) * (1.f / D_);
      const float var = fmaxf(dpp_add<DPP_ROR8>(sum8(s2[0] + s2[1])) * (1.f / D_) - mean * mean, 0.f);
      const float sd = rbf(__builtin_amdgcn_sqrtf(rbf(rbf(var) + 1e-5f)));
      const float rs = __builtin_amdgcn_rcpf(sd);
      const int nt = srow >> 8, col = (srow >> 4) & 15, k4 = (srow >> 2) & 3, rr = srow & 3;
      if (row == 0 && srow < S_) __builtin_amdgcn_raw_buffer_store_b64(u32x2{__float_as_uint(rs), __float_as_uint(-rbf(mean) * rs)}, rw,
                                                                       O_ST1 + (((nt * 2 + (rr >> 1)) * 64 + k4 * 16 + col) * 4 + (rr & 1) * 2) * 4, 0, SC1);
    }
    u32 draw;
    sld32(draw, (const u32*)ssmD + (d >> 1));
    *(u32x2*)(smem + CKV_OFF + tid * 8) = u32x2{0, 0};
    if (lane < 15) *(u32*)(smem + wv * CH_BYTES + (lane >> 3) * FC_BYTES + (lane & 7) * 4) = 0;
    const int rho = row + 1;
    u32 sh = (rho & 1) * 16;
    unsigned aAddr = sbase + wv * CH_BYTES + ((rho >> 1) & 1) * FC_BYTES + 2 * (64 * kg + (rho & ~3));
    pin(sh); pin(aAddr);
    unsigned bA[8];
#pragma unroll
    for (int e = 0; e < 8; ++e) { const int g = 32 + 2 * row + 1 - 8 * kg - e; bA[e] = sbase + CKV_OFF + 16 * ((4 * g + wv) ^ ((g >> 2) & 7)); pin(bA[e]); }
    {
      const int po[1] = {O_FK + b * 256 + wv * 64 + (lane & 3) * 16};
      poll<1, 0>(rw, po, tag);
      STAMP(3);
      const unsigned ko0 = (lane ^ (lane >> 4)) * 16;
      dma16x4<CKV_OFF + 2048>(sbase + wv * 4096, ko0, ko0 ^ 64, ws + O_KVG + b * 16384 + wv * 4096);
    }
    drain_bar();
    if (tid < 8) st_flag(ws, O_FX + (tid * 20 + (b >> 3)) * 128 + (b & 7) * 8, tag);
    STAMP(4);
    f32x4 acc[8];
    u32x2 ra[2][8][2]; u32x4 rb[2][8]; u32 carry[8]; u32x4 A[8];
    u32x4 pf[2]; u32 mraw;
    auto load_n = [&](auto E, auto N) {
      constexpr int e = E.value, n = N.value, q = n / 3, bf = e & 1;
      if constexpr (n % 3 == 0) ds_b64<512 * q + 16 * e>(ra[bf][q][0], aAddr);
      else if constexpr (n % 3 == 1) ds_b64<512 * q + 16 * e + 8>(ra[bf][q][1], aAddr);
      else ds_b128<2048 * q>(rb[bf][q], bA[e]);
    };
    auto build = [&](auto E, auto AA, auto I) {
      constexpr int e = E.value, a = AA.value, i = I.value, bf = e & 1;
      if constexpr (i == 0) A[a][0] = alignbit(ra[bf][a][0][1], ra[bf][a][0][0], sh);
      else if constexpr (i == 1) A[a][1] = alignbit(ra[bf][a][1][0], ra[bf][a][0][1], sh);
      else if constexpr (i == 2) A[a][2] = alignbit(ra[bf][a][1][1], ra[bf][a][1][0], sh);
      else { A[a][3] = alignbit(carry[a], ra[bf][a][1][1], sh); carry[a] = ra[bf][a][0][0]; }
    };
    const int pa1[2] = {O_FX + ((b & 7) * 20 + (lane >> 2)) * 128 + (lane & 3) * 16,
                        (lane < 48 ? O_FA1 + ((b & 7) * 12 + (lane >> 2)) * 128 : O_FX + ((b & 7) * 20 + 4 + (lane >> 2)) * 128) + (lane & 3) * 16};
    static_for<0, 8>([&](auto I) { ds_b32<512 * I.value + 128>(carry[I.value], aAddr); });
    static_for<0, 24>([&](auto N) { load_n(IC<7>{}, N); });
    wait_lgkm<0>();
    static_for<0, 4>([&](auto I) { build(IC<7>{}, IC<0>{}, I); });
    asm volatile("s_nop 1");
    static_for<0, 8>([&](auto EE) {
      constexpr int e = 7 - EE.value, bf = e & 1;
      const u32x4 A0 = A[0];
      static_for<0, 8>([&](auto AA) {
        constexpr int a = AA.value;
        static_for<0, 8 - a>([&](auto BB) {
          constexpr int bb = BB.value, n = conv_gap(a, bb);
          mfma_<(e == 7 && a == 0)>(acc[a + bb], a == 0 ? A0 : A[a], rb[bf][bb]);
          if constexpr (e > 0 && n < 24) load_n(IC<(e > 0 ? e - 1 : 0)>{}, IC<n>{});
          if constexpr (n < 28) build(IC<e>{}, IC<1 + n / 4>{}, IC<n % 4>{});
          if constexpr (e > 0 && n == 30) wait_lgkm<0>();
          if constexpr (e > 0 && (n == 31 || n == 32)) { build(IC<(e > 0 ? e - 1 : 0)>{}, IC<0>{}, IC<2 * (n - 31)>{}); build(IC<(e > 0 ? e - 1 : 0)>{}, IC<0>{}, IC<2 * (n - 31) + 1>{}); }
          if constexpr (e == 3 && n == 28) gld128<0>(pf[0], pa1[0], ws);
          if constexpr (e == 3 && n == 29) gld128<0>(pf[1], pa1[1], ws);
          if constexpr (e == 2 && n == 33) {
            wait_vm<0>(); pin(pf[0]); pin(pf[1]);
            if (!__all(tag_ok(pf[0], tag) && tag_ok(pf[1], tag))) poll<2, 1>(rw, pa1, tag);
            dma16x4<CX_OFF>(sbase + wv * 4096, l16, l16, ws + O_XG + b * 16384 + wv * 4096);
          }
          if constexpr (e == 1 && n == 28) dma16x4<CS_OFF>(sbase + wv * 4096, l16, l16, ws + O_ST1 + wv * 4096);
          if constexpr (e == 1 && n == 34) gld32(mraw, (((lane & 7) < MODP ? (lane & 7) : 0) * NMOD + ((lane >> 3) & 3) * D_) * 4, ws + O_MODP + d * 4);
        });
      });
    });
    STAMP(5);
    asm volatile("s_waitcnt vmcnt(0)\n\ts_nop 7\n\ts_barrier" : "+v"(acc[0]), "+v"(acc[1]), "+v"(acc[2]), "+v"(acc[3]), "+v"(acc[4]), "+v"(acc[5]), "+v"(acc[6]), "+v"(acc[7]), "+v"(mraw), "+s"(draw) :: "memory");
    const float mv = rbf(sum8((lane & 7) < MODP ? __uint_as_float(mraw) : 0.f));
    const float m_ga = rdl(mv, 16), c0 = rdl(mv, 0) * m_ga, c1 = rbf(1.f + rdl(mv, 8)) * m_ga;
    const float Dv = (d & 1) ? bfhi(draw) : bflo(draw);
#pragma unroll
    for (int np = 0; np < 4; ++np) {
      u32x4 o;
#pragma unroll
      for (int u = 0; u < 2; ++u) {
        const int nt = 2 * np + u;
        const int tg = 32 * nt + 2 * row + (kg >> 1), g = 32 + tg;
        const u32x2 kvw = *(const u32x2*)(smem + CKV_OFF + 16 * ((4 * g + wv) ^ ((g >> 2) & 7)) + ((kg & 1) ? 0 : 8));
        const u32x2 xw = *(const u32x2*)(smem + CX_OFF + tg * 64 + wv * 16 + (kg & 1) * 8);
        const f32x4 sA = *(const f32x4*)(smem + CS_OFF + (nt * 2) * 1024 + l16);
        const f32x4 sB = *(const f32x4*)(smem + CS_OFF + (nt * 2 + 1) * 1024 + l16);
        const float kv4[4] = {bfhi(kvw[1]), bflo(kvw[1]), bfhi(kvw[0]), bflo(kvw[0])};
        const float x4[4] = {bflo(xw[0]), bfhi(xw[0]), bflo(xw[1]), bfhi(xw[1])};
        const float rs4[4] = {sA[0], sA[2], sB[0], sB[2]};
        const float nb4[4] = {sA[1], sA[3], sB[1], sB[3]};
        float r4[4];
#pragma unroll
        for (int r = 0; r < 4; ++r) {
          const float yb = fmaf(kv4[r], Dv, acc[nt][r]);
          const float n1 = fmaf(x4[r], rs4[r], nb4[r]);
          const float h = fmaf(n1, c1, c0);
          r4[r] = fmaf(h, yb, x4[r]);
        }
        o[2 * u] = pack_bf16(r4[0], r4[1]); o[2 * u + 1] = pack_bf16(r4[2], r4[3]);
      }
      __builtin_amdgcn_raw_buffer_store_b128(o, rw, l16 + np * 1024, O_X2T + d * (TP_ * 2), SC1);
    }
  }
  STAMP(6);
  drain_bar();
  if (tid < 8) st_flag(ws, O_F2 + (tid * 32 + (b >> 3)) * 128 + (b & 7) * 8, tag);
  STAMP(7);

  int bs = blockIdx.x;
  asm volatile("" : "+s"(bs));
  const int xcd = bs & 7, idx = bs >> 3;
  const int mt = (xcd & 1) * 8 + (idx & 7), nc = (xcd >> 1) * 4 + (idx >> 3);
  const int M = mt * 2 + ((nc >> 2) & 1), N = (nc >> 3) * 4 + (nc & 3);
  const int kg = lane >> 4;
  u32x4 WD[60];
  const char* wdp = ws + O_W2F + (N * 48 + wv * 12) * 5120;
  auto issue_wd = [&](auto I) { constexpr int i = I.value; gld128a<(i & 3) * 1024>(WD[i], l16, wdp + (i >> 2) * 4096); };
  {
    unsigned xoff[2];
#pragma unroll
    for (int p = 0; p < 2; ++p) {
      const int k = 4 * p + (lane >> 4);
      xoff[p] = (k * TP_ + (((lane & 15) ^ (2 * k)) * 8)) * 2;
    }
    const char* xsrc = ws + O_X2T + (8 * wv * TP_ + mt * 128) * 2;
    const char* wsrc = ws + O_W1F + (nc * 4 + wv) * W1_STEPS * 3072;
    const unsigned ldsw = sbase + wv * 2048;
    u32x4 W[G1_R][3];
    auto issue_w = [&](auto I) { constexpr int i = I.value; gld128<(i % 3) * 1024>(W[(i / 3) % G1_R][i % 3], l16, wsrc + (i / 3) * 3072); };
    auto issue = [&](auto O) {
      constexpr int kind = G1S.kind[O.value], i = G1S.idx[O.value];
      if constexpr (kind == 0) dma16<((i >> 1) % G1_SLOTS) * 8192 + (i & 1) * 1024>(ldsw, xoff[i & 1], xsrc + (size_t)(i >> 1) * 32 * TP_ * 2);
      else if constexpr (kind == 1) issue_w(IC<i>{});
      else issue_wd(IC<i>{});
    };
    unsigned xa[8];
#pragma unroll
    for (int jj = 0; jj < 8; ++jj) {
      const int rr = 4 * kg + ((lane & 15) >> 2);
      xa[jj] = sbase + rr * 256 + ((((jj + 2 * wv) & 7) ^ (rr & 7)) * 32) + (lane & 3) * 8;
      pin(xa[jj]);
    }
    u32x4 ones = {0x3f803f80u, 0x3f803f80u, 0x3f803f80u, 0x3f803f80u};
    pin(ones);
    {
      const int fb = O_F2 + (b & 7) * 32 * 128 + (lane & 3) * 16;
      const int pa[1] = {fb + (20 + (lane < 48 ? lane >> 2 : 0)) * 128};
      poll<1, 2>(rw, pa, tag);
      static_for<0, 3 * (G1_R - 1)>(issue_w);
      static_for<0, G1_WD0>(issue_wd);
      STAMP(8);
      if (wv == 0) {
        const int pc[2] = {fb + (lane >> 2) * 128, fb + (16 + ((lane >> 2) & 3)) * 128};
        poll<2, 1>(rw, pc, tag);
      }
      wait_vm<0>();
      bar();
    }
    STAMP(9);
    static_for<0, G1S.first[0]>(issue);
    f32x4 acc[8][3], gram[2], sum1[2];
    u32x2 X[G1_LA + 1][2];
    auto xread = [&](auto T, auto H) {
      constexpr int t = T.value, h = H.value;
      ds_tr<((t >> 3) % G1_SLOTS) * 8192 + h * 4096>(X[t % (G1_LA + 1)][h], xa[t & 7]);
    };
    wait_vm<G1S.first[0] - 2>();
    bar();
    STAMP(10);
    static_for<0, G1_LA>([&](auto T) { xread(T, IC<0>{}); xread(T, IC<1>{}); });
    static_for<0, 160>([&](auto T) {
      constexpr int t = T.value, ks = t >> 3, jj = t & 7, tn = t + G1_LA;
      wait_lgkm<2 * ((tn <= 160 ? tn : 160) - t - 1)>();
      if constexpr (tn < 160 && (tn & 7) == 0) {
        static_assert(g1_pos(0, 2 * (tn / 8) + 1) < G1S.first[t], "not issued yet");
        wait_vm<G1S.first[t] - 1 - g1_pos(0, 2 * (tn / 8) + 1)>();
        bar();
      }
      const u32x4 xb = cat(X[t % (G1_LA + 1)][0], X[t % (G1_LA + 1)][1]);
      mfma_<ks == 0>(acc[jj][0], W[ks % G1_R][0], xb);
      if constexpr (tn < 160) xread(IC<tn>{}, IC<0>{});
      mfma_<ks == 0>(acc[jj][1], W[ks % G1_R][1], xb);
      if constexpr (tn < 160) xread(IC<tn>{}, IC<1>{});
      mfma_<ks == 0>(acc[jj][2], W[ks % G1_R][2], xb);
      static_for<G1S.first[t], G1S.first[t + 1]>(issue);
      if constexpr (jj < 2) { mfma_<ks == 0>(gram[jj], xb, xb); mfma_<ks == 0>(sum1[jj], ones, xb); }
    });
    STAMP(11);
    wait_vm<G1S.n - 1 - g1_pos(1, 62)>();
    asm volatile("" : "+v"(gram[0]), "+v"(gram[1]), "+v"(sum1[0]), "+v"(sum1[1]) :: "memory");
    f32x2* stat = (f32x2*)(smem + G1_STAT);
#pragma unroll
    for (int sj = 0; sj < 2; ++sj) {
      const int c3 = lane & 3;
      const float q = c3 == 0 ? gram[sj][0] : c3 == 1 ? gram[sj][1] : c3 == 2 ? gram[sj][2] : gram[sj][3];
      const float mean = sum1[sj][0] * (1.f / D_);
      const float var = fmaxf(q * (1.f / D_) - mean * mean, 0.f);
      const float sd = rbf(__builtin_amdgcn_sqrtf(rbf(rbf(var) + 1e-5f)));
      const float rs = __builtin_amdgcn_rcpf(sd);
      if (((lane & 15) >> 2) == kg) stat[((sj + 2 * wv) & 7) * 16 + (lane & 15)] = f32x2{__uint_as_float(pack_bf16(-mean, sd)), rs * rs};
    }
    asm volatile("s_waitcnt lgkmcnt(0)\n\ts_barrier" ::: "memory");
    u32* stage = (u32*)smem;
    f32x2 st[8];
#pragma unroll
    for (int jj = 0; jj < 8; ++jj) st[jj] = stat[((jj + 2 * wv) & 7) * 16 + (lane & 15)];
    u32x4 xc[8];
#pragma unroll
    for (int jj = 0; jj < 8; ++jj) {
      const u32 p = __float_as_uint(st[jj][0]);
      xc[jj] = u32x4{__builtin_amdgcn_perm(p, p, 0x01000100u), p, __builtin_amdgcn_perm(p, p, 0x03020302u), 0u};
      pin(xc[jj]);
    }
    asm volatile("s_nop 1");
#pragma unroll
    for (int jj = 0; jj < 8; ++jj)
#pragma unroll
      for (int i = 0; i < 3; ++i) mfma(acc[jj][i], W[20 % G1_R][i], xc[jj]);
    asm volatile("s_nop 7");
#pragma unroll
    for (int jj = 0; jj < 8; ++jj) {
      const int m16 = (jj + 2 * wv) & 7;
#pragma unroll
      for (int i = 0; i < 3; ++i) {
        pin(acc[jj][i]);
        const float g0 = __int_as_float(max(__float_as_int(acc[jj][i][0]), 0)), g1 = __int_as_float(max(__float_as_int(acc[jj][i][2]), 0));
        const int cI = 3 * wv + i;
        stage[(m16 * 3 + (cI >> 2)) * 256 + ((cI & 3) * 16 + (lane & 15)) * 4 + kg] =
            pack_bf16(g0 * st[jj][1] * acc[jj][i][1], g1 * st[jj][1] * acc[jj][i][3]);
      }
    }
    asm volatile("s_waitcnt lgkmcnt(0)\n\ts_barrier" ::: "memory");
#pragma unroll
    for (int i = 0; i < 6; ++i) {
      const int m16 = wv * 2 + i / 3, f3 = i % 3;
      const int g16 = mt * 8 + m16;
      __builtin_amdgcn_raw_buffer_store_b128(*(const u32x4*)(stage + (m16 * 3 + f3) * 256 + lane * 4), rw, l16,
                                             O_H2F + (((g16 >> 2) * 48 + 3 * nc + f3) * 4 + (g16 & 3)) * 1024, SC1);
    }
  }
  STAMP(12);
  drain_bar();
  if (tid < 16) st_flag(ws, O_F3 + (mt * 16 + tid) * 128 + nc * 8, tag);
  static_for<G1_WDN, 60>(issue_wd);
  {
    const char* xs = ws + O_X2T + (N * 80 * TP_ + M * 64) * 2;
    const unsigned xo = ((lane >> 3) * TP_ + (lane & 7) * 8) * 2;
    dma16<XR_OFF>(sbase + wv * 1024, xo, xs + (size_t)wv * 8 * TP_ * 2);
    dma16<XR_OFF + 4096>(sbase + wv * 1024, xo, xs + (size_t)(wv + 4) * 8 * TP_ * 2);
    dma16<XR_OFF + 8192>(sbase + (wv & 1) * 1024, xo, xs + (size_t)((wv & 1) + 8) * 8 * TP_ * 2);
  }
  float* gbl = (float*)(smem + XR_OFF + 10240);
  if (tid < 80) {
    float g = 0.f;
#pragma unroll
    for (int p = 0; p < MODP; ++p) g += __uint_as_float(__builtin_amdgcn_raw_buffer_load_b32(rw, tid * 4, O_MODP + (p * NMOD + 5 * D_ + N * 80) * 4, SC1));
    gbl[tid] = rbf(g);
  }
  const char* hp = ws + O_H2F + (M * 48 + wv * 12) * 4096;
  unsigned hoff[4];
#pragma unroll
  for (int jj = 0; jj < 4; ++jj) { hoff[jj] = ((jj + wv + 1) & 3) * 1024 + l16; pin(hoff[jj]); }
  {
    const int po[1] = {O_F3 + (mt * 16 + nc) * 128 + wv * 32 + (lane & 1) * 16};
    poll<1, 0>(rw, po, tag);
    wait_vm<0>();
  }
  STAMP(13);
  u32x4 H[G2_R][4];
  auto issue_h = [&](auto O) { constexpr int o = O.value; gld128<0>(H[(o >> 2) % G2_R][o & 3], hoff[o & 3], hp + (o >> 2) * 4096); };
  static_for<0, 4 * G2_PRE>(issue_h);
  f32x4 acc[4][5];
  static_for<0, 12>([&](auto KS) {
    constexpr int ks = KS.value;
    constexpr int i0 = (4 * G2_PRE + 5 * ks < 48) ? 4 * G2_PRE + 5 * ks : 48;
    static_assert(i0 >= 4 * (ks + 1) && (i0 + 5 > 48 ? 48 : i0 + 5) <= 4 * (ks + G2_R), "ring");
    wait_vm<i0 - 4 * (ks + 1)>();
    static_for<0, 20>([&](auto G) {
      constexpr int g = G.value, jj = g / 5, i = g % 5;
      mfma_a<ks == 0>(acc[jj][i], WD[ks * 5 + i], H[ks % G2_R][jj]);
      if constexpr ((g & 3) == 0 && i0 + g / 4 < 48) issue_h(IC<i0 + g / 4>{});
    });
  });
  STAMP(14);
  asm volatile("s_nop 7");
  static_for<0, 15>([&](auto G) {
    constexpr int jj = G.value / 5, i = G.value % 5;
    ds_w128<(jj * 5 + i) * 1024>(sbase + ((jj + wv + 1) & 3) * 15360 + l16, acc[jj][i]);
  });
  asm volatile("s_waitcnt lgkmcnt(0)\n\ts_barrier" ::: "memory");
  const f32x4* part = (const f32x4*)(smem + wv * 15360);
  const unsigned xad = sbase + XR_OFF + (4 * kg + ((lane & 15) >> 2)) * 128 + wv * 32 + (lane & 3) * 8;
  u32x2 xw[5];
  static_for<0, 5>([&](auto I) { ds_tr<I.value * 2048>(xw[I.value], xad); });
  wait_lgkm<0>();
  const int pos = M * 64 + wv * 16 + (lane & 15);
  const int l8 = (pos >> 3) & 63;
  const int tok = 256 * (2 * (pos >> 9) + ((pos >> 2) & 1)) + 16 * (l8 & 15) + 4 * (l8 >> 4) + (pos & 3);
#pragma unroll
  for (int i = 0; i < 5; ++i) {
    pin(xw[i]); pin(acc[3][i]);
    f32x4 o = acc[3][i];
#pragma unroll
    for (int sl = 0; sl < 3; ++sl) o += part[(sl * 5 + i) * 64 + lane];
    const float x4[4] = {bflo(xw[i][0]), bfhi(xw[i][0]), bflo(xw[i][1]), bfhi(xw[i][1])};
    const f32x4 gb = *(const f32x4*)(gbl + i * 16 + 4 * kg);
    float r[4];
#pragma unroll
    for (int q = 0; q < 4; ++q) r[q] = fmaf(gb[q], o[q], x4[q]);
    *(u32x2*)(out + (size_t)tok * D_ + N * 80 + i * 16 + 4 * kg) = u32x2{pack_bf16(r[0], r[1]), pack_bf16(r[2], r[3])};
  }
  if (b == 0 && tid == 0) st_flag(ws, O_EPOCH, tag);
  STAMP_END();
}

extern "C" void kernel_launch(void* const* d_in, const int* in_sizes, int n_in, void* d_out, int out_size,
                              void* d_ws, size_t ws_size, hipStream_t stream) {
  k_fused<<<256, 256, 0, stream>>>((const u16*)d_in[0], (const u16*)d_in[1], (const u16*)d_in[2], (const u16*)d_in[3],
                                   (const u16*)d_in[4], (const u16*)d_in[5], (const u16*)d_in[7], (const u16*)d_in[8],
                                   (const u16*)d_in[9], (const u16*)d_in[10], (const u16*)d_in[11], (u16*)d_out, (char*)d_ws);
}
```

```cpp
#include <hip/hip_runtime.h>
#include <stdint.h>

typedef unsigned short u16;
typedef unsigned int u32;
typedef __attribute__((ext_vector_type(2))) __bf16 bf16x2;
typedef __attribute__((ext_vector_type(4))) float f32x4;
typedef __attribute__((ext_vector_type(2))) float f32x2;
typedef __attribute__((ext_vector_type(4))) u32 u32x4;
typedef __attribute__((ext_vector_type(2))) u32 u32x2;

constexpr int S_ = 2048, D_ = 640, F_ = 1536;
constexpr int TP_ = S_ + 64;
constexpr int NMOD = 6 * D_;
constexpr int MODP = 4;
constexpr int CWG = 160;

__device__ __forceinline__ float bflo(u32 w) { return __uint_as_float(w << 16); }
__device__ __forceinline__ float bfhi(u32 w) { return __uint_as_float(w & 0xffff0000u); }
__device__ __forceinline__ u32 pack_bf16(float lo, float hi) {
  bf16x2 v; v[0] = (__bf16)lo; v[1] = (__bf16)hi; return __builtin_bit_cast(u32, v);
}
__device__ __forceinline__ float rbf(float x) { __bf16 b = (__bf16)x; return (float)b; }
#define SCHED() __builtin_amdgcn_sched_barrier(0)
template <int V> struct IC { static constexpr int value = V; };
template <int B, int E, class Fn> __device__ __forceinline__ void static_for(Fn&& f) {
  if constexpr (B < E) { f(IC<B>{}); static_for<B + 1, E>(f); }
}
__device__ __forceinline__ void mfma(f32x4& c, u32x4 a, u32x4 b) {
  asm volatile("v_mfma_f32_16x16x32_bf16 %0, %1, %2, %0" : "+v"(c) : "v"(a), "v"(b));
}
__device__ __forceinline__ void mfma0(f32x4& c, u32x4 a, u32x4 b) {
  asm volatile("v_mfma_f32_16x16x32_bf16 %0, %1, %2, 0" : "=&v"(c) : "v"(a), "v"(b));
}
template <bool FIRST> __device__ __forceinline__ void mfma_(f32x4& c, u32x4 a, u32x4 b) { if constexpr (FIRST) mfma0(c, a, b); else mfma(c, a, b); }
__device__ __forceinline__ u32 alignbit(u32 hi, u32 lo, u32 sh) {
  u32 d; asm volatile("v_alignbit_b32 %0, %1, %2, %3" : "=v"(d) : "v"(hi), "v"(lo), "v"(sh)); return d;
}
template <int OFF> __device__ __forceinline__ void ds_tr(u32x2& dst, unsigned addr) {
  asm volatile("ds_read_b64_tr_b16 %0, %1 offset:%2" : "=v"(dst) : "v"(addr), "i"(OFF) : "memory");
}
template <int OFF> __device__ __forceinline__ void ds_b32(u32& dst, unsigned addr) {
  asm volatile("ds_read_b32 %0, %1 offset:%2" : "=v"(dst) : "v"(addr), "i"(OFF) : "memory");
}
template <int OFF> __device__ __forceinline__ void ds_b64(u32x2& dst, unsigned addr) {
  asm volatile("ds_read_b64 %0, %1 offset:%2" : "=v"(dst) : "v"(addr), "i"(OFF) : "memory");
}
template <int OFF> __device__ __forceinline__ void ds_b128(u32x4& dst, unsigned addr) {
  asm volatile("ds_read_b128 %0, %1 offset:%2" : "=v"(dst) : "v"(addr), "i"(OFF) : "memory");
}
template <int OFF> __device__ __forceinline__ void ds_w128(unsigned addr, f32x4 v) {
  asm volatile("ds_write_b128 %0, %1 offset:%2" :: "v"(addr), "v"(v), "i"(OFF) : "memory");
}
template <int OFF> __device__ __forceinline__ void gld128(u32x4& dst, unsigned voff, const char* sb) {
  asm volatile("global_load_dwordx4 %0, %1, %2 offset:%3 sc1" : "=v"(dst) : "v"(voff), "s"(sb), "i"(OFF) : "memory");
}
__device__ __forceinline__ void gld32(u32& dst, unsigned voff, const char* sb) {
  asm volatile("global_load_dword %0, %1, %2 sc1" : "=v"(dst) : "v"(voff), "s"(sb) : "memory");
}
template <int LOFF> __device__ __forceinline__ void dma16(unsigned ldsb, unsigned voff, const char* sb) {
  asm volatile("s_add_u32 m0, %0, %3\n\ts_nop 0\n\tglobal_load_lds_dwordx4 %1, %2 sc1" :: "s"(ldsb), "v"(voff), "s"(sb), "i"(LOFF) : "memory", "scc");
}
template <int LOFF> __device__ __forceinline__ void dma16x4(unsigned ldsb, unsigned v0, unsigned v1, const char* sb) {
  asm volatile("s_add_u32 m0, %0, %4\n\ts_nop 0\n\tglobal_load_lds_dwordx4 %1, %3 sc1\n\tglobal_load_lds_dwordx4 %2, %3 offset:1024 sc1\n\t"
               "global_load_lds_dwordx4 %1, %3 offset:2048 sc1\n\tglobal_load_lds_dwordx4 %2, %3 offset:3072 sc1"
               :: "s"(ldsb), "v"(v0), "v"(v1), "s"(sb), "i"(LOFF) : "memory", "scc");
}
__device__ __forceinline__ void sld32(u32& dst, const void* p) { asm volatile("s_load_dword %0, %1, 0x0" : "=s"(dst) : "s"(p) : "memory"); }
template <int N> __device__ __forceinline__ void wait_vm() { asm volatile("s_waitcnt vmcnt(%0)" :: "i"(N) : "memory"); }
template <int N> __device__ __forceinline__ void wait_lgkm() { asm volatile("s_waitcnt lgkmcnt(%0)" :: "i"(N) : "memory"); }
__device__ __forceinline__ void bar() { asm volatile("s_barrier" ::: "memory"); }
template <class T> __device__ __forceinline__ void pin(T& a) { asm volatile("" : "+v"(a)); }
__device__ __forceinline__ u32x4 cat(u32x2 a, u32x2 b) { return u32x4{a[0], a[1], b[0], b[1]}; }

typedef __amdgpu_buffer_rsrc_t rsrc_t;
__device__ __forceinline__ rsrc_t mk_rsrc(const void* p, int bytes) { return __builtin_amdgcn_make_buffer_rsrc((void*)p, 0, bytes, 0x00020000); }
template <int CTRL> __device__ __forceinline__ float dpp_add(float x) {
  return x + __builtin_bit_cast(float, __builtin_amdgcn_mov_dpp(__builtin_bit_cast(int, x), CTRL, 0xf, 0xf, true));
}
constexpr int DPP_XOR1 = 0xB1, DPP_XOR2 = 0x4E, DPP_HMIRROR = 0x141, DPP_ROR8 = 0x128;
__device__ __forceinline__ float sum8(float x) { return dpp_add<DPP_HMIRROR>(dpp_add<DPP_XOR2>(dpp_add<DPP_XOR1>(x))); }
__device__ __forceinline__ float tred32(float a, float b) {
  auto r = __builtin_amdgcn_permlane32_swap(__float_as_uint(a), __float_as_uint(b), false, false);
  return __uint_as_float(r[0]) + __uint_as_float(r[1]);
}
__device__ __forceinline__ float tred16(float a, float b) {
  auto r = __builtin_amdgcn_permlane16_swap(__float_as_uint(a), __float_as_uint(b), false, false);
  return __uint_as_float(r[0]) + __uint_as_float(r[1]);
}
__device__ __forceinline__ float rdl(float x, int l) { return __int_as_float(__builtin_amdgcn_readlane(__float_as_int(x), l)); }

typedef unsigned long long u64;
#define GAS __attribute__((address_space(1)))
constexpr int SC1 = 16;
#define NT 2
#define NTS " nt"
template <int OFF> __device__ __forceinline__ void gld128a(u32x4& dst, unsigned voff, const char* sb) {
  asm volatile("global_load_dwordx4 %0, %1, %2 offset:%3 sc1" : "=a"(dst) : "v"(voff), "s"(sb), "i"(OFF) : "memory");
}
template <bool FIRST> __device__ __forceinline__ void mfma_a(f32x4& c, u32x4 a, u32x4 b) {
  if constexpr (FIRST) asm volatile("v_mfma_f32_16x16x32_bf16 %0, %1, %2, 0" : "=&v"(c) : "a"(a), "v"(b));
  else asm volatile("v_mfma_f32_16x16x32_bf16 %0, %1, %2, %0" : "+v"(c) : "a"(a), "v"(b));
}
typedef __attribute__((ext_vector_type(8))) __bf16 bf16x8;
__device__ __forceinline__ f32x4 mfma_b(u32x4 a, u32x4 b, f32x4 c) {
  return __builtin_amdgcn_mfma_f32_16x16x32_bf16(__builtin_bit_cast(bf16x8, a), __builtin_bit_cast(bf16x8, b), c, 0, 0, 0);
}
template <int LOFF> __device__ __forceinline__ void dma16p(unsigned ldsb, unsigned voff, const char* sb) {
  asm volatile("s_add_u32 m0, %0, %3\n\ts_nop 0\n\tglobal_load_lds_dwordx4 %1, %2" NTS :: "s"(ldsb), "v"(voff), "s"(sb), "i"(LOFF) : "memory", "scc");
}
template <int LOFF> __device__ __forceinline__ void dma16x4p(unsigned ldsb, unsigned v0, const char* sb) {
  asm volatile("s_add_u32 m0, %0, %3\n\ts_nop 0\n\tglobal_load_lds_dwordx4 %1, %2" NTS "\n\tglobal_load_lds_dwordx4 %1, %2 offset:1024" NTS "\n\t"
               "global_load_lds_dwordx4 %1, %2 offset:2048" NTS "\n\tglobal_load_lds_dwordx4 %1, %2 offset:3072" NTS
               :: "s"(ldsb), "v"(v0), "s"(sb), "i"(LOFF) : "memory", "scc");
}

#define STAMP(I)
#define STAMP_DECL()
#define STAMP_END()

__device__ __forceinline__ void gld128p(u32x4& dst, unsigned voff, const char* sb) {
  asm volatile("global_load_dwordx4 %0, %1, %2" NTS : "=v"(dst) : "v"(voff), "s"(sb) : "memory");
}
template <int OFF> __device__ __forceinline__ void gld16p(u32& dst, unsigned voff, const char* sb) {
  asm volatile("global_load_ushort %0, %1, %2 offset:%3" : "=v"(dst) : "v"(voff), "s"(sb), "i"(OFF) : "memory");
}
#define CPF_DONE() asm volatile("" :: "v"(cpf))
constexpr int W1_STEPS = 21;
constexpr int O_EPOCH = 0;
constexpr int O_FK = 4096;
constexpr int O_FA0 = O_FK + 160 * 256;
constexpr int O_FA1 = O_FA0 + 8 * 12 * 128;
constexpr int O_FX = O_FA1 + 8 * 12 * 128;
constexpr int O_F2 = O_FX + 8 * 20 * 128;
constexpr int O_F3 = O_F2 + 8 * 32 * 128;
constexpr int O_MODP = O_F3 + 256 * 128;
constexpr int O_ST1 = O_MODP + MODP * NMOD * 4;
constexpr int O_KVG = O_ST1 + 16384;
constexpr int O_XG = O_KVG + S_ * D_ * 2;
constexpr int O_X2T = O_XG + S_ * D_ * 2;
constexpr int O_W1F = O_X2T + TP_ * D_ * 2;
constexpr int O_W2F = O_W1F + 64 * W1_STEPS * 3 * 1024;
constexpr int O_H2F = O_W2F + 8 * 48 * 5 * 1024;
constexpr int WS_BYTES = O_H2F + S_ * F_ * 2;
static_assert(O_MODP % 256 == 0 && O_ST1 % 256 == 0 && O_KVG % 256 == 0 && O_X2T % 256 == 0 && O_W1F % 256 == 0, "alignment");

constexpr int SPIN_CAP = 1 << 16;
constexpr unsigned CODE_BYTES = 40 * 1024;
__device__ __forceinline__ void st_flag(char* ws, int off, u64 tag) {
  __hip_atomic_store((GAS u64*)(ws + off), tag, __ATOMIC_RELAXED, __HIP_MEMORY_SCOPE_AGENT);
}
__device__ __forceinline__ bool tag_ok(u32x4 v, u64 tag) {
  const u32 lo = (u32)tag, hi = (u32)(tag >> 32);
  return ((v[0] ^ lo) | (v[1] ^ hi) | (v[2] ^ lo) | (v[3] ^ hi)) == 0;
}
template <int NL, int SLEEP> __device__ __forceinline__ void poll(rsrc_t rw, const int (&off)[NL], u64 tag) {
#pragma unroll 1
  for (int sp = 0; sp < SPIN_CAP; ++sp) {
    bool ok = true;
#pragma unroll
    for (int i = 0; i < NL; ++i) ok &= tag_ok(__builtin_amdgcn_raw_buffer_load_b128(rw, off[i], 0, SC1), tag);
    if (__all(ok)) break;
    if (SLEEP) __builtin_amdgcn_s_sleep(SLEEP);
    asm volatile("" ::: "memory");
  }
  asm volatile("" ::: "memory");
}
__device__ __forceinline__ u64 next_tag(u32x2 ep) {
  u32 lo, hi;
  asm volatile("s_waitcnt lgkmcnt(0)\n\ts_add_u32 %0, %2, 1\n\ts_addc_u32 %1, %3, 0" : "=&s"(lo), "=&s"(hi) : "s"(ep[0]), "s"(ep[1]) : "memory", "scc");
  return ((u64)hi << 32) | lo;
}
__device__ __forceinline__ void drain_bar() { asm volatile("s_waitcnt vmcnt(0) lgkmcnt(0)\n\ts_barrier" ::: "memory"); }

constexpr int FC_BYTES = 2080 * 2;
constexpr int CH_BYTES = 2 * FC_BYTES;
constexpr int CKV_OFF = 33280;
constexpr int CX_OFF = CKV_OFF + 2048 + 16384, CS_OFF = CX_OFF + 16384;
constexpr int conv_gap(int a, int b) { int n = 0; for (int i = 0; i < a; ++i) n += 8 - i; return n + b; }
constexpr int G1_R = 6;
constexpr int G1_SLOTS = 8;
constexpr int G1_LA = 4;
constexpr int G1_STAT = G1_SLOTS * 8192;
constexpr int G1_XPRE = 3;
#define WD0 8
constexpr int G1_WD0 = WD0;
constexpr int G1_WD = WD0 + 22;
struct G1Sched { int kind[192]; int idx[192]; int first[162]; int n; };
constexpr G1Sched g1_make() {
  G1Sched S{};
  int mk[128] = {}, mi[128] = {}, ms[128] = {}, nm = 0;
  for (int st = 0; st <= 20; ++st) {
    if (st >= G1_R - 1) for (int i = 0; i < 3; ++i) { mk[nm] = 1; mi[nm] = 3 * st + i; ms[nm] = st; ++nm; }
    if (st < 20) for (int p = 0; p < 2; ++p) { mk[nm] = 0; mi[nm] = 2 * st + p; ms[nm] = st; ++nm; }
  }
  int n = 0, m = 0, wd = G1_WD0;
  for (; m < 2 * G1_XPRE; ++m) { S.kind[n] = mk[m]; S.idx[n] = mi[m]; ++n; }
  for (int t = 0; t < 160; ++t) {
    S.first[t] = n;
    const int ks = t >> 3, jj = t & 7;
    if (m < nm && ms[m] <= ks + G1_R - 1) { S.kind[n] = mk[m]; S.idx[n] = mi[m]; ++n; ++m; }
    else if (wd < G1_WD && t < 150 && m >= nm && jj < 6) { S.kind[n] = 2; S.idx[n] = wd; ++n; ++wd; }
  }
  S.first[160] = n; S.n = n;
  return S;
}
constexpr G1Sched G1S = g1_make();
constexpr int g1_pos(int kind, int idx) { for (int i = 0; i < G1S.n; ++i) if (G1S.kind[i] == kind && G1S.idx[i] == idx) return i; return -1; }
constexpr int g1_wd_count() { int c = G1_WD0; for (int i = 0; i < G1S.n; ++i) c += G1S.kind[i] == 2; return c; }
constexpr int G1_WDN = g1_wd_count();
static_assert(g1_pos(1, 62) >= 0 && g1_pos(0, 39) >= 0, "schedule incomplete");
constexpr int G2_R = 8, G2_PRE = 4;
constexpr int XR_OFF = 69632;
constexpr int LDS_BYTES = 96 * 1024;
constexpr int AG_OFF = 0, AU_OFF = 20480, AD_OFF = 40960, AM_OFF = 61440;

__global__ __launch_bounds__(256, 1) void k_fused(const u16* __restrict__ xIn, const u16* __restrict__ kIn, const u16* __restrict__ vIn,
                                                  const u16* __restrict__ shk, const u16* __restrict__ ssmk, const u16* __restrict__ ssmD,
                                                  const u16* __restrict__ wg, const u16* __restrict__ wu, const u16* __restrict__ wd,
                                                  const u16* __restrict__ cIn, const u16* __restrict__ wmod,
                                                  u16* __restrict__ out, char* __restrict__ ws) {
  __shared__ __attribute__((aligned(16))) char smem[LDS_BYTES];
  STAMP_DECL();
  STAMP(0);
  u32 cpf = 0;
  if (blockIdx.x >= 240) {
    uint64_t pc; asm volatile("s_getpc_b64 %0" : "=s"(pc));
    const unsigned co = (((blockIdx.x >> 3) & 1) * 256 + threadIdx.x) * 128;
    asm volatile("global_load_dword %0, %1, %2" : "=v"(cpf) : "v"(co < CODE_BYTES ? co : 0u), "s"(pc & ~(uint64_t)127) : "memory");
  }
  asm volatile("" :: "s"(xIn), "s"(kIn), "s"(vIn), "s"(shk), "s"(ssmk), "s"(ssmD), "s"(wg), "s"(wu), "s"(wd), "s"(cIn), "s"(wmod), "s"(out), "s"(ws));
  u32x2 ep;
  asm volatile("s_load_dwordx2 %0, %1, 0x0" : "=s"(ep) : "s"(ws) : "memory");
  u64 tag;
  const int tid = threadIdx.x, lane = tid & 63;
  const int wv = __builtin_amdgcn_readfirstlane(tid >> 6);
  const int b = blockIdx.x;
  const unsigned sbase = (unsigned)(size_t)smem;
  const unsigned l16 = lane * 16;
  const rsrc_t rw = mk_rsrc(ws, WS_BYTES);
  u32x4 um[5]; u32 uc[5];
  if (b >= CWG) {
    const int j = b - CWG, r8 = lane >> 3, cp = lane & 7;
    const int ug = j % 24, kq = j / 24;
    const int uk = j & 3, ucol = 30 + (j < 80 ? j >> 2 : 0);
    static_for<0, 5>([&](auto I) { gld16p<I.value * 16>(uc[I.value], r8 * 2, (const char*)(cIn + uk * 160 + wv * 40)); });
    static_for<0, 5>([&](auto I) { gld128p(um[I.value], r8 * (NMOD * 2) + cp * 16, (const char*)(wmod + (size_t)(uk * 160 + wv * 40 + I.value * 8) * NMOD + ucol * 64)); });
    const unsigned key = (r8 >> 1) * 2;
    const unsigned vg = r8 * (F_ * 2) + ((cp ^ key) * 16);
    const char* gs = (const char*)(wg + (size_t)(kq * 160 + wv * 40) * F_ + ug * 64);
    const char* us = (const char*)(wu + (size_t)(kq * 160 + wv * 40) * F_ + ug * 64);
    static_for<0, 5>([&](auto I) {
      dma16p<AG_OFF + I.value * 1024>(sbase + wv * 5120, vg, gs + I.value * (8 * F_ * 2));
      dma16p<AU_OFF + I.value * 1024>(sbase + wv * 5120, vg ^ 16, us + I.value * (8 * F_ * 2));
    });
  }
  int b2 = b;
  asm volatile("" : "+s"(b2));
  if (b2 >= CWG) {
    const int j = b - CWG;
    const int r8 = lane >> 3, cp = lane & 7;
    const int ks2 = j >> 1, hf = j & 1;
    const int ug = j % 24, kq = j / 24;
    float* LC = (float*)(smem + AM_OFF);
    u16* LS = (u16*)(smem + AM_OFF + 640);
    u32* LN = (u32*)(smem + AM_OFF + 1024);
    float* LP = (float*)(smem + AM_OFF + 1280);
    auto silu = [](u32 c) { const float x = bflo(c); return rbf(x * __builtin_amdgcn_rcpf(1.f + __expf(-x))); };
    auto colsum = [&](f32x2 (&acc)[4]) {
      const float a0 = tred16(tred32(acc[0][0], acc[2][0]), tred32(acc[1][0], acc[3][0]));
      const float a1 = tred16(tred32(acc[0][1], acc[2][1]), tred32(acc[1][1], acc[3][1]));
      const float e0 = dpp_add<DPP_ROR8>(a0), e1 = dpp_add<DPP_ROR8>(a1);
      return (lane & 8) ? e1 : e0;
    };
    const int uk = j & 3, ucol = 30 + (j < 80 ? j >> 2 : 0);
    tag = next_tag(ep);
    if (tid == 0) *LN = 0;
    {
      float sv[5];
      wait_vm<15>();
#pragma unroll
      for (int it = 0; it < 5; ++it) { pin(uc[it]); sv[it] = silu(uc[it]); pin(sv[it]); }
      wait_vm<10>();
      f32x2 acc[4];
#pragma unroll
      for (int it = 0; it < 5; ++it) {
        pin(um[it]);
        const f32x2 s2 = {sv[it], sv[it]};
#pragma unroll
        for (int q = 0; q < 4; ++q) {
          const f32x2 w2 = {bflo(um[it][q]), bfhi(um[it][q])};
          acc[q] = it ? __builtin_elementwise_fma(s2, w2, acc[q]) : s2 * w2;
        }
      }
      LP[wv * 64 + cp * 8 + r8] = colsum(acc);
    }
    asm volatile("s_waitcnt lgkmcnt(0)\n\ts_barrier" ::: "memory");
    STAMP(1);
    if (wv == 0 && j < 80) {
      const float m = (LP[lane] + LP[64 + lane]) + (LP[128 + lane] + LP[192 + lane]);
      __builtin_amdgcn_raw_buffer_store_b32(__float_as_uint(m), rw, O_MODP + (uk * NMOD + ucol * 64 + lane) * 4, 0, SC1);
      wait_vm<0>();
      if (lane < 8) st_flag(ws, O_FA0 + (lane * 12 + (j >> 3)) * 128 + (j & 7) * 8, tag);
    } else {
      wait_vm<0>();
    }
    bar();
    STAMP(2);
    CPF_DONE();
    const int kg = lane >> 4, q4 = (lane & 15) >> 2, p4 = lane & 3;
    if (wv == 0 || wv == 3) {
      const char* ds = (const char*)(wd + (size_t)(ks2 * 32 + (wv & 1) * 16) * D_ + hf * 320);
      static_for<0, 10>([&](auto I) { dma16p<AD_OFF + (I.value >> 1) * 4096 + (I.value & 1) * 1024>(sbase + (wv & 1) * 2048, r8 * (D_ * 2) + cp * 16, ds + (I.value & 1) * (8 * D_ * 2) + (I.value >> 1) * 128); });
      const int po[1] = {O_FA0 + ((b & 7) * 12 + (lane < 40 ? lane >> 2 : 0)) * 128 + (lane & 3) * 16};
      poll<1, 1>(rw, po, tag);
      STAMP(3);
      float mm[3];
#pragma unroll
      for (int i = 0; i < 3; ++i) {
        mm[i] = 0.f;
#pragma unroll
        for (int p = 0; p < MODP; ++p)
          mm[i] += __uint_as_float(__builtin_amdgcn_raw_buffer_load_b32(rw, lane * 4 + i * 256, O_MODP + (p * NMOD + (wv ? 4 : 3) * D_ + kq * 160) * 4, SC1));
      }
#pragma unroll
      for (int i = 0; i < 3; ++i)
        if (i * 64 + lane < 160) { if (wv) LC[i * 64 + lane] = rbf(1.f + rbf(mm[i])); else LS[i * 64 + lane] = (u16)pack_bf16(mm[i], 0.f); }
    } else {
      const int gt = (wv - 1) * 96 + (95 - j);
      if (gt < 160) {
        const rsrc_t rm = mk_rsrc(wmod, D_ * NMOD * 2);
        const int gk = gt & 3, gq = gt >> 2, gcol = gq < 30 ? gq : gq + 20;
        u32x4 wm[20]; u32 cv[3];
#pragma unroll
        for (int i = 0; i < 3; ++i) cv[i] = cIn[gk * 160 + (i * 64 + lane < 160 ? i * 64 + lane : 0)];
#pragma unroll
        for (int it = 0; it < 20; ++it) wm[it] = __builtin_amdgcn_raw_buffer_load_b128(rm, r8 * (NMOD * 2) + cp * 16, (gk * 160 + it * 8) * (NMOD * 2) + gcol * 128, NT);
        SCHED();
        float sl[3], sv[20];
#pragma unroll
        for (int i = 0; i < 3; ++i) sl[i] = silu(cv[i]);
#pragma unroll
        for (int it = 0; it < 20; ++it) sv[it] = __shfl(sl[it >> 3], (it & 7) * 8 + r8);
        f32x2 acc[4];
#pragma unroll
        for (int it = 0; it < 20; ++it) {
          const f32x2 s2 = {sv[it], sv[it]};
#pragma unroll
          for (int q = 0; q < 4; ++q) {
            const f32x2 w2 = {bflo(wm[it][q]), bfhi(wm[it][q])};
            acc[q] = it ? __builtin_elementwise_fma(s2, w2, acc[q]) : s2 * w2;
          }
        }
        __builtin_amdgcn_raw_buffer_store_b32(__float_as_uint(colsum(acc)), rw, O_MODP + (gk * NMOD + gcol * 64 + cp * 8 + r8) * 4, 0, SC1);
      }
      wait_vm<0>();
      u32 old = 0;
      if (lane == 0) old = __hip_atomic_fetch_add(LN, 1u, __ATOMIC_RELAXED, __HIP_MEMORY_SCOPE_WORKGROUP);
      if (__builtin_amdgcn_readfirstlane(old) == 1 && lane < 8) st_flag(ws, O_FA1 + (lane * 12 + (j >> 3)) * 128 + (j & 7) * 8, tag);
    }
    asm volatile("s_waitcnt lgkmcnt(0)\n\ts_barrier" ::: "memory");
    STAMP(4);
    {
      const int rr = 4 * kg + q4;
      u32x2 lo[10], hi[10], sbl[5], sbh[5]; u32x4 cl[5], ch[5];
      unsigned ad[2];
#pragma unroll
      for (int t = 0; t < 2; ++t) ad[t] = sbase + (p4 >> 1) * AU_OFF + rr * 128 + (((wv * 2 + t) ^ (((rr >> 1) & 3) * 2 + (p4 >> 1))) * 16) + (p4 & 1) * 8;
      static_for<0, 10>([&](auto I) { constexpr int i = I.value; ds_tr<(i >> 1) * 4096>(lo[i], ad[i & 1]); ds_tr<(i >> 1) * 4096 + 2048>(hi[i], ad[i & 1]); });
      const unsigned ac = sbase + AM_OFF + kg * 16, as = sbase + AM_OFF + 640 + kg * 8;
      static_for<0, 5>([&](auto I) { constexpr int i = I.value; ds_b128<i * 128>(cl[i], ac); ds_b128<i * 128 + 64>(ch[i], ac); ds_b64<i * 64>(sbl[i], as); ds_b64<i * 64 + 32>(sbh[i], as); });
      u32x2 dl[5], dh[5];
      const unsigned add = sbase + AD_OFF + (8 * kg + q4) * 128 + p4 * 8;
#pragma unroll
      for (int i = 0; i < 5; ++i) { const int tl = wv * 5 + i; const unsigned a2 = add + (tl >> 2) * 4096 + (tl & 3) * 32; ds_tr<0>(dl[i], a2); ds_tr<512>(dh[i], a2); }
      wait_lgkm<0>();
#pragma unroll
      for (int i = 0; i < 10; ++i) { pin(lo[i]); pin(hi[i]); }
#pragma unroll
      for (int i = 0; i < 5; ++i) { pin(cl[i]); pin(ch[i]); pin(sbl[i]); pin(sbh[i]); pin(dl[i]); pin(dh[i]); }
      const int frow = 2 * (4 * (q4 & 1) + p4) + (q4 >> 1);
      const u32x4 ones = {0x3f803f80u, 0x3f803f80u, 0x3f803f80u, 0x3f803f80u};
      f32x4 aU[2] = {}, aW[2] = {};
#pragma unroll
      for (int i = 0; i < 10; ++i) {
        const int ks = i >> 1, t = i & 1;
        const u32x4 raw = cat(lo[i], hi[i]);
        u32x4 sc;
#pragma unroll
        for (int q = 0; q < 2; ++q) {
          sc[q] = pack_bf16(bflo(raw[q]) * __uint_as_float(cl[ks][2 * q]), bfhi(raw[q]) * __uint_as_float(cl[ks][2 * q + 1]));
          sc[2 + q] = pack_bf16(bflo(raw[2 + q]) * __uint_as_float(ch[ks][2 * q]), bfhi(raw[2 + q]) * __uint_as_float(ch[ks][2 * q + 1]));
        }
        aW[t] = mfma_b(raw, cat(sbl[ks], sbh[ks]), aW[t]);
        aU[t] = mfma_b(sc, ones, aU[t]);
        const int T = ug * 8 + wv * 2 + t;
        const int sw3 = T / 3, ti = T - sw3 * 3;
        __builtin_amdgcn_raw_buffer_store_b128(sc, rw, (kg * 16 + frow) * 16, O_W1F + ((sw3 * W1_STEPS + kq * 5 + ks) * 3 + ti) * 1024, SC1);
      }
#pragma unroll
      for (int i = 0; i < 5; ++i) __builtin_amdgcn_raw_buffer_store_b128(cat(dl[i], dh[i]), rw, l16, O_W2F + (((hf * 4 + wv) * 48 + ks2) * 5 + i) * 1024, SC1);
      const f32x4 su = (lane & 4) ? aU[1] : aU[0], sw = (lane & 4) ? aW[1] : aW[0];
      const float U = p4 == 0 ? su[0] : p4 == 1 ? su[1] : p4 == 2 ? su[2] : su[3];
      const float W = p4 == 0 ? sw[0] : p4 == 1 ? sw[1] : p4 == 2 ? sw[2] : sw[3];
      const float u1 = rbf(U), u2 = rbf(U - u1), u3 = U - u1 - u2;
      const float w1 = rbf(W), w2 = rbf(W - w1), w3 = W - w1 - w2;
      const int T = ug * 8 + wv * 2 + ((lane >> 2) & 1);
      const int sw3 = T / 3, ti = T - sw3 * 3;
      const int crow = 2 * (4 * (kg & 1) + p4) + (kg >> 1);
      if (!(lane & 8))
        __builtin_amdgcn_raw_buffer_store_b128(u32x4{pack_bf16(u1, u2), pack_bf16(u3, w1), pack_bf16(w2, w3), 0u}, rw,
                                               O_W1F + (((sw3 * W1_STEPS + 20) * 3 + ti) * 64 + kq * 16 + crow) * 16, 0, SC1);
    }
    STAMP(5);
  } else {
    const int d = (b >> 1) * 8 + (b & 1) + 2 * wv;
    const int row = lane & 15, kg = lane >> 4;
    u32 xx[16]; u32x4 xs[5];
    const int tt = b & 31, ct = b >> 5;
    const int ov = ((lane >> 2) * 2048 + (lane & 3)) * 16, os = (ct * 16 * 2048 + (tt * 8 + wv * 2) * 4) * 16;
    const int srow = (b * 4 + wv) * 4 + kg;
    {
      const rsrc_t rk = mk_rsrc(kIn, S_ * D_ * 2), rv = mk_rsrc(vIn, S_ * D_ * 2);
      const int vo = ct * 256 + lane * 4, so = (tt * 64 + wv * 16) * 1280;
      const int sh0 = so ? so - 3 * 1280 : 0;
      u32 kk[19], vv[16];
      const u32x4 taps = *(const u32x4*)(shk + (ct * 64 + lane) * 8);
#pragma unroll
      for (int q = 0; q < 3; ++q) kk[q] = __builtin_amdgcn_raw_buffer_load_b32(rk, vo + q * 1280, sh0, NT);
#pragma unroll
      for (int q = 0; q < 8; ++q) kk[3 + q] = __builtin_amdgcn_raw_buffer_load_b32(rk, vo + (q & 1) * 1280, so + (q & 14) * 1280, NT);
#pragma unroll
      for (int q = 0; q < 8; ++q) vv[q] = __builtin_amdgcn_raw_buffer_load_b32(rv, vo + (q & 1) * 1280, so + (q & 14) * 1280, NT);
#pragma unroll
      for (int q = 8; q < 16; ++q) kk[3 + q] = __builtin_amdgcn_raw_buffer_load_b32(rk, vo + (q & 1) * 1280, so + (q & 14) * 1280, NT);
#pragma unroll
      for (int q = 8; q < 16; ++q) vv[q] = __builtin_amdgcn_raw_buffer_load_b32(rv, vo + (q & 1) * 1280, so + (q & 14) * 1280, NT);
      SCHED();
      const rsrc_t rx = mk_rsrc(xIn, S_ * D_ * 2);
#pragma unroll
      for (int q = 0; q < 16; ++q) xx[q] = __builtin_amdgcn_raw_buffer_load_b32(rx, vo + (q & 1) * 1280, so + (q & 14) * 1280, NT);
#pragma unroll
      for (int q = 0; q < 5; ++q) xs[q] = __builtin_amdgcn_raw_buffer_load_b128(rx, srow * 1280 + row * 16 + q * 256, 0, NT);
      SCHED();
      if (so == 0) { kk[0] = 0; kk[1] = 0; kk[2] = 0; }
      const f32x2 w0 = {bflo(taps[0]), bflo(taps[2])}, w1 = {bfhi(taps[0]), bfhi(taps[2])};
      const f32x2 w2 = {bflo(taps[1]), bflo(taps[3])}, w3 = {bfhi(taps[1]), bfhi(taps[3])};
#pragma unroll
      for (int r = 0; r < 2; ++r) {
        f32x2 kv[8];
#pragma unroll
        for (int q = 0; q < 8; ++q) {
          const int i = 8 * r + q;
          f32x2 a = f32x2{bflo(kk[i]), bfhi(kk[i])} * w3;
          a = __builtin_elementwise_fma(f32x2{bflo(kk[i + 1]), bfhi(kk[i + 1])}, w2, a);
          a = __builtin_elementwise_fma(f32x2{bflo(kk[i + 2]), bfhi(kk[i + 2])}, w1, a);
          a = __builtin_elementwise_fma(f32x2{bflo(kk[i + 3]), bfhi(kk[i + 3])}, w0, a);
          kv[q] = a * f32x2{bflo(vv[i]), bfhi(vv[i])};
        }
        u32x4 o0, o1;
#pragma unroll
        for (int t = 0; t < 4; ++t) {
          o0[t] = pack_bf16(kv[7 - 2 * t][0], kv[6 - 2 * t][0]);
          o1[t] = pack_bf16(kv[7 - 2 * t][1], kv[6 - 2 * t][1]);
        }
        __builtin_amdgcn_raw_buffer_store_b128(o0, rw, ov + r * 64, O_KVG + os, SC1);
        __builtin_amdgcn_raw_buffer_store_b128(o1, rw, ov + r * 64, O_KVG + os + 16384, SC1);
      }
    }
    STAMP(1);
    tag = next_tag(ep);
    drain_bar();
    if (tid < 32) st_flag(ws, O_FK + ((b & ~31) + tid) * 256 + (b & 31) * 8, tag);
    STAMP(2);
    {
      const char* fs = (const char*)(ssmk + (size_t)d * S_);
      dma16x4p<32>(sbase + wv * CH_BYTES, l16, fs);
      dma16x4p<FC_BYTES + 28>(sbase + wv * CH_BYTES, l16, fs);
    }
#pragma unroll
    for (int r = 0; r < 2; ++r) {
      u32x4 p0, p1;
#pragma unroll
      for (int t = 0; t < 4; ++t) {
        p0[t] = __builtin_amdgcn_perm(xx[8 * r + 2 * t + 1], xx[8 * r + 2 * t], 0x05040100u);
        p1[t] = __builtin_amdgcn_perm(xx[8 * r + 2 * t + 1], xx[8 * r + 2 * t], 0x07060302u);
      }
      __builtin_amdgcn_raw_buffer_store_b128(p0, rw, ov + r * 64, O_XG + os, SC1);
      __builtin_amdgcn_raw_buffer_store_b128(p1, rw, ov + r * 64, O_XG + os + 16384, SC1);
    }
    {
      const bf16x2 ones = {(__bf16)1.0f, (__bf16)1.0f};
      float s1[2] = {0.f, 0.f}, s2[2] = {0.f, 0.f};
#pragma unroll
      for (int i = 0; i < 5; ++i)
#pragma unroll
        for (int q = 0; q < 4; ++q) {
          const u32 raw = xs[i][q];
          const bf16x2 v = __builtin_bit_cast(bf16x2, raw);
          s1[q & 1] = __builtin_amdgcn_fdot2_f32_bf16(v, ones, s1[q & 1], false);
          s2[q & 1] = __builtin_amdgcn_fdot2_f32_bf16(v, v, s2[q & 1], false);
        }
      const float mean = dpp_add<DPP_ROR8>(sum8(s1[0] + s1[1])) * (1.f / D_);
      const float var = fmaxf(dpp_add<DPP_ROR8>(sum8(s2[0] + s2[1])) * (1.f / D_) - mean * mean, 0.f);
      const float sd = rbf(__builtin_amdgcn_sqrtf(rbf(rbf(var) + 1e-5f)));
      const float rs = __builtin_amdgcn_rcpf(sd);
      const int nt = srow >> 8, col = (srow >> 4) & 15, k4 = (srow >> 2) & 3, rr = srow & 3;
      if (row == 0 && srow < S_) __builtin_amdgcn_raw_buffer_store_b64(u32x2{__float_as_uint(rs), __float_as_uint(-rbf(mean) * rs)}, rw,
                                                                       O_ST1 + (((nt * 2 + (rr >> 1)) * 64 + k4 * 16 + col) * 4 + (rr & 1) * 2) * 4, 0, SC1);
    }
    u32 draw;
    sld32(draw, (const u32*)ssmD + (d >> 1));
    *(u32x2*)(smem + CKV_OFF + tid * 8) = u32x2{0, 0};
    if (lane < 15) *(u32*)(smem + wv * CH_BYTES + (lane >> 3) * FC_BYTES + (lane & 7) * 4) = 0;
    const int rho = row + 1;
    u32 sh = (rho & 1) * 16;
    unsigned aAddr = sbase + wv * CH_BYTES + ((rho >> 1) & 1) * FC_BYTES + 2 * (64 * kg + (rho & ~3));
    pin(sh); pin(aAddr);
    unsigned bA[8];
#pragma unroll
    for (int e = 0; e < 8; ++e) { const int g = 32 + 2 * row + 1 - 8 * kg - e; bA[e] = sbase + CKV_OFF + 16 * ((4 * g + wv) ^ ((g >> 2) & 7)); pin(bA[e]); }
    {
      const int po[1] = {O_FK + b * 256 + wv * 64 + (lane & 3) * 16};
      poll<1, 0>(rw, po, tag);
      STAMP(3);
      const unsigned ko0 = (lane ^ (lane >> 4)) * 16;
      dma16x4<CKV_OFF + 2048>(sbase + wv * 4096, ko0, ko0 ^ 64, ws + O_KVG + b * 16384 + wv * 4096);
    }
    drain_bar();
    if (tid < 8) st_flag(ws, O_FX + (tid * 20 + (b >> 3)) * 128 + (b & 7) * 8, tag);
    STAMP(4);
    f32x4 acc[8];
    u32x2 ra[2][8][2]; u32x4 rb[2][8]; u32 carry[8]; u32x4 A[8];
    u32x4 pf[2]; u32 mraw;
    auto load_n = [&](auto E, auto N) {
      constexpr int e = E.value, n = N.value, q = n / 3, bf = e & 1;
      if constexpr (n % 3 == 0) ds_b64<512 * q + 16 * e>(ra[bf][q][0], aAddr);
      else if constexpr (n % 3 == 1) ds_b64<512 * q + 16 * e + 8>(ra[bf][q][1], aAddr);
      else ds_b128<2048 * q>(rb[bf][q], bA[e]);
    };
    auto build = [&](auto E, auto AA, auto I) {
      constexpr int e = E.value, a = AA.value, i = I.value, bf = e & 1;
      if constexpr (i == 0) A[a][0] = alignbit(ra[bf][a][0][1], ra[bf][a][0][0], sh);
      else if constexpr (i == 1) A[a][1] = alignbit(ra[bf][a][1][0], ra[bf][a][0][1], sh);
      else if constexpr (i == 2) A[a][2] = alignbit(ra[bf][a][1][1], ra[bf][a][1][0], sh);
      else { A[a][3] = alignbit(carry[a], ra[bf][a][1][1], sh); carry[a] = ra[bf][a][0][0]; }
    };
    const int pa1[2] = {O_FX + ((b & 7) * 20 + (lane >> 2)) * 128 + (lane & 3) * 16,
                        (lane < 48 ? O_FA1 + ((b & 7) * 12 + (lane >> 2)) * 128 : O_FX + ((b & 7) * 20 + 4 + (lane >> 2)) * 128) + (lane & 3) * 16};
    static_for<0, 8>([&](auto I) { ds_b32<512 * I.value + 128>(carry[I.value], aAddr); });
    static_for<0, 24>([&](auto N) { load_n(IC<7>{}, N); });
    wait_lgkm<0>();
    static_for<0, 4>([&](auto I) { build(IC<7>{}, IC<0>{}, I); });
    asm volatile("s_nop 1");
    static_for<0, 8>([&](auto EE) {
      constexpr int e = 7 - EE.value, bf = e & 1;
      const u32x4 A0 = A[0];
      static_for<0, 8>([&](auto AA) {
        constexpr int a = AA.value;
        static_for<0, 8 - a>([&](auto BB) {
          constexpr int bb = BB.value, n = conv_gap(a, bb);
          mfma_<(e == 7 && a == 0)>(acc[a + bb], a == 0 ? A0 : A[a], rb[bf][bb]);
          if constexpr (e > 0 && n < 24) load_n(IC<(e > 0 ? e - 1 : 0)>{}, IC<n>{});
          if constexpr (n < 28) build(IC<e>{}, IC<1 + n / 4>{}, IC<n % 4>{});
          if constexpr (e > 0 && n == 30) wait_lgkm<0>();
          if constexpr (e > 0 && (n == 31 || n == 32)) { build(IC<(e > 0 ? e - 1 : 0)>{}, IC<0>{}, IC<2 * (n - 31)>{}); build(IC<(e > 0 ? e - 1 : 0)>{}, IC<0>{}, IC<2 * (n - 31) + 1>{}); }
          if constexpr (e == 3 && n == 28) gld128<0>(pf[0], pa1[0], ws);
          if constexpr (e == 3 && n == 29) gld128<0>(pf[1], pa1[1], ws);
          if constexpr (e == 2 && n == 33) {
            wait_vm<0>(); pin(pf[0]); pin(pf[1]);
            if (!__all(tag_ok(pf[0], tag) && tag_ok(pf[1], tag))) poll<2, 1>(rw, pa1, tag);
            dma16x4<CX_OFF>(sbase + wv * 4096, l16, l16, ws + O_XG + b * 16384 + wv * 4096);
          }
          if constexpr (e == 1 && n == 28) dma16x4<CS_OFF>(sbase + wv * 4096, l16, l16, ws + O_ST1 + wv * 4096);
          if constexpr (e == 1 && n == 34) gld32(mraw, (((lane & 7) < MODP ? (lane & 7) : 0) * NMOD + ((lane >> 3) & 3) * D_) * 4, ws + O_MODP + d * 4);
        });
      });
    });
    STAMP(5);
    asm volatile("s_waitcnt vmcnt(0)\n\ts_nop 7\n\ts_barrier" : "+v"(acc[0]), "+v"(acc[1]), "+v"(acc[2]), "+v"(acc[3]), "+v"(acc[4]), "+v"(acc[5]), "+v"(acc[6]), "+v"(acc[7]), "+v"(mraw), "+s"(draw) :: "memory");
    const float mv = rbf(sum8((lane & 7) < MODP ? __uint_as_float(mraw) : 0.f));
    const float m_ga = rdl(mv, 16), c0 = rdl(mv, 0) * m_ga, c1 = rbf(1.f + rdl(mv, 8)) * m_ga;
    const float Dv = (d & 1) ? bfhi(draw) : bflo(draw);
#pragma unroll
    for (int np = 0; np < 4; ++np) {
      u32x4 o;
#pragma unroll
      for (int u = 0; u < 2; ++u) {
        const int nt = 2 * np + u;
        const int tg = 32 * nt + 2 * row + (kg >> 1), g = 32 + tg;
        const u32x2 kvw = *(const u32x2*)(smem + CKV_OFF + 16 * ((4 * g + wv) ^ ((g >> 2) & 7)) + ((kg & 1) ? 0 : 8));
        const u32x2 xw = *(const u32x2*)(smem + CX_OFF + tg * 64 + wv * 16 + (kg & 1) * 8);
        const f32x4 sA = *(const f32x4*)(smem + CS_OFF + (nt * 2) * 1024 + l16);
        const f32x4 sB = *(const f32x4*)(smem + CS_OFF + (nt * 2 + 1) * 1024 + l16);
        const float kv4[4] = {bfhi(kvw[1]), bflo(kvw[1]), bfhi(kvw[0]), bflo(kvw[0])};
        const float x4[4] = {bflo(xw[0]), bfhi(xw[0]), bflo(xw[1]), bfhi(xw[1])};
        const float rs4[4] = {sA[0], sA[2], sB[0], sB[2]};
        const float nb4[4] = {sA[1], sA[3], sB[1], sB[3]};
        float r4[4];
#pragma unroll
        for (int r = 0; r < 4; ++r) {
          const float yb = fmaf(kv4[r], Dv, acc[nt][r]);
          const float n1 = fmaf(x4[r], rs4[r], nb4[r]);
          const float h = fmaf(n1, c1, c0);
          r4[r] = fmaf(h, yb, x4[r]);
        }
        o[2 * u] = pack_bf16(r4[0], r4[1]); o[2 * u + 1] = pack_bf16(r4[2], r4[3]);
      }
      __builtin_amdgcn_raw_buffer_store_b128(o, rw, l16 + np * 1024, O_X2T + d * (TP_ * 2), SC1);
    }
  }
  STAMP(6);
  drain_bar();
  if (tid < 8) st_flag(ws, O_F2 + (tid * 32 + (b >> 3)) * 128 + (b & 7) * 8, tag);
  STAMP(7);

  int bs = blockIdx.x;
  asm volatile("" : "+s"(bs));
  const int xcd = bs & 7, idx = bs >> 3;
  const int mt = (xcd & 1) * 8 + (idx & 7), nc = (xcd >> 1) * 4 + (idx >> 3);
  const int M = mt * 2 + ((nc >> 2) & 1), N = (nc >> 3) * 4 + (nc & 3);
  const int kg = lane >> 4;
  u32x4 WD[60];
  const char* wdp = ws + O_W2F + (N * 48 + wv * 12) * 5120;
  auto issue_wd = [&](auto I) { constexpr int i = I.value; gld128a<(i & 3) * 1024>(WD[i], l16, wdp + (i >> 2) * 4096); };
  {
    unsigned xoff[2];
#pragma unroll
    for (int p = 0; p < 2; ++p) {
      const int k = 4 * p + (lane >> 4);
      xoff[p] = (k * TP_ + (((lane & 15) ^ (2 * k)) * 8)) * 2;
    }
    const char* xsrc = ws + O_X2T + (8 * wv * TP_ + mt * 128) * 2;
    const char* wsrc = ws + O_W1F + (nc * 4 + wv) * W1_STEPS * 3072;
    const unsigned ldsw = sbase + wv * 2048;
    u32x4 W[G1_R][3];
    auto issue_w = [&](auto I) { constexpr int i = I.value; gld128<(i % 3) * 1024>(W[(i / 3) % G1_R][i % 3], l16, wsrc + (i / 3) * 3072); };
    auto issue = [&](auto O) {
      constexpr int kind = G1S.kind[O.value], i = G1S.idx[O.value];
      if constexpr (kind == 0) dma16<((i >> 1) % G1_SLOTS) * 8192 + (i & 1) * 1024>(ldsw, xoff[i & 1], xsrc + (size_t)(i >> 1) * 32 * TP_ * 2);
      else if constexpr (kind == 1) issue_w(IC<i>{});
      else issue_wd(IC<i>{});
    };
    unsigned xa[8];
#pragma unroll
    for (int jj = 0; jj < 8; ++jj) {
      const int rr = 4 * kg + ((lane & 15) >> 2);
      xa[jj] = sbase + rr * 256 + ((((jj + 2 * wv) & 7) ^ (rr & 7)) * 32) + (lane & 3) * 8;
      pin(xa[jj]);
    }
    u32x4 ones = {0x3f803f80u, 0x3f803f80u, 0x3f803f80u, 0x3f803f80u};
    pin(ones);
    {
      const int fb = O_F2 + (b & 7) * 32 * 128 + (lane & 3) * 16;
      const int pa[1] = {fb + (20 + (lane < 48 ? lane >> 2 : 0)) * 128};
      poll<1, 2>(rw, pa, tag);
      static_for<0, 3 * (G1_R - 1)>(issue_w);
      static_for<0, G1_WD0>(issue_wd);
      STAMP(8);
      if (wv == 0) {
        const int pc[2] = {fb + (lane >> 2) * 128, fb + (16 + ((lane >> 2) & 3)) * 128};
        poll<2, 1>(rw, pc, tag);
      }
      wait_vm<0>();
      bar();
    }
    STAMP(9);
    static_for<0, G1S.first[0]>(issue);
    f32x4 acc[8][3], gram[2], sum1[2];
    u32x2 X[G1_LA + 1][2];
    auto xread = [&](auto T, auto H) {
      constexpr int t = T.value, h = H.value;
      ds_tr<((t >> 3) % G1_SLOTS) * 8192 + h * 4096>(X[t % (G1_LA + 1)][h], xa[t & 7]);
    };
    wait_vm<G1S.first[0] - 2>();
    bar();
    STAMP(10);
    static_for<0, G1_LA>([&](auto T) { xread(T, IC<0>{}); xread(T, IC<1>{}); });
    static_for<0, 160>([&](auto T) {
      constexpr int t = T.value, ks = t >> 3, jj = t & 7, tn = t + G1_LA;
      wait_lgkm<2 * ((tn <= 160 ? tn : 160) - t - 1)>();
      if constexpr (tn < 160 && (tn & 7) == 0) {
        static_assert(g1_pos(0, 2 * (tn / 8) + 1) < G1S.first[t], "not issued yet");
        wait_vm<G1S.first[t] - 1 - g1_pos(0, 2 * (tn / 8) + 1)>();
        bar();
      }
      const u32x4 xb = cat(X[t % (G1_LA + 1)][0], X[t % (G1_LA + 1)][1]);
      mfma_<ks == 0>(acc[jj][0], W[ks % G1_R][0], xb);
      if constexpr (tn < 160) xread(IC<tn>{}, IC<0>{});
      mfma_<ks == 0>(acc[jj][1], W[ks % G1_R][1], xb);
      if constexpr (tn < 160) xread(IC<tn>{}, IC<1>{});
      mfma_<ks == 0>(acc[jj][2], W[ks % G1_R][2], xb);
      static_for<G1S.first[t], G1S.first[t + 1]>(issue);
      if constexpr (jj < 2) { mfma_<ks == 0>(gram[jj], xb, xb); mfma_<ks == 0>(sum1[jj], ones, xb); }
    });
    STAMP(11);
    wait_vm<G1S.n - 1 - g1_pos(1, 62)>();
    asm volatile("" : "+v"(gram[0]), "+v"(gram[1]), "+v"(sum1[0]), "+v"(sum1[1]) :: "memory");
    f32x2* stat = (f32x2*)(smem + G1_STAT);
#pragma unroll
    for (int sj = 0; sj < 2; ++sj) {
      const int c3 = lane & 3;
      const float q = c3 == 0 ? gram[sj][0] : c3 == 1 ? gram[sj][1] : c3 == 2 ? gram[sj][2] : gram[sj][3];
      const float mean = sum1[sj][0] * (1.f / D_);
      const float var = fmaxf(q * (1.f / D_) - mean * mean, 0.f);
      const float sd = rbf(__builtin_amdgcn_sqrtf(rbf(rbf(var) + 1e-5f)));
      const float rs = __builtin_amdgcn_rcpf(sd);
      if (((lane & 15) >> 2) == kg) stat[((sj + 2 * wv) & 7) * 16 + (lane & 15)] = f32x2{__uint_as_float(pack_bf16(-mean, sd)), rs * rs};
    }
    asm volatile("s_waitcnt lgkmcnt(0)\n\ts_barrier" ::: "memory");
    u32* stage = (u32*)smem;
    f32x2 st[8];
#pragma unroll
    for (int jj = 0; jj < 8; ++jj) st[jj] = stat[((jj + 2 * wv) & 7) * 16 + (lane & 15)];
    u32x4 xc[8];
#pragma unroll
    for (int jj = 0; jj < 8; ++jj) {
      const u32 p = __float_as_uint(st[jj][0]);
      xc[jj] = u32x4{__builtin_amdgcn_perm(p, p, 0x01000100u), p, __builtin_amdgcn_perm(p, p, 0x03020302u), 0u};
      pin(xc[jj]);
    }
    asm volatile("s_nop 1");
#pragma unroll
    for (int jj = 0; jj < 8; ++jj)
#pragma unroll
      for (int i = 0; i < 3; ++i) mfma(acc[jj][i], W[20 % G1_R][i], xc[jj]);
    asm volatile("s_nop 7");
#pragma unroll
    for (int jj = 0; jj < 8; ++jj) {
      const int m16 = (jj + 2 * wv) & 7;
#pragma unroll
      for (int i = 0; i < 3; ++i) {
        pin(acc[jj][i]);
        const float g0 = __int_as_float(max(__float_as_int(acc[jj][i][0]), 0)), g1 = __int_as_float(max(__float_as_int(acc[jj][i][2]), 0));
        const int cI = 3 * wv + i;
        stage[(m16 * 3 + (cI >> 2)) * 256 + ((cI & 3) * 16 + (lane & 15)) * 4 + kg] =
            pack_bf16(g0 * st[jj][1] * acc[jj][i][1], g1 * st[jj][1] * acc[jj][i][3]);
      }
    }
    asm volatile("s_waitcnt lgkmcnt(0)\n\ts_barrier" ::: "memory");
#pragma unroll
    for (int i = 0; i < 6; ++i) {
      const int m16 = wv * 2 + i / 3, f3 = i % 3;
      const int g16 = mt * 8 + m16;
      __builtin_amdgcn_raw_buffer_store_b128(*(const u32x4*)(stage + (m16 * 3 + f3) * 256 + lane * 4), rw, l16,
                                             O_H2F + (((g16 >> 2) * 48 + 3 * nc + f3) * 4 + (g16 & 3)) * 1024, SC1);
    }
  }
  STAMP(12);
  drain_bar();
  if (tid < 16) st_flag(ws, O_F3 + (mt * 16 + tid) * 128 + nc * 8, tag);
  static_for<G1_WDN, 60>(issue_wd);
  {
    const char* xs = ws + O_X2T + (N * 80 * TP_ + M * 64) * 2;
    const unsigned xo = ((lane >> 3) * TP_ + (lane & 7) * 8) * 2;
    dma16<XR_OFF>(sbase + wv * 1024, xo, xs + (size_t)wv * 8 * TP_ * 2);
    dma16<XR_OFF + 4096>(sbase + wv * 1024, xo, xs + (size_t)(wv + 4) * 8 * TP_ * 2);
    dma16<XR_OFF + 8192>(sbase + (wv & 1) * 1024, xo, xs + (size_t)((wv & 1) + 8) * 8 * TP_ * 2);
  }
  float* gbl = (float*)(smem + XR_OFF + 10240);
  if (tid < 80) {
    float g = 0.f;
#pragma unroll
    for (int p = 0; p < MODP; ++p) g += __uint_as_float(__builtin_amdgcn_raw_buffer_load_b32(rw, tid * 4, O_MODP + (p * NMOD + 5 * D_ + N * 80) * 4, SC1));
    gbl[tid] = rbf(g);
  }
  const char* hp = ws + O_H2F + (M * 48 + wv * 12) * 4096;
  unsigned hoff[4];
#pragma unroll
  for (int jj = 0; jj < 4; ++jj) { hoff[jj] = ((jj + wv + 1) & 3) * 1024 + l16; pin(hoff[jj]); }
  {
    const int po[1] = {O_F3 + (mt * 16 + nc) * 128 + wv * 32 + (lane & 1) * 16};
    poll<1, 0>(rw, po, tag);
    wait_vm<0>();
  }
  STAMP(13);
  u32x4 H[G2_R][4];
  auto issue_h = [&](auto O) { constexpr int o = O.value; gld128<0>(H[(o >> 2) % G2_R][o & 3], hoff[o & 3], hp + (o >> 2) * 4096); };
  static_for<0, 4 * G2_PRE>(issue_h);
  f32x4 acc[4][5];
  static_for<0, 12>([&](auto KS) {
    constexpr int ks = KS.value;
    constexpr int i0 = (4 * G2_PRE + 5 * ks < 48) ? 4 * G2_PRE + 5 * ks : 48;
    static_assert(i0 >= 4 * (ks + 1) && (i0 + 5 > 48 ? 48 : i0 + 5) <= 4 * (ks + G2_R), "ring");
    wait_vm<i0 - 4 * (ks + 1)>();
    static_for<0, 20>([&](auto G) {
      constexpr int g = G.value, jj = g / 5, i = g % 5;
      mfma_a<ks == 0>(acc[jj][i], WD[ks * 5 + i], H[ks % G2_R][jj]);
      if constexpr ((g & 3) == 0 && i0 + g / 4 < 48) issue_h(IC<i0 + g / 4>{});
    });
  });
  STAMP(14);
  asm volatile("s_nop 7");
  static_for<0, 15>([&](auto G) {
    constexpr int jj = G.value / 5, i = G.value % 5;
    ds_w128<(jj * 5 + i) * 1024>(sbase + ((jj + wv + 1) & 3) * 15360 + l16, acc[jj][i]);
  });
  asm volatile("s_waitcnt lgkmcnt(0)\n\ts_barrier" ::: "memory");
  const f32x4* part = (const f32x4*)(smem + wv * 15360);
  const unsigned xad = sbase + XR_OFF + (4 * kg + ((lane & 15) >> 2)) * 128 + wv * 32 + (lane & 3) * 8;
  u32x2 xw[5];
  static_for<0, 5>([&](auto I) { ds_tr<I.value * 2048>(xw[I.value], xad); });
  wait_lgkm<0>();
  const int pos = M * 64 + wv * 16 + (lane & 15);
  const int l8 = (pos >> 3) & 63;
  const int tok = 256 * (2 * (pos >> 9) + ((pos >> 2) & 1)) + 16 * (l8 & 15) + 4 * (l8 >> 4) + (pos & 3);
#pragma unroll
  for (int i = 0; i < 5; ++i) {
    pin(xw[i]); pin(acc[3][i]);
    f32x4 o = acc[3][i];
#pragma unroll
    for (int sl = 0; sl < 3; ++sl) o += part[(sl * 5 + i) * 64 + lane];
    const float x4[4] = {bflo(xw[i][0]), bfhi(xw[i][0]), bflo(xw[i][1]), bfhi(xw[i][1])};
    const f32x4 gb = *(const f32x4*)(gbl + i * 16 + 4 * kg);
    float r[4];
#pragma unroll
    for (int q = 0; q < 4; ++q) r[q] = fmaf(gb[q], o[q], x4[q]);
    *(u32x2*)(out + (size_t)tok * D_ + N * 80 + i * 16 + 4 * kg) = u32x2{pack_bf16(r[0], r[1]), pack_bf16(r[2], r[3])};
  }
  if (b == 0 && tid == 0) st_flag(ws, O_EPOCH, tag);
  STAMP_END();
}

extern "C" void kernel_launch(void* const* d_in, const int* in_sizes, int n_in, void* d_out, int out_size,
                              void* d_ws, size_t ws_size, hipStream_t stream) {
  k_fused<<<256, 256, 0, stream>>>((const u16*)d_in[0], (const u16*)d_in[1], (const u16*)d_in[2], (const u16*)d_in[3],
                                   (const u16*)d_in[4], (const u16*)d_in[5], (const u16*)d_in[7], (const u16*)d_in[8],
                                   (const u16*)d_in[9], (const u16*)d_in[10], (const u16*)d_in[11], (u16*)d_out, (char*)d_ws + ((ws_size - WS_BYTES) & ~(size_t)4095));
}
```

```cpp
#define PROBE 1
#include <hip/hip_runtime.h>
#include <stdint.h>

typedef unsigned short u16;
typedef unsigned int u32;
typedef __attribute__((ext_vector_type(2))) __bf16 bf16x2;
typedef __attribute__((ext_vector_type(4))) float f32x4;
typedef __attribute__((ext_vector_type(2))) float f32x2;
typedef __attribute__((ext_vector_type(4))) u32 u32x4;
typedef __attribute__((ext_vector_type(2))) u32 u32x2;

constexpr int S_ = 2048, D_ = 640, F_ = 1536;
constexpr int TP_ = S_ + 64;
constexpr int NMOD = 6 * D_;
constexpr int MODP = 4;
constexpr int CWG = 160;

__device__ __forceinline__ float bflo(u32 w) { return __uint_as_float(w << 16); }
__device__ __forceinline__ float bfhi(u32 w) { return __uint_as_float(w & 0xffff0000u); }
__device__ __forceinline__ u32 pack_bf16(float lo, float hi) {
  bf16x2 v; v[0] = (__bf16)lo; v[1] = (__bf16)hi; return __builtin_bit_cast(u32, v);
}
__device__ __forceinline__ float rbf(float x) { __bf16 b = (__bf16)x; return (float)b; }
#define SCHED() __builtin_amdgcn_sched_barrier(0)
template <int V> struct IC { static constexpr int value = V; };
template <int B, int E, class Fn> __device__ __forceinline__ void static_for(Fn&& f) {
  if constexpr (B < E) { f(IC<B>{}); static_for<B + 1, E>(f); }
}
__device__ __forceinline__ void mfma(f32x4& c, u32x4 a, u32x4 b) {
  asm volatile("v_mfma_f32_16x16x32_bf16 %0, %1, %2, %0" : "+v"(c) : "v"(a), "v"(b));
}
__device__ __forceinline__ void mfma0(f32x4& c, u32x4 a, u32x4 b) {
  asm volatile("v_mfma_f32_16x16x32_bf16 %0, %1, %2, 0" : "=&v"(c) : "v"(a), "v"(b));
}
template <bool FIRST> __device__ __forceinline__ void mfma_(f32x4& c, u32x4 a, u32x4 b) { if constexpr (FIRST) mfma0(c, a, b); else mfma(c, a, b); }
__device__ __forceinline__ u32 alignbit(u32 hi, u32 lo, u32 sh) {
  u32 d; asm volatile("v_alignbit_b32 %0, %1, %2, %3" : "=v"(d) : "v"(hi), "v"(lo), "v"(sh)); return d;
}
template <int OFF> __device__ __forceinline__ void ds_tr(u32x2& dst, unsigned addr) {
  asm volatile("ds_read_b64_tr_b16 %0, %1 offset:%2" : "=v"(dst) : "v"(addr), "i"(OFF) : "memory");
}
template <int OFF> __device__ __forceinline__ void ds_b32(u32& dst, unsigned addr) {
  asm volatile("ds_read_b32 %0, %1 offset:%2" : "=v"(dst) : "v"(addr), "i"(OFF) : "memory");
}
template <int OFF> __device__ __forceinline__ void ds_b64(u32x2& dst, unsigned addr) {
  asm volatile("ds_read_b64 %0, %1 offset:%2" : "=v"(dst) : "v"(addr), "i"(OFF) : "memory");
}
template <int OFF> __device__ __forceinline__ void ds_b128(u32x4& dst, unsigned addr) {
  asm volatile("ds_read_b128 %0, %1 offset:%2" : "=v"(dst) : "v"(addr), "i"(OFF) : "memory");
}
template <int OFF> __device__ __forceinline__ void ds_w128(unsigned addr, f32x4 v) {
  asm volatile("ds_write_b128 %0, %1 offset:%2" :: "v"(addr), "v"(v), "i"(OFF) : "memory");
}
template <int OFF> __device__ __forceinline__ void gld128(u32x4& dst, unsigned voff, const char* sb) {
  asm volatile("global_load_dwordx4 %0, %1, %2 offset:%3 sc1" : "=v"(dst) : "v"(voff), "s"(sb), "i"(OFF) : "memory");
}
__device__ __forceinline__ void gld32(u32& dst, unsigned voff, const char* sb) {
  asm volatile("global_load_dword %0, %1, %2 sc1" : "=v"(dst) : "v"(voff), "s"(sb) : "memory");
}
template <int LOFF> __device__ __forceinline__ void dma16(unsigned ldsb, unsigned voff, const char* sb) {
  asm volatile("s_add_u32 m0, %0, %3\n\ts_nop 0\n\tglobal_load_lds_dwordx4 %1, %2 sc1" :: "s"(ldsb), "v"(voff), "s"(sb), "i"(LOFF) : "memory", "scc");
}
template <int LOFF> __device__ __forceinline__ void dma16x4(unsigned ldsb, unsigned v0, unsigned v1, const char* sb) {
  asm volatile("s_add_u32 m0, %0, %4\n\ts_nop 0\n\tglobal_load_lds_dwordx4 %1, %3 sc1\n\tglobal_load_lds_dwordx4 %2, %3 offset:1024 sc1\n\t"
               "global_load_lds_dwordx4 %1, %3 offset:2048 sc1\n\tglobal_load_lds_dwordx4 %2, %3 offset:3072 sc1"
               :: "s"(ldsb), "v"(v0), "v"(v1), "s"(sb), "i"(LOFF) : "memory", "scc");
}
__device__ __forceinline__ void sld32(u32& dst, const void* p) { asm volatile("s_load_dword %0, %1, 0x0" : "=s"(dst) : "s"(p) : "memory"); }
template <int N> __device__ __forceinline__ void wait_vm() { asm volatile("s_waitcnt vmcnt(%0)" :: "i"(N) : "memory"); }
template <int N> __device__ __forceinline__ void wait_lgkm() { asm volatile("s_waitcnt lgkmcnt(%0)" :: "i"(N) : "memory"); }
__device__ __forceinline__ void bar() { asm volatile("s_barrier" ::: "memory"); }
template <class T> __device__ __forceinline__ void pin(T& a) { asm volatile("" : "+v"(a)); }
__device__ __forceinline__ u32x4 cat(u32x2 a, u32x2 b) { return u32x4{a[0], a[1], b[0], b[1]}; }

typedef __amdgpu_buffer_rsrc_t rsrc_t;
__device__ __forceinline__ rsrc_t mk_rsrc(const void* p, int bytes) { return __builtin_amdgcn_make_buffer_rsrc((void*)p, 0, bytes, 0x00020000); }
template <int CTRL> __device__ __forceinline__ float dpp_add(float x) {
  return x + __builtin_bit_cast(float, __builtin_amdgcn_mov_dpp(__builtin_bit_cast(int, x), CTRL, 0xf, 0xf, true));
}
constexpr int DPP_XOR1 = 0xB1, DPP_XOR2 = 0x4E, DPP_HMIRROR = 0x141, DPP_ROR8 = 0x128;
__device__ __forceinline__ float sum8(float x) { return dpp_add<DPP_HMIRROR>(dpp_add<DPP_XOR2>(dpp_add<DPP_XOR1>(x))); }
__device__ __forceinline__ float tred32(float a, float b) {
  auto r = __builtin_amdgcn_permlane32_swap(__float_as_uint(a), __float_as_uint(b), false, false);
  return __uint_as_float(r[0]) + __uint_as_float(r[1]);
}
__device__ __forceinline__ float tred16(float a, float b) {
  auto r = __builtin_amdgcn_permlane16_swap(__float_as_uint(a), __float_as_uint(b), false, false);
  return __uint_as_float(r[0]) + __uint_as_float(r[1]);
}
__device__ __forceinline__ float rdl(float x, int l) { return __int_as_float(__builtin_amdgcn_readlane(__float_as_int(x), l)); }

typedef unsigned long long u64;
#define GAS __attribute__((address_space(1)))
constexpr int SC1 = 16;
#define NT 2
#define NTS " nt"
template <int OFF> __device__ __forceinline__ void gld128a(u32x4& dst, unsigned voff, const char* sb) {
  asm volatile("global_load_dwordx4 %0, %1, %2 offset:%3 sc1" : "=a"(dst) : "v"(voff), "s"(sb), "i"(OFF) : "memory");
}
template <bool FIRST> __device__ __forceinline__ void mfma_a(f32x4& c, u32x4 a, u32x4 b) {
  if constexpr (FIRST) asm volatile("v_mfma_f32_16x16x32_bf16 %0, %1, %2, 0" : "=&v"(c) : "a"(a), "v"(b));
  else asm volatile("v_mfma_f32_16x16x32_bf16 %0, %1, %2, %0" : "+v"(c) : "a"(a), "v"(b));
}
typedef __attribute__((ext_vector_type(8))) __bf16 bf16x8;
__device__ __forceinline__ f32x4 mfma_b(u32x4 a, u32x4 b, f32x4 c) {
  return __builtin_amdgcn_mfma_f32_16x16x32_bf16(__builtin_bit_cast(bf16x8, a), __builtin_bit_cast(bf16x8, b), c, 0, 0, 0);
}
template <int LOFF> __device__ __forceinline__ void dma16p(unsigned ldsb, unsigned voff, const char* sb) {
  asm volatile("s_add_u32 m0, %0, %3\n\ts_nop 0\n\tglobal_load_lds_dwordx4 %1, %2" NTS :: "s"(ldsb), "v"(voff), "s"(sb), "i"(LOFF) : "memory", "scc");
}
template <int LOFF> __device__ __forceinline__ void dma16x4p(unsigned ldsb, unsigned v0, const char* sb) {
  asm volatile("s_add_u32 m0, %0, %3\n\ts_nop 0\n\tglobal_load_lds_dwordx4 %1, %2" NTS "\n\tglobal_load_lds_dwordx4 %1, %2 offset:1024" NTS "\n\t"
               "global_load_lds_dwordx4 %1, %2 offset:2048" NTS "\n\tglobal_load_lds_dwordx4 %1, %2 offset:3072" NTS
               :: "s"(ldsb), "v"(v0), "s"(sb), "i"(LOFF) : "memory", "scc");
}

#define STAMP(I) do { if constexpr ((I) == 0 || (I) == PROBE) { SCHED(); unsigned long long r_; asm volatile("s_memrealtime %0\n\ts_waitcnt lgkmcnt(0)" : "=s"(r_) :: "memory"); ts_[(I) ? 1 : 0] = r_; SCHED(); } } while (0)
#define STAMP_DECL() unsigned long long ts_[2] = {}
#define STAMP_END() do { asm volatile("s_waitcnt vmcnt(0)" ::: "memory"); STAMP(15); unsigned long long e_, n_; asm volatile("s_memrealtime %0\n\ts_waitcnt lgkmcnt(0)" : "=s"(e_) :: "memory"); \
  if (blockIdx.x < 160 && ts_[1] > ts_[0]) { e_ += ts_[1] - ts_[0]; int g_ = 0; do { asm volatile("s_memrealtime %0\n\ts_waitcnt lgkmcnt(0)" : "=s"(n_) :: "memory"); } while (n_ < e_ && ++g_ < 1000000); } } while (0)

__device__ __forceinline__ void gld128p(u32x4& dst, unsigned voff, const char* sb) {
  asm volatile("global_load_dwordx4 %0, %1, %2" NTS : "=v"(dst) : "v"(voff), "s"(sb) : "memory");
}
template <int OFF> __device__ __forceinline__ void gld16p(u32& dst, unsigned voff, const char* sb) {
  asm volatile("global_load_ushort %0, %1, %2 offset:%3" : "=v"(dst) : "v"(voff), "s"(sb), "i"(OFF) : "memory");
}
#define CPF_DONE() asm volatile("" :: "v"(cpf))
constexpr int W1_STEPS = 21;
constexpr int O_EPOCH = 0;
constexpr int O_FK = 4096;
constexpr int O_FA0 = O_FK + 160 * 256;
constexpr int O_FA1 = O_FA0 + 8 * 12 * 128;
constexpr int O_FX = O_FA1 + 8 * 12 * 128;
constexpr int O_F2 = O_FX + 8 * 20 * 128;
constexpr int O_F3 = O_F2 + 8 * 32 * 128;
constexpr int O_MODP = O_F3 + 256 * 128;
constexpr int O_ST1 = O_MODP + MODP * NMOD * 4;
constexpr int O_KVG = O_ST1 + 16384;
constexpr int O_XG = O_KVG + S_ * D_ * 2;
constexpr int O_X2T = O_XG + S_ * D_ * 2;
constexpr int O_W1F = O_X2T + TP_ * D_ * 2;
constexpr int O_W2F = O_W1F + 64 * W1_STEPS * 3 * 1024;
constexpr int O_H2F = O_W2F + 8 * 48 * 5 * 1024;
constexpr int WS_BYTES = O_H2F + S_ * F_ * 2;
static_assert(O_MODP % 256 == 0 && O_ST1 % 256 == 0 && O_KVG % 256 == 0 && O_X2T % 256 == 0 && O_W1F % 256 == 0, "alignment");

constexpr int SPIN_CAP = 1 << 16;
constexpr unsigned CODE_BYTES = 40 * 1024;
__device__ __forceinline__ void st_flag(char* ws, int off, u64 tag) {
  __hip_atomic_store((GAS u64*)(ws + off), tag, __ATOMIC_RELAXED, __HIP_MEMORY_SCOPE_AGENT);
}
__device__ __forceinline__ bool tag_ok(u32x4 v, u64 tag) {
  const u32 lo = (u32)tag, hi = (u32)(tag >> 32);
  return ((v[0] ^ lo) | (v[1] ^ hi) | (v[2] ^ lo) | (v[3] ^ hi)) == 0;
}
template <int NL, int SLEEP> __device__ __forceinline__ void poll(rsrc_t rw, const int (&off)[NL], u64 tag) {
#pragma unroll 1
  for (int sp = 0; sp < SPIN_CAP; ++sp) {
    bool ok = true;
#pragma unroll
    for (int i = 0; i < NL; ++i) ok &= tag_ok(__builtin_amdgcn_raw_buffer_load_b128(rw, off[i], 0, SC1), tag);
    if (__all(ok)) break;
    if (SLEEP) __builtin_amdgcn_s_sleep(SLEEP);
    asm volatile("" ::: "memory");
  }
  asm volatile("" ::: "memory");
}
__device__ __forceinline__ u64 next_tag(u32x2 ep) {
  u32 lo, hi;
  asm volatile("s_waitcnt lgkmcnt(0)\n\ts_add_u32 %0, %2, 1\n\ts_addc_u32 %1, %3, 0" : "=&s"(lo), "=&s"(hi) : "s"(ep[0]), "s"(ep[1]) : "memory", "scc");
  return ((u64)hi << 32) | lo;
}
__device__ __forceinline__ void drain_bar() { asm volatile("s_waitcnt vmcnt(0) lgkmcnt(0)\n\ts_barrier" ::: "memory"); }

constexpr int FC_BYTES = 2080 * 2;
constexpr int CH_BYTES = 2 * FC_BYTES;
constexpr int CKV_OFF = 33280;
constexpr int CX_OFF = CKV_OFF + 2048 + 16384, CS_OFF = CX_OFF + 16384;
constexpr int conv_gap(int a, int b) { int n = 0; for (int i = 0; i < a; ++i) n += 8 - i; return n + b; }
constexpr int G1_R = 6;
constexpr int G1_SLOTS = 8;
constexpr int G1_LA = 4;
constexpr int G1_STAT = G1_SLOTS * 8192;
constexpr int G1_XPRE = 3;
#define WD0 8
constexpr int G1_WD0 = WD0;
constexpr int G1_WD = WD0 + 22;
struct G1Sched { int kind[192]; int idx[192]; int first[162]; int n; };
constexpr G1Sched g1_make() {
  G1Sched S{};
  int mk[128] = {}, mi[128] = {}, ms[128] = {}, nm = 0;
  for (int st = 0; st <= 20; ++st) {
    if (st >= G1_R - 1) for (int i = 0; i < 3; ++i) { mk[nm] = 1; mi[nm] = 3 * st + i; ms[nm] = st; ++nm; }
    if (st < 20) for (int p = 0; p < 2; ++p) { mk[nm] = 0; mi[nm] = 2 * st + p; ms[nm] = st; ++nm; }
  }
  int n = 0, m = 0, wd = G1_WD0;
  for (; m < 2 * G1_XPRE; ++m) { S.kind[n] = mk[m]; S.idx[n] = mi[m]; ++n; }
  for (int t = 0; t < 160; ++t) {
    S.first[t] = n;
    const int ks = t >> 3, jj = t & 7;
    if (m < nm && ms[m] <= ks + G1_R - 1) { S.kind[n] = mk[m]; S.idx[n] = mi[m]; ++n; ++m; }
    else if (wd < G1_WD && t < 150 && m >= nm && jj < 6) { S.kind[n] = 2; S.idx[n] = wd; ++n; ++wd; }
  }
  S.first[160] = n; S.n = n;
  return S;
}
constexpr G1Sched G1S = g1_make();
constexpr int g1_pos(int kind, int idx) { for (int i = 0; i < G1S.n; ++i) if (G1S.kind[i] == kind && G1S.idx[i] == idx) return i; return -1; }
constexpr int g1_wd_count() { int c = G1_WD0; for (int i = 0; i < G1S.n; ++i) c += G1S.kind[i] == 2; return c; }
constexpr int G1_WDN = g1_wd_count();
static_assert(g1_pos(1, 62) >= 0 && g1_pos(0, 39) >= 0, "schedule incomplete");
constexpr int G2_R = 8, G2_PRE = 4;
constexpr int XR_OFF = 69632;
constexpr int LDS_BYTES = 96 * 1024;
constexpr int AG_OFF = 0, AU_OFF = 20480, AD_OFF = 40960, AM_OFF = 61440;

__global__ __launch_bounds__(256, 1) void k_fused(const u16* __restrict__ xIn, const u16* __restrict__ kIn, const u16* __restrict__ vIn,
                                                  const u16* __restrict__ shk, const u16* __restrict__ ssmk, const u16* __restrict__ ssmD,
                                                  const u16* __restrict__ wg, const u16* __restrict__ wu, const u16* __restrict__ wd,
                                                  const u16* __restrict__ cIn, const u16* __restrict__ wmod,
                                                  u16* __restrict__ out, char* __restrict__ ws) {
  __shared__ __attribute__((aligned(16))) char smem[LDS_BYTES];
  STAMP_DECL();
  STAMP(0);
  u32 cpf = 0;
  if (blockIdx.x >= 240) {
    uint64_t pc; asm volatile("s_getpc_b64 %0" : "=s"(pc));
    const unsigned co = (((blockIdx.x >> 3) & 1) * 256 + threadIdx.x) * 128;
    asm volatile("global_load_dword %0, %1, %2" : "=v"(cpf) : "v"(co < CODE_BYTES ? co : 0u), "s"(pc & ~(uint64_t)127) : "memory");
  }
  asm volatile("" :: "s"(xIn), "s"(kIn), "s"(vIn), "s"(shk), "s"(ssmk), "s"(ssmD), "s"(wg), "s"(wu), "s"(wd), "s"(cIn), "s"(wmod), "s"(out), "s"(ws));
  u32x2 ep;
  asm volatile("s_load_dwordx2 %0, %1, 0x0" : "=s"(ep) : "s"(ws) : "memory");
  u64 tag;
  const int tid = threadIdx.x, lane = tid & 63;
  const int wv = __builtin_amdgcn_readfirstlane(tid >> 6);
  const int b = blockIdx.x;
  const unsigned sbase = (unsigned)(size_t)smem;
  const unsigned l16 = lane * 16;
  const rsrc_t rw = mk_rsrc(ws, WS_BYTES);
  u32x4 um[5]; u32 uc[5];
  if (b >= CWG) {
    const int j = b - CWG, r8 = lane >> 3, cp = lane & 7;
    const int ug = j % 24, kq = j / 24;
    const int uk = j & 3, ucol = 30 + (j < 80 ? j >> 2 : 0);
    static_for<0, 5>([&](auto I) { gld16p<I.value * 16>(uc[I.value], r8 * 2, (const char*)(cIn + uk * 160 + wv * 40)); });
    static_for<0, 5>([&](auto I) { gld128p(um[I.value], r8 * (NMOD * 2) + cp * 16, (const char*)(wmod + (size_t)(uk * 160 + wv * 40 + I.value * 8) * NMOD + ucol * 64)); });
    const unsigned key = (r8 >> 1) * 2;
    const unsigned vg = r8 * (F_ * 2) + ((cp ^ key) * 16);
    const char* gs = (const char*)(wg + (size_t)(kq * 160 + wv * 40) * F_ + ug * 64);
    const char* us = (const char*)(wu + (size_t)(kq * 160 + wv * 40) * F_ + ug * 64);
    static_for<0, 5>([&](auto I) {
      dma16p<AG_OFF + I.value * 1024>(sbase + wv * 5120, vg, gs + I.value * (8 * F_ * 2));
      dma16p<AU_OFF + I.value * 1024>(sbase + wv * 5120, vg ^ 16, us + I.value * (8 * F_ * 2));
    });
  }
  int b2 = b;
  asm volatile("" : "+s"(b2));
  if (b2 >= CWG) {
    const int j = b - CWG;
    const int r8 = lane >> 3, cp = lane & 7;
    const int ks2 = j >> 1, hf = j & 1;
    const int ug = j % 24, kq = j / 24;
    float* LC = (float*)(smem + AM_OFF);
    u16* LS = (u16*)(smem + AM_OFF + 640);
    u32* LN = (u32*)(smem + AM_OFF + 1024);
    float* LP = (float*)(smem + AM_OFF + 1280);
    auto silu = [](u32 c) { const float x = bflo(c); return rbf(x * __builtin_amdgcn_rcpf(1.f + __expf(-x))); };
    auto colsum = [&](f32x2 (&acc)[4]) {
      const float a0 = tred16(tred32(acc[0][0], acc[2][0]), tred32(acc[1][0], acc[3][0]));
      const float a1 = tred16(tred32(acc[0][1], acc[2][1]), tred32(acc[1][1], acc[3][1]));
      const float e0 = dpp_add<DPP_ROR8>(a0), e1 = dpp_add<DPP_ROR8>(a1);
      return (lane & 8) ? e1 : e0;
    };
    const int uk = j & 3, ucol = 30 + (j < 80 ? j >> 2 : 0);
    tag = next_tag(ep);
    if (tid == 0) *LN = 0;
    {
      float sv[5];
      wait_vm<15>();
#pragma unroll
      for (int it = 0; it < 5; ++it) { pin(uc[it]); sv[it] = silu(uc[it]); pin(sv[it]); }
      wait_vm<10>();
      f32x2 acc[4];
#pragma unroll
      for (int it = 0; it < 5; ++it) {
        pin(um[it]);
        const f32x2 s2 = {sv[it], sv[it]};
#pragma unroll
        for (int q = 0; q < 4; ++q) {
          const f32x2 w2 = {bflo(um[it][q]), bfhi(um[it][q])};
          acc[q] = it ? __builtin_elementwise_fma(s2, w2, acc[q]) : s2 * w2;
        }
      }
      LP[wv * 64 + cp * 8 + r8] = colsum(acc);
    }
    asm volatile("s_waitcnt lgkmcnt(0)\n\ts_barrier" ::: "memory");
    STAMP(1);
    if (wv == 0 && j < 80) {
      const float m = (LP[lane] + LP[64 + lane]) + (LP[128 + lane] + LP[192 + lane]);
      __builtin_amdgcn_raw_buffer_store_b32(__float_as_uint(m), rw, O_MODP + (uk * NMOD + ucol * 64 + lane) * 4, 0, SC1);
      wait_vm<0>();
      if (lane < 8) st_flag(ws, O_FA0 + (lane * 12 + (j >> 3)) * 128 + (j & 7) * 8, tag);
    } else {
      wait_vm<0>();
    }
    bar();
    STAMP(2);
    CPF_DONE();
    const int kg = lane >> 4, q4 = (lane & 15) >> 2, p4 = lane & 3;
    if (wv == 0 || wv == 3) {
      const char* ds = (const char*)(wd + (size_t)(ks2 * 32 + (wv & 1) * 16) * D_ + hf * 320);
      static_for<0, 10>([&](auto I) { dma16p<AD_OFF + (I.value >> 1) * 4096 + (I.value & 1) * 1024>(sbase + (wv & 1) * 2048, r8 * (D_ * 2) + cp * 16, ds + (I.value & 1) * (8 * D_ * 2) + (I.value >> 1) * 128); });
      const int po[1] = {O_FA0 + ((b & 7) * 12 + (lane < 40 ? lane >> 2 : 0)) * 128 + (lane & 3) * 16};
      poll<1, 1>(rw, po, tag);
      STAMP(3);
      float mm[3];
#pragma unroll
      for (int i = 0; i < 3; ++i) {
        mm[i] = 0.f;
#pragma unroll
        for (int p = 0; p < MODP; ++p)
          mm[i] += __uint_as_float(__builtin_amdgcn_raw_buffer_load_b32(rw, lane * 4 + i * 256, O_MODP + (p * NMOD + (wv ? 4 : 3) * D_ + kq * 160) * 4, SC1));
      }
#pragma unroll
      for (int i = 0; i < 3; ++i)
        if (i * 64 + lane < 160) { if (wv) LC[i * 64 + lane] = rbf(1.f + rbf(mm[i])); else LS[i * 64 + lane] = (u16)pack_bf16(mm[i], 0.f); }
    } else {
      const int gt = (wv - 1) * 96 + (95 - j);
      if (gt < 160) {
        const rsrc_t rm = mk_rsrc(wmod, D_ * NMOD * 2);
        const int gk = gt & 3, gq = gt >> 2, gcol = gq < 30 ? gq : gq + 20;
        u32x4 wm[20]; u32 cv[3];
#pragma unroll
        for (int i = 0; i < 3; ++i) cv[i] = cIn[gk * 160 + (i * 64 + lane < 160 ? i * 64 + lane : 0)];
#pragma unroll
        for (int it = 0; it < 20; ++it) wm[it] = __builtin_amdgcn_raw_buffer_load_b128(rm, r8 * (NMOD * 2) + cp * 16, (gk * 160 + it * 8) * (NMOD * 2) + gcol * 128, NT);
        SCHED();
        float sl[3], sv[20];
#pragma unroll
        for (int i = 0; i < 3; ++i) sl[i] = silu(cv[i]);
#pragma unroll
        for (int it = 0; it < 20; ++it) sv[it] = __shfl(sl[it >> 3], (it & 7) * 8 + r8);
        f32x2 acc[4];
#pragma unroll
        for (int it = 0; it < 20; ++it) {
          const f32x2 s2 = {sv[it], sv[it]};
#pragma unroll
          for (int q = 0; q < 4; ++q) {
            const f32x2 w2 = {bflo(wm[it][q]), bfhi(wm[it][q])};
            acc[q] = it ? __builtin_elementwise_fma(s2, w2, acc[q]) : s2 * w2;
          }
        }
        __builtin_amdgcn_raw_buffer_store_b32(__float_as_uint(colsum(acc)), rw, O_MODP + (gk * NMOD + gcol * 64 + cp * 8 + r8) * 4, 0, SC1);
      }
      wait_vm<0>();
      u32 old = 0;
      if (lane == 0) old = __hip_atomic_fetch_add(LN, 1u, __ATOMIC_RELAXED, __HIP_MEMORY_SCOPE_WORKGROUP);
      if (__builtin_amdgcn_readfirstlane(old) == 1 && lane < 8) st_flag(ws, O_FA1 + (lane * 12 + (j >> 3)) * 128 + (j & 7) * 8, tag);
    }
    asm volatile("s_waitcnt lgkmcnt(0)\n\ts_barrier" ::: "memory");
    STAMP(4);
    {
      const int rr = 4 * kg + q4;
      u32x2 lo[10], hi[10], sbl[5], sbh[5]; u32x4 cl[5], ch[5];
      unsigned ad[2];
#pragma unroll
      for (int t = 0; t < 2; ++t) ad[t] = sbase + (p4 >> 1) * AU_OFF + rr * 128 + (((wv * 2 + t) ^ (((rr >> 1) & 3) * 2 + (p4 >> 1))) * 16) + (p4 & 1) * 8;
      static_for<0, 10>([&](auto I) { constexpr int i = I.value; ds_tr<(i >> 1) * 4096>(lo[i], ad[i & 1]); ds_tr<(i >> 1) * 4096 + 2048>(hi[i], ad[i & 1]); });
      const unsigned ac = sbase + AM_OFF + kg * 16, as = sbase + AM_OFF + 640 + kg * 8;
      static_for<0, 5>([&](auto I) { constexpr int i = I.value; ds_b128<i * 128>(cl[i], ac); ds_b128<i * 128 + 64>(ch[i], ac); ds_b64<i * 64>(sbl[i], as); ds_b64<i * 64 + 32>(sbh[i], as); });
      u32x2 dl[5], dh[5];
      const unsigned add = sbase + AD_OFF + (8 * kg + q4) * 128 + p4 * 8;
#pragma unroll
      for (int i = 0; i < 5; ++i) { const int tl = wv * 5 + i; const unsigned a2 = add + (tl >> 2) * 4096 + (tl & 3) * 32; ds_tr<0>(dl[i], a2); ds_tr<512>(dh[i], a2); }
      wait_lgkm<0>();
#pragma unroll
      for (int i = 0; i < 10; ++i) { pin(lo[i]); pin(hi[i]); }
#pragma unroll
      for (int i = 0; i < 5; ++i) { pin(cl[i]); pin(ch[i]); pin(sbl[i]); pin(sbh[i]); pin(dl[i]); pin(dh[i]); }
      const int frow = 2 * (4 * (q4 & 1) + p4) + (q4 >> 1);
      const u32x4 ones = {0x3f803f80u, 0x3f803f80u, 0x3f803f80u, 0x3f803f80u};
      f32x4 aU[2] = {}, aW[2] = {};
#pragma unroll
      for (int i = 0; i < 10; ++i) {
        const int ks = i >> 1, t = i & 1;
        const u32x4 raw = cat(lo[i], hi[i]);
        u32x4 sc;
#pragma unroll
        for (int q = 0; q < 2; ++q) {
          sc[q] = pack_bf16(bflo(raw[q]) * __uint_as_float(cl[ks][2 * q]), bfhi(raw[q]) * __uint_as_float(cl[ks][2 * q + 1]));
          sc[2 + q] = pack_bf16(bflo(raw[2 + q]) * __uint_as_float(ch[ks][2 * q]), bfhi(raw[2 + q]) * __uint_as_float(ch[ks][2 * q + 1]));
        }
        aW[t] = mfma_b(raw, cat(sbl[ks], sbh[ks]), aW[t]);
        aU[t] = mfma_b(sc, ones, aU[t]);
        const int T = ug * 8 + wv * 2 + t;
        const int sw3 = T / 3, ti = T - sw3 * 3;
        __builtin_amdgcn_raw_buffer_store_b128(sc, rw, (kg * 16 + frow) * 16, O_W1F + ((sw3 * W1_STEPS + kq * 5 + ks) * 3 + ti) * 1024, SC1);
      }
#pragma unroll
      for (int i = 0; i < 5; ++i) __builtin_amdgcn_raw_buffer_store_b128(cat(dl[i], dh[i]), rw, l16, O_W2F + (((hf * 4 + wv) * 48 + ks2) * 5 + i) * 1024, SC1);
      const f32x4 su = (lane & 4) ? aU[1] : aU[0], sw = (lane & 4) ? aW[1] : aW[0];
      const float U = p4 == 0 ? su[0] : p4 == 1 ? su[1] : p4 == 2 ? su[2] : su[3];
      const float W = p4 == 0 ? sw[0] : p4 == 1 ? sw[1] : p4 == 2 ? sw[2] : sw[3];
      const float u1 = rbf(U), u2 = rbf(U - u1), u3 = U - u1 - u2;
      const float w1 = rbf(W), w2 = rbf(W - w1), w3 = W - w1 - w2;
      const int T = ug * 8 + wv * 2 + ((lane >> 2) & 1);
      const int sw3 = T / 3, ti = T - sw3 * 3;
      const int crow = 2 * (4 * (kg & 1) + p4) + (kg >> 1);
      if (!(lane & 8))
        __builtin_amdgcn_raw_buffer_store_b128(u32x4{pack_bf16(u1, u2), pack_bf16(u3, w1), pack_bf16(w2, w3), 0u}, rw,
                                               O_W1F + (((sw3 * W1_STEPS + 20) * 3 + ti) * 64 + kq * 16 + crow) * 16, 0, SC1);
    }
    STAMP(5);
  } else {
    const int d = (b >> 1) * 8 + (b & 1) + 2 * wv;
    const int row = lane & 15, kg = lane >> 4;
    u32 xx[16]; u32x4 xs[5];
    const int tt = b & 31, ct = b >> 5;
    const int ov = ((lane >> 2) * 2048 + (lane & 3)) * 16, os = (ct * 16 * 2048 + (tt * 8 + wv * 2) * 4) * 16;
    const int srow = (b * 4 + wv) * 4 + kg;
    {
      const rsrc_t rk = mk_rsrc(kIn, S_ * D_ * 2), rv = mk_rsrc(vIn, S_ * D_ * 2);
      const int vo = ct * 256 + lane * 4, so = (tt * 64 + wv * 16) * 1280;
      const int sh0 = so ? so - 3 * 1280 : 0;
      u32 kk[19], vv[16];
      const u32x4 taps = *(const u32x4*)(shk + (ct * 64 + lane) * 8);
#pragma unroll
      for (int q = 0; q < 3; ++q) kk[q] = __builtin_amdgcn_raw_buffer_load_b32(rk, vo + q * 1280, sh0, NT);
#pragma unroll
      for (int q = 0; q < 8; ++q) kk[3 + q] = __builtin_amdgcn_raw_buffer_load_b32(rk, vo + (q & 1) * 1280, so + (q & 14) * 1280, NT);
#pragma unroll
      for (int q = 0; q < 8; ++q) vv[q] = __builtin_amdgcn_raw_buffer_load_b32(rv, vo + (q & 1) * 1280, so + (q & 14) * 1280, NT);
#pragma unroll
      for (int q = 8; q < 16; ++q) kk[3 + q] = __builtin_amdgcn_raw_buffer_load_b32(rk, vo + (q & 1) * 1280, so + (q & 14) * 1280, NT);
#pragma unroll
      for (int q = 8; q < 16; ++q) vv[q] = __builtin_amdgcn_raw_buffer_load_b32(rv, vo + (q & 1) * 1280, so + (q & 14) * 1280, NT);
      SCHED();
      const rsrc_t rx = mk_rsrc(xIn, S_ * D_ * 2);
#pragma unroll
      for (int q = 0; q < 16; ++q) xx[q] = __builtin_amdgcn_raw_buffer_load_b32(rx, vo + (q & 1) * 1280, so + (q & 14) * 1280, NT);
#pragma unroll
      for (int q = 0; q < 5; ++q) xs[q] = __builtin_amdgcn_raw_buffer_load_b128(rx, srow * 1280 + row * 16 + q * 256, 0, NT);
      SCHED();
      if (so == 0) { kk[0] = 0; kk[1] = 0; kk[2] = 0; }
      const f32x2 w0 = {bflo(taps[0]), bflo(taps[2])}, w1 = {bfhi(taps[0]), bfhi(taps[2])};
      const f32x2 w2 = {bflo(taps[1]), bflo(taps[3])}, w3 = {bfhi(taps[1]), bfhi(taps[3])};
#pragma unroll
      for (int r = 0; r < 2; ++r) {
        f32x2 kv[8];
#pragma unroll
        for (int q = 0; q < 8; ++q) {
          const int i = 8 * r + q;
          f32x2 a = f32x2{bflo(kk[i]), bfhi(kk[i])} * w3;
          a = __builtin_elementwise_fma(f32x2{bflo(kk[i + 1]), bfhi(kk[i + 1])}, w2, a);
          a = __builtin_elementwise_fma(f32x2{bflo(kk[i + 2]), bfhi(kk[i + 2])}, w1, a);
          a = __builtin_elementwise_fma(f32x2{bflo(kk[i + 3]), bfhi(kk[i + 3])}, w0, a);
          kv[q] = a * f32x2{bflo(vv[i]), bfhi(vv[i])};
        }
        u32x4 o0, o1;
#pragma unroll
        for (int t = 0; t < 4; ++t) {
          o0[t] = pack_bf16(kv[7 - 2 * t][0], kv[6 - 2 * t][0]);
          o1[t] = pack_bf16(kv[7 - 2 * t][1], kv[6 - 2 * t][1]);
        }
        __builtin_amdgcn_raw_buffer_store_b128(o0, rw, ov + r * 64, O_KVG + os, SC1);
        __builtin_amdgcn_raw_buffer_store_b128(o1, rw, ov + r * 64, O_KVG + os + 16384, SC1);
      }
    }
    STAMP(1);
    tag = next_tag(ep);
    drain_bar();
    if (tid < 32) st_flag(ws, O_FK + ((b & ~31) + tid) * 256 + (b & 31) * 8, tag);
    STAMP(2);
    {
      const char* fs = (const char*)(ssmk + (size_t)d * S_);
      dma16x4p<32>(sbase + wv * CH_BYTES, l16, fs);
      dma16x4p<FC_BYTES + 28>(sbase + wv * CH_BYTES, l16, fs);
    }
#pragma unroll
    for (int r = 0; r < 2; ++r) {
      u32x4 p0, p1;
#pragma unroll
      for (int t = 0; t < 4; ++t) {
        p0[t] = __builtin_amdgcn_perm(xx[8 * r + 2 * t + 1], xx[8 * r + 2 * t], 0x05040100u);
        p1[t] = __builtin_amdgcn_perm(xx[8 * r + 2 * t + 1], xx[8 * r + 2 * t], 0x07060302u);
      }
      __builtin_amdgcn_raw_buffer_store_b128(p0, rw, ov + r * 64, O_XG + os, SC1);
      __builtin_amdgcn_raw_buffer_store_b128(p1, rw, ov + r * 64, O_XG + os + 16384, SC1);
    }
    {
      const bf16x2 ones = {(__bf16)1.0f, (__bf16)1.0f};
      float s1[2] = {0.f, 0.f}, s2[2] = {0.f, 0.f};
#pragma unroll
      for (int i = 0; i < 5; ++i)
#pragma unroll
        for (int q = 0; q < 4; ++q) {
          const u32 raw = xs[i][q];
          const bf16x2 v = __builtin_bit_cast(bf16x2, raw);
          s1[q & 1] = __builtin_amdgcn_fdot2_f32_bf16(v, ones, s1[q & 1], false);
          s2[q & 1] = __builtin_amdgcn_fdot2_f32_bf16(v, v, s2[q & 1], false);
        }
      const float mean = dpp_add<DPP_ROR8>(sum8(s1[0] + s1[1])) * (1.f / D_);
      const float var = fmaxf(dpp_add<DPP_ROR8>(sum8(s2[0] + s2[1])) * (1.f / D_) - mean * mean, 0.f);
      const float sd = rbf(__builtin_amdgcn_sqrtf(rbf(rbf(var) + 1e-5f)));
      const float rs = __builtin_amdgcn_rcpf(sd);
      const int nt = srow >> 8, col = (srow >> 4) & 15, k4 = (srow >> 2) & 3, rr = srow & 3;
      if (row == 0 && srow < S_) __builtin_amdgcn_raw_buffer_store_b64(u32x2{__float_as_uint(rs), __float_as_uint(-rbf(mean) * rs)}, rw,
                                                                       O_ST1 + (((nt * 2 + (rr >> 1)) * 64 + k4 * 16 + col) * 4 + (rr & 1) * 2) * 4, 0, SC1);
    }
    u32 draw;
    sld32(draw, (const u32*)ssmD + (d >> 1));
    *(u32x2*)(smem + CKV_OFF + tid * 8) = u32x2{0, 0};
    if (lane < 15) *(u32*)(smem + wv * CH_BYTES + (lane >> 3) * FC_BYTES + (lane & 7) * 4) = 0;
    const int rho = row + 1;
    u32 sh = (rho & 1) * 16;
    unsigned aAddr = sbase + wv * CH_BYTES + ((rho >> 1) & 1) * FC_BYTES + 2 * (64 * kg + (rho & ~3));
    pin(sh); pin(aAddr);
    unsigned bA[8];
#pragma unroll
    for (int e = 0; e < 8; ++e) { const int g = 32 + 2 * row + 1 - 8 * kg - e; bA[e] = sbase + CKV_OFF + 16 * ((4 * g + wv) ^ ((g >> 2) & 7)); pin(bA[e]); }
    {
      const int po[1] = {O_FK + b * 256 + wv * 64 + (lane & 3) * 16};
      poll<1, 0>(rw, po, tag);
      STAMP(3);
      const unsigned ko0 = (lane ^ (lane >> 4)) * 16;
      dma16x4<CKV_OFF + 2048>(sbase + wv * 4096, ko0, ko0 ^ 64, ws + O_KVG + b * 16384 + wv * 4096);
    }
    drain_bar();
    if (tid < 8) st_flag(ws, O_FX + (tid * 20 + (b >> 3)) * 128 + (b & 7) * 8, tag);
    STAMP(4);
    f32x4 acc[8];
    u32x2 ra[2][8][2]; u32x4 rb[2][8]; u32 carry[8]; u32x4 A[8];
    u32x4 pf[2]; u32 mraw;
    auto load_n = [&](auto E, auto N) {
      constexpr int e = E.value, n = N.value, q = n / 3, bf = e & 1;
      if constexpr (n % 3 == 0) ds_b64<512 * q + 16 * e>(ra[bf][q][0], aAddr);
      else if constexpr (n % 3 == 1) ds_b64<512 * q + 16 * e + 8>(ra[bf][q][1], aAddr);
      else ds_b128<2048 * q>(rb[bf][q], bA[e]);
    };
    auto build = [&](auto E, auto AA, auto I) {
      constexpr int e = E.value, a = AA.value, i = I.value, bf = e & 1;
      if constexpr (i == 0) A[a][0] = alignbit(ra[bf][a][0][1], ra[bf][a][0][0], sh);
      else if constexpr (i == 1) A[a][1] = alignbit(ra[bf][a][1][0], ra[bf][a][0][1], sh);
      else if constexpr (i == 2) A[a][2] = alignbit(ra[bf][a][1][1], ra[bf][a][1][0], sh);
      else { A[a][3] = alignbit(carry[a], ra[bf][a][1][1], sh); carry[a] = ra[bf][a][0][0]; }
    };
    const int pa1[2] = {O_FX + ((b & 7) * 20 + (lane >> 2)) * 128 + (lane & 3) * 16,
                        (lane < 48 ? O_FA1 + ((b & 7) * 12 + (lane >> 2)) * 128 : O_FX + ((b & 7) * 20 + 4 + (lane >> 2)) * 128) + (lane & 3) * 16};
    static_for<0, 8>([&](auto I) { ds_b32<512 * I.value + 128>(carry[I.value], aAddr); });
    static_for<0, 24>([&](auto N) { load_n(IC<7>{}, N); });
    wait_lgkm<0>();
    static_for<0, 4>([&](auto I) { build(IC<7>{}, IC<0>{}, I); });
    asm volatile("s_nop 1");
    static_for<0, 8>([&](auto EE) {
      constexpr int e = 7 - EE.value, bf = e & 1;
      const u32x4 A0 = A[0];
      static_for<0, 8>([&](auto AA) {
        constexpr int a = AA.value;
        static_for<0, 8 - a>([&](auto BB) {
          constexpr int bb = BB.value, n = conv_gap(a, bb);
          mfma_<(e == 7 && a == 0)>(acc[a + bb], a == 0 ? A0 : A[a], rb[bf][bb]);
          if constexpr (e > 0 && n < 24) load_n(IC<(e > 0 ? e - 1 : 0)>{}, IC<n>{});
          if constexpr (n < 28) build(IC<e>{}, IC<1 + n / 4>{}, IC<n % 4>{});
          if constexpr (e > 0 && n == 30) wait_lgkm<0>();
          if constexpr (e > 0 && (n == 31 || n == 32)) { build(IC<(e > 0 ? e - 1 : 0)>{}, IC<0>{}, IC<2 * (n - 31)>{}); build(IC<(e > 0 ? e - 1 : 0)>{}, IC<0>{}, IC<2 * (n - 31) + 1>{}); }
          if constexpr (e == 3 && n == 28) gld128<0>(pf[0], pa1[0], ws);
          if constexpr (e == 3 && n == 29) gld128<0>(pf[1], pa1[1], ws);
          if constexpr (e == 2 && n == 33) {
            wait_vm<0>(); pin(pf[0]); pin(pf[1]);
            if (!__all(tag_ok(pf[0], tag) && tag_ok(pf[1], tag))) poll<2, 1>(rw, pa1, tag);
            dma16x4<CX_OFF>(sbase + wv * 4096, l16, l16, ws + O_XG + b * 16384 + wv * 4096);
          }
          if constexpr (e == 1 && n == 28) dma16x4<CS_OFF>(sbase + wv * 4096, l16, l16, ws + O_ST1 + wv * 4096);
          if constexpr (e == 1 && n == 34) gld32(mraw, (((lane & 7) < MODP ? (lane & 7) : 0) * NMOD + ((lane >> 3) & 3) * D_) * 4, ws + O_MODP + d * 4);
        });
      });
    });
    STAMP(5);
    asm volatile("s_waitcnt vmcnt(0)\n\ts_nop 7\n\ts_barrier" : "+v"(acc[0]), "+v"(acc[1]), "+v"(acc[2]), "+v"(acc[3]), "+v"(acc[4]), "+v"(acc[5]), "+v"(acc[6]), "+v"(acc[7]), "+v"(mraw), "+s"(draw) :: "memory");
    const float mv = rbf(sum8((lane & 7) < MODP ? __uint_as_float(mraw) : 0.f));
    const float m_ga = rdl(mv, 16), c0 = rdl(mv, 0) * m_ga, c1 = rbf(1.f + rdl(mv, 8)) * m_ga;
    const float Dv = (d & 1) ? bfhi(draw) : bflo(draw);
#pragma unroll
    for (int np = 0; np < 4; ++np) {
      u32x4 o;
#pragma unroll
      for (int u = 0; u < 2; ++u) {
        const int nt = 2 * np + u;
        const int tg = 32 * nt + 2 * row + (kg >> 1), g = 32 + tg;
        const u32x2 kvw = *(const u32x2*)(smem + CKV_OFF + 16 * ((4 * g + wv) ^ ((g >> 2) & 7)) + ((kg & 1) ? 0 : 8));
        const u32x2 xw = *(const u32x2*)(smem + CX_OFF + tg * 64 + wv * 16 + (kg & 1) * 8);
        const f32x4 sA = *(const f32x4*)(smem + CS_OFF + (nt * 2) * 1024 + l16);
        const f32x4 sB = *(const f32x4*)(smem + CS_OFF + (nt * 2 + 1) * 1024 + l16);
        const float kv4[4] = {bfhi(kvw[1]), bflo(kvw[1]), bfhi(kvw[0]), bflo(kvw[0])};
        const float x4[4] = {bflo(xw[0]), bfhi(xw[0]), bflo(xw[1]), bfhi(xw[1])};
        const float rs4[4] = {sA[0], sA[2], sB[0], sB[2]};
        const float nb4[4] = {sA[1], sA[3], sB[1], sB[3]};
        float r4[4];
#pragma unroll
        for (int r = 0; r < 4; ++r) {
          const float yb = fmaf(kv4[r], Dv, acc[nt][r]);
          const float n1 = fmaf(x4[r], rs4[r], nb4[r]);
          const float h = fmaf(n1, c1, c0);
          r4[r] = fmaf(h, yb, x4[r]);
        }
        o[2 * u] = pack_bf16(r4[0], r4[1]); o[2 * u + 1] = pack_bf16(r4[2], r4[3]);
      }
      __builtin_amdgcn_raw_buffer_store_b128(o, rw, l16 + np * 1024, O_X2T + d * (TP_ * 2), SC1);
    }
  }
  STAMP(6);
  drain_bar();
  if (tid < 8) st_flag(ws, O_F2 + (tid * 32 + (b >> 3)) * 128 + (b & 7) * 8, tag);
  STAMP(7);

  int bs = blockIdx.x;
  asm volatile("" : "+s"(bs));
  const int xcd = bs & 7, idx = bs >> 3;
  const int mt = (xcd & 1) * 8 + (idx & 7), nc = (xcd >> 1) * 4 + (idx >> 3);
  const int M = mt * 2 + ((nc >> 2) & 1), N = (nc >> 3) * 4 + (nc & 3);
  const int kg = lane >> 4;
  u32x4 WD[60];
  const char* wdp = ws + O_W2F + (N * 48 + wv * 12) * 5120;
  auto issue_wd = [&](auto I) { constexpr int i = I.value; gld128a<(i & 3) * 1024>(WD[i], l16, wdp + (i >> 2) * 4096); };
  {
    unsigned xoff[2];
#pragma unroll
    for (int p = 0; p < 2; ++p) {
      const int k = 4 * p + (lane >> 4);
      xoff[p] = (k * TP_ + (((lane & 15) ^ (2 * k)) * 8)) * 2;
    }
    const char* xsrc = ws + O_X2T + (8 * wv * TP_ + mt * 128) * 2;
    const char* wsrc = ws + O_W1F + (nc * 4 + wv) * W1_STEPS * 3072;
    const unsigned ldsw = sbase + wv * 2048;
    u32x4 W[G1_R][3];
    auto issue_w = [&](auto I) { constexpr int i = I.value; gld128<(i % 3) * 1024>(W[(i / 3) % G1_R][i % 3], l16, wsrc + (i / 3) * 3072); };
    auto issue = [&](auto O) {
      constexpr int kind = G1S.kind[O.value], i = G1S.idx[O.value];
      if constexpr (kind == 0) dma16<((i >> 1) % G1_SLOTS) * 8192 + (i & 1) * 1024>(ldsw, xoff[i & 1], xsrc + (size_t)(i >> 1) * 32 * TP_ * 2);
      else if constexpr (kind == 1) issue_w(IC<i>{});
      else issue_wd(IC<i>{});
    };
    unsigned xa[8];
#pragma unroll
    for (int jj = 0; jj < 8; ++jj) {
      const int rr = 4 * kg + ((lane & 15) >> 2);
      xa[jj] = sbase + rr * 256 + ((((jj + 2 * wv) & 7) ^ (rr & 7)) * 32) + (lane & 3) * 8;
      pin(xa[jj]);
    }
    u32x4 ones = {0x3f803f80u, 0x3f803f80u, 0x3f803f80u, 0x3f803f80u};
    pin(ones);
    {
      const int fb = O_F2 + (b & 7) * 32 * 128 + (lane & 3) * 16;
      const int pa[1] = {fb + (20 + (lane < 48 ? lane >> 2 : 0)) * 128};
      poll<1, 2>(rw, pa, tag);
      static_for<0, 3 * (G1_R - 1)>(issue_w);
      static_for<0, G1_WD0>(issue_wd);
      STAMP(8);
      if (wv == 0) {
        const int pc[2] = {fb + (lane >> 2) * 128, fb + (16 + ((lane >> 2) & 3)) * 128};
        poll<2, 1>(rw, pc, tag);
      }
      wait_vm<0>();
      bar();
    }
    STAMP(9);
    static_for<0, G1S.first[0]>(issue);
    f32x4 acc[8][3], gram[2], sum1[2];
    u32x2 X[G1_LA + 1][2];
    auto xread = [&](auto T, auto H) {
      constexpr int t = T.value, h = H.value;
      ds_tr<((t >> 3) % G1_SLOTS) * 8192 + h * 4096>(X[t % (G1_LA + 1)][h], xa[t & 7]);
    };
    wait_vm<G1S.first[0] - 2>();
    bar();
    STAMP(10);
    static_for<0, G1_LA>([&](auto T) { xread(T, IC<0>{}); xread(T, IC<1>{}); });
    static_for<0, 160>([&](auto T) {
      constexpr int t = T.value, ks = t >> 3, jj = t & 7, tn = t + G1_LA;
      wait_lgkm<2 * ((tn <= 160 ? tn : 160) - t - 1)>();
      if constexpr (tn < 160 && (tn & 7) == 0) {
        static_assert(g1_pos(0, 2 * (tn / 8) + 1) < G1S.first[t], "not issued yet");
        wait_vm<G1S.first[t] - 1 - g1_pos(0, 2 * (tn / 8) + 1)>();
        bar();
      }
      const u32x4 xb = cat(X[t % (G1_LA + 1)][0], X[t % (G1_LA + 1)][1]);
      mfma_<ks == 0>(acc[jj][0], W[ks % G1_R][0], xb);
      if constexpr (tn < 160) xread(IC<tn>{}, IC<0>{});
      mfma_<ks == 0>(acc[jj][1], W[ks % G1_R][1], xb);
      if constexpr (tn < 160) xread(IC<tn>{}, IC<1>{});
      mfma_<ks == 0>(acc[jj][2], W[ks % G1_R][2], xb);
      static_for<G1S.first[t], G1S.first[t + 1]>(issue);
      if constexpr (jj < 2) { mfma_<ks == 0>(gram[jj], xb, xb); mfma_<ks == 0>(sum1[jj], ones, xb); }
    });
    STAMP(11);
    wait_vm<G1S.n - 1 - g1_pos(1, 62)>();
    asm volatile("" : "+v"(gram[0]), "+v"(gram[1]), "+v"(sum1[0]), "+v"(sum1[1]) :: "memory");
    f32x2* stat = (f32x2*)(smem + G1_STAT);
#pragma unroll
    for (int sj = 0; sj < 2; ++sj) {
      const int c3 = lane & 3;
      const float q = c3 == 0 ? gram[sj][0] : c3 == 1 ? gram[sj][1] : c3 == 2 ? gram[sj][2] : gram[sj][3];
      const float mean = sum1[sj][0] * (1.f / D_);
      const float var = fmaxf(q * (1.f / D_) - mean * mean, 0.f);
      const float sd = rbf(__builtin_amdgcn_sqrtf(rbf(rbf(var) + 1e-5f)));
      const float rs = __builtin_amdgcn_rcpf(sd);
      if (((lane & 15) >> 2) == kg) stat[((sj + 2 * wv) & 7) * 16 + (lane & 15)] = f32x2{__uint_as_float(pack_bf16(-mean, sd)), rs * rs};
    }
    asm volatile("s_waitcnt lgkmcnt(0)\n\ts_barrier" ::: "memory");
    u32* stage = (u32*)smem;
    f32x2 st[8];
#pragma unroll
    for (int jj = 0; jj < 8; ++jj) st[jj] = stat[((jj + 2 * wv) & 7) * 16 + (lane & 15)];
    u32x4 xc[8];
#pragma unroll
    for (int jj = 0; jj < 8; ++jj) {
      const u32 p = __float_as_uint(st[jj][0]);
      xc[jj] = u32x4{__builtin_amdgcn_perm(p, p, 0x01000100u), p, __builtin_amdgcn_perm(p, p, 0x03020302u), 0u};
      pin(xc[jj]);
    }
    asm volatile("s_nop 1");
#pragma unroll
    for (int jj = 0; jj < 8; ++jj)
#pragma unroll
      for (int i = 0; i < 3; ++i) mfma(acc[jj][i], W[20 % G1_R][i], xc[jj]);
    asm volatile("s_nop 7");
#pragma unroll
    for (int jj = 0; jj < 8; ++jj) {
      const int m16 = (jj + 2 * wv) & 7;
#pragma unroll
      for (int i = 0; i < 3; ++i) {
        pin(acc[jj][i]);
        const float g0 = __int_as_float(max(__float_as_int(acc[jj][i][0]), 0)), g1 = __int_as_float(max(__float_as_int(acc[jj][i][2]), 0));
        const int cI = 3 * wv + i;
        stage[(m16 * 3 + (cI >> 2)) * 256 + ((cI & 3) * 16 + (lane & 15)) * 4 + kg] =
            pack_bf16(g0 * st[jj][1] * acc[jj][i][1], g1 * st[jj][1] * acc[jj][i][3]);
      }
    }
    asm volatile("s_waitcnt lgkmcnt(0)\n\ts_barrier" ::: "memory");
#pragma unroll
    for (int i = 0; i < 6; ++i) {
      const int m16 = wv * 2 + i / 3, f3 = i % 3;
      const int g16 = mt * 8 + m16;
      __builtin_amdgcn_raw_buffer_store_b128(*(const u32x4*)(stage + (m16 * 3 + f3) * 256 + lane * 4), rw, l16,
                                             O_H2F + (((g16 >> 2) * 48 + 3 * nc + f3) * 4 + (g16 & 3)) * 1024, SC1);
    }
  }
  STAMP(12);
  drain_bar();
  if (tid < 16) st_flag(ws, O_F3 + (mt * 16 + tid) * 128 + nc * 8, tag);
  static_for<G1_WDN, 60>(issue_wd);
  {
    const char* xs = ws + O_X2T + (N * 80 * TP_ + M * 64) * 2;
    const unsigned xo = ((lane >> 3) * TP_ + (lane & 7) * 8) * 2;
    dma16<XR_OFF>(sbase + wv * 1024, xo, xs + (size_t)wv * 8 * TP_ * 2);
    dma16<XR_OFF + 4096>(sbase + wv * 1024, xo, xs + (size_t)(wv + 4) * 8 * TP_ * 2);
    dma16<XR_OFF + 8192>(sbase + (wv & 1) * 1024, xo, xs + (size_t)((wv & 1) + 8) * 8 * TP_ * 2);
  }
  float* gbl = (float*)(smem + XR_OFF + 10240);
  if (tid < 80) {
    float g = 0.f;
#pragma unroll
    for (int p = 0; p < MODP; ++p) g += __uint_as_float(__builtin_amdgcn_raw_buffer_load_b32(rw, tid * 4, O_MODP + (p * NMOD + 5 * D_ + N * 80) * 4, SC1));
    gbl[tid] = rbf(g);
  }
  const char* hp = ws + O_H2F + (M * 48 + wv * 12) * 4096;
  unsigned hoff[4];
#pragma unroll
  for (int jj = 0; jj < 4; ++jj) { hoff[jj] = ((jj + wv + 1) & 3) * 1024 + l16; pin(hoff[jj]); }
  {
    const int po[1] = {O_F3 + (mt * 16 + nc) * 128 + wv * 32 + (lane & 1) * 16};
    poll<1, 0>(rw, po, tag);
    wait_vm<0>();
  }
  STAMP(13);
  u32x4 H[G2_R][4];
  auto issue_h = [&](auto O) { constexpr int o = O.value; gld128<0>(H[(o >> 2) % G2_R][o & 3], hoff[o & 3], hp + (o >> 2) * 4096); };
  static_for<0, 4 * G2_PRE>(issue_h);
  f32x4 acc[4][5];
  static_for<0, 12>([&](auto KS) {
    constexpr int ks = KS.value;
    constexpr int i0 = (4 * G2_PRE + 5 * ks < 48) ? 4 * G2_PRE + 5 * ks : 48;
    static_assert(i0 >= 4 * (ks + 1) && (i0 + 5 > 48 ? 48 : i0 + 5) <= 4 * (ks + G2_R), "ring");
    wait_vm<i0 - 4 * (ks + 1)>();
    static_for<0, 20>([&](auto G) {
      constexpr int g = G.value, jj = g / 5, i = g % 5;
      mfma_a<ks == 0>(acc[jj][i], WD[ks * 5 + i], H[ks % G2_R][jj]);
      if constexpr ((g & 3) == 0 && i0 + g / 4 < 48) issue_h(IC<i0 + g / 4>{});
    });
  });
  STAMP(14);
  asm volatile("s_nop 7");
  static_for<0, 15>([&](auto G) {
    constexpr int jj = G.value / 5, i = G.value % 5;
    ds_w128<(jj * 5 + i) * 1024>(sbase + ((jj + wv + 1) & 3) * 15360 + l16, acc[jj][i]);
  });
  asm volatile("s_waitcnt lgkmcnt(0)\n\ts_barrier" ::: "memory");
  const f32x4* part = (const f32x4*)(smem + wv * 15360);
  const unsigned xad = sbase + XR_OFF + (4 * kg + ((lane & 15) >> 2)) * 128 + wv * 32 + (lane & 3) * 8;
  u32x2 xw[5];
  static_for<0, 5>([&](auto I) { ds_tr<I.value * 2048>(xw[I.value], xad); });
  wait_lgkm<0>();
  const int pos = M * 64 + wv * 16 + (lane & 15);
  const int l8 = (pos >> 3) & 63;
  const int tok = 256 * (2 * (pos >> 9) + ((pos >> 2) & 1)) + 16 * (l8 & 15) + 4 * (l8 >> 4) + (pos & 3);
#pragma unroll
  for (int i = 0; i < 5; ++i) {
    pin(xw[i]); pin(acc[3][i]);
    f32x4 o = acc[3][i];
#pragma unroll
    for (int sl = 0; sl < 3; ++sl) o += part[(sl * 5 + i) * 64 + lane];
    const float x4[4] = {bflo(xw[i][0]), bfhi(xw[i][0]), bflo(xw[i][1]), bfhi(xw[i][1])};
    const f32x4 gb = *(const f32x4*)(gbl + i * 16 + 4 * kg);
    float r[4];
#pragma unroll
    for (int q = 0; q < 4; ++q) r[q] = fmaf(gb[q], o[q], x4[q]);
    *(u32x2*)(out + (size_t)tok * D_ + N * 80 + i * 16 + 4 * kg) = u32x2{pack_bf16(r[0], r[1]), pack_bf16(r[2], r[3])};
  }
  if (b == 0 && tid == 0) st_flag(ws, O_EPOCH, tag);
  STAMP_END();
}

extern "C" void kernel_launch(void* const* d_in, const int* in_sizes, int n_in, void* d_out, int out_size,
                              void* d_ws, size_t ws_size, hipStream_t stream) {
  k_fused<<<256, 256, 0, stream>>>((const u16*)d_in[0], (const u16*)d_in[1], (const u16*)d_in[2], (const u16*)d_in[3],
                                   (const u16*)d_in[4], (const u16*)d_in[5], (const u16*)d_in[7], (const u16*)d_in[8],
                                   (const u16*)d_in[9], (const u16*)d_in[10], (const u16*)d_in[11], (u16*)d_out, (char*)d_ws);
}
```

```cpp
#include <hip/hip_runtime.h>
#include <stdint.h>

typedef unsigned short u16;
typedef unsigned int u32;
typedef __attribute__((ext_vector_type(2))) __bf16 bf16x2;
typedef __attribute__((ext_vector_type(4))) float f32x4;
typedef __attribute__((ext_vector_type(2))) float f32x2;
typedef __attribute__((ext_vector_type(4))) u32 u32x4;
typedef __attribute__((ext_vector_type(2))) u32 u32x2;

constexpr int S_ = 2048, D_ = 640, F_ = 1536;
constexpr int TP_ = S_ + 64;
constexpr int NMOD = 6 * D_;
constexpr int MODP = 4;
constexpr int CWG = 160;

__device__ __forceinline__ float bflo(u32 w) { return __uint_as_float(w << 16); }
__device__ __forceinline__ float bfhi(u32 w) { return __uint_as_float(w & 0xffff0000u); }
__device__ __forceinline__ u32 pack_bf16(float lo, float hi) {
  bf16x2 v; v[0] = (__bf16)lo; v[1] = (__bf16)hi; return __builtin_bit_cast(u32, v);
}
__device__ __forceinline__ float rbf(float x) { __bf16 b = (__bf16)x; return (float)b; }
#define SCHED() __builtin_amdgcn_sched_barrier(0)
template <int V> struct IC { static constexpr int value = V; };
template <int B, int E, class Fn> __device__ __forceinline__ void static_for(Fn&& f) {
  if constexpr (B < E) { f(IC<B>{}); static_for<B + 1, E>(f); }
}
__device__ __forceinline__ void mfma(f32x4& c, u32x4 a, u32x4 b) {
  asm volatile("v_mfma_f32_16x16x32_bf16 %0, %1, %2, %0" : "+v"(c) : "v"(a), "v"(b));
}
__device__ __forceinline__ void mfma0(f32x4& c, u32x4 a, u32x4 b) {
  asm volatile("v_mfma_f32_16x16x32_bf16 %0, %1, %2, 0" : "=&v"(c) : "v"(a), "v"(b));
}
template <bool FIRST> __device__ __forceinline__ void mfma_(f32x4& c, u32x4 a, u32x4 b) { if constexpr (FIRST) mfma0(c, a, b); else mfma(c, a, b); }
__device__ __forceinline__ u32 alignbit(u32 hi, u32 lo, u32 sh) {
  u32 d; asm volatile("v_alignbit_b32 %0, %1, %2, %3" : "=v"(d) : "v"(hi), "v"(lo), "v"(sh)); return d;
}
template <int OFF> __device__ __forceinline__ void ds_tr(u32x2& dst, unsigned addr) {
  asm volatile("ds_read_b64_tr_b16 %0, %1 offset:%2" : "=v"(dst) : "v"(addr), "i"(OFF) : "memory");
}
template <int OFF> __device__ __forceinline__ void ds_b32(u32& dst, unsigned addr) {
  asm volatile("ds_read_b32 %0, %1 offset:%2" : "=v"(dst) : "v"(addr), "i"(OFF) : "memory");
}
template <int OFF> __device__ __forceinline__ void ds_b64(u32x2& dst, unsigned addr) {
  asm volatile("ds_read_b64 %0, %1 offset:%2" : "=v"(dst) : "v"(addr), "i"(OFF) : "memory");
}
template <int OFF> __device__ __forceinline__ void ds_b128(u32x4& dst, unsigned addr) {
  asm volatile("ds_read_b128 %0, %1 offset:%2" : "=v"(dst) : "v"(addr), "i"(OFF) : "memory");
}
template <int OFF> __device__ __forceinline__ void ds_w128(unsigned addr, f32x4 v) {
  asm volatile("ds_write_b128 %0, %1 offset:%2" :: "v"(addr), "v"(v), "i"(OFF) : "memory");
}
template <int OFF> __device__ __forceinline__ void gld128(u32x4& dst, unsigned voff, const char* sb) {
  asm volatile("global_load_dwordx4 %0, %1, %2 offset:%3 sc1" : "=v"(dst) : "v"(voff), "s"(sb), "i"(OFF) : "memory");
}
__device__ __forceinline__ void gld32(u32& dst, unsigned voff, const char* sb) {
  asm volatile("global_load_dword %0, %1, %2 sc1" : "=v"(dst) : "v"(voff), "s"(sb) : "memory");
}
template <int LOFF> __device__ __forceinline__ void dma16(unsigned ldsb, unsigned voff, const char* sb) {
  asm volatile("s_add_u32 m0, %0, %3\n\ts_nop 0\n\tglobal_load_lds_dwordx4 %1, %2 sc1" :: "s"(ldsb), "v"(voff), "s"(sb), "i"(LOFF) : "memory", "scc");
}
template <int LOFF> __device__ __forceinline__ void dma16x4(unsigned ldsb, unsigned v0, unsigned v1, const char* sb) {
  asm volatile("s_add_u32 m0, %0, %4\n\ts_nop 0\n\tglobal_load_lds_dwordx4 %1, %3 sc1\n\tglobal_load_lds_dwordx4 %2, %3 offset:1024 sc1\n\t"
               "global_load_lds_dwordx4 %1, %3 offset:2048 sc1\n\tglobal_load_lds_dwordx4 %2, %3 offset:3072 sc1"
               :: "s"(ldsb), "v"(v0), "v"(v1), "s"(sb), "i"(LOFF) : "memory", "scc");
}
__device__ __forceinline__ void sld32(u32& dst, const void* p) { asm volatile("s_load_dword %0, %1, 0x0" : "=s"(dst) : "s"(p) : "memory"); }
template <int N> __device__ __forceinline__ void wait_vm() { asm volatile("s_waitcnt vmcnt(%0)" :: "i"(N) : "memory"); }
template <int N> __device__ __forceinline__ void wait_lgkm() { asm volatile("s_waitcnt lgkmcnt(%0)" :: "i"(N) : "memory"); }
__device__ __forceinline__ void bar() { asm volatile("s_barrier" ::: "memory"); }
template <class T> __device__ __forceinline__ void pin(T& a) { asm volatile("" : "+v"(a)); }
__device__ __forceinline__ u32x4 cat(u32x2 a, u32x2 b) { return u32x4{a[0], a[1], b[0], b[1]}; }

typedef __amdgpu_buffer_rsrc_t rsrc_t;
__device__ __forceinline__ rsrc_t mk_rsrc(const void* p, int bytes) { return __builtin_amdgcn_make_buffer_rsrc((void*)p, 0, bytes, 0x00020000); }
template <int CTRL> __device__ __forceinline__ float dpp_add(float x) {
  return x + __builtin_bit_cast(float, __builtin_amdgcn_mov_dpp(__builtin_bit_cast(int, x), CTRL, 0xf, 0xf, true));
}
constexpr int DPP_XOR1 = 0xB1, DPP_XOR2 = 0x4E, DPP_HMIRROR = 0x141, DPP_ROR8 = 0x128;
__device__ __forceinline__ float sum8(float x) { return dpp_add<DPP_HMIRROR>(dpp_add<DPP_XOR2>(dpp_add<DPP_XOR1>(x))); }
__device__ __forceinline__ float tred32(float a, float b) {
  auto r = __builtin_amdgcn_permlane32_swap(__float_as_uint(a), __float_as_uint(b), false, false);
  return __uint_as_float(r[0]) + __uint_as_float(r[1]);
}
__device__ __forceinline__ float tred16(float a, float b) {
  auto r = __builtin_amdgcn_permlane16_swap(__float_as_uint(a), __float_as_uint(b), false, false);
  return __uint_as_float(r[0]) + __uint_as_float(r[1]);
}
__device__ __forceinline__ float rdl(float x, int l) { return __int_as_float(__builtin_amdgcn_readlane(__float_as_int(x), l)); }

typedef unsigned long long u64;
#define GAS __attribute__((address_space(1)))
constexpr int SC1 = 16;
#define NT 2
#define NTS " nt"
template <int OFF> __device__ __forceinline__ void gld128a(u32x4& dst, unsigned voff, const char* sb) {
  asm volatile("global_load_dwordx4 %0, %1, %2 offset:%3 sc1" : "=a"(dst) : "v"(voff), "s"(sb), "i"(OFF) : "memory");
}
template <bool FIRST> __device__ __forceinline__ void mfma_a(f32x4& c, u32x4 a, u32x4 b) {
  if constexpr (FIRST) asm volatile("v_mfma_f32_16x16x32_bf16 %0, %1, %2, 0" : "=&v"(c) : "a"(a), "v"(b));
  else asm volatile("v_mfma_f32_16x16x32_bf16 %0, %1, %2, %0" : "+v"(c) : "a"(a), "v"(b));
}
typedef __attribute__((ext_vector_type(8))) __bf16 bf16x8;
__device__ __forceinline__ f32x4 mfma_b(u32x4 a, u32x4 b, f32x4 c) {
  return __builtin_amdgcn_mfma_f32_16x16x32_bf16(__builtin_bit_cast(bf16x8, a), __builtin_bit_cast(bf16x8, b), c, 0, 0, 0);
}
template <int LOFF> __device__ __forceinline__ void dma16p(unsigned ldsb, unsigned voff, const char* sb) {
  asm volatile("s_add_u32 m0, %0, %3\n\ts_nop 0\n\tglobal_load_lds_dwordx4 %1, %2" NTS :: "s"(ldsb), "v"(voff), "s"(sb), "i"(LOFF) : "memory", "scc");
}
template <int LOFF> __device__ __forceinline__ void dma16x4p(unsigned ldsb, unsigned v0, const char* sb) {
  asm volatile("s_add_u32 m0, %0, %3\n\ts_nop 0\n\tglobal_load_lds_dwordx4 %1, %2" NTS "\n\tglobal_load_lds_dwordx4 %1, %2 offset:1024" NTS "\n\t"
               "global_load_lds_dwordx4 %1, %2 offset:2048" NTS "\n\tglobal_load_lds_dwordx4 %1, %2 offset:3072" NTS
               :: "s"(ldsb), "v"(v0), "s"(sb), "i"(LOFF) : "memory", "scc");
}

#define STAMP(I)
#define STAMP_DECL()
#define STAMP_END()

__device__ __forceinline__ void gld128p(u32x4& dst, unsigned voff, const char* sb) {
  asm volatile("global_load_dwordx4 %0, %1, %2" NTS : "=v"(dst) : "v"(voff), "s"(sb) : "memory");
}
template <int OFF> __device__ __forceinline__ void gld16p(u32& dst, unsigned voff, const char* sb) {
  asm volatile("global_load_ushort %0, %1, %2 offset:%3" : "=v"(dst) : "v"(voff), "s"(sb), "i"(OFF) : "memory");
}
#define CPF_DONE() asm volatile("" :: "v"(cpf))
constexpr int W1_STEPS = 21;
constexpr int O_EPOCH = 0;
constexpr int O_FK = 4096;
constexpr int O_FA0 = O_FK + 160 * 256;
constexpr int O_FA1 = O_FA0 + 8 * 12 * 128;
constexpr int O_FX = O_FA1 + 8 * 12 * 128;
constexpr int O_F2 = O_FX + 8 * 20 * 128;
constexpr int O_F3 = O_F2 + 8 * 32 * 128;
constexpr int O_MODP = O_F3 + 256 * 128;
constexpr int O_ST1 = O_MODP + MODP * NMOD * 4;
constexpr int O_KVG = O_ST1 + 16384;
constexpr int O_XG = O_KVG + S_ * D_ * 2;
constexpr int O_X2T = O_XG + S_ * D_ * 2;
constexpr int O_W1F = O_X2T + TP_ * D_ * 2;
constexpr int O_W2F = O_W1F + 64 * W1_STEPS * 3 * 1024;
constexpr int O_H2F = O_W2F + 8 * 48 * 5 * 1024;
constexpr int WS_BYTES = O_H2F + S_ * F_ * 2;
static_assert(O_MODP % 256 == 0 && O_ST1 % 256 == 0 && O_KVG % 256 == 0 && O_X2T % 256 == 0 && O_W1F % 256 == 0, "alignment");

constexpr int SPIN_CAP = 1 << 16;
constexpr unsigned CODE_BYTES = 40 * 1024;
__device__ __forceinline__ void st_flag(char* ws, int off, u64 tag) {
  __hip_atomic_store((GAS u64*)(ws + off), tag, __ATOMIC_RELAXED, __HIP_MEMORY_SCOPE_AGENT);
}
__device__ __forceinline__ bool tag_ok(u32x4 v, u64 tag) {
  const u32 lo = (u32)tag, hi = (u32)(tag >> 32);
  return ((v[0] ^ lo) | (v[1] ^ hi) | (v[2] ^ lo) | (v[3] ^ hi)) == 0;
}
template <int NL, int SLEEP> __device__ __forceinline__ void poll(rsrc_t rw, const int (&off)[NL], u64 tag) {
#pragma unroll 1
  for (int sp = 0; sp < SPIN_CAP; ++sp) {
    bool ok = true;
#pragma unroll
    for (int i = 0; i < NL; ++i) ok &= tag_ok(__builtin_amdgcn_raw_buffer_load_b128(rw, off[i], 0, SC1), tag);
    if (__all(ok)) break;
    if (SLEEP) __builtin_amdgcn_s_sleep(SLEEP);
    asm volatile("" ::: "memory");
  }
  asm volatile("" ::: "memory");
}
__device__ __forceinline__ u64 next_tag(u32x2 ep) {
  u32 lo, hi;
  asm volatile("s_waitcnt lgkmcnt(0)\n\ts_add_u32 %0, %2, 1\n\ts_addc_u32 %1, %3, 0" : "=&s"(lo), "=&s"(hi) : "s"(ep[0]), "s"(ep[1]) : "memory", "scc");
  return ((u64)hi << 32) | lo;
}
__device__ __forceinline__ void drain_bar() { asm volatile("s_waitcnt vmcnt(0) lgkmcnt(0)\n\ts_barrier" ::: "memory"); }

constexpr int FC_BYTES = 2080 * 2;
constexpr int CH_BYTES = 2 * FC_BYTES;
constexpr int CKV_OFF = 33280;
constexpr int CX_OFF = CKV_OFF + 2048 + 16384, CS_OFF = CX_OFF + 16384;
constexpr int conv_gap(int a, int b) { int n = 0; for (int i = 0; i < a; ++i) n += 8 - i; return n + b; }
constexpr int G1_R = 6;
constexpr int G1_SLOTS = 8;
constexpr int G1_LA = 4;
constexpr int G1_STAT = G1_SLOTS * 8192;
constexpr int G1_XPRE = 3;
#define WD0 8
constexpr int G1_WD0 = WD0;
constexpr int G1_WD = WD0 + 22;
struct G1Sched { int kind[192]; int idx[192]; int first[162]; int n; };
constexpr G1Sched g1_make() {
  G1Sched S{};
  int mk[128] = {}, mi[128] = {}, ms[128] = {}, nm = 0;
  for (int st = 0; st <= 20; ++st) {
    if (st >= G1_R - 1) for (int i = 0; i < 3; ++i) { mk[nm] = 1; mi[nm] = 3 * st + i; ms[nm] = st; ++nm; }
    if (st < 20) for (int p = 0; p < 2; ++p) { mk[nm] = 0; mi[nm] = 2 * st + p; ms[nm] = st; ++nm; }
  }
  int n = 0, m = 0, wd = G1_WD0;
  for (; m < 2 * G1_XPRE; ++m) { S.kind[n] = mk[m]; S.idx[n] = mi[m]; ++n; }
  for (int t = 0; t < 160; ++t) {
    S.first[t] = n;
    const int ks = t >> 3, jj = t & 7;
    if (m < nm && ms[m] <= ks + G1_R - 1) { S.kind[n] = mk[m]; S.idx[n] = mi[m]; ++n; ++m; }
    else if (wd < G1_WD && t < 150 && m >= nm && jj < 6) { S.kind[n] = 2; S.idx[n] = wd; ++n; ++wd; }
  }
  S.first[160] = n; S.n = n;
  return S;
}
constexpr G1Sched G1S = g1_make();
constexpr int g1_pos(int kind, int idx) { for (int i = 0; i < G1S.n; ++i) if (G1S.kind[i] == kind && G1S.idx[i] == idx) return i; return -1; }
constexpr int g1_wd_count() { int c = G1_WD0; for (int i = 0; i < G1S.n; ++i) c += G1S.kind[i] == 2; return c; }
constexpr int G1_WDN = g1_wd_count();
static_assert(g1_pos(1, 62) >= 0 && g1_pos(0, 39) >= 0, "schedule incomplete");
constexpr int G2_R = 8, G2_PRE = 4;
constexpr int XR_OFF = 69632;
constexpr int LDS_BYTES = 96 * 1024;
constexpr int AG_OFF = 0, AU_OFF = 20480, AD_OFF = 40960, AM_OFF = 61440;

__global__ __launch_bounds__(256, 1) void k_fused(const u16* __restrict__ xIn, const u16* __restrict__ kIn, const u16* __restrict__ vIn,
                                                  const u16* __restrict__ shk, const u16* __restrict__ ssmk, const u16* __restrict__ ssmD,
                                                  const u16* __restrict__ wg, const u16* __restrict__ wu, const u16* __restrict__ wd,
                                                  const u16* __restrict__ cIn, const u16* __restrict__ wmod,
                                                  u16* __restrict__ out, char* __restrict__ ws) {
  __shared__ __attribute__((aligned(16))) char smem[LDS_BYTES];
  STAMP_DECL();
  STAMP(0);
  u32 cpf = 0;
  if (blockIdx.x >= 240) {
    uint64_t pc; asm volatile("s_getpc_b64 %0" : "=s"(pc));
    const unsigned co = (((blockIdx.x >> 3) & 1) * 256 + threadIdx.x) * 128;
    asm volatile("global_load_dword %0, %1, %2" : "=v"(cpf) : "v"(co < CODE_BYTES ? co : 0u), "s"(pc & ~(uint64_t)127) : "memory");
  }
  asm volatile("" :: "s"(xIn), "s"(kIn), "s"(vIn), "s"(shk), "s"(ssmk), "s"(ssmD), "s"(wg), "s"(wu), "s"(wd), "s"(cIn), "s"(wmod), "s"(out), "s"(ws));
  u32x2 ep;
  asm volatile("s_load_dwordx2 %0, %1, 0x0" : "=s"(ep) : "s"(ws) : "memory");
  u64 tag;
  const int tid = threadIdx.x, lane = tid & 63;
  const int wv = __builtin_amdgcn_readfirstlane(tid >> 6);
  const int b = blockIdx.x;
  const unsigned sbase = (unsigned)(size_t)smem;
  const unsigned l16 = lane * 16;
  const rsrc_t rw = mk_rsrc(ws, WS_BYTES);
  u32x4 um[5]; u32 uc[5];
  if (b >= CWG) {
    const int j = b - CWG, r8 = lane >> 3, cp = lane & 7;
    const int ug = j % 24, kq = j / 24;
    const int uk = j & 3, ucol = 30 + (j < 80 ? j >> 2 : 0);
    static_for<0, 5>([&](auto I) { gld16p<I.value * 16>(uc[I.value], r8 * 2, (const char*)(cIn + uk * 160 + wv * 40)); });
    static_for<0, 5>([&](auto I) { gld128p(um[I.value], r8 * (NMOD * 2) + cp * 16, (const char*)(wmod + (size_t)(uk * 160 + wv * 40 + I.value * 8) * NMOD + ucol * 64)); });
    const unsigned key = (r8 >> 1) * 2;
    const unsigned vg = r8 * (F_ * 2) + ((cp ^ key) * 16);
    const char* gs = (const char*)(wg + (size_t)(kq * 160 + wv * 40) * F_ + ug * 64);
    const char* us = (const char*)(wu + (size_t)(kq * 160 + wv * 40) * F_ + ug * 64);
    static_for<0, 5>([&](auto I) {
      dma16p<AG_OFF + I.value * 1024>(sbase + wv * 5120, vg, gs + I.value * (8 * F_ * 2));
      dma16p<AU_OFF + I.value * 1024>(sbase + wv * 5120, vg ^ 16, us + I.value * (8 * F_ * 2));
    });
  }
  int b2 = b;
  asm volatile("" : "+s"(b2));
  if (b2 >= CWG) {
    const int j = b - CWG;
    const int r8 = lane >> 3, cp = lane & 7;
    const int ks2 = j >> 1, hf = j & 1;
    const int ug = j % 24, kq = j / 24;
    float* LC = (float*)(smem + AM_OFF);
    u16* LS = (u16*)(smem + AM_OFF + 640);
    u32* LN = (u32*)(smem + AM_OFF + 1024);
    float* LP = (float*)(smem + AM_OFF + 1280);
    auto silu = [](u32 c) { const float x = bflo(c); return rbf(x * __builtin_amdgcn_rcpf(1.f + __expf(-x))); };
    auto colsum = [&](f32x2 (&acc)[4]) {
      const float a0 = tred16(tred32(acc[0][0], acc[2][0]), tred32(acc[1][0], acc[3][0]));
      const float a1 = tred16(tred32(acc[0][1], acc[2][1]), tred32(acc[1][1], acc[3][1]));
      const float e0 = dpp_add<DPP_ROR8>(a0), e1 = dpp_add<DPP_ROR8>(a1);
      return (lane & 8) ? e1 : e0;
    };
    const int uk = j & 3, ucol = 30 + (j < 80 ? j >> 2 : 0);
    tag = next_tag(ep);
    if (tid == 0) *LN = 0;
    {
      float sv[5];
      wait_vm<15>();
#pragma unroll
      for (int it = 0; it < 5; ++it) { pin(uc[it]); sv[it] = silu(uc[it]); pin(sv[it]); }
      wait_vm<10>();
      f32x2 acc[4];
#pragma unroll
      for (int it = 0; it < 5; ++it) {
        pin(um[it]);
        const f32x2 s2 = {sv[it], sv[it]};
#pragma unroll
        for (int q = 0; q < 4; ++q) {
          const f32x2 w2 = {bflo(um[it][q]), bfhi(um[it][q])};
          acc[q] = it ? __builtin_elementwise_fma(s2, w2, acc[q]) : s2 * w2;
        }
      }
      LP[wv * 64 + cp * 8 + r8] = colsum(acc);
    }
    asm volatile("s_waitcnt lgkmcnt(0)\n\ts_barrier" ::: "memory");
    STAMP(1);
    if (wv == 0 && j < 80) {
      const float m = (LP[lane] + LP[64 + lane]) + (LP[128 + lane] + LP[192 + lane]);
      __builtin_amdgcn_raw_buffer_store_b32(__float_as_uint(m), rw, O_MODP + (uk * NMOD + ucol * 64 + lane) * 4, 0, SC1);
      wait_vm<0>();
      if (lane < 8) st_flag(ws, O_FA0 + (lane * 12 + (j >> 3)) * 128 + (j & 7) * 8, tag);
    } else {
      wait_vm<0>();
    }
    bar();
    STAMP(2);
    CPF_DONE();
    const int kg = lane >> 4, q4 = (lane & 15) >> 2, p4 = lane & 3;
    if (wv == 0 || wv == 3) {
      const char* ds = (const char*)(wd + (size_t)(ks2 * 32 + (wv & 1) * 16) * D_ + hf * 320);
      static_for<0, 10>([&](auto I) { dma16p<AD_OFF + (I.value >> 1) * 4096 + (I.value & 1) * 1024>(sbase + (wv & 1) * 2048, r8 * (D_ * 2) + cp * 16, ds + (I.value & 1) * (8 * D_ * 2) + (I.value >> 1) * 128); });
      const int po[1] = {O_FA0 + ((b & 7) * 12 + (lane < 40 ? lane >> 2 : 0)) * 128 + (lane & 3) * 16};
      poll<1, 1>(rw, po, tag);
      STAMP(3);
      float mm[3];
#pragma unroll
      for (int i = 0; i < 3; ++i) {
        mm[i] = 0.f;
#pragma unroll
        for (int p = 0; p < MODP; ++p)
          mm[i] += __uint_as_float(__builtin_amdgcn_raw_buffer_load_b32(rw, lane * 4 + i * 256, O_MODP + (p * NMOD + (wv ? 4 : 3) * D_ + kq * 160) * 4, SC1));
      }
#pragma unroll
      for (int i = 0; i < 3; ++i)
        if (i * 64 + lane < 160) { if (wv) LC[i * 64 + lane] = rbf(1.f + rbf(mm[i])); else LS[i * 64 + lane] = (u16)pack_bf16(mm[i], 0.f); }
    } else {
      const int gt = (wv - 1) * 96 + (95 - j);
      if (gt < 160) {
        const rsrc_t rm = mk_rsrc(wmod, D_ * NMOD * 2);
        const int gk = gt & 3, gq = gt >> 2, gcol = gq < 30 ? gq : gq + 20;
        u32x4 wm[20]; u32 cv[3];
#pragma unroll
        for (int i = 0; i < 3; ++i) cv[i] = cIn[gk * 160 + (i * 64 + lane < 160 ? i * 64 + lane : 0)];
#pragma unroll
        for (int it = 0; it < 20; ++it) wm[it] = __builtin_amdgcn_raw_buffer_load_b128(rm, r8 * (NMOD * 2) + cp * 16, (gk * 160 + it * 8) * (NMOD * 2) + gcol * 128, NT);
        SCHED();
        float sl[3], sv[20];
#pragma unroll
        for (int i = 0; i < 3; ++i) sl[i] = silu(cv[i]);
#pragma unroll
        for (int it = 0; it < 20; ++it) sv[it] = __shfl(sl[it >> 3], (it & 7) * 8 + r8);
        f32x2 acc[4];
#pragma unroll
        for (int it = 0; it < 20; ++it) {
          const f32x2 s2 = {sv[it], sv[it]};
#pragma unroll
          for (int q = 0; q < 4; ++q) {
            const f32x2 w2 = {bflo(wm[it][q]), bfhi(wm[it][q])};
            acc[q] = it ? __builtin_elementwise_fma(s2, w2, acc[q]) : s2 * w2;
          }
        }
        __builtin_amdgcn_raw_buffer_store_b32(__float_as_uint(colsum(acc)), rw, O_MODP + (gk * NMOD + gcol * 64 + cp * 8 + r8) * 4, 0, SC1);
      }
      wait_vm<0>();
      u32 old = 0;
      if (lane == 0) old = __hip_atomic_fetch_add(LN, 1u, __ATOMIC_RELAXED, __HIP_MEMORY_SCOPE_WORKGROUP);
      if (__builtin_amdgcn_readfirstlane(old) == 1 && lane < 8) st_flag(ws, O_FA1 + (lane * 12 + (j >> 3)) * 128 + (j & 7) * 8, tag);
    }
    asm volatile("s_waitcnt lgkmcnt(0)\n\ts_barrier" ::: "memory");
    STAMP(4);
    {
      const int rr = 4 * kg + q4;
      u32x2 lo[10], hi[10], sbl[5], sbh[5]; u32x4 cl[5], ch[5];
      unsigned ad[2];
#pragma unroll
      for (int t = 0; t < 2; ++t) ad[t] = sbase + (p4 >> 1) * AU_OFF + rr * 128 + (((wv * 2 + t) ^ (((rr >> 1) & 3) * 2 + (p4 >> 1))) * 16) + (p4 & 1) * 8;
      static_for<0, 10>([&](auto I) { constexpr int i = I.value; ds_tr<(i >> 1) * 4096>(lo[i], ad[i & 1]); ds_tr<(i >> 1) * 4096 + 2048>(hi[i], ad[i & 1]); });
      const unsigned ac = sbase + AM_OFF + kg * 16, as = sbase + AM_OFF + 640 + kg * 8;
      static_for<0, 5>([&](auto I) { constexpr int i = I.value; ds_b128<i * 128>(cl[i], ac); ds_b128<i * 128 + 64>(ch[i], ac); ds_b64<i * 64>(sbl[i], as); ds_b64<i * 64 + 32>(sbh[i], as); });
      u32x2 dl[5], dh[5];
      const unsigned add = sbase + AD_OFF + (8 * kg + q4) * 128 + p4 * 8;
#pragma unroll
      for (int i = 0; i < 5; ++i) { const int tl = wv * 5 + i; const unsigned a2 = add + (tl >> 2) * 4096 + (tl & 3) * 32; ds_tr<0>(dl[i], a2); ds_tr<512>(dh[i], a2); }
      wait_lgkm<0>();
#pragma unroll
      for (int i = 0; i < 10; ++i) { pin(lo[i]); pin(hi[i]); }
#pragma unroll
      for (int i = 0; i < 5; ++i) { pin(cl[i]); pin(ch[i]); pin(sbl[i]); pin(sbh[i]); pin(dl[i]); pin(dh[i]); }
      const int frow = 2 * (4 * (q4 & 1) + p4) + (q4 >> 1);
      const u32x4 ones = {0x3f803f80u, 0x3f803f80u, 0x3f803f80u, 0x3f803f80u};
      f32x4 aU[2] = {}, aW[2] = {};
#pragma unroll
      for (int i = 0; i < 10; ++i) {
        const int ks = i >> 1, t = i & 1;
        const u32x4 raw = cat(lo[i], hi[i]);
        u32x4 sc;
#pragma unroll
        for (int q = 0; q < 2; ++q) {
          sc[q] = pack_bf16(bflo(raw[q]) * __uint_as_float(cl[ks][2 * q]), bfhi(raw[q]) * __uint_as_float(cl[ks][2 * q + 1]));
          sc[2 + q] = pack_bf16(bflo(raw[2 + q]) * __uint_as_float(ch[ks][2 * q]), bfhi(raw[2 + q]) * __uint_as_float(ch[ks][2 * q + 1]));
        }
        aW[t] = mfma_b(raw, cat(sbl[ks], sbh[ks]), aW[t]);
        aU[t] = mfma_b(sc, ones, aU[t]);
        const int T = ug * 8 + wv * 2 + t;
        const int sw3 = T / 3, ti = T - sw3 * 3;
        __builtin_amdgcn_raw_buffer_store_b128(sc, rw, (kg * 16 + frow) * 16, O_W1F + ((sw3 * W1_STEPS + kq * 5 + ks) * 3 + ti) * 1024, SC1);
      }
#pragma unroll
      for (int i = 0; i < 5; ++i) __builtin_amdgcn_raw_buffer_store_b128(cat(dl[i], dh[i]), rw, l16, O_W2F + (((hf * 4 + wv) * 48 + ks2) * 5 + i) * 1024, SC1);
      const f32x4 su = (lane & 4) ? aU[1] : aU[0], sw = (lane & 4) ? aW[1] : aW[0];
      const float U = p4 == 0 ? su[0] : p4 == 1 ? su[1] : p4 == 2 ? su[2] : su[3];
      const float W = p4 == 0 ? sw[0] : p4 == 1 ? sw[1] : p4 == 2 ? sw[2] : sw[3];
      const float u1 = rbf(U), u2 = rbf(U - u1), u3 = U - u1 - u2;
      const float w1 = rbf(W), w2 = rbf(W - w1), w3 = W - w1 - w2;
      const int T = ug * 8 + wv * 2 + ((lane >> 2) & 1);
      const int sw3 = T / 3, ti = T - sw3 * 3;
      const int crow = 2 * (4 * (kg & 1) + p4) + (kg >> 1);
      if (!(lane & 8))
        __builtin_amdgcn_raw_buffer_store_b128(u32x4{pack_bf16(u1, u2), pack_bf16(u3, w1), pack_bf16(w2, w3), 0u}, rw,
                                               O_W1F + (((sw3 * W1_STEPS + 20) * 3 + ti) * 64 + kq * 16 + crow) * 16, 0, SC1);
    }
    STAMP(5);
  } else {
    const int d = (b >> 1) * 8 + (b & 1) + 2 * wv;
    const int row = lane & 15, kg = lane >> 4;
    u32 xx[16]; u32x4 xs[5];
    const int tt = b & 31, ct = b >> 5;
    const int ov = ((lane >> 2) * 2048 + (lane & 3)) * 16, os = (ct * 16 * 2048 + (tt * 8 + wv * 2) * 4) * 16;
    const int srow = (b * 4 + wv) * 4 + kg;
    {
      const rsrc_t rk = mk_rsrc(kIn, S_ * D_ * 2), rv = mk_rsrc(vIn, S_ * D_ * 2);
      const int vo = ct * 256 + lane * 4, so = (tt * 64 + wv * 16) * 1280;
      const int sh0 = so ? so - 3 * 1280 : 0;
      u32 kk[19], vv[16];
      const u32x4 taps = *(const u32x4*)(shk + (ct * 64 + lane) * 8);
#pragma unroll
      for (int q = 0; q < 3; ++q) kk[q] = __builtin_amdgcn_raw_buffer_load_b32(rk, vo + q * 1280, sh0, NT);
#pragma unroll
      for (int q = 0; q < 8; ++q) kk[3 + q] = __builtin_amdgcn_raw_buffer_load_b32(rk, vo + (q & 1) * 1280, so + (q & 14) * 1280, NT);
#pragma unroll
      for (int q = 0; q < 8; ++q) vv[q] = __builtin_amdgcn_raw_buffer_load_b32(rv, vo + (q & 1) * 1280, so + (q & 14) * 1280, NT);
#pragma unroll
      for (int q = 8; q < 16; ++q) kk[3 + q] = __builtin_amdgcn_raw_buffer_load_b32(rk, vo + (q & 1) * 1280, so + (q & 14) * 1280, NT);
#pragma unroll
      for (int q = 8; q < 16; ++q) vv[q] = __builtin_amdgcn_raw_buffer_load_b32(rv, vo + (q & 1) * 1280, so + (q & 14) * 1280, NT);
      SCHED();
      const rsrc_t rx = mk_rsrc(xIn, S_ * D_ * 2);
#pragma unroll
      for (int q = 0; q < 16; ++q) xx[q] = __builtin_amdgcn_raw_buffer_load_b32(rx, vo + (q & 1) * 1280, so + (q & 14) * 1280, NT);
#pragma unroll
      for (int q = 0; q < 5; ++q) xs[q] = __builtin_amdgcn_raw_buffer_load_b128(rx, srow * 1280 + row * 16 + q * 256, 0, NT);
      SCHED();
      if (so == 0) { kk[0] = 0; kk[1] = 0; kk[2] = 0; }
      const f32x2 w0 = {bflo(taps[0]), bflo(taps[2])}, w1 = {bfhi(taps[0]), bfhi(taps[2])};
      const f32x2 w2 = {bflo(taps[1]), bflo(taps[3])}, w3 = {bfhi(taps[1]), bfhi(taps[3])};
#pragma unroll
      for (int r = 0; r < 2; ++r) {
        f32x2 kv[8];
#pragma unroll
        for (int q = 0; q < 8; ++q) {
          const int i = 8 * r + q;
          f32x2 a = f32x2{bflo(kk[i]), bfhi(kk[i])} * w3;
          a = __builtin_elementwise_fma(f32x2{bflo(kk[i + 1]), bfhi(kk[i + 1])}, w2, a);
          a = __builtin_elementwise_fma(f32x2{bflo(kk[i + 2]), bfhi(kk[i + 2])}, w1, a);
          a = __builtin_elementwise_fma(f32x2{bflo(kk[i + 3]), bfhi(kk[i + 3])}, w0, a);
          kv[q] = a * f32x2{bflo(vv[i]), bfhi(vv[i])};
        }
        u32x4 o0, o1;
#pragma unroll
        for (int t = 0; t < 4; ++t) {
          o0[t] = pack_bf16(kv[7 - 2 * t][0], kv[6 - 2 * t][0]);
          o1[t] = pack_bf16(kv[7 - 2 * t][1], kv[6 - 2 * t][1]);
        }
        __builtin_amdgcn_raw_buffer_store_b128(o0, rw, ov + r * 64, O_KVG + os, SC1);
        __builtin_amdgcn_raw_buffer_store_b128(o1, rw, ov + r * 64, O_KVG + os + 16384, SC1);
      }
    }
    STAMP(1);
    tag = next_tag(ep);
    drain_bar();
    if (tid < 32) st_flag(ws, O_FK + ((b & ~31) + tid) * 256 + (b & 31) * 8, tag);
    STAMP(2);
    {
      const char* fs = (const char*)(ssmk + (size_t)d * S_);
      dma16x4p<32>(sbase + wv * CH_BYTES, l16, fs);
      dma16x4p<FC_BYTES + 28>(sbase + wv * CH_BYTES, l16, fs);
    }
#pragma unroll
    for (int r = 0; r < 2; ++r) {
      u32x4 p0, p1;
#pragma unroll
      for (int t = 0; t < 4; ++t) {
        p0[t] = __builtin_amdgcn_perm(xx[8 * r + 2 * t + 1], xx[8 * r + 2 * t], 0x05040100u);
        p1[t] = __builtin_amdgcn_perm(xx[8 * r + 2 * t + 1], xx[8 * r + 2 * t], 0x07060302u);
      }
      __builtin_amdgcn_raw_buffer_store_b128(p0, rw, ov + r * 64, O_XG + os, SC1);
      __builtin_amdgcn_raw_buffer_store_b128(p1, rw, ov + r * 64, O_XG + os + 16384, SC1);
    }
    {
      const bf16x2 ones = {(__bf16)1.0f, (__bf16)1.0f};
      float s1[2] = {0.f, 0.f}, s2[2] = {0.f, 0.f};
#pragma unroll
      for (int i = 0; i < 5; ++i)
#pragma unroll
        for (int q = 0; q < 4; ++q) {
          const u32 raw = xs[i][q];
          const bf16x2 v = __builtin_bit_cast(bf16x2, raw);
          s1[q & 1] = __builtin_amdgcn_fdot2_f32_bf16(v, ones, s1[q & 1], false);
          s2[q & 1] = __builtin_amdgcn_fdot2_f32_bf16(v, v, s2[q & 1], false);
        }
      const float mean = dpp_add<DPP_ROR8>(sum8(s1[0] + s1[1])) * (1.f / D_);
      const float var = fmaxf(dpp_add<DPP_ROR8>(sum8(s2[0] + s2[1])) * (1.f / D_) - mean * mean, 0.f);
      const float sd = rbf(__builtin_amdgcn_sqrtf(rbf(rbf(var) + 1e-5f)));
      const float rs = __builtin_amdgcn_rcpf(sd);
      const int nt = srow >> 8, col = (srow >> 4) & 15, k4 = (srow >> 2) & 3, rr = srow & 3;
      if (row == 0 && srow < S_) __builtin_amdgcn_raw_buffer_store_b64(u32x2{__float_as_uint(rs), __float_as_uint(-rbf(mean) * rs)}, rw,
                                                                       O_ST1 + (((nt * 2 + (rr >> 1)) * 64 + k4 * 16 + col) * 4 + (rr & 1) * 2) * 4, 0, SC1);
    }
    u32 draw;
    sld32(draw, (const u32*)ssmD + (d >> 1));
    *(u32x2*)(smem + CKV_OFF + tid * 8) = u32x2{0, 0};
    if (lane < 15) *(u32*)(smem + wv * CH_BYTES + (lane >> 3) * FC_BYTES + (lane & 7) * 4) = 0;
    const int rho = row + 1;
    u32 sh = (rho & 1) * 16;
    unsigned aAddr = sbase + wv * CH_BYTES + ((rho >> 1) & 1) * FC_BYTES + 2 * (64 * kg + (rho & ~3));
    pin(sh); pin(aAddr);
    unsigned bA[8];
#pragma unroll
    for (int e = 0; e < 8; ++e) { const int g = 32 + 2 * row + 1 - 8 * kg - e; bA[e] = sbase + CKV_OFF + 16 * ((4 * g + wv) ^ ((g >> 2) & 7)); pin(bA[e]); }
    {
      const int po[1] = {O_FK + b * 256 + wv * 64 + (lane & 3) * 16};
      poll<1, 0>(rw, po, tag);
      STAMP(3);
      const unsigned ko0 = (lane ^ (lane >> 4)) * 16;
      dma16x4<CKV_OFF + 2048>(sbase + wv * 4096, ko0, ko0 ^ 64, ws + O_KVG + b * 16384 + wv * 4096);
    }
    drain_bar();
    if (tid < 8) st_flag(ws, O_FX + (tid * 20 + (b >> 3)) * 128 + (b & 7) * 8, tag);
    STAMP(4);
    f32x4 acc[8];
    u32x2 ra[2][8][2]; u32x4 rb[2][8]; u32 carry[8]; u32x4 A[8];
    u32x4 pf[2]; u32 mraw;
    auto load_n = [&](auto E, auto N) {
      constexpr int e = E.value, n = N.value, q = n / 3, bf = e & 1;
      if constexpr (n % 3 == 0) ds_b64<512 * q + 16 * e>(ra[bf][q][0], aAddr);
      else if constexpr (n % 3 == 1) ds_b64<512 * q + 16 * e + 8>(ra[bf][q][1], aAddr);
      else ds_b128<2048 * q>(rb[bf][q], bA[e]);
    };
    auto build = [&](auto E, auto AA, auto I) {
      constexpr int e = E.value, a = AA.value, i = I.value, bf = e & 1;
      if constexpr (i == 0) A[a][0] = alignbit(ra[bf][a][0][1], ra[bf][a][0][0], sh);
      else if constexpr (i == 1) A[a][1] = alignbit(ra[bf][a][1][0], ra[bf][a][0][1], sh);
      else if constexpr (i == 2) A[a][2] = alignbit(ra[bf][a][1][1], ra[bf][a][1][0], sh);
      else { A[a][3] = alignbit(carry[a], ra[bf][a][1][1], sh); carry[a] = ra[bf][a][0][0]; }
    };
    const int pa1[2] = {O_FX + ((b & 7) * 20 + (lane >> 2)) * 128 + (lane & 3) * 16,
                        (lane < 48 ? O_FA1 + ((b & 7) * 12 + (lane >> 2)) * 128 : O_FX + ((b & 7) * 20 + 4 + (lane >> 2)) * 128) + (lane & 3) * 16};
    static_for<0, 8>([&](auto I) { ds_b32<512 * I.value + 128>(carry[I.value], aAddr); });
    static_for<0, 24>([&](auto N) { load_n(IC<7>{}, N); });
    wait_lgkm<0>();
    static_for<0, 4>([&](auto I) { build(IC<7>{}, IC<0>{}, I); });
    asm volatile("s_nop 1");
    static_for<0, 8>([&](auto EE) {
      constexpr int e = 7 - EE.value, bf = e & 1;
      const u32x4 A0 = A[0];
      static_for<0, 8>([&](auto AA) {
        constexpr int a = AA.value;
        static_for<0, 8 - a>([&](auto BB) {
          constexpr int bb = BB.value, n = conv_gap(a, bb);
          mfma_<(e == 7 && a == 0)>(acc[a + bb], a == 0 ? A0 : A[a], rb[bf][bb]);
          if constexpr (e > 0 && n < 24) load_n(IC<(e > 0 ? e - 1 : 0)>{}, IC<n>{});
          if constexpr (n < 28) build(IC<e>{}, IC<1 + n / 4>{}, IC<n % 4>{});
          if constexpr (e > 0 && n == 30) wait_lgkm<0>();
          if constexpr (e > 0 && (n == 31 || n == 32)) { build(IC<(e > 0 ? e - 1 : 0)>{}, IC<0>{}, IC<2 * (n - 31)>{}); build(IC<(e > 0 ? e - 1 : 0)>{}, IC<0>{}, IC<2 * (n - 31) + 1>{}); }
          if constexpr (e == 3 && n == 28) gld128<0>(pf[0], pa1[0], ws);
          if constexpr (e == 3 && n == 29) gld128<0>(pf[1], pa1[1], ws);
          if constexpr (e == 2 && n == 33) {
            wait_vm<0>(); pin(pf[0]); pin(pf[1]);
            if (!__all(tag_ok(pf[0], tag) && tag_ok(pf[1], tag))) poll<2, 1>(rw, pa1, tag);
            dma16x4<CX_OFF>(sbase + wv * 4096, l16, l16, ws + O_XG + b * 16384 + wv * 4096);
          }
          if constexpr (e == 1 && n == 28) dma16x4<CS_OFF>(sbase + wv * 4096, l16, l16, ws + O_ST1 + wv * 4096);
          if constexpr (e == 1 && n == 34) gld32(mraw, (((lane & 7) < MODP ? (lane & 7) : 0) * NMOD + ((lane >> 3) & 3) * D_) * 4, ws + O_MODP + d * 4);
        });
      });
    });
    STAMP(5);
    asm volatile("s_waitcnt vmcnt(0)\n\ts_nop 7\n\ts_barrier" : "+v"(acc[0]), "+v"(acc[1]), "+v"(acc[2]), "+v"(acc[3]), "+v"(acc[4]), "+v"(acc[5]), "+v"(acc[6]), "+v"(acc[7]), "+v"(mraw), "+s"(draw) :: "memory");
    const float mv = rbf(sum8((lane & 7) < MODP ? __uint_as_float(mraw) : 0.f));
    const float m_ga = rdl(mv, 16), c0 = rdl(mv, 0) * m_ga, c1 = rbf(1.f + rdl(mv, 8)) * m_ga;
    const float Dv = (d & 1) ? bfhi(draw) : bflo(draw);
#pragma unroll
    for (int np = 0; np < 4; ++np) {
      u32x4 o;
#pragma unroll
      for (int u = 0; u < 2; ++u) {
        const int nt = 2 * np + u;
        const int tg = 32 * nt + 2 * row + (kg >> 1), g = 32 + tg;
        const u32x2 kvw = *(const u32x2*)(smem + CKV_OFF + 16 * ((4 * g + wv) ^ ((g >> 2) & 7)) + ((kg & 1) ? 0 : 8));
        const u32x2 xw = *(const u32x2*)(smem + CX_OFF + tg * 64 + wv * 16 + (kg & 1) * 8);
        const f32x4 sA = *(const f32x4*)(smem + CS_OFF + (nt * 2) * 1024 + l16);
        const f32x4 sB = *(const f32x4*)(smem + CS_OFF + (nt * 2 + 1) * 1024 + l16);
        const float kv4[4] = {bfhi(kvw[1]), bflo(kvw[1]), bfhi(kvw[0]), bflo(kvw[0])};
        const float x4[4] = {bflo(xw[0]), bfhi(xw[0]), bflo(xw[1]), bfhi(xw[1])};
        const float rs4[4] = {sA[0], sA[2], sB[0], sB[2]};
        const float nb4[4] = {sA[1], sA[3], sB[1], sB[3]};
        float r4[4];
#pragma unroll
        for (int r = 0; r < 4; ++r) {
          const float yb = fmaf(kv4[r], Dv, acc[nt][r]);
          const float n1 = fmaf(x4[r], rs4[r], nb4[r]);
          const float h = fmaf(n1, c1, c0);
          r4[r] = fmaf(h, yb, x4[r]);
        }
        o[2 * u] = pack_bf16(r4[0], r4[1]); o[2 * u + 1] = pack_bf16(r4[2], r4[3]);
      }
      __builtin_amdgcn_raw_buffer_store_b128(o, rw, l16 + np * 1024, O_X2T + d * (TP_ * 2), SC1);
    }
  }
  STAMP(6);
  drain_bar();
  if (tid < 8) st_flag(ws, O_F2 + (tid * 32 + (b >> 3)) * 128 + (b & 7) * 8, tag);
  STAMP(7);

  int bs = blockIdx.x;
  asm volatile("" : "+s"(bs));
  const int xcd = bs & 7, idx = bs >> 3;
  const int mt = (xcd & 1) * 8 + (idx & 7), nc = (xcd >> 1) * 4 + (idx >> 3);
  const int M = mt * 2 + ((nc >> 2) & 1), N = (nc >> 3) * 4 + (nc & 3);
  const int kg = lane >> 4;
  u32x4 WD[60];
  const char* wdp = ws + O_W2F + (N * 48 + wv * 12) * 5120;
  auto issue_wd = [&](auto I) { constexpr int i = I.value; gld128a<(i & 3) * 1024>(WD[i], l16, wdp + (i >> 2) * 4096); };
  {
    unsigned xoff[2];
#pragma unroll
    for (int p = 0; p < 2; ++p) {
      const int k = 4 * p + (lane >> 4);
      xoff[p] = (k * TP_ + (((lane & 15) ^ (2 * k)) * 8)) * 2;
    }
    const char* xsrc = ws + O_X2T + (8 * wv * TP_ + mt * 128) * 2;
    const char* wsrc = ws + O_W1F + (nc * 4 + wv) * W1_STEPS * 3072;
    const unsigned ldsw = sbase + wv * 2048;
    u32x4 W[G1_R][3];
    auto issue_w = [&](auto I) { constexpr int i = I.value; gld128<(i % 3) * 1024>(W[(i / 3) % G1_R][i % 3], l16, wsrc + (i / 3) * 3072); };
    auto issue = [&](auto O) {
      constexpr int kind = G1S.kind[O.value], i = G1S.idx[O.value];
      if constexpr (kind == 0) dma16<((i >> 1) % G1_SLOTS) * 8192 + (i & 1) * 1024>(ldsw, xoff[i & 1], xsrc + (size_t)(i >> 1) * 32 * TP_ * 2);
      else if constexpr (kind == 1) issue_w(IC<i>{});
      else issue_wd(IC<i>{});
    };
    unsigned xa[8];
#pragma unroll
    for (int jj = 0; jj < 8; ++jj) {
      const int rr = 4 * kg + ((lane & 15) >> 2);
      xa[jj] = sbase + rr * 256 + ((((jj + 2 * wv) & 7) ^ (rr & 7)) * 32) + (lane & 3) * 8;
      pin(xa[jj]);
    }
    u32x4 ones = {0x3f803f80u, 0x3f803f80u, 0x3f803f80u, 0x3f803f80u};
    pin(ones);
    {
      const int fb = O_F2 + (b & 7) * 32 * 128 + (lane & 3) * 16;
      const int pa[1] = {fb + (20 + (lane < 48 ? lane >> 2 : 0)) * 128};
      poll<1, 2>(rw, pa, tag);
      static_for<0, 3 * (G1_R - 1)>(issue_w);
      static_for<0, G1_WD0>(issue_wd);
      STAMP(8);
      if (wv == 0) {
        const int pc[2] = {fb + (lane >> 2) * 128, fb + (16 + ((lane >> 2) & 3)) * 128};
        poll<2, 1>(rw, pc, tag);
      }
      wait_vm<0>();
      bar();
    }
    STAMP(9);
    static_for<0, G1S.first[0]>(issue);
    f32x4 acc[8][3], gram[2], sum1[2];
    u32x2 X[G1_LA + 1][2];
    auto xread = [&](auto T, auto H) {
      constexpr int t = T.value, h = H.value;
      ds_tr<((t >> 3) % G1_SLOTS) * 8192 + h * 4096>(X[t % (G1_LA + 1)][h], xa[t & 7]);
    };
    wait_vm<G1S.first[0] - 2>();
    bar();
    STAMP(10);
    static_for<0, G1_LA>([&](auto T) { xread(T, IC<0>{}); xread(T, IC<1>{}); });
    static_for<0, 160>([&](auto T) {
      constexpr int t = T.value, ks = t >> 3, jj = t & 7, tn = t + G1_LA;
      wait_lgkm<2 * ((tn <= 160 ? tn : 160) - t - 1)>();
      if constexpr (tn < 160 && (tn & 7) == 0) {
        static_assert(g1_pos(0, 2 * (tn / 8) + 1) < G1S.first[t], "not issued yet");
        wait_vm<G1S.first[t] - 1 - g1_pos(0, 2 * (tn / 8) + 1)>();
        bar();
      }
      const u32x4 xb = cat(X[t % (G1_LA + 1)][0], X[t % (G1_LA + 1)][1]);
      mfma_<ks == 0>(acc[jj][0], W[ks % G1_R][0], xb);
      if constexpr (tn < 160) xread(IC<tn>{}, IC<0>{});
      mfma_<ks == 0>(acc[jj][1], W[ks % G1_R][1], xb);
      if constexpr (tn < 160) xread(IC<tn>{}, IC<1>{});
      mfma_<ks == 0>(acc[jj][2], W[ks % G1_R][2], xb);
      static_for<G1S.first[t], G1S.first[t + 1]>(issue);
      if constexpr (jj < 2) { mfma_<ks == 0>(gram[jj], xb, xb); mfma_<ks == 0>(sum1[jj], ones, xb); }
    });
    STAMP(11);
    wait_vm<G1S.n - 1 - g1_pos(1, 62)>();
    asm volatile("" : "+v"(gram[0]), "+v"(gram[1]), "+v"(sum1[0]), "+v"(sum1[1]) :: "memory");
    f32x2* stat = (f32x2*)(smem + G1_STAT);
#pragma unroll
    for (int sj = 0; sj < 2; ++sj) {
      const int c3 = lane & 3;
      const float q = c3 == 0 ? gram[sj][0] : c3 == 1 ? gram[sj][1] : c3 == 2 ? gram[sj][2] : gram[sj][3];
      const float mean = sum1[sj][0] * (1.f / D_);
      const float var = fmaxf(q * (1.f / D_) - mean * mean, 0.f);
      const float sd = rbf(__builtin_amdgcn_sqrtf(rbf(rbf(var) + 1e-5f)));
      const float rs = __builtin_amdgcn_rcpf(sd);
      if (((lane & 15) >> 2) == kg) stat[((sj + 2 * wv) & 7) * 16 + (lane & 15)] = f32x2{__uint_as_float(pack_bf16(-mean, sd)), rs * rs};
    }
    asm volatile("s_waitcnt lgkmcnt(0)\n\ts_barrier" ::: "memory");
    u32* stage = (u32*)smem;
    f32x2 st[8];
#pragma unroll
    for (int jj = 0; jj < 8; ++jj) st[jj] = stat[((jj + 2 * wv) & 7) * 16 + (lane & 15)];
    u32x4 xc[8];
#pragma unroll
    for (int jj = 0; jj < 8; ++jj) {
      const u32 p = __float_as_uint(st[jj][0]);
      xc[jj] = u32x4{__builtin_amdgcn_perm(p, p, 0x01000100u), p, __builtin_amdgcn_perm(p, p, 0x03020302u), 0u};
      pin(xc[jj]);
    }
    asm volatile("s_nop 1");
#pragma unroll
    for (int jj = 0; jj < 8; ++jj)
#pragma unroll
      for (int i = 0; i < 3; ++i) mfma(acc[jj][i], W[20 % G1_R][i], xc[jj]);
    asm volatile("s_nop 7");
#pragma unroll
    for (int jj = 0; jj < 8; ++jj) {
      const int m16 = (jj + 2 * wv) & 7;
#pragma unroll
      for (int i = 0; i < 3; ++i) {
        pin(acc[jj][i]);
        const float g0 = __int_as_float(max(__float_as_int(acc[jj][i][0]), 0)), g1 = __int_as_float(max(__float_as_int(acc[jj][i][2]), 0));
        const int cI = 3 * wv + i;
        stage[(m16 * 3 + (cI >> 2)) * 256 + ((cI & 3) * 16 + (lane & 15)) * 4 + kg] =
            pack_bf16(g0 * st[jj][1] * acc[jj][i][1], g1 * st[jj][1] * acc[jj][i][3]);
      }
    }
    asm volatile("s_waitcnt lgkmcnt(0)\n\ts_barrier" ::: "memory");
#pragma unroll
    for (int i = 0; i < 6; ++i) {
      const int m16 = wv * 2 + i / 3, f3 = i % 3;
      const int g16 = mt * 8 + m16;
      __builtin_amdgcn_raw_buffer_store_b128(*(const u32x4*)(stage + (m16 * 3 + f3) * 256 + lane * 4), rw, l16,
                                             O_H2F + (((g16 >> 2) * 48 + 3 * nc + f3) * 4 + (g16 & 3)) * 1024, SC1);
    }
  }
  STAMP(12);
  drain_bar();
  if (tid < 16) st_flag(ws, O_F3 + (mt * 16 + tid) * 128 + nc * 8, tag);
  static_for<G1_WDN, 60>(issue_wd);
  {
    const char* xs = ws + O_X2T + (N * 80 * TP_ + M * 64) * 2;
    const unsigned xo = ((lane >> 3) * TP_ + (lane & 7) * 8) * 2;
    dma16<XR_OFF>(sbase + wv * 1024, xo, xs + (size_t)wv * 8 * TP_ * 2);
    dma16<XR_OFF + 4096>(sbase + wv * 1024, xo, xs + (size_t)(wv + 4) * 8 * TP_ * 2);
    dma16<XR_OFF + 8192>(sbase + (wv & 1) * 1024, xo, xs + (size_t)((wv & 1) + 8) * 8 * TP_ * 2);
  }
  float* gbl = (float*)(smem + XR_OFF + 10240);
  if (tid < 80) {
    float g = 0.f;
#pragma unroll
    for (int p = 0; p < MODP; ++p) g += __uint_as_float(__builtin_amdgcn_raw_buffer_load_b32(rw, tid * 4, O_MODP + (p * NMOD + 5 * D_ + N * 80) * 4, SC1));
    gbl[tid] = rbf(g);
  }
  const char* hp = ws + O_H2F + (M * 48 + wv * 12) * 4096;
  unsigned hoff[4];
#pragma unroll
  for (int jj = 0; jj < 4; ++jj) { hoff[jj] = ((jj + wv + 1) & 3) * 1024 + l16; pin(hoff[jj]); }
  {
    const int po[1] = {O_F3 + (mt * 16 + nc) * 128 + wv * 32 + (lane & 1) * 16};
    poll<1, 0>(rw, po, tag);
    wait_vm<0>();
  }
  STAMP(13);
  u32x4 H[G2_R][4];
  auto issue_h = [&](auto O) { constexpr int o = O.value; gld128<0>(H[(o >> 2) % G2_R][o & 3], hoff[o & 3], hp + (o >> 2) * 4096); };
  static_for<0, 4 * G2_PRE>(issue_h);
  f32x4 acc[4][5];
  static_for<0, 12>([&](auto KS) {
    constexpr int ks = KS.value;
    constexpr int i0 = (4 * G2_PRE + 5 * ks < 48) ? 4 * G2_PRE + 5 * ks : 48;
    static_assert(i0 >= 4 * (ks + 1) && (i0 + 5 > 48 ? 48 : i0 + 5) <= 4 * (ks + G2_R), "ring");
    wait_vm<i0 - 4 * (ks + 1)>();
    static_for<0, 20>([&](auto G) {
      constexpr int g = G.value, jj = g / 5, i = g % 5;
      mfma_a<ks == 0>(acc[jj][i], WD[ks * 5 + i], H[ks % G2_R][jj]);
      if constexpr ((g & 3) == 0 && i0 + g / 4 < 48) issue_h(IC<i0 + g / 4>{});
    });
  });
  STAMP(14);
  asm volatile("s_nop 7");
  static_for<0, 15>([&](auto G) {
    constexpr int jj = G.value / 5, i = G.value % 5;
    ds_w128<(jj * 5 + i) * 1024>(sbase + ((jj + wv + 1) & 3) * 15360 + l16, acc[jj][i]);
  });
  asm volatile("s_waitcnt lgkmcnt(0)\n\ts_barrier" ::: "memory");
  const f32x4* part = (const f32x4*)(smem + wv * 15360);
  const unsigned xad = sbase + XR_OFF + (4 * kg + ((lane & 15) >> 2)) * 128 + wv * 32 + (lane & 3) * 8;
  u32x2 xw[5];
  static_for<0, 5>([&](auto I) { ds_tr<I.value * 2048>(xw[I.value], xad); });
  wait_lgkm<0>();
  const int pos = M * 64 + wv * 16 + (lane & 15);
  const int l8 = (pos >> 3) & 63;
  const int tok = 256 * (2 * (pos >> 9) + ((pos >> 2) & 1)) + 16 * (l8 & 15) + 4 * (l8 >> 4) + (pos & 3);
#pragma unroll
  for (int i = 0; i < 5; ++i) {
    pin(xw[i]); pin(acc[3][i]);
    f32x4 o = acc[3][i];
#pragma unroll
    for (int sl = 0; sl < 3; ++sl) o += part[(sl * 5 + i) * 64 + lane];
    const float x4[4] = {bflo(xw[i][0]), bfhi(xw[i][0]), bflo(xw[i][1]), bfhi(xw[i][1])};
    const f32x4 gb = *(const f32x4*)(gbl + i * 16 + 4 * kg);
    float r[4];
#pragma unroll
    for (int q = 0; q < 4; ++q) r[q] = fmaf(gb[q], o[q], x4[q]);
    *(u32x2*)(out + (size_t)tok * D_ + N * 80 + i * 16 + 4 * kg) = u32x2{pack_bf16(r[0], r[1]), pack_bf16(r[2], r[3])};
  }
  if (b == 0 && tid == 0) st_flag(ws, O_EPOCH, tag);
  STAMP_END();
}

extern "C" void kernel_launch(void* const* d_in, const int* in_sizes, int n_in, void* d_out, int out_size,
                              void* d_ws, size_t ws_size, hipStream_t stream) {
  k_fused<<<256, 256, 0, stream>>>((const u16*)d_in[0], (const u16*)d_in[1], (const u16*)d_in[2], (const u16*)d_in[3],
                                   (const u16*)d_in[4], (const u16*)d_in[5], (const u16*)d_in[7], (const u16*)d_in[8],
                                   (const u16*)d_in[9], (const u16*)d_in[10], (const u16*)d_in[11], (u16*)d_out, (char*)d_ws);
}
```

```cpp
#include <hip/hip_runtime.h>
#include <stdint.h>

typedef unsigned short u16;
typedef unsigned int u32;
typedef __attribute__((ext_vector_type(2))) __bf16 bf16x2;
typedef __attribute__((ext_vector_type(4))) float f32x4;
typedef __attribute__((ext_vector_type(2))) float f32x2;
typedef __attribute__((ext_vector_type(4))) u32 u32x4;
typedef __attribute__((ext_vector_type(2))) u32 u32x2;

constexpr int S_ = 2048, D_ = 640, F_ = 1536;
constexpr int TP_ = S_ + 64;
constexpr int NMOD = 6 * D_;
constexpr int MODP = 5;
constexpr int CWG = 160;

__device__ __forceinline__ float bflo(u32 w) { return __uint_as_float(w << 16); }
__device__ __forceinline__ float bfhi(u32 w) { return __uint_as_float(w & 0xffff0000u); }
__device__ __forceinline__ u32 pack_bf16(float lo, float hi) {
  bf16x2 v; v[0] = (__bf16)lo; v[1] = (__bf16)hi; return __builtin_bit_cast(u32, v);
}
__device__ __forceinline__ float rbf(float x) { __bf16 b = (__bf16)x; return (float)b; }
#define SCHED() __builtin_amdgcn_sched_barrier(0)
template <int V> struct IC { static constexpr int value = V; };
template <int B, int E, class Fn> __device__ __forceinline__ void static_for(Fn&& f) {
  if constexpr (B < E) { f(IC<B>{}); static_for<B + 1, E>(f); }
}
__device__ __forceinline__ void mfma(f32x4& c, u32x4 a, u32x4 b) {
  asm volatile("v_mfma_f32_16x16x32_bf16 %0, %1, %2, %0" : "+v"(c) : "v"(a), "v"(b));
}
__device__ __forceinline__ void mfma0(f32x4& c, u32x4 a, u32x4 b) {
  asm volatile("v_mfma_f32_16x16x32_bf16 %0, %1, %2, 0" : "=&v"(c) : "v"(a), "v"(b));
}
template <bool FIRST> __device__ __forceinline__ void mfma_(f32x4& c, u32x4 a, u32x4 b) { if constexpr (FIRST) mfma0(c, a, b); else mfma(c, a, b); }
__device__ __forceinline__ u32 alignbit(u32 hi, u32 lo, u32 sh) {
  u32 d; asm volatile("v_alignbit_b32 %0, %1, %2, %3" : "=v"(d) : "v"(hi), "v"(lo), "v"(sh)); return d;
}
template <int OFF> __device__ __forceinline__ void ds_tr(u32x2& dst, unsigned addr) {
  asm volatile("ds_read_b64_tr_b16 %0, %1 offset:%2" : "=v"(dst) : "v"(addr), "i"(OFF) : "memory");
}
template <int OFF> __device__ __forceinline__ void ds_b32(u32& dst, unsigned addr) {
  asm volatile("ds_read_b32 %0, %1 offset:%2" : "=v"(dst) : "v"(addr), "i"(OFF) : "memory");
}
template <int OFF> __device__ __forceinline__ void ds_b64(u32x2& dst, unsigned addr) {
  asm volatile("ds_read_b64 %0, %1 offset:%2" : "=v"(dst) : "v"(addr), "i"(OFF) : "memory");
}
template <int OFF> __device__ __forceinline__ void ds_b128(u32x4& dst, unsigned addr) {
  asm volatile("ds_read_b128 %0, %1 offset:%2" : "=v"(dst) : "v"(addr), "i"(OFF) : "memory");
}
template <int OFF> __device__ __forceinline__ void ds_w128(unsigned addr, f32x4 v) {
  asm volatile("ds_write_b128 %0, %1 offset:%2" :: "v"(addr), "v"(v), "i"(OFF) : "memory");
}
template <int OFF> __device__ __forceinline__ void gld128(u32x4& dst, unsigned voff, const char* sb) {
  asm volatile("global_load_dwordx4 %0, %1, %2 offset:%3 sc1" : "=v"(dst) : "v"(voff), "s"(sb), "i"(OFF) : "memory");
}
__device__ __forceinline__ void gld32(u32& dst, unsigned voff, const char* sb) {
  asm volatile("global_load_dword %0, %1, %2 sc1" : "=v"(dst) : "v"(voff), "s"(sb) : "memory");
}
template <int LOFF> __device__ __forceinline__ void dma16(unsigned ldsb, unsigned voff, const char* sb) {
  asm volatile("s_add_u32 m0, %0, %3\n\ts_nop 0\n\tglobal_load_lds_dwordx4 %1, %2 sc1" :: "s"(ldsb), "v"(voff), "s"(sb), "i"(LOFF) : "memory", "scc");
}
template <int LOFF> __device__ __forceinline__ void dma16x4(unsigned ldsb, unsigned v0, unsigned v1, const char* sb) {
  asm volatile("s_add_u32 m0, %0, %4\n\ts_nop 0\n\tglobal_load_lds_dwordx4 %1, %3 sc1\n\tglobal_load_lds_dwordx4 %2, %3 offset:1024 sc1\n\t"
               "global_load_lds_dwordx4 %1, %3 offset:2048 sc1\n\tglobal_load_lds_dwordx4 %2, %3 offset:3072 sc1"
               :: "s"(ldsb), "v"(v0), "v"(v1), "s"(sb), "i"(LOFF) : "memory", "scc");
}
__device__ __forceinline__ void sld32(u32& dst, const void* p) { asm volatile("s_load_dword %0, %1, 0x0" : "=s"(dst) : "s"(p) : "memory"); }
template <int N> __device__ __forceinline__ void wait_vm() { asm volatile("s_waitcnt vmcnt(%0)" :: "i"(N) : "memory"); }
template <int N> __device__ __forceinline__ void wait_lgkm() { asm volatile("s_waitcnt lgkmcnt(%0)" :: "i"(N) : "memory"); }
__device__ __forceinline__ void bar() { asm volatile("s_barrier" ::: "memory"); }
template <class T> __device__ __forceinline__ void pin(T& a) { asm volatile("" : "+v"(a)); }
__device__ __forceinline__ u32x4 cat(u32x2 a, u32x2 b) { return u32x4{a[0], a[1], b[0], b[1]}; }

typedef __amdgpu_buffer_rsrc_t rsrc_t;
__device__ __forceinline__ rsrc_t mk_rsrc(const void* p, int bytes) { return __builtin_amdgcn_make_buffer_rsrc((void*)p, 0, bytes, 0x00020000); }
template <int CTRL> __device__ __forceinline__ float dpp_add(float x) {
  return x + __builtin_bit_cast(float, __builtin_amdgcn_mov_dpp(__builtin_bit_cast(int, x), CTRL, 0xf, 0xf, true));
}
constexpr int DPP_XOR1 = 0xB1, DPP_XOR2 = 0x4E, DPP_HMIRROR = 0x141, DPP_ROR8 = 0x128;
__device__ __forceinline__ float sum8(float x) { return dpp_add<DPP_HMIRROR>(dpp_add<DPP_XOR2>(dpp_add<DPP_XOR1>(x))); }
__device__ __forceinline__ float tred32(float a, float b) {
  auto r = __builtin_amdgcn_permlane32_swap(__float_as_uint(a), __float_as_uint(b), false, false);
  return __uint_as_float(r[0]) + __uint_as_float(r[1]);
}
__device__ __forceinline__ float tred16(float a, float b) {
  auto r = __builtin_amdgcn_permlane16_swap(__float_as_uint(a), __float_as_uint(b), false, false);
  return __uint_as_float(r[0]) + __uint_as_float(r[1]);
}
__device__ __forceinline__ float rdl(float x, int l) { return __int_as_float(__builtin_amdgcn_readlane(__float_as_int(x), l)); }

typedef unsigned long long u64;
#define GAS __attribute__((address_space(1)))
constexpr int SC1 = 16;
#define A0_DELAY 16
#define F_LATE 1
#define A_DELAY 2
#define FXE 7
#define PE 2
#define NT 2
#define NTS " nt"
template <int OFF> __device__ __forceinline__ void gld128a(u32x4& dst, unsigned voff, const char* sb) {
  asm volatile("global_load_dwordx4 %0, %1, %2 offset:%3 sc1" : "=a"(dst) : "v"(voff), "s"(sb), "i"(OFF) : "memory");
}
template <bool FIRST> __device__ __forceinline__ void mfma_a(f32x4& c, u32x4 a, u32x4 b) {
  if constexpr (FIRST) asm volatile("v_mfma_f32_16x16x32_bf16 %0, %1, %2, 0" : "=&v"(c) : "a"(a), "v"(b));
  else asm volatile("v_mfma_f32_16x16x32_bf16 %0, %1, %2, %0" : "+v"(c) : "a"(a), "v"(b));
}
typedef __attribute__((ext_vector_type(8))) __bf16 bf16x8;
__device__ __forceinline__ f32x4 mfma_b(u32x4 a, u32x4 b, f32x4 c) {
  return __builtin_amdgcn_mfma_f32_16x16x32_bf16(__builtin_bit_cast(bf16x8, a), __builtin_bit_cast(bf16x8, b), c, 0, 0, 0);
}
template <int LOFF> __device__ __forceinline__ void dma16p(unsigned ldsb, unsigned voff, const char* sb) {
  asm volatile("s_add_u32 m0, %0, %3\n\ts_nop 0\n\tglobal_load_lds_dwordx4 %1, %2" NTS :: "s"(ldsb), "v"(voff), "s"(sb), "i"(LOFF) : "memory", "scc");
}
template <int LOFF> __device__ __forceinline__ void dma16x4p(unsigned ldsb, unsigned v0, const char* sb) {
  asm volatile("s_add_u32 m0, %0, %3\n\ts_nop 0\n\tglobal_load_lds_dwordx4 %1, %2" NTS "\n\tglobal_load_lds_dwordx4 %1, %2 offset:1024" NTS "\n\t"
               "global_load_lds_dwordx4 %1, %2 offset:2048" NTS "\n\tglobal_load_lds_dwordx4 %1, %2 offset:3072" NTS
               :: "s"(ldsb), "v"(v0), "s"(sb), "i"(LOFF) : "memory", "scc");
}

#define STAMP(I)
#define STAMP_DECL()
#define STAMP_END()

__device__ __forceinline__ void gld128p(u32x4& dst, unsigned voff, const char* sb) {
  asm volatile("global_load_dwordx4 %0, %1, %2" NTS : "=v"(dst) : "v"(voff), "s"(sb) : "memory");
}
template <int OFF> __device__ __forceinline__ void gld32p(u32& dst, unsigned voff, const char* sb) {
  asm volatile("global_load_dword %0, %1, %2 offset:%3" NTS : "=v"(dst) : "v"(voff), "s"(sb), "i"(OFF) : "memory");
}
template <int OFF> __device__ __forceinline__ void gld16p(u32& dst, unsigned voff, const char* sb) {
  asm volatile("global_load_ushort %0, %1, %2 offset:%3" : "=v"(dst) : "v"(voff), "s"(sb), "i"(OFF) : "memory");
}
#define CPF_DONE() asm volatile("" :: "v"(cpf))
constexpr int W1_STEPS = 21;
constexpr int O_EPOCH = 0;
constexpr int O_FK = 4096;
constexpr int O_FA0 = O_FK + 160 * 256;
constexpr int O_FA1 = O_FA0 + 8 * 12 * 128;
constexpr int O_FX = O_FA1 + 8 * 12 * 128;
constexpr int O_F2 = O_FX + 8 * 20 * 128;
constexpr int O_F3 = O_F2 + 8 * 32 * 128;
constexpr int O_MODP = O_F3 + 256 * 128;
constexpr int O_ST1 = O_MODP + MODP * NMOD * 4;
constexpr int O_KVG = O_ST1 + 16384;
constexpr int O_XG = O_KVG + S_ * D_ * 2;
constexpr int O_X2T = O_XG + S_ * D_ * 2;
constexpr int O_W1F = O_X2T + TP_ * D_ * 2;
constexpr int O_W2F = O_W1F + 64 * W1_STEPS * 3 * 1024;
constexpr int O_H2F = O_W2F + 8 * 48 * 5 * 1024;
constexpr int WS_BYTES = O_H2F + S_ * F_ * 2;
static_assert(O_MODP % 256 == 0 && O_ST1 % 256 == 0 && O_KVG % 256 == 0 && O_X2T % 256 == 0 && O_W1F % 256 == 0, "alignment");

constexpr int SPIN_CAP = 1 << 16;
constexpr unsigned CODE_BYTES = 40 * 1024;
__device__ __forceinline__ void st_flag(char* ws, int off, u64 tag) {
  __hip_atomic_store((GAS u64*)(ws + off), tag, __ATOMIC_RELAXED, __HIP_MEMORY_SCOPE_AGENT);
}
__device__ __forceinline__ bool tag_ok(u32x4 v, u64 tag) {
  const u32 lo = (u32)tag, hi = (u32)(tag >> 32);
  return ((v[0] ^ lo) | (v[1] ^ hi) | (v[2] ^ lo) | (v[3] ^ hi)) == 0;
}
template <int NL, int SLEEP> __device__ __forceinline__ void poll(rsrc_t rw, const int (&off)[NL], u64 tag) {
#pragma unroll 1
  for (int sp = 0; sp < SPIN_CAP; ++sp) {
    bool ok = true;
#pragma unroll
    for (int i = 0; i < NL; ++i) ok &= tag_ok(__builtin_amdgcn_raw_buffer_load_b128(rw, off[i], 0, SC1), tag);
    if (__all(ok)) break;
    if (SLEEP) __builtin_amdgcn_s_sleep(SLEEP);
    asm volatile("" ::: "memory");
  }
  asm volatile("" ::: "memory");
}
__device__ __forceinline__ u64 next_tag(u32x2 ep) {
  u32 lo, hi;
  asm volatile("s_waitcnt lgkmcnt(0)\n\ts_add_u32 %0, %2, 1\n\ts_addc_u32 %1, %3, 0" : "=&s"(lo), "=&s"(hi) : "s"(ep[0]), "s"(ep[1]) : "memory", "scc");
  return ((u64)hi << 32) | lo;
}
__device__ __forceinline__ void drain_bar() { asm volatile("s_waitcnt vmcnt(0) lgkmcnt(0)\n\ts_barrier" ::: "memory"); }

constexpr int FC_BYTES = 2080 * 2;
constexpr int CH_BYTES = 2 * FC_BYTES;
constexpr int CKV_OFF = 33280;
constexpr int CX_OFF = CKV_OFF + 2048 + 16384, CS_OFF = CX_OFF + 16384;
constexpr int conv_gap(int a, int b) { int n = 0; for (int i = 0; i < a; ++i) n += 8 - i; return n + b; }
constexpr int G1_R = 6;
constexpr int G1_SLOTS = 8;
constexpr int G1_LA = 4;
constexpr int G1_STAT = G1_SLOTS * 8192;
constexpr int G1_XPRE = 3;
#define WD0 8
constexpr int G1_WD0 = WD0;
constexpr int G1_WD = WD0 + 22;
struct G1Sched { int kind[192]; int idx[192]; int first[162]; int n; };
constexpr G1Sched g1_make() {
  G1Sched S{};
  int mk[128] = {}, mi[128] = {}, ms[128] = {}, nm = 0;
  for (int st = 0; st <= 20; ++st) {
    if (st >= G1_R - 1) for (int i = 0; i < 3; ++i) { mk[nm] = 1; mi[nm] = 3 * st + i; ms[nm] = st; ++nm; }
    if (st < 20) for (int p = 0; p < 2; ++p) { mk[nm] = 0; mi[nm] = 2 * st + p; ms[nm] = st; ++nm; }
  }
  int n = 0, m = 0, wd = G1_WD0;
  for (; m < 2 * G1_XPRE; ++m) { S.kind[n] = mk[m]; S.idx[n] = mi[m]; ++n; }
  for (int t = 0; t < 160; ++t) {
    S.first[t] = n;
    const int ks = t >> 3, jj = t & 7;
    if (m < nm && ms[m] <= ks + G1_R - 1) { S.kind[n] = mk[m]; S.idx[n] = mi[m]; ++n; ++m; }
    else if (wd < G1_WD && t < 150 && m >= nm && jj < 6) { S.kind[n] = 2; S.idx[n] = wd; ++n; ++wd; }
  }
  S.first[160] = n; S.n = n;
  return S;
}
constexpr G1Sched G1S = g1_make();
constexpr int g1_pos(int kind, int idx) { for (int i = 0; i < G1S.n; ++i) if (G1S.kind[i] == kind && G1S.idx[i] == idx) return i; return -1; }
constexpr int g1_wd_count() { int c = G1_WD0; for (int i = 0; i < G1S.n; ++i) c += G1S.kind[i] == 2; return c; }
constexpr int G1_WDN = g1_wd_count();
static_assert(g1_pos(1, 62) >= 0 && g1_pos(0, 39) >= 0, "schedule incomplete");
constexpr int G2_R = 8, G2_PRE = 4;
constexpr int XR_OFF = 69632;
constexpr int LDS_BYTES = 96 * 1024;
constexpr int AG_OFF = 0, AU_OFF = 20480, AD_OFF = 40960, AM_OFF = 61440;

__global__ __launch_bounds__(256, 1) void k_fused(const u16* __restrict__ xIn, const u16* __restrict__ kIn, const u16* __restrict__ vIn,
                                                  const u16* __restrict__ shk, const u16* __restrict__ ssmk, const u16* __restrict__ ssmD,
                                                  const u16* __restrict__ wg, const u16* __restrict__ wu, const u16* __restrict__ wd,
                                                  const u16* __restrict__ cIn, const u16* __restrict__ wmod,
                                                  u16* __restrict__ out, char* __restrict__ ws) {
  __shared__ __attribute__((aligned(16))) char smem[LDS_BYTES];
  STAMP_DECL();
  STAMP(0);
  u32 cpf = 0;
  if (blockIdx.x >= 240) {
    uint64_t pc; asm volatile("s_getpc_b64 %0" : "=s"(pc));
    const unsigned co = (((blockIdx.x >> 3) & 1) * 256 + threadIdx.x) * 128;
    asm volatile("global_load_dword %0, %1, %2" : "=v"(cpf) : "v"(co < CODE_BYTES ? co : 0u), "s"(pc & ~(uint64_t)127) : "memory");
  }
  asm volatile("" :: "s"(xIn), "s"(kIn), "s"(vIn), "s"(shk), "s"(ssmk), "s"(ssmD), "s"(wg), "s"(wu), "s"(wd), "s"(cIn), "s"(wmod), "s"(out), "s"(ws));
  u32x2 ep;
  asm volatile("s_load_dwordx2 %0, %1, 0x0" : "=s"(ep) : "s"(ws) : "memory");
  u64 tag;
  const int tid = threadIdx.x, lane = tid & 63;
  const int wv = __builtin_amdgcn_readfirstlane(tid >> 6);
  const int b = blockIdx.x;
  const unsigned sbase = (unsigned)(size_t)smem;
  const unsigned l16 = lane * 16;
  const rsrc_t rw = mk_rsrc(ws, WS_BYTES);
  if (b >= CWG) {
    const int j = b - CWG;
    const int r8 = lane >> 3, cp = lane & 7;
    const int ks2 = j >> 1, hf = j & 1;
    const int ug = j % 24, kq = j / 24;
    float* LC = (float*)(smem + AM_OFF);
    u16* LS = (u16*)(smem + AM_OFF + 640);
    const int gt = wv * 96 + (95 - j);
    const bool gact = gt < 300;
    const int gcol = gt / MODP, gk = gt - gcol * MODP;
    u32x4 wm[16]; u32 cv[2];
    if (A0_DELAY) __builtin_amdgcn_s_sleep(A0_DELAY);
    if (gact) {
      static_for<0, 2>([&](auto I) { gld16p<I.value * 128>(cv[I.value], lane * 2, (const char*)(cIn + gk * 128)); });
      static_for<0, 16>([&](auto I) { gld128p(wm[I.value], r8 * (NMOD * 2) + cp * 16, (const char*)(wmod + (size_t)(gk * 128 + I.value * 8) * NMOD + gcol * 64)); });
    }
    tag = next_tag(ep);
    if (gact) {
      wait_vm<16>();
      float sl[2], sv[16];
#pragma unroll
      for (int i = 0; i < 2; ++i) { pin(cv[i]); const float x = bflo(cv[i]); sl[i] = rbf(x * __builtin_amdgcn_rcpf(1.f + __expf(-x))); }
#pragma unroll
      for (int it = 0; it < 16; ++it) { sv[it] = __shfl(sl[it >> 3], (it & 7) * 8 + r8); pin(sv[it]); }
      f32x2 acc[4];
      static_for<0, 4>([&](auto G) {
        constexpr int g = G.value;
        wait_vm<12 - 4 * g>();
#pragma unroll
        for (int it = 4 * g; it < 4 * g + 4; ++it) {
          pin(wm[it]);
          const f32x2 s2 = {sv[it], sv[it]};
#pragma unroll
          for (int q = 0; q < 4; ++q) {
            const f32x2 w2 = {bflo(wm[it][q]), bfhi(wm[it][q])};
            acc[q] = it ? __builtin_elementwise_fma(s2, w2, acc[q]) : s2 * w2;
          }
        }
#pragma unroll
        for (int q = 0; q < 4; ++q) pin(acc[q]);
      });
      const float a0 = tred16(tred32(acc[0][0], acc[2][0]), tred32(acc[1][0], acc[3][0]));
      const float a1 = tred16(tred32(acc[0][1], acc[2][1]), tred32(acc[1][1], acc[3][1]));
      const float e0 = dpp_add<DPP_ROR8>(a0), e1 = dpp_add<DPP_ROR8>(a1);
      __builtin_amdgcn_raw_buffer_store_b32(__float_as_uint((lane & 8) ? e1 : e0), rw, O_MODP + (gk * NMOD + gcol * 64 + cp * 8 + r8) * 4, 0, SC1);
    }
    STAMP(1);
    {
      const unsigned vg = r8 * (F_ * 2) + ((cp ^ ((r8 >> 1) * 2)) * 16);
      const char* gs = (const char*)(wg + (size_t)(kq * 160 + wv * 40) * F_ + ug * 64);
      const char* us = (const char*)(wu + (size_t)(kq * 160 + wv * 40) * F_ + ug * 64);
      static_for<0, 5>([&](auto I) {
        dma16p<AG_OFF + I.value * 1024>(sbase + wv * 5120, vg, gs + I.value * (8 * F_ * 2));
        dma16p<AU_OFF + I.value * 1024>(sbase + wv * 5120, vg ^ 16, us + I.value * (8 * F_ * 2));
      });
    }
    wait_vm<10>();
    bar();
    if (tid < 8) st_flag(ws, O_FA0 + (tid * 12 + (j >> 3)) * 128 + (j & 7) * 8, tag);
    STAMP(2);
    const int kg = lane >> 4, q4 = (lane & 15) >> 2, p4 = lane & 3;
    {
      const char* ds = (const char*)(wd + (size_t)(ks2 * 32 + wv * 8) * D_ + hf * 320);
      static_for<0, 5>([&](auto I) { dma16p<AD_OFF + I.value * 4096>(sbase + wv * 1024, r8 * (D_ * 2) + cp * 16, ds + I.value * 128); });
    }
    if (wv == 0) {
      const int po[1] = {O_FA0 + ((b & 7) * 12 + (lane < 48 ? lane >> 2 : 0)) * 128 + (lane & 3) * 16};
      poll<1, 1>(rw, po, tag);
      STAMP(3);
      f32x4 mm[2];
      const int lo = lane < 40 ? lane * 16 : 0;
#pragma unroll
      for (int i = 0; i < 2; ++i) {
        mm[i] = f32x4{0.f, 0.f, 0.f, 0.f};
#pragma unroll
        for (int p = 0; p < MODP; ++p)
          mm[i] += __builtin_bit_cast(f32x4, __builtin_amdgcn_raw_buffer_load_b128(rw, lo, O_MODP + (p * NMOD + (3 + i) * D_ + kq * 160) * 4, SC1));
      }
      if (lane < 40) {
        *(u32x2*)(LS + lane * 4) = u32x2{pack_bf16(mm[0][0], mm[0][1]), pack_bf16(mm[0][2], mm[0][3])};
        *(f32x4*)(LC + lane * 4) = f32x4{rbf(1.f + rbf(mm[1][0])), rbf(1.f + rbf(mm[1][1])), rbf(1.f + rbf(mm[1][2])), rbf(1.f + rbf(mm[1][3]))};
      }
    }
    wait_vm<0>();
    asm volatile("s_waitcnt lgkmcnt(0)\n\ts_barrier" ::: "memory");
    CPF_DONE();
    STAMP(4);
    {
      const int rr = 4 * kg + q4;
      u32x2 lo[10], hi[10], sbl[5], sbh[5]; u32x4 cl[5], ch[5];
      unsigned ad[2];
#pragma unroll
      for (int t = 0; t < 2; ++t) ad[t] = sbase + (p4 >> 1) * AU_OFF + rr * 128 + (((wv * 2 + t) ^ (((rr >> 1) & 3) * 2 + (p4 >> 1))) * 16) + (p4 & 1) * 8;
      static_for<0, 10>([&](auto I) { constexpr int i = I.value; ds_tr<(i >> 1) * 4096>(lo[i], ad[i & 1]); ds_tr<(i >> 1) * 4096 + 2048>(hi[i], ad[i & 1]); });
      const unsigned ac = sbase + AM_OFF + kg * 16, as = sbase + AM_OFF + 640 + kg * 8;
      static_for<0, 5>([&](auto I) { constexpr int i = I.value; ds_b128<i * 128>(cl[i], ac); ds_b128<i * 128 + 64>(ch[i], ac); ds_b64<i * 64>(sbl[i], as); ds_b64<i * 64 + 32>(sbh[i], as); });
      u32x2 dl[5], dh[5];
      const unsigned add = sbase + AD_OFF + (8 * kg + q4) * 128 + p4 * 8;
#pragma unroll
      for (int i = 0; i < 5; ++i) { const int tl = wv * 5 + i; const unsigned a2 = add + (tl >> 2) * 4096 + (tl & 3) * 32; ds_tr<0>(dl[i], a2); ds_tr<512>(dh[i], a2); }
      wait_lgkm<0>();
#pragma unroll
      for (int i = 0; i < 10; ++i) { pin(lo[i]); pin(hi[i]); }
#pragma unroll
      for (int i = 0; i < 5; ++i) { pin(cl[i]); pin(ch[i]); pin(sbl[i]); pin(sbh[i]); pin(dl[i]); pin(dh[i]); }
      const int frow = 2 * (4 * (q4 & 1) + p4) + (q4 >> 1);
      const u32x4 ones = {0x3f803f80u, 0x3f803f80u, 0x3f803f80u, 0x3f803f80u};
      f32x4 aU[2] = {}, aW[2] = {};
#pragma unroll
      for (int i = 0; i < 10; ++i) {
        const int ks = i >> 1, t = i & 1;
        const u32x4 raw = cat(lo[i], hi[i]);
        u32x4 sc;
#pragma unroll
        for (int q = 0; q < 2; ++q) {
          sc[q] = pack_bf16(bflo(raw[q]) * __uint_as_float(cl[ks][2 * q]), bfhi(raw[q]) * __uint_as_float(cl[ks][2 * q + 1]));
          sc[2 + q] = pack_bf16(bflo(raw[2 + q]) * __uint_as_float(ch[ks][2 * q]), bfhi(raw[2 + q]) * __uint_as_float(ch[ks][2 * q + 1]));
        }
        aW[t] = mfma_b(raw, cat(sbl[ks], sbh[ks]), aW[t]);
        aU[t] = mfma_b(sc, ones, aU[t]);
        const int T = ug * 8 + wv * 2 + t;
        const int sw3 = T / 3, ti = T - sw3 * 3;
        __builtin_amdgcn_raw_buffer_store_b128(sc, rw, (kg * 16 + frow) * 16, O_W1F + ((sw3 * W1_STEPS + kq * 5 + ks) * 3 + ti) * 1024, SC1);
      }
#pragma unroll
      for (int i = 0; i < 5; ++i) __builtin_amdgcn_raw_buffer_store_b128(cat(dl[i], dh[i]), rw, l16, O_W2F + (((hf * 4 + wv) * 48 + ks2) * 5 + i) * 1024, SC1);
      const f32x4 su = (lane & 4) ? aU[1] : aU[0], sw = (lane & 4) ? aW[1] : aW[0];
      const float U = p4 == 0 ? su[0] : p4 == 1 ? su[1] : p4 == 2 ? su[2] : su[3];
      const float W = p4 == 0 ? sw[0] : p4 == 1 ? sw[1] : p4 == 2 ? sw[2] : sw[3];
      const float u1 = rbf(U), u2 = rbf(U - u1), u3 = U - u1 - u2;
      const float w1 = rbf(W), w2 = rbf(W - w1), w3 = W - w1 - w2;
      const int T = ug * 8 + wv * 2 + ((lane >> 2) & 1);
      const int sw3 = T / 3, ti = T - sw3 * 3;
      const int crow = 2 * (4 * (kg & 1) + p4) + (kg >> 1);
      if (!(lane & 8))
        __builtin_amdgcn_raw_buffer_store_b128(u32x4{pack_bf16(u1, u2), pack_bf16(u3, w1), pack_bf16(w2, w3), 0u}, rw,
                                               O_W1F + (((sw3 * W1_STEPS + 20) * 3 + ti) * 64 + kq * 16 + crow) * 16, 0, SC1);
    }
    STAMP(5);
  } else {
    const int d = (b >> 1) * 8 + (b & 1) + 2 * wv;
    const int row = lane & 15, kg = lane >> 4;
    const int tt = b & 31, ct = b >> 5;
    const int ov = ((lane >> 2) * 2048 + (lane & 3)) * 16, os = (ct * 16 * 2048 + (tt * 8 + wv * 2) * 4) * 16;
    {
      const rsrc_t rk = mk_rsrc(kIn, S_ * D_ * 2), rv = mk_rsrc(vIn, S_ * D_ * 2);
      const int vo = ct * 256 + lane * 4, so = (tt * 64 + wv * 16) * 1280;
      const int sh0 = so ? so - 3 * 1280 : 0;
      u32 kk[19], vv[16];
      const u32x4 taps = *(const u32x4*)(shk + (ct * 64 + lane) * 8);
#pragma unroll
      for (int q = 0; q < 3; ++q) kk[q] = __builtin_amdgcn_raw_buffer_load_b32(rk, vo + q * 1280, sh0, NT);
#pragma unroll
      for (int q = 0; q < 8; ++q) kk[3 + q] = __builtin_amdgcn_raw_buffer_load_b32(rk, vo + (q & 1) * 1280, so + (q & 14) * 1280, NT);
#pragma unroll
      for (int q = 0; q < 8; ++q) vv[q] = __builtin_amdgcn_raw_buffer_load_b32(rv, vo + (q & 1) * 1280, so + (q & 14) * 1280, NT);
#pragma unroll
      for (int q = 8; q < 16; ++q) kk[3 + q] = __builtin_amdgcn_raw_buffer_load_b32(rk, vo + (q & 1) * 1280, so + (q & 14) * 1280, NT);
#pragma unroll
      for (int q = 8; q < 16; ++q) vv[q] = __builtin_amdgcn_raw_buffer_load_b32(rv, vo + (q & 1) * 1280, so + (q & 14) * 1280, NT);
      SCHED();
      if (!F_LATE) {
        const char* fs = (const char*)(ssmk + (size_t)d * S_);
        dma16x4p<32>(sbase + wv * CH_BYTES, l16, fs);
        dma16x4p<FC_BYTES + 28>(sbase + wv * CH_BYTES, l16, fs);
      }
      SCHED();
      if (so == 0) { kk[0] = 0; kk[1] = 0; kk[2] = 0; }
      const f32x2 w0 = {bflo(taps[0]), bflo(taps[2])}, w1 = {bfhi(taps[0]), bfhi(taps[2])};
      const f32x2 w2 = {bflo(taps[1]), bflo(taps[3])}, w3 = {bfhi(taps[1]), bfhi(taps[3])};
#pragma unroll
      for (int r = 0; r < 2; ++r) {
        f32x2 kv[8];
#pragma unroll
        for (int q = 0; q < 8; ++q) {
          const int i = 8 * r + q;
          f32x2 a = f32x2{bflo(kk[i]), bfhi(kk[i])} * w3;
          a = __builtin_elementwise_fma(f32x2{bflo(kk[i + 1]), bfhi(kk[i + 1])}, w2, a);
          a = __builtin_elementwise_fma(f32x2{bflo(kk[i + 2]), bfhi(kk[i + 2])}, w1, a);
          a = __builtin_elementwise_fma(f32x2{bflo(kk[i + 3]), bfhi(kk[i + 3])}, w0, a);
          kv[q] = a * f32x2{bflo(vv[i]), bfhi(vv[i])};
        }
        u32x4 o0, o1;
#pragma unroll
        for (int t = 0; t < 4; ++t) {
          o0[t] = pack_bf16(kv[7 - 2 * t][0], kv[6 - 2 * t][0]);
          o1[t] = pack_bf16(kv[7 - 2 * t][1], kv[6 - 2 * t][1]);
        }
        __builtin_amdgcn_raw_buffer_store_b128(o0, rw, ov + r * 64, O_KVG + os, SC1);
        __builtin_amdgcn_raw_buffer_store_b128(o1, rw, ov + r * 64, O_KVG + os + 16384, SC1);
      }
    }
    STAMP(1);
    tag = next_tag(ep);
    const int stg = b * 2 + (wv - 2);
    const bool xact = wv >= 2 && b < 128;
    u32 xx[5][8];
    u32* LN = (u32*)(smem + CS_OFF + 16384);
    if (tid == 0) *LN = 0;
      if (F_LATE) {
        const char* fs = (const char*)(ssmk + (size_t)d * S_);
        dma16x4p<32>(sbase + wv * CH_BYTES, l16, fs);
        dma16x4p<FC_BYTES + 28>(sbase + wv * CH_BYTES, l16, fs);
      }
    if (xact) {
      const char* xp = (const char*)xIn + stg * 8 * 1280;
      static_for<0, 40>([&](auto I) { constexpr int t = I.value / 5, c = I.value % 5; gld32p<c * 256 + (t & 1) * 1280>(xx[c][t], lane * 4, xp + (t & 6) * 1280); });
      wait_vm<40 + 8 * F_LATE>();
    } else {
      wait_vm<8 * F_LATE>();
    }
    asm volatile("s_waitcnt lgkmcnt(0)\n\ts_barrier" ::: "memory");
    if (tid < 32) st_flag(ws, O_FK + ((b & ~31) + tid) * 256 + (b & 31) * 8, tag);
    STAMP(2);
    u32 draw;
    sld32(draw, (const u32*)ssmD + (d >> 1));
    *(u32x2*)(smem + CKV_OFF + tid * 8) = u32x2{0, 0};
    if (lane < 15) *(u32*)(smem + wv * CH_BYTES + (lane >> 3) * FC_BYTES + (lane & 7) * 4) = 0;
    const int rho = row + 1;
    u32 sh = (rho & 1) * 16;
    unsigned aAddr = sbase + wv * CH_BYTES + ((rho >> 1) & 1) * FC_BYTES + 2 * (64 * kg + (rho & ~3));
    pin(sh); pin(aAddr);
    unsigned bA[8];
#pragma unroll
    for (int e = 0; e < 8; ++e) { const int g = 32 + 2 * row + 1 - 8 * kg - e; bA[e] = sbase + CKV_OFF + 16 * ((4 * g + wv) ^ ((g >> 2) & 7)); pin(bA[e]); }
    if (wv < 2) {
      const int po[1] = {O_FK + b * 256 + wv * 128 + (lane & 7) * 16};
      poll<1, 0>(rw, po, tag);
      STAMP(3);
      const unsigned ko0 = (lane ^ (lane >> 4)) * 16;
      dma16x4<CKV_OFF + 2048>(sbase + wv * 8192, ko0, ko0 ^ 64, ws + O_KVG + b * 16384 + wv * 8192);
      dma16x4<CKV_OFF + 2048 + 4096>(sbase + wv * 8192, ko0, ko0 ^ 64, ws + O_KVG + b * 16384 + wv * 8192 + 4096);
      wait_vm<0>();
    } else if (xact) {
      wait_vm<0>();
#pragma unroll
      for (int c = 0; c < 5; ++c)
#pragma unroll
        for (int t = 0; t < 8; ++t) pin(xx[c][t]);
#pragma unroll
      for (int c = 0; c < 5; ++c) {
        u32x4 p0, p1;
#pragma unroll
        for (int t = 0; t < 4; ++t) {
          p0[t] = __builtin_amdgcn_perm(xx[c][2 * t + 1], xx[c][2 * t], 0x05040100u);
          p1[t] = __builtin_amdgcn_perm(xx[c][2 * t + 1], xx[c][2 * t], 0x07060302u);
        }
        const int so = O_XG + (c * 16 * 2048 + stg * 4) * 16;
        __builtin_amdgcn_raw_buffer_store_b128(p0, rw, ov, so, SC1);
        __builtin_amdgcn_raw_buffer_store_b128(p1, rw, ov, so + 16384, SC1);
      }
      const bf16x2 ones = {(__bf16)1.0f, (__bf16)1.0f};
      float s1[8], s2[8];
#pragma unroll
      for (int t = 0; t < 8; ++t) {
        s1[t] = 0.f; s2[t] = 0.f;
#pragma unroll
        for (int c = 0; c < 5; ++c) {
          const bf16x2 v = __builtin_bit_cast(bf16x2, xx[c][t]);
          s1[t] = __builtin_amdgcn_fdot2_f32_bf16(v, ones, s1[t], false);
          s2[t] = __builtin_amdgcn_fdot2_f32_bf16(v, v, s2[t], false);
        }
      }
      float q1[2], q2[2];
#pragma unroll
      for (int e = 0; e < 2; ++e) {
        q1[e] = dpp_add<DPP_ROR8>(tred16(tred32(s1[e], s1[4 + e]), tred32(s1[2 + e], s1[6 + e])));
        q2[e] = dpp_add<DPP_ROR8>(tred16(tred32(s2[e], s2[4 + e]), tred32(s2[2 + e], s2[6 + e])));
      }
      const float mean = sum8((lane & 8) ? q1[1] : q1[0]) * (1.f / D_);
      const float var = fmaxf(sum8((lane & 8) ? q2[1] : q2[0]) * (1.f / D_) - mean * mean, 0.f);
      const float sd = rbf(__builtin_amdgcn_sqrtf(rbf(rbf(var) + 1e-5f)));
      const float rs = __builtin_amdgcn_rcpf(sd);
      const int s = stg * 8 + (lane >> 3);
      const int nt = s >> 8, col = (s >> 4) & 15, k4 = (s >> 2) & 3, rr = s & 3;
      if ((lane & 7) == 0) __builtin_amdgcn_raw_buffer_store_b64(u32x2{__float_as_uint(rs), __float_as_uint(-rbf(mean) * rs)}, rw,
                                                                 O_ST1 + (((nt * 2 + (rr >> 1)) * 64 + k4 * 16 + col) * 4 + (rr & 1) * 2) * 4, 0, SC1);
      wait_vm<0>();
      u32 old = 0;
      if (lane == 0) old = __hip_atomic_fetch_add(LN, 1u, __ATOMIC_RELAXED, __HIP_MEMORY_SCOPE_WORKGROUP);
      if (__builtin_amdgcn_readfirstlane(old) == 1 && lane < 8) st_flag(ws, O_FX + (lane * 20 + (b >> 3)) * 128 + (b & 7) * 8, tag);
    } else {
      wait_vm<0>();
    }
    asm volatile("s_waitcnt lgkmcnt(0)\n\ts_barrier" ::: "memory");
    STAMP(4);
    f32x4 acc[8];
    u32x2 ra[2][8][2]; u32x4 rb[2][8]; u32 carry[8]; u32x4 A[8];
    u32x4 pf[2]; u32 mraw;
    auto load_n = [&](auto E, auto N) {
      constexpr int e = E.value, n = N.value, q = n / 3, bf = e & 1;
      if constexpr (n % 3 == 0) ds_b64<512 * q + 16 * e>(ra[bf][q][0], aAddr);
      else if constexpr (n % 3 == 1) ds_b64<512 * q + 16 * e + 8>(ra[bf][q][1], aAddr);
      else ds_b128<2048 * q>(rb[bf][q], bA[e]);
    };
    auto build = [&](auto E, auto AA, auto I) {
      constexpr int e = E.value, a = AA.value, i = I.value, bf = e & 1;
      if constexpr (i == 0) A[a][0] = alignbit(ra[bf][a][0][1], ra[bf][a][0][0], sh);
      else if constexpr (i == 1) A[a][1] = alignbit(ra[bf][a][1][0], ra[bf][a][0][1], sh);
      else if constexpr (i == 2) A[a][2] = alignbit(ra[bf][a][1][1], ra[bf][a][1][0], sh);
      else { A[a][3] = alignbit(carry[a], ra[bf][a][1][1], sh); carry[a] = ra[bf][a][0][0]; }
    };
    const int pa1[2] = {O_FX + ((b & 7) * 20 + (lane >> 2)) * 128 + (lane & 3) * 16,
                        O_FA0 + ((b & 7) * 12 + (lane < 48 ? lane >> 2 : 0)) * 128 + (lane & 3) * 16};
    static_for<0, 8>([&](auto I) { ds_b32<512 * I.value + 128>(carry[I.value], aAddr); });
    static_for<0, 24>([&](auto N) { load_n(IC<7>{}, N); });
    wait_lgkm<0>();
    static_for<0, 4>([&](auto I) { build(IC<7>{}, IC<0>{}, I); });
    asm volatile("s_nop 1");
    static_for<0, 8>([&](auto EE) {
      constexpr int e = 7 - EE.value, bf = e & 1;
      const u32x4 A0 = A[0];
      static_for<0, 8>([&](auto AA) {
        constexpr int a = AA.value;
        static_for<0, 8 - a>([&](auto BB) {
          constexpr int bb = BB.value, n = conv_gap(a, bb);
          mfma_<(e == 7 && a == 0)>(acc[a + bb], a == 0 ? A0 : A[a], rb[bf][bb]);
          if constexpr (e > 0 && n < 24) load_n(IC<(e > 0 ? e - 1 : 0)>{}, IC<n>{});
          if constexpr (n < 28) build(IC<e>{}, IC<1 + n / 4>{}, IC<n % 4>{});
          if constexpr (e > 0 && n == 30) wait_lgkm<0>();
          if constexpr (e > 0 && (n == 31 || n == 32)) { build(IC<(e > 0 ? e - 1 : 0)>{}, IC<0>{}, IC<2 * (n - 31)>{}); build(IC<(e > 0 ? e - 1 : 0)>{}, IC<0>{}, IC<2 * (n - 31) + 1>{}); }
          if constexpr (e == PE + 1 && n == 28) gld128<0>(pf[0], pa1[0], ws);
          if constexpr (e == PE + 1 && n == 29) gld128<0>(pf[1], pa1[1], ws);
          if constexpr (e == PE && n == 33) {
            wait_vm<0>(); pin(pf[0]); pin(pf[1]);
            if (!__all(tag_ok(pf[0], tag) && tag_ok(pf[1], tag))) poll<2, 1>(rw, pa1, tag);
            dma16x4<CX_OFF>(sbase + wv * 4096, l16, l16, ws + O_XG + b * 16384 + wv * 4096);
          }
          if constexpr (e == PE - 1 && n == 26) dma16x4<CS_OFF>(sbase + wv * 4096, l16, l16, ws + O_ST1 + wv * 4096);
          if constexpr (e == PE - 1 && n == 34) gld32(mraw, (((lane & 7) < MODP ? (lane & 7) : 0) * NMOD + ((lane >> 3) & 3) * D_) * 4, ws + O_MODP + d * 4);
        });
      });
    });
    STAMP(5);
    asm volatile("s_waitcnt vmcnt(0)\n\ts_nop 7\n\ts_barrier" : "+v"(acc[0]), "+v"(acc[1]), "+v"(acc[2]), "+v"(acc[3]), "+v"(acc[4]), "+v"(acc[5]), "+v"(acc[6]), "+v"(acc[7]), "+v"(mraw), "+s"(draw) :: "memory");
    const float mv = rbf(sum8((lane & 7) < MODP ? __uint_as_float(mraw) : 0.f));
    const float m_ga = rdl(mv, 16), c0 = rdl(mv, 0) * m_ga, c1 = rbf(1.f + rdl(mv, 8)) * m_ga;
    const float Dv = (d & 1) ? bfhi(draw) : bflo(draw);
#pragma unroll
    for (int np = 0; np < 4; ++np) {
      u32x4 o;
#pragma unroll
      for (int u = 0; u < 2; ++u) {
        const int nt = 2 * np + u;
        const int tg = 32 * nt + 2 * row + (kg >> 1), g = 32 + tg;
        const u32x2 kvw = *(const u32x2*)(smem + CKV_OFF + 16 * ((4 * g + wv) ^ ((g >> 2) & 7)) + ((kg & 1) ? 0 : 8));
        const u32x2 xw = *(const u32x2*)(smem + CX_OFF + tg * 64 + wv * 16 + (kg & 1) * 8);
        const f32x4 sA = *(const f32x4*)(smem + CS_OFF + (nt * 2) * 1024 + l16);
        const f32x4 sB = *(const f32x4*)(smem + CS_OFF + (nt * 2 + 1) * 1024 + l16);
        const float kv4[4] = {bfhi(kvw[1]), bflo(kvw[1]), bfhi(kvw[0]), bflo(kvw[0])};
        const float x4[4] = {bflo(xw[0]), bfhi(xw[0]), bflo(xw[1]), bfhi(xw[1])};
        const float rs4[4] = {sA[0], sA[2], sB[0], sB[2]};
        const float nb4[4] = {sA[1], sA[3], sB[1], sB[3]};
        float r4[4];
#pragma unroll
        for (int r = 0; r < 4; ++r) {
          const float yb = fmaf(kv4[r], Dv, acc[nt][r]);
          const float n1 = fmaf(x4[r], rs4[r], nb4[r]);
          const float h = fmaf(n1, c1, c0);
          r4[r] = fmaf(h, yb, x4[r]);
        }
        o[2 * u] = pack_bf16(r4[0], r4[1]); o[2 * u + 1] = pack_bf16(r4[2], r4[3]);
      }
      __builtin_amdgcn_raw_buffer_store_b128(o, rw, l16 + np * 1024, O_X2T + d * (TP_ * 2), SC1);
    }
  }
  STAMP(6);
  drain_bar();
  if (tid < 8) st_flag(ws, O_F2 + (tid * 32 + (b >> 3)) * 128 + (b & 7) * 8, tag);
  STAMP(7);

  int bs = blockIdx.x;
  asm volatile("" : "+s"(bs));
  const int xcd = bs & 7, idx = bs >> 3;
  const int mt = (xcd & 1) * 8 + (idx & 7), nc = (xcd >> 1) * 4 + (idx >> 3);
  const int M = mt * 2 + ((nc >> 2) & 1), N = (nc >> 3) * 4 + (nc & 3);
  const int kg = lane >> 4;
  u32x4 WD[60];
  const char* wdp = ws + O_W2F + (N * 48 + wv * 12) * 5120;
  auto issue_wd = [&](auto I) { constexpr int i = I.value; gld128a<(i & 3) * 1024>(WD[i], l16, wdp + (i >> 2) * 4096); };
  {
    unsigned xoff[2];
#pragma unroll
    for (int p = 0; p < 2; ++p) {
      const int k = 4 * p + (lane >> 4);
      xoff[p] = (k * TP_ + (((lane & 15) ^ (2 * k)) * 8)) * 2;
    }
    const char* xsrc = ws + O_X2T + (8 * wv * TP_ + mt * 128) * 2;
    const char* wsrc = ws + O_W1F + (nc * 4 + wv) * W1_STEPS * 3072;
    const unsigned ldsw = sbase + wv * 2048;
    u32x4 W[G1_R][3];
    auto issue_w = [&](auto I) { constexpr int i = I.value; gld128<(i % 3) * 1024>(W[(i / 3) % G1_R][i % 3], l16, wsrc + (i / 3) * 3072); };
    auto issue = [&](auto O) {
      constexpr int kind = G1S.kind[O.value], i = G1S.idx[O.value];
      if constexpr (kind == 0) dma16<((i >> 1) % G1_SLOTS) * 8192 + (i & 1) * 1024>(ldsw, xoff[i & 1], xsrc + (size_t)(i >> 1) * 32 * TP_ * 2);
      else if constexpr (kind == 1) issue_w(IC<i>{});
      else issue_wd(IC<i>{});
    };
    unsigned xa[8];
#pragma unroll
    for (int jj = 0; jj < 8; ++jj) {
      const int rr = 4 * kg + ((lane & 15) >> 2);
      xa[jj] = sbase + rr * 256 + ((((jj + 2 * wv) & 7) ^ (rr & 7)) * 32) + (lane & 3) * 8;
      pin(xa[jj]);
    }
    u32x4 ones = {0x3f803f80u, 0x3f803f80u, 0x3f803f80u, 0x3f803f80u};
    pin(ones);
    {
      const int fb = O_F2 + (b & 7) * 32 * 128 + (lane & 3) * 16;
      const int pa[1] = {fb + (20 + (lane < 48 ? lane >> 2 : 0)) * 128};
      poll<1, 2>(rw, pa, tag);
      static_for<0, 3 * (G1_R - 1)>(issue_w);
      static_for<0, G1_WD0>(issue_wd);
      STAMP(8);
      if (wv == 0) {
        const int pc[2] = {fb + (lane >> 2) * 128, fb + (16 + ((lane >> 2) & 3)) * 128};
        poll<2, 1>(rw, pc, tag);
      }
      wait_vm<0>();
      bar();
    }
    STAMP(9);
    static_for<0, G1S.first[0]>(issue);
    f32x4 acc[8][3], gram[2], sum1[2];
    u32x2 X[G1_LA + 1][2];
    auto xread = [&](auto T, auto H) {
      constexpr int t = T.value, h = H.value;
      ds_tr<((t >> 3) % G1_SLOTS) * 8192 + h * 4096>(X[t % (G1_LA + 1)][h], xa[t & 7]);
    };
    wait_vm<G1S.first[0] - 2>();
    bar();
    STAMP(10);
    static_for<0, G1_LA>([&](auto T) { xread(T, IC<0>{}); xread(T, IC<1>{}); });
    static_for<0, 160>([&](auto T) {
      constexpr int t = T.value, ks = t >> 3, jj = t & 7, tn = t + G1_LA;
      wait_lgkm<2 * ((tn <= 160 ? tn : 160) - t - 1)>();
      if constexpr (tn < 160 && (tn & 7) == 0) {
        static_assert(g1_pos(0, 2 * (tn / 8) + 1) < G1S.first[t], "not issued yet");
        wait_vm<G1S.first[t] - 1 - g1_pos(0, 2 * (tn / 8) + 1)>();
        bar();
      }
      const u32x4 xb = cat(X[t % (G1_LA + 1)][0], X[t % (G1_LA + 1)][1]);
      mfma_<ks == 0>(acc[jj][0], W[ks % G1_R][0], xb);
      if constexpr (tn < 160) xread(IC<tn>{}, IC<0>{});
      mfma_<ks == 0>(acc[jj][1], W[ks % G1_R][1], xb);
      if constexpr (tn < 160) xread(IC<tn>{}, IC<1>{});
      mfma_<ks == 0>(acc[jj][2], W[ks % G1_R][2], xb);
      static_for<G1S.first[t], G1S.first[t + 1]>(issue);
      if constexpr (jj < 2) { mfma_<ks == 0>(gram[jj], xb, xb); mfma_<ks == 0>(sum1[jj], ones, xb); }
    });
    STAMP(11);
    wait_vm<G1S.n - 1 - g1_pos(1, 62)>();
    asm volatile("" : "+v"(gram[0]), "+v"(gram[1]), "+v"(sum1[0]), "+v"(sum1[1]) :: "memory");
    f32x2* stat = (f32x2*)(smem + G1_STAT);
#pragma unroll
    for (int sj = 0; sj < 2; ++sj) {
      const int c3 = lane & 3;
      const float q = c3 == 0 ? gram[sj][0] : c3 == 1 ? gram[sj][1] : c3 == 2 ? gram[sj][2] : gram[sj][3];
      const float mean = sum1[sj][0] * (1.f / D_);
      const float var = fmaxf(q * (1.f / D_) - mean * mean, 0.f);
      const float sd = rbf(__builtin_amdgcn_sqrtf(rbf(rbf(var) + 1e-5f)));
      const float rs = __builtin_amdgcn_rcpf(sd);
      if (((lane & 15) >> 2) == kg) stat[((sj + 2 * wv) & 7) * 16 + (lane & 15)] = f32x2{__uint_as_float(pack_bf16(-mean, sd)), rs * rs};
    }
    asm volatile("s_waitcnt lgkmcnt(0)\n\ts_barrier" ::: "memory");
    u32* stage = (u32*)smem;
    f32x2 st[8];
#pragma unroll
    for (int jj = 0; jj < 8; ++jj) st[jj] = stat[((jj + 2 * wv) & 7) * 16 + (lane & 15)];
    u32x4 xc[8];
#pragma unroll
    for (int jj = 0; jj < 8; ++jj) {
      const u32 p = __float_as_uint(st[jj][0]);
      xc[jj] = u32x4{__builtin_amdgcn_perm(p, p, 0x01000100u), p, __builtin_amdgcn_perm(p, p, 0x03020302u), 0u};
      pin(xc[jj]);
    }
    asm volatile("s_nop 1");
#pragma unroll
    for (int jj = 0; jj < 8; ++jj)
#pragma unroll
      for (int i = 0; i < 3; ++i) mfma(acc[jj][i], W[20 % G1_R][i], xc[jj]);
    asm volatile("s_nop 7");
#pragma unroll
    for (int jj = 0; jj < 8; ++jj) {
      const int m16 = (jj + 2 * wv) & 7;
#pragma unroll
      for (int i = 0; i < 3; ++i) {
        pin(acc[jj][i]);
        const float g0 = __int_as_float(max(__float_as_int(acc[jj][i][0]), 0)), g1 = __int_as_float(max(__float_as_int(acc[jj][i][2]), 0));
        const int cI = 3 * wv + i;
        stage[(m16 * 3 + (cI >> 2)) * 256 + ((cI & 3) * 16 + (lane & 15)) * 4 + kg] =
            pack_bf16(g0 * st[jj][1] * acc[jj][i][1], g1 * st[jj][1] * acc[jj][i][3]);
      }
    }
    asm volatile("s_waitcnt lgkmcnt(0)\n\ts_barrier" ::: "memory");
#pragma unroll
    for (int i = 0; i < 6; ++i) {
      const int m16 = wv * 2 + i / 3, f3 = i % 3;
      const int g16 = mt * 8 + m16;
      __builtin_amdgcn_raw_buffer_store_b128(*(const u32x4*)(stage + (m16 * 3 + f3) * 256 + lane * 4), rw, l16,
                                             O_H2F + (((g16 >> 2) * 48 + 3 * nc + f3) * 4 + (g16 & 3)) * 1024, SC1);
    }
  }
  STAMP(12);
  drain_bar();
  if (tid < 16) st_flag(ws, O_F3 + (mt * 16 + tid) * 128 + nc * 8, tag);
  static_for<G1_WDN, 60>(issue_wd);
  {
    const char* xs = ws + O_X2T + (N * 80 * TP_ + M * 64) * 2;
    const unsigned xo = ((lane >> 3) * TP_ + (lane & 7) * 8) * 2;
    dma16<XR_OFF>(sbase + wv * 1024, xo, xs + (size_t)wv * 8 * TP_ * 2);
    dma16<XR_OFF + 4096>(sbase + wv * 1024, xo, xs + (size_t)(wv + 4) * 8 * TP_ * 2);
    dma16<XR_OFF + 8192>(sbase + (wv & 1) * 1024, xo, xs + (size_t)((wv & 1) + 8) * 8 * TP_ * 2);
  }
  float* gbl = (float*)(smem + XR_OFF + 10240);
  if (tid < 80) {
    float g = 0.f;
#pragma unroll
    for (int p = 0; p < MODP; ++p) g += __uint_as_float(__builtin_amdgcn_raw_buffer_load_b32(rw, tid * 4, O_MODP + (p * NMOD + 5 * D_ + N * 80) * 4, SC1));
    gbl[tid] = rbf(g);
  }
  const char* hp = ws + O_H2F + (M * 48 + wv * 12) * 4096;
  unsigned hoff[4];
#pragma unroll
  for (int jj = 0; jj < 4; ++jj) { hoff[jj] = ((jj + wv + 1) & 3) * 1024 + l16; pin(hoff[jj]); }
  {
    const int po[1] = {O_F3 + (mt * 16 + nc) * 128 + wv * 32 + (lane & 1) * 16};
    poll<1, 0>(rw, po, tag);
    wait_vm<0>();
  }
  STAMP(13);
  u32x4 H[G2_R][4];
  auto issue_h = [&](auto O) { constexpr int o = O.value; gld128<0>(H[(o >> 2) % G2_R][o & 3], hoff[o & 3], hp + (o >> 2) * 4096); };
  static_for<0, 4 * G2_PRE>(issue_h);
  f32x4 acc[4][5];
  static_for<0, 12>([&](auto KS) {
    constexpr int ks = KS.value;
    constexpr int i0 = (4 * G2_PRE + 5 * ks < 48) ? 4 * G2_PRE + 5 * ks : 48;
    static_assert(i0 >= 4 * (ks + 1) && (i0 + 5 > 48 ? 48 : i0 + 5) <= 4 * (ks + G2_R), "ring");
    wait_vm<i0 - 4 * (ks + 1)>();
    static_for<0, 20>([&](auto G) {
      constexpr int g = G.value, jj = g / 5, i = g % 5;
      mfma_a<ks == 0>(acc[jj][i], WD[ks * 5 + i], H[ks % G2_R][jj]);
      if constexpr ((g & 3) == 0 && i0 + g / 4 < 48) issue_h(IC<i0 + g / 4>{});
    });
  });
  STAMP(14);
  asm volatile("s_nop 7");
  static_for<0, 15>([&](auto G) {
    constexpr int jj = G.value / 5, i = G.value % 5;
    ds_w128<(jj * 5 + i) * 1024>(sbase + ((jj + wv + 1) & 3) * 15360 + l16, acc[jj][i]);
  });
  asm volatile("s_waitcnt lgkmcnt(0)\n\ts_barrier" ::: "memory");
  const f32x4* part = (const f32x4*)(smem + wv * 15360);
  const unsigned xad = sbase + XR_OFF + (4 * kg + ((lane & 15) >> 2)) * 128 + wv * 32 + (lane & 3) * 8;
  u32x2 xw[5];
  static_for<0, 5>([&](auto I) { ds_tr<I.value * 2048>(xw[I.value], xad); });
  wait_lgkm<0>();
  const int pos = M * 64 + wv * 16 + (lane & 15);
  const int l8 = (pos >> 3) & 63;
  const int tok = 256 * (2 * (pos >> 9) + ((pos >> 2) & 1)) + 16 * (l8 & 15) + 4 * (l8 >> 4) + (pos & 3);
#pragma unroll
  for (int i = 0; i < 5; ++i) {
    pin(xw[i]); pin(acc[3][i]);
    f32x4 o = acc[3][i];
#pragma unroll
    for (int sl = 0; sl < 3; ++sl) o += part[(sl * 5 + i) * 64 + lane];
    const float x4[4] = {bflo(xw[i][0]), bfhi(xw[i][0]), bflo(xw[i][1]), bfhi(xw[i][1])};
    const f32x4 gb = *(const f32x4*)(gbl + i * 16 + 4 * kg);
    float r[4];
#pragma unroll
    for (int q = 0; q < 4; ++q) r[q] = fmaf(gb[q], o[q], x4[q]);
    *(u32x2*)(out + (size_t)tok * D_ + N * 80 + i * 16 + 4 * kg) = u32x2{pack_bf16(r[0], r[1]), pack_bf16(r[2], r[3])};
  }
  if (b == 0 && tid == 0) st_flag(ws, O_EPOCH, tag);
  STAMP_END();
}

extern "C" void kernel_launch(void* const* d_in, const int* in_sizes, int n_in, void* d_out, int out_size,
                              void* d_ws, size_t ws_size, hipStream_t stream) {
  k_fused<<<256, 256, 0, stream>>>((const u16*)d_in[0], (const u16*)d_in[1], (const u16*)d_in[2], (const u16*)d_in[3],
                                   (const u16*)d_in[4], (const u16*)d_in[5], (const u16*)d_in[7], (const u16*)d_in[8],
                                   (const u16*)d_in[9], (const u16*)d_in[10], (const u16*)d_in[11], (u16*)d_out, (char*)d_ws);
}
```

```cpp
#include <hip/hip_runtime.h>
#include <stdint.h>

typedef unsigned short u16;
typedef unsigned int u32;
typedef __attribute__((ext_vector_type(2))) __bf16 bf16x2;
typedef __attribute__((ext_vector_type(4))) float f32x4;
typedef __attribute__((ext_vector_type(2))) float f32x2;
typedef __attribute__((ext_vector_type(4))) u32 u32x4;
typedef __attribute__((ext_vector_type(2))) u32 u32x2;

constexpr int S_ = 2048, D_ = 640, F_ = 1536;
constexpr int TP_ = S_ + 64;
constexpr int NMOD = 6 * D_;
constexpr int MODP = 5;
constexpr int CWG = 160;

__device__ __forceinline__ float bflo(u32 w) { return __uint_as_float(w << 16); }
__device__ __forceinline__ float bfhi(u32 w) { return __uint_as_float(w & 0xffff0000u); }
__device__ __forceinline__ u32 pack_bf16(float lo, float hi) {
  bf16x2 v; v[0] = (__bf16)lo; v[1] = (__bf16)hi; return __builtin_bit_cast(u32, v);
}
__device__ __forceinline__ float rbf(float x) { __bf16 b = (__bf16)x; return (float)b; }
#define SCHED() __builtin_amdgcn_sched_barrier(0)
template <int V> struct IC { static constexpr int value = V; };
template <int B, int E, class Fn> __device__ __forceinline__ void static_for(Fn&& f) {
  if constexpr (B < E) { f(IC<B>{}); static_for<B + 1, E>(f); }
}
__device__ __forceinline__ void mfma(f32x4& c, u32x4 a, u32x4 b) {
  asm volatile("v_mfma_f32_16x16x32_bf16 %0, %1, %2, %0" : "+v"(c) : "v"(a), "v"(b));
}
__device__ __forceinline__ void mfma0(f32x4& c, u32x4 a, u32x4 b) {
  asm volatile("v_mfma_f32_16x16x32_bf16 %0, %1, %2, 0" : "=&v"(c) : "v"(a), "v"(b));
}
template <bool FIRST> __device__ __forceinline__ void mfma_(f32x4& c, u32x4 a, u32x4 b) { if constexpr (FIRST) mfma0(c, a, b); else mfma(c, a, b); }
__device__ __forceinline__ u32 alignbit(u32 hi, u32 lo, u32 sh) {
  u32 d; asm volatile("v_alignbit_b32 %0, %1, %2, %3" : "=v"(d) : "v"(hi), "v"(lo), "v"(sh)); return d;
}
template <int OFF> __device__ __forceinline__ void ds_tr(u32x2& dst, unsigned addr) {
  asm volatile("ds_read_b64_tr_b16 %0, %1 offset:%2" : "=v"(dst) : "v"(addr), "i"(OFF) : "memory");
}
template <int OFF> __device__ __forceinline__ void ds_b32(u32& dst, unsigned addr) {
  asm volatile("ds_read_b32 %0, %1 offset:%2" : "=v"(dst) : "v"(addr), "i"(OFF) : "memory");
}
template <int OFF> __device__ __forceinline__ void ds_b64(u32x2& dst, unsigned addr) {
  asm volatile("ds_read_b64 %0, %1 offset:%2" : "=v"(dst) : "v"(addr), "i"(OFF) : "memory");
}
template <int OFF> __device__ __forceinline__ void ds_b128(u32x4& dst, unsigned addr) {
  asm volatile("ds_read_b128 %0, %1 offset:%2" : "=v"(dst) : "v"(addr), "i"(OFF) : "memory");
}
template <int OFF> __device__ __forceinline__ void ds_w128(unsigned addr, f32x4 v) {
  asm volatile("ds_write_b128 %0, %1 offset:%2" :: "v"(addr), "v"(v), "i"(OFF) : "memory");
}
template <int OFF> __device__ __forceinline__ void gld128(u32x4& dst, unsigned voff, const char* sb) {
  asm volatile("global_load_dwordx4 %0, %1, %2 offset:%3 sc1" : "=v"(dst) : "v"(voff), "s"(sb), "i"(OFF) : "memory");
}
__device__ __forceinline__ void gld32(u32& dst, unsigned voff, const char* sb) {
  asm volatile("global_load_dword %0, %1, %2 sc1" : "=v"(dst) : "v"(voff), "s"(sb) : "memory");
}
template <int LOFF> __device__ __forceinline__ void dma16(unsigned ldsb, unsigned voff, const char* sb) {
  asm volatile("s_add_u32 m0, %0, %3\n\ts_nop 0\n\tglobal_load_lds_dwordx4 %1, %2 sc1" :: "s"(ldsb), "v"(voff), "s"(sb), "i"(LOFF) : "memory", "scc");
}
template <int LOFF> __device__ __forceinline__ void dma16x4(unsigned ldsb, unsigned v0, unsigned v1, const char* sb) {
  asm volatile("s_add_u32 m0, %0, %4\n\ts_nop 0\n\tglobal_load_lds_dwordx4 %1, %3 sc1\n\tglobal_load_lds_dwordx4 %2, %3 offset:1024 sc1\n\t"
               "global_load_lds_dwordx4 %1, %3 offset:2048 sc1\n\tglobal_load_lds_dwordx4 %2, %3 offset:3072 sc1"
               :: "s"(ldsb), "v"(v0), "v"(v1), "s"(sb), "i"(LOFF) : "memory", "scc");
}
__device__ __forceinline__ void sld32(u32& dst, const void* p) { asm volatile("s_load_dword %0, %1, 0x0" : "=s"(dst) : "s"(p) : "memory"); }
template <int N> __device__ __forceinline__ void wait_vm() { asm volatile("s_waitcnt vmcnt(%0)" :: "i"(N) : "memory"); }
template <int N> __device__ __forceinline__ void wait_lgkm() { asm volatile("s_waitcnt lgkmcnt(%0)" :: "i"(N) : "memory"); }
__device__ __forceinline__ void bar() { asm volatile("s_barrier" ::: "memory"); }
template <class T> __device__ __forceinline__ void pin(T& a) { asm volatile("" : "+v"(a)); }
__device__ __forceinline__ u32x4 cat(u32x2 a, u32x2 b) { return u32x4{a[0], a[1], b[0], b[1]}; }

typedef __amdgpu_buffer_rsrc_t rsrc_t;
__device__ __forceinline__ rsrc_t mk_rsrc(const void* p, int bytes) { return __builtin_amdgcn_make_buffer_rsrc((void*)p, 0, bytes, 0x00020000); }
template <int CTRL> __device__ __forceinline__ float dpp_add(float x) {
  return x + __builtin_bit_cast(float, __builtin_amdgcn_mov_dpp(__builtin_bit_cast(int, x), CTRL, 0xf, 0xf, true));
}
constexpr int DPP_XOR1 = 0xB1, DPP_XOR2 = 0x4E, DPP_HMIRROR = 0x141, DPP_ROR8 = 0x128;
__device__ __forceinline__ float sum8(float x) { return dpp_add<DPP_HMIRROR>(dpp_add<DPP_XOR2>(dpp_add<DPP_XOR1>(x))); }
__device__ __forceinline__ float tred32(float a, float b) {
  auto r = __builtin_amdgcn_permlane32_swap(__float_as_uint(a), __float_as_uint(b), false, false);
  return __uint_as_float(r[0]) + __uint_as_float(r[1]);
}
__device__ __forceinline__ float tred16(float a, float b) {
  auto r = __builtin_amdgcn_permlane16_swap(__float_as_uint(a), __float_as_uint(b), false, false);
  return __uint_as_float(r[0]) + __uint_as_float(r[1]);
}
__device__ __forceinline__ float rdl(float x, int l) { return __int_as_float(__builtin_amdgcn_readlane(__float_as_int(x), l)); }

typedef unsigned long long u64;
#define GAS __attribute__((address_space(1)))
constexpr int SC1 = 16;
#define A0_DELAY 16
#define F_LATE 1
#define A1_DELAY 4
#define A3_DELAY 24
#define AU_DELAY 16
#define A_DELAY 2
#define FXE 7
#define PE 4
#define NT 2
#define NTS " nt"
template <int OFF> __device__ __forceinline__ void gld128a(u32x4& dst, unsigned voff, const char* sb) {
  asm volatile("global_load_dwordx4 %0, %1, %2 offset:%3 sc1" : "=a"(dst) : "v"(voff), "s"(sb), "i"(OFF) : "memory");
}
template <bool FIRST> __device__ __forceinline__ void mfma_a(f32x4& c, u32x4 a, u32x4 b) {
  if constexpr (FIRST) asm volatile("v_mfma_f32_16x16x32_bf16 %0, %1, %2, 0" : "=&v"(c) : "a"(a), "v"(b));
  else asm volatile("v_mfma_f32_16x16x32_bf16 %0, %1, %2, %0" : "+v"(c) : "a"(a), "v"(b));
}
typedef __attribute__((ext_vector_type(8))) __bf16 bf16x8;
__device__ __forceinline__ f32x4 mfma_b(u32x4 a, u32x4 b, f32x4 c) {
  return __builtin_amdgcn_mfma_f32_16x16x32_bf16(__builtin_bit_cast(bf16x8, a), __builtin_bit_cast(bf16x8, b), c, 0, 0, 0);
}
template <int LOFF> __device__ __forceinline__ void dma16p(unsigned ldsb, unsigned voff, const char* sb) {
  asm volatile("s_add_u32 m0, %0, %3\n\ts_nop 0\n\tglobal_load_lds_dwordx4 %1, %2" NTS :: "s"(ldsb), "v"(voff), "s"(sb), "i"(LOFF) : "memory", "scc");
}
template <int LOFF> __device__ __forceinline__ void dma16x4p(unsigned ldsb, unsigned v0, const char* sb) {
  asm volatile("s_add_u32 m0, %0, %3\n\ts_nop 0\n\tglobal_load_lds_dwordx4 %1, %2" NTS "\n\tglobal_load_lds_dwordx4 %1, %2 offset:1024" NTS "\n\t"
               "global_load_lds_dwordx4 %1, %2 offset:2048" NTS "\n\tglobal_load_lds_dwordx4 %1, %2 offset:3072" NTS
               :: "s"(ldsb), "v"(v0), "s"(sb), "i"(LOFF) : "memory", "scc");
}

#undef DBGC
#define STAMP(I)
#define STAMP_DECL()
#define STAMP_END()

__device__ __forceinline__ void gld128p(u32x4& dst, unsigned voff, const char* sb) {
  asm volatile("global_load_dwordx4 %0, %1, %2" NTS : "=v"(dst) : "v"(voff), "s"(sb) : "memory");
}
template <int OFF> __device__ __forceinline__ void gld32p(u32& dst, unsigned voff, const char* sb) {
  asm volatile("global_load_dword %0, %1, %2 offset:%3" NTS : "=v"(dst) : "v"(voff), "s"(sb), "i"(OFF) : "memory");
}
template <int OFF> __device__ __forceinline__ void gld16p(u32& dst, unsigned voff, const char* sb) {
  asm volatile("global_load_ushort %0, %1, %2 offset:%3" : "=v"(dst) : "v"(voff), "s"(sb), "i"(OFF) : "memory");
}
#define CPF_DONE() asm volatile("" :: "v"(cpf))
#define X_EARLY 0
template <int OFF> __device__ __forceinline__ void dma16n(unsigned voff, const char* sb) {
  asm volatile("global_load_lds_dwordx4 %0, %1 offset:%2 sc1" :: "v"(voff), "s"(sb), "i"(OFF) : "memory");
}
__device__ __forceinline__ u64 v_ne64(u32x2 v, u64 tag) { u64 m; asm volatile("v_cmp_ne_u64 %0, %1, %2" : "=s"(m) : "v"(v), "s"(tag)); return m; }
__device__ __forceinline__ float v_lo(u32 w) { float d; asm volatile("v_lshlrev_b32 %0, 16, %1" : "=v"(d) : "v"(w)); return d; }
__device__ __forceinline__ float v_hi(u32 w) { float d; asm volatile("v_and_b32 %0, 0xffff0000, %1" : "=v"(d) : "v"(w)); return d; }
__device__ __forceinline__ float v_fma(float a, float b, float c) { float d; asm volatile("v_fma_f32 %0, %1, %2, %3" : "=v"(d) : "v"(a), "v"(b), "v"(c)); return d; }
__device__ __forceinline__ float v_mul(float a, float b) { float d; asm volatile("v_mul_f32 %0, %1, %2" : "=v"(d) : "v"(a), "v"(b)); return d; }
__device__ __forceinline__ float v_add(float a, float b) { float d; asm volatile("v_add_f32 %0, %1, %2" : "=v"(d) : "v"(a), "v"(b)); return d; }
#define STAMPD(I)
#define STAMPL(I) STAMP(I)
__device__ __forceinline__ float v_relu(float a) { float d; asm volatile("v_max_i32 %0, 0, %1" : "=v"(d) : "v"(a)); return d; }
__device__ __forceinline__ u32 v_cvt(float a, float b) { u32 d; asm volatile("v_cvt_pk_bf16_f32 %0, %1, %2" : "=v"(d) : "v"(a), "v"(b)); return d; }
__device__ __forceinline__ u32 v_perm(u32 a, u32 sel) { u32 d; asm volatile("v_perm_b32 %0, %1, %1, %2" : "=v"(d) : "v"(a), "s"(sel)); return d; }
__device__ __forceinline__ void v_swap32(u32& a, u32& b) { asm volatile("v_permlane32_swap_b32 %0, %1" : "+v"(a), "+v"(b)); }
__device__ __forceinline__ void v_swap16(u32& a, u32& b) { asm volatile("v_permlane16_swap_b32 %0, %1" : "+v"(a), "+v"(b)); }
template <int OFF> __device__ __forceinline__ void ds_f128(f32x4& dst, unsigned addr) {
  asm volatile("ds_read_b128 %0, %1 offset:%2" : "=v"(dst) : "v"(addr), "i"(OFF) : "memory");
}
#define STAMPG(I)
#define STAMPE(I) STAMP(I)
__device__ __forceinline__ void asm_and3(u32& d, u32 a, u32 b, u32 c) { asm volatile("v_bitop3_b32 %0, %1, %2, %3 bitop3:0x80" : "=v"(d) : "v"(a), "v"(b), "v"(c)); }
__device__ __forceinline__ void asm_ne1(unsigned long long& m, u32 a) { asm volatile("v_cmp_ne_u32 %0, 1, %1" : "=s"(m) : "v"(a)); }
__device__ __forceinline__ void ds_w32(unsigned addr, u32 v) { asm volatile("ds_write_b32 %0, %1" :: "v"(addr), "v"(v) : "memory"); }
template <int OP> __device__ __forceinline__ void asm_op1(u32& d, u32 a) {
  if constexpr (OP == 0) asm volatile("v_mul_f32 %0, 0x3acccccd, %1" : "=v"(d) : "v"(a));
  else if constexpr (OP == 1) asm volatile("v_mov_b32 %0, %1" : "=v"(d) : "v"(a));
  else if constexpr (OP == 2) asm volatile("v_max_f32 %0, 0, %1" : "=v"(d) : "v"(a));
  else if constexpr (OP == 3) asm volatile("v_cvt_pk_bf16_f32 %0, 0, %1" : "=v"(d) : "v"(a));
  else if constexpr (OP == 4) asm volatile("v_add_f32 %0, 0x3727c5ac, %1" : "=v"(d) : "v"(a));
  else asm volatile("v_sqrt_f32 %0, %1" : "=v"(d) : "v"(a));
}
template <int OP> __device__ __forceinline__ void asm_op2(u32& d, u32 a, u32 b) {
  if constexpr (OP == 0) asm volatile("v_add_f32 %0, %1, %2" : "=v"(d) : "v"(a), "v"(b));
  else if constexpr (OP == 1) asm volatile("v_fma_f32 %0, -%1, %1, %2" : "=v"(d) : "v"(a), "v"(b));
  else asm volatile("v_cvt_pk_bf16_f32 %0, -%1, %2" : "=v"(d) : "v"(a), "v"(b));
}
constexpr int W1_STEPS = 21;
constexpr int O_EPOCH = 0;
constexpr int O_FK = 4096;
constexpr int O_FA0 = O_FK + 160 * 256;
constexpr int O_FW = O_FA0 + 8 * 12 * 128;
constexpr int O_FU = O_FW + 8 * 12 * 128;
constexpr int O_FX = O_FU + 8 * 14 * 128;
constexpr int O_F2 = O_FX + 8 * 32 * 128;
constexpr int O_F3 = O_F2 + 8 * 32 * 128;
constexpr int O_SG = O_F3 + 256 * 128;
constexpr int O_MODP = O_SG + 32 * 8192;
constexpr int O_ST1 = O_MODP + MODP * NMOD * 4;
constexpr int O_KVG = O_ST1 + 16384;
constexpr int O_XG = O_KVG + S_ * D_ * 2;
constexpr int O_X2T = O_XG + S_ * D_ * 2;
constexpr int O_W1F = O_X2T + TP_ * D_ * 2;
constexpr int O_W2F = O_W1F + 64 * W1_STEPS * 3 * 1024;
constexpr int O_H2F = O_W2F + 8 * 48 * 5 * 1024;
constexpr int WS_BYTES = O_H2F + S_ * F_ * 2;
static_assert(O_MODP % 256 == 0 && O_ST1 % 256 == 0 && O_KVG % 256 == 0 && O_X2T % 256 == 0 && O_W1F % 256 == 0, "alignment");

constexpr int SPIN_CAP = 1 << 16;
constexpr unsigned CODE_BYTES = 38 * 1024;
__device__ __forceinline__ void st_flag(char* ws, int off, u64 tag) {
  __hip_atomic_store((GAS u64*)(ws + off), tag, __ATOMIC_RELAXED, __HIP_MEMORY_SCOPE_AGENT);
}
__device__ __forceinline__ bool tag_ok(u32x4 v, u64 tag) {
  const u32 lo = (u32)tag, hi = (u32)(tag >> 32);
  return ((v[0] ^ lo) | (v[1] ^ hi) | (v[2] ^ lo) | (v[3] ^ hi)) == 0;
}
template <int NL, int SLEEP> __device__ __forceinline__ void poll(rsrc_t rw, const int (&off)[NL], u64 tag) {
#pragma unroll 1
  for (int sp = 0; sp < SPIN_CAP; ++sp) {
    bool ok = true;
#pragma unroll
    for (int i = 0; i < NL; ++i) ok &= tag_ok(__builtin_amdgcn_raw_buffer_load_b128(rw, off[i], 0, SC1), tag);
    if (__all(ok)) break;
    if (SLEEP) __builtin_amdgcn_s_sleep(SLEEP);
    asm volatile("" ::: "memory");
  }
  asm volatile("" ::: "memory");
}
__device__ __forceinline__ u64 next_tag(u32x2 ep) {
  u32 lo, hi;
  asm volatile("s_waitcnt lgkmcnt(0)\n\ts_add_u32 %0, %2, 1\n\ts_addc_u32 %1, %3, 0" : "=&s"(lo), "=&s"(hi) : "s"(ep[0]), "s"(ep[1]) : "memory", "scc");
  return ((u64)hi << 32) | lo;
}
__device__ __forceinline__ void drain_bar() { asm volatile("s_waitcnt vmcnt(0) lgkmcnt(0)\n\ts_barrier" ::: "memory"); }

constexpr int FC_BYTES = 2064 * 2;
constexpr int CH_BYTES = 4 * FC_BYTES;
constexpr int CKV_OFF = 66048;
constexpr int CX_OFF = CKV_OFF + 2048 + 16384, CS_OFF = CX_OFF + 16384;
constexpr int conv_gap(int a, int b) { int n = 0; for (int i = 0; i < a; ++i) n += 8 - i; return n + b; }
struct CSched { int rd[290]; int op[290]; };
constexpr int CBE = PE - 2;
constexpr CSched conv_make() {
  CSched S{};
  int rd = 0, op = 0, landed = 0;
  for (int g = 0; g < 288; ++g) {
    const int e = 7 - g / 36, n = g % 36;
    S.rd[g] = rd; S.op[g] = op;
    const bool open = e < CBE || (e == CBE && n >= 24);
    bool ds = e > 0 && n < 24;
    if (open && !ds && rd < 24 && (e == 0 || n < 33)) { ++rd; ds = true; }
    if (e > 0 ? n == 34 : n == 12) landed = rd;
    int k = ds ? 1 : 2;
    while (k-- > 0 && op < (landed / 3) * 16) ++op;
  }
  S.rd[288] = rd; S.op[288] = op;
  return S;
}
constexpr CSched CS = conv_make();
static_assert(CS.rd[288 - 36 + 10] == 24, "reads must be out before the last wait");
constexpr int G_XCHK = 36 * (7 - PE) + 24;
struct CVSched { int kind[290]; int idx[290]; };
constexpr CVSched cv_make() {
  CVSched S{};
  for (int g = 0; g < 288; g += 4) {
    const int dx = g - (G_XCHK + 4);
    if (g >= G_XCHK - 44 && g <= G_XCHK - 36) { S.kind[g] = 2; S.idx[g] = (g - (G_XCHK - 44)) / 4; }
    else if (dx >= 0 && dx < 32) { S.kind[g] = 3 + dx / 16; S.idx[g] = (dx / 4) & 3; }
    else if (dx == 32) S.kind[g] = 5;
  }
  return S;
}
constexpr CVSched CV = cv_make();
constexpr int cv_younger(int g, int kind, int idx) {
  int n = -1;
  for (int i = 0; i <= g; ++i) { if (n >= 0 && CV.kind[i]) ++n; if (CV.kind[i] == kind && CV.idx[i] == idx) n = 0; }
  return n;
}
static_assert(G_XCHK % 4 == 0, "conv request schedule");
constexpr int G1_R = 7;
constexpr int G1_SLOTS = 8;
constexpr int G1_LA = 4;
constexpr int G1_SP = G1_SLOTS * 8192;
constexpr int G1_WPRE = 5;
constexpr int g1_ks(int t) { return t < 128 ? t >> 3 : 16 + (t & 3); }
constexpr int g1_j(int t) { return t < 128 ? t & 7 : (t - 128) >> 2; }
struct G1Sched { int kind[256]; int idx[256]; int first[162]; int n; };
#define T_SPOLL 85
#define XPRE 4
constexpr int T_SST = 37;
#define WA 4
#define WD_G1 48
#define WD_TEND 148
constexpr int T_WPOLL = 5, T_WCHK = 29;
constexpr G1Sched g1_make() {
  G1Sched S{};
  int n = 0, x = 0, w = 3 * (G1_WPRE + WA), wd = 0, rs = 0, credit = 0;
  for (; x < 2 * XPRE; ++x) { S.kind[n] = 0; S.idx[n] = x; ++n; }
  for (int t = 0; t < 160; ++t) {
    S.first[t] = n;
    const int ks = t < 128 ? t >> 3 : 16;
    credit += 3;
    if (credit > 6) credit = 6;
    int kind = -1, idx = 0;
    const bool xok = x < 40 && (x >> 1) <= ks + ((t & 7) >= 4 ? 7 : 6), wok = w < 60 && w / 3 <= ks + G1_R - 1;
    const int alive = 3 + 3 * (ks < G1_WPRE ? WA : (ks < G1_WPRE + WA ? G1_WPRE + WA - ks : 0));
    if (t == T_WPOLL) kind = 4;
    else if (t == T_SST) kind = 5;
    else if (t == T_SPOLL || t == T_SPOLL + 1) { kind = 6; idx = t - T_SPOLL; }
    else if (t >= 135 && (t & 3) == 3) { kind = 7; idx = (t - 135) >> 2; }
    else if (credit >= 4) {
      if (xok && (x >> 1) <= ks + 5 + (ks > 10) * 2 && (!wok || (x >> 1) < w / 3)) { kind = 0; idx = x++; }
      else if (wok) { kind = 1; idx = w++; }
      else if (rs < 3) { kind = 3; idx = rs++; }
      else if (t > T_WCHK && t < WD_TEND && wd < WD_G1 && wd + alive < 63) { kind = 2; idx = wd++; }
      else if (xok) { kind = 0; idx = x++; }
    }
    if (kind >= 0) { S.kind[n] = kind; S.idx[n] = idx; ++n; credit -= 4; }
  }
  S.first[160] = n; S.n = n;
  return S;
}
constexpr G1Sched G1S = g1_make();
constexpr int g1_pos(int kind, int idx) { for (int i = 0; i < G1S.n; ++i) if (G1S.kind[i] == kind && G1S.idx[i] == idx) return i; return -1; }
constexpr int g1_wd_count() { int c = 0; for (int i = 0; i < G1S.n; ++i) c += G1S.kind[i] == 2; return c; }
constexpr int G1_WDN = g1_wd_count();
constexpr int imax(int a, int b) { return a > b ? a : b; }
constexpr int g1_need(int t) {
  const int s = t == 124 ? 19 : (t + 4) / 8;
  int p = imax(g1_pos(0, 2 * s + 1), g1_pos(1, 3 * s + 2));
  if (t == 108) p = imax(p, g1_pos(6, 1));
  if (t == T_WCHK - 1) p = imax(p, g1_pos(4, 0));
  return p;
}
static_assert(g1_pos(1, 59) >= 0 && g1_pos(0, 39) >= 0 && g1_pos(7, 6) >= 0, "schedule incomplete");
constexpr int G2_R = 28, G2_PRE = 16;
constexpr int XR_OFF = 69632;
constexpr int LDS_BYTES = 118 * 1024;
constexpr int AG_OFF = 0, AU_OFF = 20480, AD_OFF = 40960, AM_OFF = 61440;

__global__ __launch_bounds__(256, 1) void k_fused(const u16* __restrict__ xIn, const u16* __restrict__ kIn, const u16* __restrict__ vIn,
                                                  const u16* __restrict__ shk, const u16* __restrict__ ssmk, const u16* __restrict__ ssmD,
                                                  const u16* __restrict__ wg, const u16* __restrict__ wu, const u16* __restrict__ wd,
                                                  const u16* __restrict__ cIn, const u16* __restrict__ wmod,
                                                  u16* __restrict__ out, char* __restrict__ ws) {
  __shared__ __attribute__((aligned(16))) char smem[LDS_BYTES];
  STAMP_DECL();
  STAMP(0);
  u32 cpf = 0;
  if (blockIdx.x >= 240) {
    uint64_t pc; asm volatile("s_getpc_b64 %0" : "=s"(pc));
    const unsigned co = (((blockIdx.x >> 3) & 1) * 256 + threadIdx.x) * 128;
    asm volatile("global_load_dword %0, %1, %2" : "=v"(cpf) : "v"(co < CODE_BYTES ? co : 0u), "s"(pc & ~(uint64_t)127) : "memory");
  }
  asm volatile("" :: "s"(xIn), "s"(kIn), "s"(vIn), "s"(shk), "s"(ssmk), "s"(ssmD), "s"(wg), "s"(wu), "s"(wd), "s"(cIn), "s"(wmod), "s"(out), "s"(ws));
  u32x2 ep;
  asm volatile("s_load_dwordx2 %0, %1, 0x0" : "=s"(ep) : "s"(ws) : "memory");
  u64 tag;
  const int tid = threadIdx.x, lane = tid & 63;
  const int wv = __builtin_amdgcn_readfirstlane(tid >> 6);
  const int b = blockIdx.x;
  const unsigned sbase = (unsigned)(size_t)smem;
  const unsigned l16 = lane * 16;
  const rsrc_t rw = mk_rsrc(ws, WS_BYTES);
  int bs = blockIdx.x;
  asm volatile("" : "+s"(bs));
  const int xcd = bs & 7, idx = bs >> 3;
  const int mt = (xcd & 1) * 8 + (idx & 7), nc = (xcd >> 1) * 4 + (idx >> 3);
  const int M = mt * 2 + ((nc >> 2) & 1), N = (nc >> 3) * 4 + (nc & 3);
  u32x4 WD[60];
  const char* wdp = ws + O_W2F + (N * 48 + wv * 12) * 5120;
  auto issue_wd = [&](auto I) { constexpr int i = I.value; gld128a<(i & 3) * 1024>(WD[i], l16, wdp + (i >> 2) * 4096); };
  if (b >= CWG) {
    const int j = b - CWG;
    const int r8 = lane >> 3, cp = lane & 7;
    const int ks2 = j >> 1, hf = j & 1;
    const int ug = j % 24, kq = j / 24;
    float* LC = (float*)(smem + AM_OFF);
    u16* LS = (u16*)(smem + AM_OFF + 640);
    const int gt = wv * 96 + j;
    const bool gact = gt < 300, urgent = gt < 100;
    const int gq = gt / MODP, gk = gt - gq * MODP, gcol = urgent ? 30 + gq : (gq < 50 ? gq - 20 : gq);
    u32x4 wm[16]; u32 cv[2];
    if (!urgent) __builtin_amdgcn_s_sleep(A0_DELAY);
    if (gact) {
      static_for<0, 2>([&](auto I) { gld16p<I.value * 128>(cv[I.value], lane * 2, (const char*)(cIn + gk * 128)); });
      static_for<0, 16>([&](auto I) { gld128p(wm[I.value], r8 * (NMOD * 2) + cp * 16, (const char*)(wmod + (size_t)(gk * 128 + I.value * 8) * NMOD + gcol * 64)); });
    }
    tag = next_tag(ep);
    if (gact) {
      wait_vm<16>();
      float sl[2], sv[16];
#pragma unroll
      for (int i = 0; i < 2; ++i) { pin(cv[i]); const float x = bflo(cv[i]); sl[i] = rbf(x * __builtin_amdgcn_rcpf(1.f + __expf(-x))); }
#pragma unroll
      for (int it = 0; it < 16; ++it) { sv[it] = __shfl(sl[it >> 3], (it & 7) * 8 + r8); pin(sv[it]); }
      f32x2 acc[4];
      static_for<0, 4>([&](auto G) {
        constexpr int g = G.value;
        wait_vm<12 - 4 * g>();
#pragma unroll
        for (int it = 4 * g; it < 4 * g + 4; ++it) {
          pin(wm[it]);
          const f32x2 s2 = {sv[it], sv[it]};
#pragma unroll
          for (int q = 0; q < 4; ++q) {
            const f32x2 w2 = {bflo(wm[it][q]), bfhi(wm[it][q])};
            acc[q] = it ? __builtin_elementwise_fma(s2, w2, acc[q]) : s2 * w2;
          }
        }
#pragma unroll
        for (int q = 0; q < 4; ++q) pin(acc[q]);
      });
      const float a0 = tred16(tred32(acc[0][0], acc[2][0]), tred32(acc[1][0], acc[3][0]));
      const float a1 = tred16(tred32(acc[0][1], acc[2][1]), tred32(acc[1][1], acc[3][1]));
      const float e0 = dpp_add<DPP_ROR8>(a0), e1 = dpp_add<DPP_ROR8>(a1);
      __builtin_amdgcn_raw_buffer_store_b32(__float_as_uint((lane & 8) ? e1 : e0), rw, O_MODP + (gk * NMOD + gcol * 64 + cp * 8 + r8) * 4, 0, SC1);
      if (urgent) {
        wait_vm<0>();
        if (lane < 8) st_flag(ws, O_FU + (lane * 14 + (gt >> 3)) * 128 + (gt & 7) * 8, tag);
        __builtin_amdgcn_s_sleep(AU_DELAY);
      }
    }
    STAMPE(1);
    {
      const unsigned vg = r8 * (F_ * 2) + ((cp ^ ((r8 >> 1) * 2)) * 16);
      const char* gs = (const char*)(wg + (size_t)(kq * 160 + wv * 40) * F_ + ug * 64);
      const char* us = (const char*)(wu + (size_t)(kq * 160 + wv * 40) * F_ + ug * 64);
      static_for<0, 5>([&](auto I) {
        dma16p<AG_OFF + I.value * 1024>(sbase + wv * 5120, vg, gs + I.value * (8 * F_ * 2));
        dma16p<AU_OFF + I.value * 1024>(sbase + wv * 5120, vg ^ 16, us + I.value * (8 * F_ * 2));
      });
    }
    wait_vm<10>();
    bar();
    if (tid < 8) st_flag(ws, O_FA0 + (tid * 12 + (j >> 3)) * 128 + (j & 7) * 8, tag);
    STAMPE(2);
    const int kg = lane >> 4, q4 = (lane & 15) >> 2, p4 = lane & 3;
    if (wv == 0) {
      const int po[1] = {O_FU + ((b & 7) * 14 + (lane < 50 ? lane >> 2 : 0)) * 128 + (lane & 3) * 16};
      poll<1, 1>(rw, po, tag);
      STAMPE(3);
      f32x4 mm[2];
      const int lo = lane < 40 ? lane * 16 : 0;
#pragma unroll
      for (int i = 0; i < 2; ++i) {
        mm[i] = f32x4{0.f, 0.f, 0.f, 0.f};
#pragma unroll
        for (int p = 0; p < MODP; ++p)
          mm[i] += __builtin_bit_cast(f32x4, __builtin_amdgcn_raw_buffer_load_b128(rw, lo, O_MODP + (p * NMOD + (3 + i) * D_ + kq * 160) * 4, SC1));
      }
      if (lane < 40) {
        *(u32x2*)(LS + lane * 4) = u32x2{pack_bf16(mm[0][0], mm[0][1]), pack_bf16(mm[0][2], mm[0][3])};
        *(f32x4*)(LC + lane * 4) = f32x4{rbf(1.f + rbf(mm[1][0])), rbf(1.f + rbf(mm[1][1])), rbf(1.f + rbf(mm[1][2])), rbf(1.f + rbf(mm[1][3]))};
      }
    }
    drain_bar();
    STAMPE(4);
    CPF_DONE();
    if (wv != 0) {
      const char* ds = (const char*)(wd + (size_t)(ks2 * 32) * D_ + hf * 320);
      const unsigned vd = r8 * (D_ * 2) + cp * 16;
      static_for<0, 5>([&](auto I) { dma16p<AD_OFF + I.value * 4096>(sbase + wv * 1024, vd, ds + wv * (8 * D_ * 2) + I.value * 128); });
      dma16p<AD_OFF>(sbase + (wv - 1) * 4096, vd, ds + (wv - 1) * 128);
      if (wv < 3) dma16p<AD_OFF>(sbase + (wv + 2) * 4096, vd, ds + (wv + 2) * 128);
    }
    u32x4 raw[10];
    {
      const int rr = 4 * kg + q4;
      u32x2 lo[10], hi[10];
      unsigned ad[2];
#pragma unroll
      for (int t = 0; t < 2; ++t) ad[t] = sbase + (p4 >> 1) * AU_OFF + rr * 128 + (((wv * 2 + t) ^ (((rr >> 1) & 3) * 2 + (p4 >> 1))) * 16) + (p4 & 1) * 8;
      static_for<0, 10>([&](auto I) { constexpr int i = I.value; ds_tr<(i >> 1) * 4096>(lo[i], ad[i & 1]); ds_tr<(i >> 1) * 4096 + 2048>(hi[i], ad[i & 1]); });
      wait_lgkm<0>();
      const int src = ((lane & 48) + ((lane & 1) * 8 + ((lane & 15) >> 1))) * 4;
#pragma unroll
      for (int i = 0; i < 10; ++i) {
        pin(lo[i]); pin(hi[i]);
        raw[i] = u32x4{(u32)__builtin_amdgcn_ds_bpermute(src, lo[i][0]), (u32)__builtin_amdgcn_ds_bpermute(src, lo[i][1]),
                       (u32)__builtin_amdgcn_ds_bpermute(src, hi[i][0]), (u32)__builtin_amdgcn_ds_bpermute(src, hi[i][1])};
      }
#pragma unroll
      for (int i = 0; i < 10; ++i) pin(raw[i]);
    }
    {
      u32x2 sbl[5], sbh[5]; u32x4 cl[5], ch[5];
      const unsigned ac = sbase + AM_OFF + kg * 16, as = sbase + AM_OFF + 640 + kg * 8;
      static_for<0, 5>([&](auto I) { constexpr int i = I.value; ds_b128<i * 128>(cl[i], ac); ds_b128<i * 128 + 64>(ch[i], ac); ds_b64<i * 64>(sbl[i], as); ds_b64<i * 64 + 32>(sbh[i], as); });
      wait_lgkm<0>();
#pragma unroll
      for (int i = 0; i < 5; ++i) { pin(cl[i]); pin(ch[i]); pin(sbl[i]); pin(sbh[i]); }
      const u32x4 ones = {0x3f803f80u, 0x3f803f80u, 0x3f803f80u, 0x3f803f80u};
      f32x4 aU[2] = {}, aW[2] = {};
#pragma unroll
      for (int i = 0; i < 10; ++i) {
        const int ks = i >> 1, t = i & 1;
        u32x4 sc;
#pragma unroll
        for (int q = 0; q < 2; ++q) {
          sc[q] = pack_bf16(bflo(raw[i][q]) * __uint_as_float(cl[ks][2 * q]), bfhi(raw[i][q]) * __uint_as_float(cl[ks][2 * q + 1]));
          sc[2 + q] = pack_bf16(bflo(raw[i][2 + q]) * __uint_as_float(ch[ks][2 * q]), bfhi(raw[i][2 + q]) * __uint_as_float(ch[ks][2 * q + 1]));
        }
        aW[t] = mfma_b(raw[i], cat(sbl[ks], sbh[ks]), aW[t]);
        aU[t] = mfma_b(sc, ones, aU[t]);
        const int T = ug * 8 + wv * 2 + t;
        const int sw3 = T / 3, ti = T - sw3 * 3;
        __builtin_amdgcn_raw_buffer_store_b128(sc, rw, l16, O_W1F + ((sw3 * W1_STEPS + kq * 5 + ks) * 3 + ti) * 1024, SC1);
      }
      const f32x4 su = (lane & 4) ? aU[1] : aU[0], sw = (lane & 4) ? aW[1] : aW[0];
      const float U = p4 == 0 ? su[0] : p4 == 1 ? su[1] : p4 == 2 ? su[2] : su[3];
      const float W = p4 == 0 ? sw[0] : p4 == 1 ? sw[1] : p4 == 2 ? sw[2] : sw[3];
      const float u1 = rbf(U), u2 = rbf(U - u1), u3 = U - u1 - u2;
      const float w1 = rbf(W), w2 = rbf(W - w1), w3 = W - w1 - w2;
      const int T = ug * 8 + wv * 2 + ((lane >> 2) & 1);
      const int sw3 = T / 3, ti = T - sw3 * 3;
      if (!(lane & 8))
        __builtin_amdgcn_raw_buffer_store_b128(u32x4{pack_bf16(u1, u2), pack_bf16(u3, w1), pack_bf16(w2, w3), 0u}, rw,
                                               O_W1F + (((sw3 * W1_STEPS + 20) * 3 + ti) * 64 + kq * 16 + 4 * kg + p4) * 16, 0, SC1);
    }
    STAMPE(5);
  } else {
    const int d = (b >> 1) * 8 + (b & 1) + 2 * wv;
    const int row = lane & 15, kg = lane >> 4;
    const int tt = b & 31, ct = b >> 5;
    const int ov = ((lane >> 2) * 2048 + (lane & 3)) * 16, os = (ct * 16 * 2048 + (tt * 8 + wv * 2) * 4) * 16;
    const int stg = b * 2 + (wv - 2);
    const bool xact = wv >= 2 && b < 128;
    u32 xx[5][8];
    u32 draw;
    unsigned aAddr, bA[8];
    const unsigned fl = sbase + wv * CH_BYTES;
    {
      const rsrc_t rk = mk_rsrc(kIn, S_ * D_ * 2), rv = mk_rsrc(vIn, S_ * D_ * 2);
      const int vo = ct * 256 + lane * 4, so = (tt * 64 + wv * 16) * 1280;
      const int sh0 = so ? so - 3 * 1280 : 0;
      u32 kk[19], vv[16];
      const u32x4 taps = *(const u32x4*)(shk + (ct * 64 + lane) * 8);
#pragma unroll
      for (int q = 0; q < 3; ++q) kk[q] = __builtin_amdgcn_raw_buffer_load_b32(rk, vo + q * 1280, sh0, NT);
#pragma unroll
      for (int q = 0; q < 8; ++q) kk[3 + q] = __builtin_amdgcn_raw_buffer_load_b32(rk, vo + (q & 1) * 1280, so + (q & 14) * 1280, NT);
#pragma unroll
      for (int q = 0; q < 8; ++q) vv[q] = __builtin_amdgcn_raw_buffer_load_b32(rv, vo + (q & 1) * 1280, so + (q & 14) * 1280, NT);
#pragma unroll
      for (int q = 8; q < 16; ++q) kk[3 + q] = __builtin_amdgcn_raw_buffer_load_b32(rk, vo + (q & 1) * 1280, so + (q & 14) * 1280, NT);
#pragma unroll
      for (int q = 8; q < 16; ++q) vv[q] = __builtin_amdgcn_raw_buffer_load_b32(rv, vo + (q & 1) * 1280, so + (q & 14) * 1280, NT);
      SCHED();
      SCHED();
      sld32(draw, (const u32*)ssmD + (d >> 1));
      aAddr = fl + (row & 3) * FC_BYTES + 2 * (64 * kg + (row & ~3));
      pin(aAddr);
#pragma unroll
      for (int e = 0; e < 8; ++e) { const int g = 32 + 2 * row + 1 - 8 * kg - e; bA[e] = sbase + CKV_OFF + 16 * ((4 * g + wv) ^ ((g >> 2) & 7)); pin(bA[e]); }
      *(u32x2*)(smem + CKV_OFF + tid * 8) = u32x2{0, 0};
      if (lane < 13) *(u32*)(smem + wv * CH_BYTES + (lane < 7 ? FC_BYTES + lane * 4 : 3 * FC_BYTES + (lane - 7) * 4)) = 0;
      SCHED();
      if (so == 0) { kk[0] = 0; kk[1] = 0; kk[2] = 0; }
      const f32x2 w0 = {bflo(taps[0]), bflo(taps[2])}, w1 = {bfhi(taps[0]), bfhi(taps[2])};
      const f32x2 w2 = {bflo(taps[1]), bflo(taps[3])}, w3 = {bfhi(taps[1]), bfhi(taps[3])};
#pragma unroll
      for (int r = 0; r < 2; ++r) {
        f32x2 kv[8];
#pragma unroll
        for (int q = 0; q < 8; ++q) {
          const int i = 8 * r + q;
          f32x2 a = f32x2{bflo(kk[i]), bfhi(kk[i])} * w3;
          a = __builtin_elementwise_fma(f32x2{bflo(kk[i + 1]), bfhi(kk[i + 1])}, w2, a);
          a = __builtin_elementwise_fma(f32x2{bflo(kk[i + 2]), bfhi(kk[i + 2])}, w1, a);
          a = __builtin_elementwise_fma(f32x2{bflo(kk[i + 3]), bfhi(kk[i + 3])}, w0, a);
          kv[q] = a * f32x2{bflo(vv[i]), bfhi(vv[i])};
        }
        u32x4 o0, o1;
#pragma unroll
        for (int t = 0; t < 4; ++t) {
          o0[t] = pack_bf16(kv[7 - 2 * t][0], kv[6 - 2 * t][0]);
          o1[t] = pack_bf16(kv[7 - 2 * t][1], kv[6 - 2 * t][1]);
        }
        if (r == 0 && X_EARLY) {
#pragma unroll
          for (int q = 11; q < 19; ++q) pin(kk[q]);
#pragma unroll
          for (int q = 8; q < 16; ++q) pin(vv[q]);
          if (xact) {
      const char* xp = (const char*)xIn + stg * 8 * 1280;
      static_for<0, 40>([&](auto I) { constexpr int t = I.value / 5, c = I.value % 5; gld32p<c * 256 + (t & 1) * 1280>(xx[c][t], lane * 4, xp + (t & 6) * 1280); });
          }
        }
        __builtin_amdgcn_raw_buffer_store_b128(o0, rw, ov + r * 64, O_KVG + os, SC1);
        __builtin_amdgcn_raw_buffer_store_b128(o1, rw, ov + r * 64, O_KVG + os + 16384, SC1);
      }
    }
    STAMPE(1);
    tag = next_tag(ep);
    {
      const char* fs = (const char*)(ssmk + (size_t)d * S_);
      dma16x4p<FC_BYTES + 28>(fl, l16, fs);
      dma16x4p<3 * FC_BYTES + 24>(fl, l16, fs);
    }
    if (xact && !X_EARLY) {
      const char* xp = (const char*)xIn + stg * 8 * 1280;
      static_for<0, 40>([&](auto I) { constexpr int t = I.value / 5, c = I.value % 5; gld32p<c * 256 + (t & 1) * 1280>(xx[c][t], lane * 4, xp + (t & 6) * 1280); });
      wait_vm<48>();
    } else {
      wait_vm<8>();
    }
    asm volatile("s_waitcnt lgkmcnt(0)\n\ts_barrier" ::: "memory");
    if (tid < 32) st_flag(ws, O_FK + ((b & ~31) + tid) * 256 + (b & 31) * 8, tag);
    STAMPE(2);
    int pa1[3] = {O_FX + ((b & 7) * 32 + (lane >> 2)) * 128 + (lane & 3) * 16, O_FX + ((b & 7) * 32 + 16 + (lane >> 2)) * 128 + (lane & 3) * 16,
                  O_FA0 + ((b & 7) * 12 + (lane < 48 ? lane >> 2 : 0)) * 128 + (lane & 3) * 16};
    const char* xgp = ws + O_XG + b * 16384 + wv * 4096;
    const char* stp = ws + O_ST1 + wv * 4096;
    const char* mdp = ws + O_MODP + d * 4;
    asm volatile("" : "+s"(xgp), "+s"(stp), "+s"(mdp));
    pin(pa1[0]); pin(pa1[1]); pin(pa1[2]);
    asm volatile("" : "+s"(draw));
    float Dv = (d & 1) ? bfhi(draw) : bflo(draw);
    unsigned ek = sbase + CKV_OFF + 16 * ((4 * (32 + 2 * row + (kg >> 1)) + wv) ^ ((row >> 1) & 7)) + ((kg & 1) ? 0 : 8);
    unsigned ex = (ek ^ 8) + (CX_OFF - CKV_OFF - 2048), es = sbase + CS_OFF + l16;
    unsigned mo = (((lane & 7) < MODP ? (lane & 7) : 0) * NMOD + ((lane >> 3) & 3) * D_) * 4;
    pin(Dv); pin(ek); pin(ex); pin(es); pin(mo);
    unsigned ko[2] = {(unsigned)(lane ^ (lane >> 4)) * 16, ((unsigned)(lane ^ (lane >> 4)) * 16) ^ 64};
    pin(ko[0]); pin(ko[1]);
    auto fcopies = [&]() {
      u32x4 m[5]; u32 lo[5], hi[5];
      const unsigned src = fl + FC_BYTES + l16, src4 = fl + FC_BYTES + 4096 + (lane & 1) * 16;
      static_for<0, 4>([&](auto I) { constexpr int i = I.value; ds_b128<i * 1024>(m[i], src); ds_b32<i * 1024>(lo[i], src - 4); ds_b32<i * 1024 + 16>(hi[i], src); });
      ds_b128<0>(m[4], src4); ds_b32<0>(lo[4], src4 - 4); ds_b32<16>(hi[4], src4);
      wait_lgkm<0>();
#pragma unroll
      for (int i = 0; i < 5; ++i) {
        pin(m[i]); pin(lo[i]); pin(hi[i]);
        if (i == 0 && lane == 0) lo[0] = 0;
        const u32 s1 = __builtin_amdgcn_alignbit(m[i][1], m[i][0], 16), s2 = __builtin_amdgcn_alignbit(m[i][2], m[i][1], 16), s3 = __builtin_amdgcn_alignbit(m[i][3], m[i][2], 16);
        char* dst = smem + wv * CH_BYTES + (i < 4 ? i * 1024 + l16 : 4096 + (lane & 1) * 16);
        *(u32x4*)dst = u32x4{__builtin_amdgcn_alignbit(m[i][0], lo[i], 16), s1, s2, s3};
        *(u32x4*)(dst + 2 * FC_BYTES) = u32x4{s1, s2, s3, __builtin_amdgcn_alignbit(hi[i], m[i][3], 16)};
      }
    };
    if (!xact) { if (wv == 0) wait_vm<1>(); else wait_vm<0>(); fcopies(); }
    if (xact) {
      wait_vm<0>();
      STAMPD(8);
#pragma unroll
      for (int c = 0; c < 5; ++c)
#pragma unroll
        for (int t = 0; t < 8; ++t) pin(xx[c][t]);
#pragma unroll
      for (int c = 0; c < 5; ++c) {
        u32x4 p0, p1;
#pragma unroll
        for (int t = 0; t < 4; ++t) {
          p0[t] = __builtin_amdgcn_perm(xx[c][2 * t + 1], xx[c][2 * t], 0x05040100u);
          p1[t] = __builtin_amdgcn_perm(xx[c][2 * t + 1], xx[c][2 * t], 0x07060302u);
        }
        const int so = O_XG + (c * 16 * 2048 + stg * 4) * 16;
        __builtin_amdgcn_raw_buffer_store_b128(p0, rw, ov, so, SC1);
        __builtin_amdgcn_raw_buffer_store_b128(p1, rw, ov, so + 16384, SC1);
      }
      const bf16x2 ones = {(__bf16)1.0f, (__bf16)1.0f};
      float s1[8], s2[8];
#pragma unroll
      for (int t = 0; t < 8; ++t) {
        s1[t] = 0.f; s2[t] = 0.f;
#pragma unroll
        for (int c = 0; c < 5; ++c) {
          const bf16x2 v = __builtin_bit_cast(bf16x2, xx[c][t]);
          s1[t] = __builtin_amdgcn_fdot2_f32_bf16(v, ones, s1[t], false);
          s2[t] = __builtin_amdgcn_fdot2_f32_bf16(v, v, s2[t], false);
        }
      }
      float q1[2], q2[2];
#pragma unroll
      for (int e = 0; e < 2; ++e) {
        q1[e] = dpp_add<DPP_ROR8>(tred16(tred32(s1[e], s1[4 + e]), tred32(s1[2 + e], s1[6 + e])));
        q2[e] = dpp_add<DPP_ROR8>(tred16(tred32(s2[e], s2[4 + e]), tred32(s2[2 + e], s2[6 + e])));
      }
      const float mean = sum8((lane & 8) ? q1[1] : q1[0]) * (1.f / D_);
      const float var = fmaxf(sum8((lane & 8) ? q2[1] : q2[0]) * (1.f / D_) - mean * mean, 0.f);
      const float sd = rbf(__builtin_amdgcn_sqrtf(rbf(rbf(var) + 1e-5f)));
      const float rs = __builtin_amdgcn_rcpf(sd);
      const int s = stg * 8 + (lane >> 3);
      const int nt = s >> 8, col = (s >> 4) & 15, k4 = (s >> 2) & 3, rr = s & 3;
      if ((lane & 7) == 0) __builtin_amdgcn_raw_buffer_store_b64(u32x2{__float_as_uint(rs), __float_as_uint(-rbf(mean) * rs)}, rw,
                                                                 O_ST1 + (((nt * 2 + (rr >> 1)) * 64 + k4 * 16 + col) * 4 + (rr & 1) * 2) * 4, 0, SC1);
      STAMPD(9);
      fcopies();
      wait_vm<0>();
      if (lane < 8) st_flag(ws, O_FX + (lane * 32 + (stg >> 3)) * 128 + (stg & 7) * 8, tag);
    } else if (wv < 2) {
      const int po[1] = {O_FK + b * 256 + wv * 128 + (lane & 7) * 16};
      poll<1, 0>(rw, po, tag);
      STAMPE(3);
      dma16x4<CKV_OFF + 2048>(sbase + wv * 8192, ko[0], ko[1], ws + O_KVG + b * 16384 + wv * 8192);
      dma16x4<CKV_OFF + 2048 + 4096>(sbase + wv * 8192, ko[0], ko[1], ws + O_KVG + b * 16384 + wv * 8192 + 4096);
      wait_vm<0>();
    }
    STAMPD(10);
    asm volatile("s_waitcnt lgkmcnt(0)\n\ts_barrier" ::: "memory");
    STAMPE(4);
    f32x4 acc[8];
    u32x2 ra[2][8][2]; u32x4 rb[2][8];
    u32x4 pf[3]; u32 mraw; u64 nm[6];
    auto load_n = [&](auto E, auto N) {
      constexpr int e = E.value, n = N.value, bf = e & 1;
      if constexpr (n < 2) ds_b64<16 * e + 8 * n>(ra[bf][0][n], aAddr);
      else if constexpr (n < 10) ds_b128<2048 * (n - 2)>(rb[bf][n - 2], bA[e]);
      else ds_b64<512 * ((n - 8) >> 1) + 16 * e + 8 * (n & 1)>(ra[bf][(n - 8) >> 1][n & 1], aAddr);
    };
    u32x2 kvw[8], xw[8]; u32x4 sA[8], sB[8];
    float T[8][4], Hh[8][4], c0, c1;
    auto kvop = [&](auto K) {
      constexpr int k = K.value, nt = k >> 3, r = k & 3;
      if constexpr ((k & 4) == 0) T[nt][r] = (r & 1) ? v_lo(kvw[nt][1 - (r >> 1)]) : v_hi(kvw[nt][1 - (r >> 1)]);
      else T[nt][r] = v_mul(T[nt][r], Dv);
    };
    auto xrd = [&](auto K) {
      constexpr int k = K.value, nt = k / 3;
      if constexpr (k % 3 == 0) ds_b64<2048 * nt>(xw[nt], ex);
      else if constexpr (k % 3 == 1) ds_b128<2048 * nt>(sA[nt], es);
      else ds_b128<2048 * nt + 1024>(sB[nt], es);
    };
    float xf[4];
    auto xop = [&](auto K) {
      constexpr int k = K.value, nt = k >> 4, ph = (k >> 2) & 3, r = k & 3;
      if constexpr (ph == 0) xf[r] = (r & 1) ? v_hi(xw[nt][r >> 1]) : v_lo(xw[nt][r >> 1]);
      else if constexpr (ph == 1) Hh[nt][r] = v_fma(xf[r], __uint_as_float((r < 2 ? sA[nt] : sB[nt])[2 * (r & 1)]), __uint_as_float((r < 2 ? sA[nt] : sB[nt])[2 * (r & 1) + 1]));
      else if constexpr (ph == 2) Hh[nt][r] = v_fma(Hh[nt][r], c1, c0);
      else T[nt][r] = v_fma(Hh[nt][r], T[nt][r], xf[r]);
    };
    static_for<0, 24>([&](auto N) { load_n(IC<7>{}, N); });
    wait_lgkm<14>();
    static_for<0, 8>([&](auto EE) {
      constexpr int e = 7 - EE.value, bf = e & 1;
      static_for<0, 8>([&](auto AA) {
        constexpr int a = AA.value;
        static_for<0, 8 - a>([&](auto BB) {
          constexpr int bb = BB.value, n = conv_gap(a, bb);
          if constexpr (e == 7 && n == 8) wait_lgkm<8>();
          mfma_<(e == 7 && a == 0)>(acc[a + bb], cat(ra[bf][a][0], ra[bf][a][1]), rb[bf][bb]);
          if constexpr (e > 0 && n < 24) load_n(IC<(e > 0 ? e - 1 : 0)>{}, IC<n>{});
          if constexpr (e == 7 && n >= 24 && n < 32) ds_b64<2048 * (n - 24)>(kvw[n - 24], ek);
          constexpr int g = 36 * (7 - e) + n;
          static_for<CS.rd[g], CS.rd[g + 1]>(xrd);
          if constexpr (e > 0 ? n == 34 : n == 12) wait_lgkm<0>();
          static_for<CS.op[g], CS.op[g + 1]>(xop);
          if constexpr ((e == 6 || e == 5) && n < 32) kvop(IC<(6 - e) * 32 + n>{});
          if constexpr (CV.kind[g] == 2) gld128<0>(pf[CV.idx[g]], pa1[CV.idx[g]], ws);
          if constexpr (CV.kind[g] == 3) { if constexpr (CV.idx[g] == 0) dma16<CX_OFF>(sbase + wv * 4096, ko[0], xgp); else dma16n<CV.idx[g] * 1024>(ko[CV.idx[g] & 1], xgp); }
          if constexpr (CV.kind[g] == 4) { if constexpr (CV.idx[g] == 0) dma16<CS_OFF>(sbase + wv * 4096, l16, stp); else dma16n<CV.idx[g] * 1024>(l16, stp); }
          if constexpr (CV.kind[g] == 5) gld32(mraw, mo, mdp);
          if constexpr (g == G_XCHK) { STAMPD(11); wait_vm<cv_younger(g, 2, 2)>(); }
          if constexpr (g >= G_XCHK && g < G_XCHK + 3) { constexpr int i = g - G_XCHK; nm[2 * i] = v_ne64(u32x2{pf[i][0], pf[i][1]}, tag); nm[2 * i + 1] = v_ne64(u32x2{pf[i][2], pf[i][3]}, tag); }
          if constexpr (g == G_XCHK + 3) {
            if (__builtin_expect(((nm[0] | nm[1]) | (nm[2] | nm[3]) | (nm[4] | nm[5])) != 0, 0)) poll<3, 1>(rw, pa1, tag);
            STAMPD(12);
          }
          if constexpr (e == CBE && n == 22) {
            STAMPD(13);
            wait_vm<cv_younger(g, 5, 0)>(); bar(); pin(mraw);
            STAMPD(14);
            const float mv = rbf(sum8((lane & 7) < MODP ? __uint_as_float(mraw) : 0.f));
            const float m_ga = rdl(mv, 16);
            c0 = rdl(mv, 0) * m_ga; c1 = rbf(1.f + rdl(mv, 8)) * m_ga;
            pin(c0); pin(c1);
          }
        });
      });
    });
    STAMPE(5);
    asm volatile("s_nop 7");
    static_for<0, 4>([&](auto NP) {
      constexpr int np = NP.value;
      static_for<(CS.op[288] > 32 * np ? CS.op[288] : 32 * np), 32 * np + 32>(xop);
      u32x4 o;
#pragma unroll
      for (int u = 0; u < 2; ++u) {
        const int nt = 2 * np + u;
        pin(acc[nt]);
        o[2 * u] = pack_bf16(fmaf(Hh[nt][0], acc[nt][0], T[nt][0]), fmaf(Hh[nt][1], acc[nt][1], T[nt][1]));
        o[2 * u + 1] = pack_bf16(fmaf(Hh[nt][2], acc[nt][2], T[nt][2]), fmaf(Hh[nt][3], acc[nt][3], T[nt][3]));
      }
      __builtin_amdgcn_raw_buffer_store_b128(o, rw, l16 + np * 1024, O_X2T + d * (TP_ * 2), SC1);
    });
  }
  const int kg = lane >> 4, c16 = lane & 15;
  const int kq0 = nc >> 2, r0 = (nc & 1) * 4 + wv, sgb = O_SG + (mt * 2 + ((nc >> 1) & 1)) * 8192;
  int sgo = (c16 >> 2) == kg ? sgb + (r0 * 4 + kq0) * 256 + c16 * 16 : WS_BYTES;
  __builtin_amdgcn_raw_buffer_store_b128(u32x4{0u, 0u, 0u, 0u}, rw, sgo, 0, SC1);
  STAMPE(6);
  drain_bar();
  if (tid < 8) st_flag(ws, O_F2 + (tid * 32 + (b >> 3)) * 128 + (b & 7) * 8, tag);
  STAMPE(7);
  if (bs >= CWG) {
    const int j = bs - CWG;
    u32x2 dl[5], dh[5];
    const unsigned add = sbase + AD_OFF + (8 * kg + (c16 >> 2)) * 128 + (lane & 3) * 8;
#pragma unroll
    for (int i = 0; i < 5; ++i) { const int tl = wv * 5 + i; const unsigned a2 = add + (tl >> 2) * 4096 + (tl & 3) * 32; ds_tr<0>(dl[i], a2); ds_tr<512>(dh[i], a2); }
    wait_lgkm<0>();
#pragma unroll
    for (int i = 0; i < 5; ++i) {
      pin(dl[i]); pin(dh[i]);
      __builtin_amdgcn_raw_buffer_store_b128(cat(dl[i], dh[i]), rw, l16, O_W2F + ((((j & 1) * 4 + wv) * 48 + (j >> 1)) * 5 + i) * 1024, SC1);
    }
    drain_bar();
    if (tid < 8) st_flag(ws, O_FW + (tid * 12 + (j >> 3)) * 128 + (j & 7) * 8, tag);
  }

  {
    unsigned xoff[2];
#pragma unroll
    for (int p = 0; p < 2; ++p) {
      const int k = 4 * p + kg;
      xoff[p] = (k * TP_ + ((c16 ^ (2 * k)) * 8)) * 2;
      pin(xoff[p]);
    }
    const char* wsrc = ws + O_W1F + (nc * 4 + wv) * W1_STEPS * 3072;
    const char *xq[4], *wq[4];
#pragma unroll
    for (int i = 0; i < 4; ++i) {
      xq[i] = ws + O_X2T + ((((kq0 + i) & 3) * 160 + 8 * wv) * TP_ + mt * 128) * 2;
      wq[i] = wsrc + ((kq0 + i) & 3) * (5 * 3072);
      asm volatile("" : "+s"(xq[i]), "+s"(wq[i]));
    }
    const unsigned ldsw = sbase + wv * 2048;
    unsigned sgl = sgb + wv * 2048 + l16;
    int hvo = kg < 3 ? ((3 * wv + kg) >> 2) * 1024 + ((3 * wv + kg) & 3) * 256 + c16 * 16 : WS_BYTES;
    const int hso = O_H2F + (mt * 8 * 48 + 3 * nc) * 1024;
    unsigned spa = sbase + G1_SP + r0 * 64 + c16 * 4;
    pin(sgl); pin(hvo); pin(sgo); pin(spa);
    u32x4 W[G1_R][3], WA_[WA > 0 ? 3 * WA : 1], WC[3], pfs[2], pfw, gran;
    int pw[1] = {O_FW + ((b & 7) * 12 + (lane < 48 ? lane >> 2 : 0)) * 128 + (lane & 3) * 16};
    unsigned xo = ((lane >> 3) * TP_ + (lane & 7) * 8) * 2;
    const char* xs = ws + O_X2T + (N * 80 * TP_ + M * 64) * 2;
    pin(pw[0]); pin(xo);
    u32 hq[4], sp[8];
    auto issue_w = [&](auto I) { constexpr int i = I.value; gld128<(i % 3) * 1024>(W[(i / 3) % G1_R][i % 3], l16, wq[i / 15] + ((i / 3) % 5) * 3072); };
    auto issue = [&](auto O) {
      constexpr int kind = G1S.kind[O.value], i = G1S.idx[O.value];
      if constexpr (kind == 0) dma16<((i >> 1) % G1_SLOTS) * 8192 + (i & 1) * 1024>(ldsw, xoff[i & 1], xq[i / 10] + ((i >> 1) % 5) * (32 * TP_ * 2));
      else if constexpr (kind == 1) issue_w(IC<i>{});
      else if constexpr (kind == 2) issue_wd(IC<i>{});
      else if constexpr (kind == 3) {
        if constexpr (i < 2) dma16<XR_OFF + i * 4096>(sbase + wv * 1024, xo, xs + (size_t)(wv + 4 * i) * 8 * TP_ * 2);
        else dma16<XR_OFF + 8192>(sbase + (wv & 1) * 1024, xo, xs + (size_t)((wv & 1) + 8) * 8 * TP_ * 2);
      }
      else if constexpr (kind == 4) gld128<0>(pfw, pw[0], ws);
      else if constexpr (kind == 5) __builtin_amdgcn_raw_buffer_store_b128(gran, rw, sgo, 0, SC1);
      else if constexpr (kind == 6) gld128<i * 1024>(pfs[i], sgl, ws);
      else __builtin_amdgcn_raw_buffer_store_b128(u32x4{hq[0], hq[1], hq[2], hq[3]}, rw, hvo, hso + ((i + r0) & 7) * (48 * 1024), SC1);
    };
    unsigned xa[8];
#pragma unroll
    for (int jj = 0; jj < 8; ++jj) {
      const int rr = 4 * kg + (c16 >> 2);
      xa[jj] = sbase + rr * 256 + ((((jj + r0) & 7) ^ (rr & 7)) * 32) + (lane & 3) * 8;
      pin(xa[jj]);
    }
    u32x4 ones = {0x3f803f80u, 0x3f803f80u, 0x3f803f80u, 0x3f803f80u};
    pin(ones);
    {
      const int fb = O_F2 + (b & 7) * 32 * 128 + (lane & 3) * 16;
      const int pa[1] = {fb + (20 + (lane < 48 ? lane >> 2 : 0)) * 128};
      poll<1, 2>(rw, pa, tag);
      static_for<0, 3 * G1_WPRE>(issue_w);
      static_for<0, 3>([&](auto I) { gld128a<I.value * 1024>(WC[I.value], l16, wsrc + 20 * 3072); });
      static_for<0, 3 * WA>([&](auto I) { constexpr int i = 3 * G1_WPRE + I.value; gld128a<(i % 3) * 1024>(WA_[I.value], l16, wq[i / 15] + ((i / 3) % 5) * 3072); });
      if (wv == 3) {
        f32x2 g = {0.f, 0.f};
#pragma unroll
        for (int p = 0; p < MODP; ++p) g += __builtin_bit_cast(f32x2, __builtin_amdgcn_raw_buffer_load_b64(rw, lane * 8, O_MODP + (p * NMOD + 5 * D_ + N * 80) * 4, SC1));
        if (lane < 40) *(f32x2*)(smem + XR_OFF + 10240 + lane * 8) = f32x2{rbf(g[0]), rbf(g[1])};
      }
      STAMPL(8);
      if (wv == 0) {
        const int pc[2] = {fb + (lane >> 2) * 128, fb + (16 + ((lane >> 2) & 3)) * 128};
        poll<2, 1>(rw, pc, tag);
      }
      wait_vm<0>();
      bar();
    }
    STAMPL(9);
    static_for<0, G1S.first[0]>(issue);
    f32x4 acc[8][3];
    u32x2 X[G1_LA + 1][2];
    auto xread = [&](auto T, auto H) {
      constexpr int t = T.value, h = H.value;
      ds_tr<(g1_ks(t) % G1_SLOTS) * 8192 + h * 4096>(X[t % (G1_LA + 1)][h], xa[g1_j(t)]);
    };
    f32x4 gram, sum1;
    u32 fs[4], fa; u64 sbad;
    unsigned spw = sbase + G1_SP + (2 * wv + (kg >> 1) + (kg & 1) * 8) * 64 + c16 * 4;
    pin(spw);
    auto fop = [&](auto K) {
      constexpr int k = K.value;
      if constexpr (k == 0) asm_and3(fa, pfs[0][1], pfs[0][3], pfs[1][1]);
      else if constexpr (k == 1) asm_and3(fa, fa, pfs[1][3], pfs[1][3]);
      else if constexpr (k == 2) asm_ne1(sbad, fa);
      else if constexpr (k == 4) v_swap32(fs[0], fs[1]);
      else if constexpr (k == 5) v_swap32(fs[2], fs[3]);
      else if constexpr (k == 6) asm_op2<0>(fs[0], fs[0], fs[1]);
      else if constexpr (k == 7) asm_op2<0>(fs[2], fs[2], fs[3]);
      else if constexpr (k == 8) v_swap16(fs[0], fs[2]);
      else if constexpr (k == 9) asm_op2<0>(fs[0], fs[0], fs[2]);
      else if constexpr (k == 10) asm_op1<0>(fs[0], fs[0]);
      else if constexpr (k == 11) asm_op1<1>(fs[1], fs[0]);
      else if constexpr (k == 12) v_swap16(fs[0], fs[1]);
      else if constexpr (k == 13) asm_op2<1>(fs[1], fs[0], fs[1]);
      else if constexpr (k == 14) asm_op1<2>(fs[1], fs[1]);
      else if constexpr (k == 15) asm_op1<3>(fs[1], fs[1]);
      else if constexpr (k == 16) asm_op1<4>(fs[1], fs[1]);
      else if constexpr (k == 17) asm_op1<3>(fs[1], fs[1]);
      else if constexpr (k == 18) asm_op1<5>(fs[1], fs[1]);
      else if constexpr (k == 19) asm_op2<2>(fs[0], fs[0], fs[1]);
      else if constexpr (k == 20) ds_w32(spw, fs[0]);
    };
    float eg[6]; u32x4 xc = {0u, 0u, 0u, 0u};
    auto eop = [&](auto J, auto K) {
      constexpr int j = J.value, k = K.value;
      if constexpr (k < 15) {
        if constexpr (k < 6) eg[k] = v_relu(acc[j][k >> 1][2 * (k & 1)]);
        else if constexpr (k < 12) eg[k - 6] = v_mul(eg[k - 6], acc[j][(k - 6) >> 1][2 * (k & 1) + 1]);
        else hq[k == 12 ? 1 : k == 13 ? 0 : 2] = v_cvt(eg[k == 12 ? 2 : k == 13 ? 0 : 4], eg[k == 12 ? 3 : k == 13 ? 1 : 5]);
      } else if constexpr (k == 15) v_swap32(hq[1], hq[3]);
      else if constexpr (k == 16) v_swap32(hq[0], hq[2]);
      else if constexpr (k == 17) v_swap16(hq[0], hq[1]);
      else v_swap16(hq[2], hq[3]);
    };
    auto xcop = [&](auto J, auto K) {
      constexpr int j = J.value;
      if constexpr (K.value == 0) xc[0] = v_perm(sp[j], 0x01000100u);
      else if constexpr (K.value == 1) xc[2] = v_perm(sp[j], 0x03020302u);
      else xc[1] = v_perm(sp[j], 0x03020100u);
    };
    hq[3] = 0;
    pin(hq[3]); pin(xc);
    wait_vm<G1S.first[0] - 2>();
    bar();
    STAMPL(10);
    static_for<0, G1_LA>([&](auto T) { xread(T, IC<0>{}); xread(T, IC<1>{}); });
    static_for<0, 160>([&](auto T) {
      constexpr int t = T.value, ks = g1_ks(t), jj = g1_j(t), tn = t + G1_LA, u = t & 3;
      if constexpr (t == 24) STAMPG(1);
      if constexpr (t == 48) STAMPG(2);
      if constexpr (t == 72) STAMPG(3);
      if constexpr (t == 96) STAMPG(4);
      if constexpr (t == 124) STAMPG(5);
      if constexpr (t == 126) STAMPG(6);
      wait_lgkm<2 * ((tn <= 160 ? tn : 160) - t - 1)>();
      if constexpr (t <= 124 && (t & 7) == 4) {
        static_assert(g1_need(t) < G1S.first[t], "not issued yet");
        wait_vm<G1S.first[t] - 1 - g1_need(t)>();
        bar();
      }
      if constexpr (t == T_WCHK) {
        if (__builtin_expect((v_ne64(u32x2{pfw[0], pfw[1]}, tag) | v_ne64(u32x2{pfw[2], pfw[3]}, tag)) != 0, 0)) poll<1, 1>(rw, pw, tag);
      }
      if constexpr (t == 35) {
        pin(gram); pin(sum1);
        const int c3 = lane & 3;
        gran = u32x4{__float_as_uint(sum1[0]), 1u, __float_as_uint(c3 == 0 ? gram[0] : c3 == 1 ? gram[1] : c3 == 2 ? gram[2] : gram[3]), 1u};
        pin(gran);
      }
      if constexpr (t == 111) {
        if (__builtin_expect(sbad != 0, 0)) {
#pragma unroll 1
          for (int spn = 0; spn < SPIN_CAP; ++spn) {
            pfs[0] = __builtin_amdgcn_raw_buffer_load_b128(rw, sgl, 0, SC1);
            pfs[1] = __builtin_amdgcn_raw_buffer_load_b128(rw, sgl + 1024, 0, SC1);
            if (__all((pfs[0][1] & pfs[0][3] & pfs[1][1] & pfs[1][3]) == 1u)) break;
            asm volatile("" ::: "memory");
          }
        }
        fs[0] = pfs[0][0]; fs[1] = pfs[1][0]; fs[2] = pfs[0][2]; fs[3] = pfs[1][2];
      }
      if constexpr (t == 124) static_for<0, 8>([&](auto J) { ds_b32<J.value * 64>(sp[J.value], spa); });
      auto shadow = [&](auto S) {
        constexpr int sl = S.value;
        if constexpr (t >= 109 && t < 120 && sl < 2) fop(IC<2 * (t - 109) + sl>{});
        if constexpr (t >= 132) {
          constexpr int k = 4 * u + sl - 2;
          if constexpr (k >= 0 && k < 14) eop(IC<jj - 1>{}, IC<k>{});
        }
        if constexpr (t >= 128 && u == 0 && sl < 2) xcop(IC<jj>{}, IC<sl>{});
      };
      const u32x4 xb = cat(X[t % (G1_LA + 1)][0], X[t % (G1_LA + 1)][1]);
      if constexpr (ks >= G1_WPRE && ks < G1_WPRE + WA) mfma_a<false>(acc[jj][0], WA_[3 * (ks - G1_WPRE) + 0], xb); else mfma_<ks == 0>(acc[jj][0], W[ks % G1_R][0], xb);
      if constexpr (tn < 160) xread(IC<tn>{}, IC<0>{});
      shadow(IC<0>{});
      if constexpr (ks >= G1_WPRE && ks < G1_WPRE + WA) mfma_a<false>(acc[jj][1], WA_[3 * (ks - G1_WPRE) + 1], xb); else mfma_<ks == 0>(acc[jj][1], W[ks % G1_R][1], xb);
      if constexpr (tn < 160) xread(IC<tn>{}, IC<1>{});
      shadow(IC<1>{});
      if constexpr (ks >= G1_WPRE && ks < G1_WPRE + WA) mfma_a<false>(acc[jj][2], WA_[3 * (ks - G1_WPRE) + 2], xb); else mfma_<ks == 0>(acc[jj][2], W[ks % G1_R][2], xb);
      if constexpr (!(t >= 128 && u == 3)) static_for<G1S.first[t], G1S.first[t + 1]>(issue);
      shadow(IC<2>{});
      shadow(IC<3>{});
      if constexpr (t < 40 && jj == 0) { mfma_<ks == 0>(gram, xb, xb); mfma_<ks == 0>(sum1, ones, xb); }
      if constexpr (t >= 128 && u == 1) xcop(IC<jj>{}, IC<2>{});
      if constexpr (t >= 128 && u == 3) {
        mfma_a<false>(acc[jj][0], WC[0], xc);
        if constexpr (t >= 132) { eop(IC<jj - 1>{}, IC<14>{}); eop(IC<jj - 1>{}, IC<15>{}); }
        mfma_a<false>(acc[jj][1], WC[1], xc);
        if constexpr (t >= 132) { eop(IC<jj - 1>{}, IC<16>{}); asm volatile("s_nop 0"); }
        mfma_a<false>(acc[jj][2], WC[2], xc);
        if constexpr (t >= 132) { eop(IC<jj - 1>{}, IC<17>{}); eop(IC<jj - 1>{}, IC<18>{}); }
        static_for<G1S.first[t], G1S.first[t + 1]>(issue);
      }
    });
    STAMPL(11);
    asm volatile("s_nop 3");
    static_for<0, 15>([&](auto K) { eop(IC<7>{}, K); });
    eop(IC<7>{}, IC<15>{}); asm volatile("s_nop 0"); eop(IC<7>{}, IC<16>{}); asm volatile("s_nop 1"); eop(IC<7>{}, IC<17>{}); eop(IC<7>{}, IC<18>{});
    __builtin_amdgcn_raw_buffer_store_b128(u32x4{hq[0], hq[1], hq[2], hq[3]}, rw, hvo, hso + ((7 + r0) & 7) * (48 * 1024), SC1);
  }
  STAMPL(12);
  float rs2;
  {
    const u32 p = *(const u32*)(smem + G1_SP + ((nc & 4) + wv) * 64 + c16 * 4);
    const float rs = __builtin_amdgcn_rcpf(bfhi(p));
    rs2 = rs * rs;
    pin(rs2);
  }
  const char* hp[4];
  unsigned pwa[3];
#pragma unroll
  for (int jj = 0; jj < 4; ++jj) { hp[jj] = ws + O_H2F + ((M * 4 + ((jj + wv + 1) & 3)) * 48 + wv * 12) * 1024; asm volatile("" : "+s"(hp[jj])); }
#pragma unroll
  for (int jj = 0; jj < 3; ++jj) { pwa[jj] = sbase + ((jj + wv + 1) & 3) * 15360 + jj * 5120 + l16; pin(pwa[jj]); }
  unsigned pra = sbase + wv * 15360 + l16, gba = sbase + XR_OFF + 10240 + kg * 16;
  unsigned xad = sbase + XR_OFF + (4 * kg + (c16 >> 2)) * 128 + wv * 32 + (lane & 3) * 8;
  int oo;
  {
    const int pos = M * 64 + wv * 16 + c16;
    const int l8 = (pos >> 3) & 63;
    const int tok = 256 * (2 * (pos >> 9) + ((pos >> 2) & 1)) + 16 * (l8 & 15) + 4 * (l8 >> 4) + (pos & 3);
    oo = (tok * D_ + N * 80 + 4 * kg) * 2;
  }
  pin(pra); pin(gba); pin(xad); pin(oo);
  int po3[1] = {O_F3 + (mt * 16 + nc) * 128 + wv * 32 + (lane & 1) * 16};
  int f3o = tid < 16 ? O_F3 + (mt * 16 + tid) * 128 + nc * 8 : WS_BYTES;
  u32x2 tag2 = {(u32)tag, (u32)(tag >> 32)};
  pin(po3[0]); pin(f3o); pin(tag2);
  drain_bar();
  if (wv == 0) __builtin_amdgcn_raw_buffer_store_b64(tag2, rw, f3o, 0, SC1);
  STAMPG(7);
  static_for<G1_WDN, 60>(issue_wd);
  poll<1, 0>(rw, po3, tag);
  STAMPL(13);
  u32x4 H[G2_R];
  auto issue_h = [&](auto O) { constexpr int o = O.value; gld128<((o % 12) & 3) * 1024>(H[o % G2_R], l16, hp[o / 12] + ((o % 12) >> 2) * 4096); };
  static_for<0, G2_PRE>(issue_h);
  f32x4 acc[4][5], pt[3][5], gbv[5];
  u32x2 xw[5];
  float G[5][4], Tt[5][4];
  static_for<0, 240>([&](auto GG) {
    constexpr int g = GG.value, o = g / 5, jj = o / 12, ks = o % 12, i = g % 5, q = g % 60;
    constexpr int i0 = (G2_PRE + (g + 3) / 4 < 48) ? G2_PRE + (g + 3) / 4 : 48;
    static_assert(i0 > o && i0 < o + G2_R, "ring");
    if constexpr (i == 0) wait_vm<i0 - 1 - o>();
    mfma_a<ks == 0>(acc[jj][i], WD[ks * 5 + i], H[o % G2_R]);
    if constexpr ((g & 3) == 0 && i0 < 48) issue_h(IC<i0>{});
    if constexpr (jj >= 1 && q >= 5 && q < 15 && (q & 1)) ds_w128<((q - 5) / 2) * 1024>(pwa[jj - 1], acc[jj - 1][(q - 5) / 2]);
    if constexpr (g >= 81 && g < 86) ds_tr<(g - 81) * 2048>(xw[g - 81], xad);
    if constexpr (g >= 86 && g < 91) ds_f128<(g - 86) * 64>(gbv[g - 86], gba);
    if constexpr (g == 96) wait_lgkm<0>();
    if constexpr (g >= 97 && g < 117) {
      constexpr int k = g - 97;
      Tt[k >> 2][k & 3] = (k & 1) ? v_hi(xw[k >> 2][(k >> 1) & 1]) : v_lo(xw[k >> 2][(k >> 1) & 1]);
      G[k >> 2][k & 3] = v_mul(gbv[k >> 2][k & 3], rs2);
    }
    if constexpr (g == 198) { wait_lgkm<0>(); bar(); }
    if constexpr (g >= 199 && g < 214) ds_f128<(g - 199) * 1024>(pt[(g - 199) / 5][(g - 199) % 5], pra);
    if constexpr (g == 208) wait_lgkm<5>();
    if constexpr (g == 218) wait_lgkm<0>();
    if constexpr (g >= 209) {
      constexpr int k0 = g < 214 ? g - 209 : 5 + 2 * (g - 214), k1 = g < 214 ? k0 + 1 : k0 + 2;
      static_for<k0, (k1 < 60 ? k1 : 60)>([&](auto K) { constexpr int k = K.value; Tt[(k % 20) >> 2][k & 3] = v_fma(G[(k % 20) >> 2][k & 3], pt[k / 20][(k % 20) >> 2][k & 3], Tt[(k % 20) >> 2][k & 3]); });
    }
  });
  STAMPL(14);
  static_for<57, 60>([&](auto K) { constexpr int k = K.value; Tt[(k % 20) >> 2][k & 3] = v_fma(G[(k % 20) >> 2][k & 3], pt[k / 20][(k % 20) >> 2][k & 3], Tt[(k % 20) >> 2][k & 3]); });
  asm volatile("s_nop 2");
  const rsrc_t ro = mk_rsrc(out, S_ * D_ * 2);
#pragma unroll
  for (int i = 0; i < 5; ++i) {
    pin(acc[3][i]);
    __builtin_amdgcn_raw_buffer_store_b64(u32x2{pack_bf16(fmaf(G[i][0], acc[3][i][0], Tt[i][0]), fmaf(G[i][1], acc[3][i][1], Tt[i][1])),
                                                pack_bf16(fmaf(G[i][2], acc[3][i][2], Tt[i][2]), fmaf(G[i][3], acc[3][i][3], Tt[i][3]))}, ro, oo + i * 32, 0, 0);
  }
  if (b == 0 && tid == 0) st_flag(ws, O_EPOCH, tag);
  STAMP_END();
}

extern "C" void kernel_launch(void* const* d_in, const int* in_sizes, int n_in, void* d_out, int out_size,
                              void* d_ws, size_t ws_size, hipStream_t stream) {
  k_fused<<<256, 256, 0, stream>>>((const u16*)d_in[0], (const u16*)d_in[1], (const u16*)d_in[2], (const u16*)d_in[3],
                                   (const u16*)d_in[4], (const u16*)d_in[5], (const u16*)d_in[7], (const u16*)d_in[8],
                                   (const u16*)d_in[9], (const u16*)d_in[10], (const u16*)d_in[11], (u16*)d_out, (char*)d_ws);
}
```

```cpp
#define TOUCH
#include <hip/hip_runtime.h>
#include <stdint.h>

typedef unsigned short u16;
typedef unsigned int u32;
typedef __attribute__((ext_vector_type(2))) __bf16 bf16x2;
typedef __attribute__((ext_vector_type(4))) float f32x4;
typedef __attribute__((ext_vector_type(2))) float f32x2;
typedef __attribute__((ext_vector_type(4))) u32 u32x4;
typedef __attribute__((ext_vector_type(2))) u32 u32x2;

constexpr int S_ = 2048, D_ = 640, F_ = 1536;
constexpr int TP_ = S_ + 64;
constexpr int NMOD = 6 * D_;
constexpr int MODP = 5;
constexpr int CWG = 160;

__device__ __forceinline__ float bflo(u32 w) { return __uint_as_float(w << 16); }
__device__ __forceinline__ float bfhi(u32 w) { return __uint_as_float(w & 0xffff0000u); }
__device__ __forceinline__ u32 pack_bf16(float lo, float hi) {
  bf16x2 v; v[0] = (__bf16)lo; v[1] = (__bf16)hi; return __builtin_bit_cast(u32, v);
}
__device__ __forceinline__ float rbf(float x) { __bf16 b = (__bf16)x; return (float)b; }
#define SCHED() __builtin_amdgcn_sched_barrier(0)
template <int V> struct IC { static constexpr int value = V; };
template <int B, int E, class Fn> __device__ __forceinline__ void static_for(Fn&& f) {
  if constexpr (B < E) { f(IC<B>{}); static_for<B + 1, E>(f); }
}
__device__ __forceinline__ void mfma(f32x4& c, u32x4 a, u32x4 b) {
  asm volatile("v_mfma_f32_16x16x32_bf16 %0, %1, %2, %0" : "+v"(c) : "v"(a), "v"(b));
}
__device__ __forceinline__ void mfma0(f32x4& c, u32x4 a, u32x4 b) {
  asm volatile("v_mfma_f32_16x16x32_bf16 %0, %1, %2, 0" : "=&v"(c) : "v"(a), "v"(b));
}
template <bool FIRST> __device__ __forceinline__ void mfma_(f32x4& c, u32x4 a, u32x4 b) { if constexpr (FIRST) mfma0(c, a, b); else mfma(c, a, b); }
__device__ __forceinline__ u32 alignbit(u32 hi, u32 lo, u32 sh) {
  u32 d; asm volatile("v_alignbit_b32 %0, %1, %2, %3" : "=v"(d) : "v"(hi), "v"(lo), "v"(sh)); return d;
}
template <int OFF> __device__ __forceinline__ void ds_tr(u32x2& dst, unsigned addr) {
  asm volatile("ds_read_b64_tr_b16 %0, %1 offset:%2" : "=v"(dst) : "v"(addr), "i"(OFF) : "memory");
}
template <int OFF> __device__ __forceinline__ void ds_b32(u32& dst, unsigned addr) {
  asm volatile("ds_read_b32 %0, %1 offset:%2" : "=v"(dst) : "v"(addr), "i"(OFF) : "memory");
}
template <int OFF> __device__ __forceinline__ void ds_b64(u32x2& dst, unsigned addr) {
  asm volatile("ds_read_b64 %0, %1 offset:%2" : "=v"(dst) : "v"(addr), "i"(OFF) : "memory");
}
template <int OFF> __device__ __forceinline__ void ds_b128(u32x4& dst, unsigned addr) {
  asm volatile("ds_read_b128 %0, %1 offset:%2" : "=v"(dst) : "v"(addr), "i"(OFF) : "memory");
}
template <int OFF> __device__ __forceinline__ void ds_w128(unsigned addr, f32x4 v) {
  asm volatile("ds_write_b128 %0, %1 offset:%2" :: "v"(addr), "v"(v), "i"(OFF) : "memory");
}
template <int OFF> __device__ __forceinline__ void gld128(u32x4& dst, unsigned voff, const char* sb) {
  asm volatile("global_load_dwordx4 %0, %1, %2 offset:%3 sc1" : "=v"(dst) : "v"(voff), "s"(sb), "i"(OFF) : "memory");
}
__device__ __forceinline__ void gld32(u32& dst, unsigned voff, const char* sb) {
  asm volatile("global_load_dword %0, %1, %2 sc1" : "=v"(dst) : "v"(voff), "s"(sb) : "memory");
}
template <int LOFF> __device__ __forceinline__ void dma16(unsigned ldsb, unsigned voff, const char* sb) {
  asm volatile("s_add_u32 m0, %0, %3\n\ts_nop 0\n\tglobal_load_lds_dwordx4 %1, %2 sc1" :: "s"(ldsb), "v"(voff), "s"(sb), "i"(LOFF) : "memory", "scc");
}
template <int LOFF> __device__ __forceinline__ void dma16x4(unsigned ldsb, unsigned v0, unsigned v1, const char* sb) {
  asm volatile("s_add_u32 m0, %0, %4\n\ts_nop 0\n\tglobal_load_lds_dwordx4 %1, %3 sc1\n\tglobal_load_lds_dwordx4 %2, %3 offset:1024 sc1\n\t"
               "global_load_lds_dwordx4 %1, %3 offset:2048 sc1\n\tglobal_load_lds_dwordx4 %2, %3 offset:3072 sc1"
               :: "s"(ldsb), "v"(v0), "v"(v1), "s"(sb), "i"(LOFF) : "memory", "scc");
}
__device__ __forceinline__ void sld32(u32& dst, const void* p) { asm volatile("s_load_dword %0, %1, 0x0" : "=s"(dst) : "s"(p) : "memory"); }
template <int N> __device__ __forceinline__ void wait_vm() { asm volatile("s_waitcnt vmcnt(%0)" :: "i"(N) : "memory"); }
template <int N> __device__ __forceinline__ void wait_lgkm() { asm volatile("s_waitcnt lgkmcnt(%0)" :: "i"(N) : "memory"); }
__device__ __forceinline__ void bar() { asm volatile("s_barrier" ::: "memory"); }
template <class T> __device__ __forceinline__ void pin(T& a) { asm volatile("" : "+v"(a)); }
__device__ __forceinline__ u32x4 cat(u32x2 a, u32x2 b) { return u32x4{a[0], a[1], b[0], b[1]}; }

typedef __amdgpu_buffer_rsrc_t rsrc_t;
__device__ __forceinline__ rsrc_t mk_rsrc(const void* p, int bytes) { return __builtin_amdgcn_make_buffer_rsrc((void*)p, 0, bytes, 0x00020000); }
template <int CTRL> __device__ __forceinline__ float dpp_add(float x) {
  return x + __builtin_bit_cast(float, __builtin_amdgcn_mov_dpp(__builtin_bit_cast(int, x), CTRL, 0xf, 0xf, true));
}
constexpr int DPP_XOR1 = 0xB1, DPP_XOR2 = 0x4E, DPP_HMIRROR = 0x141, DPP_ROR8 = 0x128;
__device__ __forceinline__ float sum8(float x) { return dpp_add<DPP_HMIRROR>(dpp_add<DPP_XOR2>(dpp_add<DPP_XOR1>(x))); }
__device__ __forceinline__ float tred32(float a, float b) {
  auto r = __builtin_amdgcn_permlane32_swap(__float_as_uint(a), __float_as_uint(b), false, false);
  return __uint_as_float(r[0]) + __uint_as_float(r[1]);
}
__device__ __forceinline__ float tred16(float a, float b) {
  auto r = __builtin_amdgcn_permlane16_swap(__float_as_uint(a), __float_as_uint(b), false, false);
  return __uint_as_float(r[0]) + __uint_as_float(r[1]);
}
__device__ __forceinline__ float rdl(float x, int l) { return __int_as_float(__builtin_amdgcn_readlane(__float_as_int(x), l)); }

typedef unsigned long long u64;
#define GAS __attribute__((address_space(1)))
constexpr int SC1 = 16;
#define A0_DELAY 16
#define F_LATE 1
#define A1_DELAY 4
#define A3_DELAY 24
#define AU_DELAY 16
#define A_DELAY 2
#define FXE 7
#define PE 4
#define NT 2
#define NTS " nt"
template <int OFF> __device__ __forceinline__ void gld128a(u32x4& dst, unsigned voff, const char* sb) {
  asm volatile("global_load_dwordx4 %0, %1, %2 offset:%3 sc1" : "=a"(dst) : "v"(voff), "s"(sb), "i"(OFF) : "memory");
}
template <bool FIRST> __device__ __forceinline__ void mfma_a(f32x4& c, u32x4 a, u32x4 b) {
  if constexpr (FIRST) asm volatile("v_mfma_f32_16x16x32_bf16 %0, %1, %2, 0" : "=&v"(c) : "a"(a), "v"(b));
  else asm volatile("v_mfma_f32_16x16x32_bf16 %0, %1, %2, %0" : "+v"(c) : "a"(a), "v"(b));
}
typedef __attribute__((ext_vector_type(8))) __bf16 bf16x8;
__device__ __forceinline__ f32x4 mfma_b(u32x4 a, u32x4 b, f32x4 c) {
  return __builtin_amdgcn_mfma_f32_16x16x32_bf16(__builtin_bit_cast(bf16x8, a), __builtin_bit_cast(bf16x8, b), c, 0, 0, 0);
}
template <int LOFF> __device__ __forceinline__ void dma16p(unsigned ldsb, unsigned voff, const char* sb) {
  asm volatile("s_add_u32 m0, %0, %3\n\ts_nop 0\n\tglobal_load_lds_dwordx4 %1, %2" NTS :: "s"(ldsb), "v"(voff), "s"(sb), "i"(LOFF) : "memory", "scc");
}
template <int LOFF> __device__ __forceinline__ void dma16x4p(unsigned ldsb, unsigned v0, const char* sb) {
  asm volatile("s_add_u32 m0, %0, %3\n\ts_nop 0\n\tglobal_load_lds_dwordx4 %1, %2" NTS "\n\tglobal_load_lds_dwordx4 %1, %2 offset:1024" NTS "\n\t"
               "global_load_lds_dwordx4 %1, %2 offset:2048" NTS "\n\tglobal_load_lds_dwordx4 %1, %2 offset:3072" NTS
               :: "s"(ldsb), "v"(v0), "s"(sb), "i"(LOFF) : "memory", "scc");
}

#undef DBGC
#define STAMP(I)
#define STAMP_DECL()
#define STAMP_END()

__device__ __forceinline__ void gld128p(u32x4& dst, unsigned voff, const char* sb) {
  asm volatile("global_load_dwordx4 %0, %1, %2" NTS : "=v"(dst) : "v"(voff), "s"(sb) : "memory");
}
template <int OFF> __device__ __forceinline__ void gld32p(u32& dst, unsigned voff, const char* sb) {
  asm volatile("global_load_dword %0, %1, %2 offset:%3" NTS : "=v"(dst) : "v"(voff), "s"(sb), "i"(OFF) : "memory");
}
template <int OFF> __device__ __forceinline__ void gld16p(u32& dst, unsigned voff, const char* sb) {
  asm volatile("global_load_ushort %0, %1, %2 offset:%3" : "=v"(dst) : "v"(voff), "s"(sb), "i"(OFF) : "memory");
}
#define CPF_DONE() asm volatile("" :: "v"(cpf))
#define X_EARLY 0
template <int OFF> __device__ __forceinline__ void dma16n(unsigned voff, const char* sb) {
  asm volatile("global_load_lds_dwordx4 %0, %1 offset:%2 sc1" :: "v"(voff), "s"(sb), "i"(OFF) : "memory");
}
__device__ __forceinline__ u64 v_ne64(u32x2 v, u64 tag) { u64 m; asm volatile("v_cmp_ne_u64 %0, %1, %2" : "=s"(m) : "v"(v), "s"(tag)); return m; }
__device__ __forceinline__ float v_lo(u32 w) { float d; asm volatile("v_lshlrev_b32 %0, 16, %1" : "=v"(d) : "v"(w)); return d; }
__device__ __forceinline__ float v_hi(u32 w) { float d; asm volatile("v_and_b32 %0, 0xffff0000, %1" : "=v"(d) : "v"(w)); return d; }
__device__ __forceinline__ float v_fma(float a, float b, float c) { float d; asm volatile("v_fma_f32 %0, %1, %2, %3" : "=v"(d) : "v"(a), "v"(b), "v"(c)); return d; }
__device__ __forceinline__ float v_mul(float a, float b) { float d; asm volatile("v_mul_f32 %0, %1, %2" : "=v"(d) : "v"(a), "v"(b)); return d; }
__device__ __forceinline__ float v_add(float a, float b) { float d; asm volatile("v_add_f32 %0, %1, %2" : "=v"(d) : "v"(a), "v"(b)); return d; }
#define STAMPD(I)
#define STAMPL(I) STAMP(I)
__device__ __forceinline__ float v_relu(float a) { float d; asm volatile("v_max_i32 %0, 0, %1" : "=v"(d) : "v"(a)); return d; }
__device__ __forceinline__ u32 v_cvt(float a, float b) { u32 d; asm volatile("v_cvt_pk_bf16_f32 %0, %1, %2" : "=v"(d) : "v"(a), "v"(b)); return d; }
__device__ __forceinline__ u32 v_perm(u32 a, u32 sel) { u32 d; asm volatile("v_perm_b32 %0, %1, %1, %2" : "=v"(d) : "v"(a), "s"(sel)); return d; }
__device__ __forceinline__ void v_swap32(u32& a, u32& b) { asm volatile("v_permlane32_swap_b32 %0, %1" : "+v"(a), "+v"(b)); }
__device__ __forceinline__ void v_swap16(u32& a, u32& b) { asm volatile("v_permlane16_swap_b32 %0, %1" : "+v"(a), "+v"(b)); }
template <int OFF> __device__ __forceinline__ void ds_f128(f32x4& dst, unsigned addr) {
  asm volatile("ds_read_b128 %0, %1 offset:%2" : "=v"(dst) : "v"(addr), "i"(OFF) : "memory");
}
#define STAMPG(I)
#define STAMPE(I) STAMP(I)
__device__ __forceinline__ void asm_and3(u32& d, u32 a, u32 b, u32 c) { asm volatile("v_bitop3_b32 %0, %1, %2, %3 bitop3:0x80" : "=v"(d) : "v"(a), "v"(b), "v"(c)); }
__device__ __forceinline__ void asm_ne1(unsigned long long& m, u32 a) { asm volatile("v_cmp_ne_u32 %0, 1, %1" : "=s"(m) : "v"(a)); }
__device__ __forceinline__ void ds_w32(unsigned addr, u32 v) { asm volatile("ds_write_b32 %0, %1" :: "v"(addr), "v"(v) : "memory"); }
template <int OP> __device__ __forceinline__ void asm_op1(u32& d, u32 a) {
  if constexpr (OP == 0) asm volatile("v_mul_f32 %0, 0x3acccccd, %1" : "=v"(d) : "v"(a));
  else if constexpr (OP == 1) asm volatile("v_mov_b32 %0, %1" : "=v"(d) : "v"(a));
  else if constexpr (OP == 2) asm volatile("v_max_f32 %0, 0, %1" : "=v"(d) : "v"(a));
  else if constexpr (OP == 3) asm volatile("v_cvt_pk_bf16_f32 %0, 0, %1" : "=v"(d) : "v"(a));
  else if constexpr (OP == 4) asm volatile("v_add_f32 %0, 0x3727c5ac, %1" : "=v"(d) : "v"(a));
  else if constexpr (OP == 5) asm volatile("v_sqrt_f32 %0, %1" : "=v"(d) : "v"(a));
  else asm volatile("v_mov_b32 %0, 0" : "=v"(d));
}
template <int OP> __device__ __forceinline__ void asm_op2(u32& d, u32 a, u32 b) {
  if constexpr (OP == 0) asm volatile("v_add_f32 %0, %1, %2" : "=v"(d) : "v"(a), "v"(b));
  else if constexpr (OP == 1) asm volatile("v_fma_f32 %0, -%1, %1, %2" : "=v"(d) : "v"(a), "v"(b));
  else asm volatile("v_cvt_pk_bf16_f32 %0, -%1, %2" : "=v"(d) : "v"(a), "v"(b));
}
template <int LOFF> __device__ __forceinline__ void xdma0(unsigned ldsb, unsigned voff, u32x4 srd, unsigned& soff, unsigned inc) {
  asm volatile("s_add_u32 m0, %1, %5\n\ts_add_u32 %0, %0, %4\n\tbuffer_load_dwordx4 %2, %3, %0 offen sc1 lds"
               : "+s"(soff) : "s"(ldsb), "v"(voff), "s"(srd), "s"(inc), "i"(LOFF) : "memory", "scc");
}
__device__ __forceinline__ void xdma1(unsigned voff, u32x4 srd, unsigned soff) {
  asm volatile("buffer_load_dwordx4 %0, %1, %2 offen offset:1024 sc1 lds" :: "v"(voff), "s"(srd), "s"(soff) : "memory");
}
template <int OFF> __device__ __forceinline__ void ds_w128u(unsigned addr, u32x4 v) {
  asm volatile("ds_write_b128 %0, %1 offset:%2" :: "v"(addr), "v"(v), "i"(OFF) : "memory");
}
constexpr int W1_STEPS = 21;
constexpr int O_EPOCH = 0;
constexpr int O_FK = 4096;
constexpr int O_FA0 = O_FK + 160 * 256;
constexpr int O_FW = O_FA0 + 8 * 12 * 128;
constexpr int O_FU = O_FW + 8 * 12 * 128;
constexpr int O_FX = O_FU + 8 * 14 * 128;
constexpr int O_F2 = O_FX + 8 * 32 * 128;
constexpr int O_F3 = O_F2 + 8 * 32 * 128;
constexpr int O_SG = O_F3 + 256 * 128;
constexpr int O_MODP = O_SG + 32 * 8192;
constexpr int O_ST1 = O_MODP + MODP * NMOD * 4;
constexpr int O_KVG = O_ST1 + 16384;
constexpr int O_XG = O_KVG + S_ * D_ * 2;
constexpr int O_X2T = O_XG + S_ * D_ * 2;
constexpr int O_W1F = O_X2T + TP_ * D_ * 2;
constexpr int O_W2F = O_W1F + 64 * W1_STEPS * 3 * 1024;
constexpr int O_H2F = O_W2F + 8 * 48 * 5 * 1024;
constexpr int WS_BYTES = O_H2F + S_ * F_ * 2;
static_assert(O_MODP % 256 == 0 && O_ST1 % 256 == 0 && O_KVG % 256 == 0 && O_X2T % 256 == 0 && O_W1F % 256 == 0, "alignment");

constexpr int SPIN_CAP = 1 << 16;
constexpr unsigned CODE_BYTES = 37 * 1024;
__device__ __forceinline__ void st_flag(char* ws, int off, u64 tag) {
  __hip_atomic_store((GAS u64*)(ws + off), tag, __ATOMIC_RELAXED, __HIP_MEMORY_SCOPE_AGENT);
}
__device__ __forceinline__ bool tag_ok(u32x4 v, u64 tag) {
  const u32 lo = (u32)tag, hi = (u32)(tag >> 32);
  return ((v[0] ^ lo) | (v[1] ^ hi) | (v[2] ^ lo) | (v[3] ^ hi)) == 0;
}
template <int NL, int SLEEP> __device__ __forceinline__ void poll(rsrc_t rw, const int (&off)[NL], u64 tag) {
#pragma unroll 1
  for (int sp = 0; sp < SPIN_CAP; ++sp) {
    bool ok = true;
#pragma unroll
    for (int i = 0; i < NL; ++i) ok &= tag_ok(__builtin_amdgcn_raw_buffer_load_b128(rw, off[i], 0, SC1), tag);
    if (__all(ok)) break;
    if (SLEEP) __builtin_amdgcn_s_sleep(SLEEP);
    asm volatile("" ::: "memory");
  }
  asm volatile("" ::: "memory");
}
__device__ __forceinline__ u64 next_tag(u32x2 ep) {
  u32 lo, hi;
  asm volatile("s_waitcnt lgkmcnt(0)\n\ts_add_u32 %0, %2, 1\n\ts_addc_u32 %1, %3, 0" : "=&s"(lo), "=&s"(hi) : "s"(ep[0]), "s"(ep[1]) : "memory", "scc");
  return ((u64)hi << 32) | lo;
}
__device__ __forceinline__ void drain_bar() { asm volatile("s_waitcnt vmcnt(0) lgkmcnt(0)\n\ts_barrier" ::: "memory"); }

constexpr int FC_BYTES = 2064 * 2;
constexpr int CH_BYTES = 4 * FC_BYTES;
constexpr int CKV_OFF = 66048;
constexpr int CX_OFF = CKV_OFF + 2048 + 16384, CS_OFF = CX_OFF + 16384;
constexpr int conv_gap(int a, int b) { int n = 0; for (int i = 0; i < a; ++i) n += 8 - i; return n + b; }
struct CSched { int rd[290]; int op[290]; };
constexpr int CBE = PE - 2;
constexpr CSched conv_make() {
  CSched S{};
  int rd = 0, op = 0, landed = 0;
  for (int g = 0; g < 288; ++g) {
    const int e = 7 - g / 36, n = g % 36;
    S.rd[g] = rd; S.op[g] = op;
    const bool open = e < CBE || (e == CBE && n >= 24);
    bool ds = e > 0 && n < 24;
    if (open && !ds && rd < 24 && (e == 0 || n < 33)) { ++rd; ds = true; }
    if (e > 0 ? n == 34 : n == 12) landed = rd;
    int k = ds ? 1 : 2;
    while (k-- > 0 && op < (landed / 3) * 16) ++op;
  }
  S.rd[288] = rd; S.op[288] = op;
  return S;
}
constexpr CSched CS = conv_make();
static_assert(CS.rd[288 - 36 + 10] == 24, "reads must be out before the last wait");
constexpr int G_XCHK = 36 * (7 - PE) + 24;
struct CVSched { int kind[290]; int idx[290]; };
constexpr CVSched cv_make() {
  CVSched S{};
  for (int g = 0; g < 288; g += 4) {
    const int dx = g - (G_XCHK + 4);
    if (g >= G_XCHK - 44 && g <= G_XCHK - 36) { S.kind[g] = 2; S.idx[g] = (g - (G_XCHK - 44)) / 4; }
    else if (dx >= 0 && dx < 32) { S.kind[g] = 3 + dx / 16; S.idx[g] = (dx / 4) & 3; }
    else if (dx == 32) S.kind[g] = 5;
  }
  return S;
}
constexpr CVSched CV = cv_make();
constexpr int cv_younger(int g, int kind, int idx) {
  int n = -1;
  for (int i = 0; i <= g; ++i) { if (n >= 0 && CV.kind[i]) ++n; if (CV.kind[i] == kind && CV.idx[i] == idx) n = 0; }
  return n;
}
static_assert(G_XCHK % 4 == 0, "conv request schedule");
constexpr int G1_R = 7;
constexpr int G1_SLOTS = 8;
constexpr int G1_LA = 4;
constexpr int G1_SP = G1_SLOTS * 8192;
constexpr int G1_WPRE = 6;
constexpr int g1_ks(int t) { return t < 128 ? t >> 3 : 16 + (t & 3); }
constexpr int g1_j(int t) { return t < 128 ? t & 7 : (t - 128) >> 2; }
struct G1Sched { int kind[256]; int idx[256]; int first[162]; int n; };
#define T_SPOLL 85
#define XPRE 4
constexpr int T_SST = 37;
#define WA 6
#define WD_G1 60
#define WD_TEND 148
constexpr int T_WPOLL = 1, T_WCHK = 21;
constexpr G1Sched g1_make() {
  G1Sched S{};
  int n = 0, x = 0, w = 3 * (G1_WPRE + WA), wd = 0, rs = 0, credit = 0;
  for (; x < 2 * XPRE; ++x) { S.kind[n] = 0; S.idx[n] = x; ++n; }
  for (int t = 0; t < 160; ++t) {
    S.first[t] = n;
    const int ks = t < 128 ? t >> 3 : 16;
    credit += 3;
    if (credit > 6) credit = 6;
    int kind = -1, idx = 0;
    const bool xok = x < 40 && (x >> 1) <= ks + ((t & 7) >= 4 ? 7 : 6), wok = w < 60 && w / 3 <= ks + G1_R - 1;
    const int alive = 3 + 3 * (ks < G1_WPRE ? WA : (ks < G1_WPRE + WA ? G1_WPRE + WA - ks : 0));
    if (t == T_WPOLL) kind = 4;
    else if (t == T_SST) kind = 5;
    else if (t == T_SPOLL || t == T_SPOLL + 1) { kind = 6; idx = t - T_SPOLL; }
    else if (t >= 135 && (t & 3) == 3) { kind = 7; idx = (t - 135) >> 2; }
    else if (credit >= 4) {
      if (xok && (x >> 1) <= ks + 5 + (ks > 10) * 2 && (!wok || (x >> 1) < w / 3)) { kind = 0; idx = x++; }
      else if (wok) { kind = 1; idx = w++; }
      else if (rs < 4 && !(x & 1)) { kind = 3; idx = rs++; }
      else if (t > T_WCHK && t < WD_TEND && wd < WD_G1 && wd + alive < 63) { kind = 2; idx = wd++; }
      else if (xok) { kind = 0; idx = x++; }
    }
    if (kind >= 0) { S.kind[n] = kind; S.idx[n] = idx; ++n; credit -= 4; }
  }
  S.first[160] = n; S.n = n;
  return S;
}
constexpr G1Sched G1S = g1_make();
constexpr int g1_pos(int kind, int idx) { for (int i = 0; i < G1S.n; ++i) if (G1S.kind[i] == kind && G1S.idx[i] == idx) return i; return -1; }
constexpr int g1_wd_count() { int c = 0; for (int i = 0; i < G1S.n; ++i) c += G1S.kind[i] == 2; return c; }
constexpr int G1_WDN = g1_wd_count();
constexpr int imax(int a, int b) { return a > b ? a : b; }
constexpr int g1_need(int t) {
  const int s = t == 124 ? 19 : (t + 4) / 8;
  int p = imax(g1_pos(0, 2 * s + 1), g1_pos(1, 3 * s + 2));
  if (t == 108) p = imax(p, g1_pos(6, 1));
  if (t == T_WCHK - 1) p = imax(p, g1_pos(4, 0));
  return p;
}
static_assert(g1_pos(1, 59) >= 0 && g1_pos(0, 39) >= 0 && g1_pos(7, 6) >= 0, "schedule incomplete");
constexpr int G2_R = 28, G2_PRE = 16;
constexpr int XR_OFF = 69632;
constexpr int LDS_BYTES = 118 * 1024;
constexpr int AG_OFF = 0, AU_OFF = 20480, AD_OFF = 40960, AM_OFF = 61440;

__global__ __launch_bounds__(256, 1) void k_fused(const u16* __restrict__ xIn, const u16* __restrict__ kIn, const u16* __restrict__ vIn,
                                                  const u16* __restrict__ shk, const u16* __restrict__ ssmk, const u16* __restrict__ ssmD,
                                                  const u16* __restrict__ wg, const u16* __restrict__ wu, const u16* __restrict__ wd,
                                                  const u16* __restrict__ cIn, const u16* __restrict__ wmod,
                                                  u16* __restrict__ out, char* __restrict__ ws) {
  __shared__ __attribute__((aligned(16))) char smem[LDS_BYTES];
  STAMP_DECL();
  STAMP(0);
  u32 cpf = 0;
  if (blockIdx.x >= 240) {
    uint64_t pc; asm volatile("s_getpc_b64 %0" : "=s"(pc));
    const unsigned co = (((blockIdx.x >> 3) & 1) * 256 + threadIdx.x) * 128;
    asm volatile("global_load_dword %0, %1, %2" : "=v"(cpf) : "v"(co < CODE_BYTES ? co : 0u), "s"(pc & ~(uint64_t)127) : "memory");
  }
  asm volatile("" :: "s"(xIn), "s"(kIn), "s"(vIn), "s"(shk), "s"(ssmk), "s"(ssmD), "s"(wg), "s"(wu), "s"(wd), "s"(cIn), "s"(wmod), "s"(out), "s"(ws));
  u32x2 ep;
  asm volatile("s_load_dwordx2 %0, %1, 0x0" : "=s"(ep) : "s"(ws) : "memory");
  u64 tag;
  const int tid = threadIdx.x, lane = tid & 63;
  const int wv = __builtin_amdgcn_readfirstlane(tid >> 6);
  const int b = blockIdx.x;
  const unsigned sbase = (unsigned)(size_t)smem;
  const unsigned l16 = lane * 16;
  const rsrc_t rw = mk_rsrc(ws, WS_BYTES);
#define TSTR 0
  u32 tch = 0;
  if (wv == 3) {
    constexpr int PER = (O_ST1 + 16384 - O_FK + 256 * 128 - 1) / (256 * 128);
    static_assert(PER <= 64, "one request per workgroup");
    asm volatile("global_load_dword %0, %1, %2" : "=v"(tch) : "v"(lane < PER || !TSTR ? O_FK + (b * PER + (lane < PER ? lane : 0)) * 128 : min((lane - PER) * TSTR + b * 128, WS_BYTES - 4)), "s"(ws) : "memory");
  }
  int bs = blockIdx.x;
  asm volatile("" : "+s"(bs));
  const int xcd = bs & 7, idx = bs >> 3;
  const int mt = (xcd & 1) * 8 + (idx & 7), nc = (xcd >> 1) * 4 + (idx >> 3);
  const int M = mt * 2 + ((nc >> 2) & 1), N = (nc >> 3) * 4 + (nc & 3);
  u32x4 WD[60];
  const char* wdp = ws + O_W2F + (N * 48 + wv * 12) * 5120 + 4096;
  const unsigned l16b = l16 + 8192;
  auto issue_wd = [&](auto I) { constexpr int i = I.value; gld128a<((i & 7) - 4) * 1024>(WD[i], (i & 8) ? l16b : l16, wdp + (i >> 4) * 16384); };
  if (b >= CWG) {
    const int j = b - CWG;
    const int r8 = lane >> 3, cp = lane & 7;
    const int ks2 = j >> 1, hf = j & 1;
    const int ug = j % 24, kq = j / 24;
    float* LC = (float*)(smem + AM_OFF);
    u16* LS = (u16*)(smem + AM_OFF + 640);
    const int gt = wv * 96 + j;
    const bool gact = gt < 300, urgent = gt < 100;
    const int gq = gt / MODP, gk = gt - gq * MODP, gcol = urgent ? 30 + gq : (gq < 50 ? gq - 20 : gq);
    u32x4 wm[16]; u32 cv[2];
    if (!urgent) __builtin_amdgcn_s_sleep(A0_DELAY);
    if (gact) {
      static_for<0, 2>([&](auto I) { gld16p<I.value * 128>(cv[I.value], lane * 2, (const char*)(cIn + gk * 128)); });
      static_for<0, 16>([&](auto I) { gld128p(wm[I.value], r8 * (NMOD * 2) + cp * 16, (const char*)(wmod + (size_t)(gk * 128 + I.value * 8) * NMOD + gcol * 64)); });
    }
    tag = next_tag(ep);
    if (gact) {
      wait_vm<16>();
      float sl[2], sv[16];
#pragma unroll
      for (int i = 0; i < 2; ++i) { pin(cv[i]); const float x = bflo(cv[i]); sl[i] = rbf(x * __builtin_amdgcn_rcpf(1.f + __expf(-x))); }
#pragma unroll
      for (int it = 0; it < 16; ++it) { sv[it] = __shfl(sl[it >> 3], (it & 7) * 8 + r8); pin(sv[it]); }
      f32x2 acc[4];
      static_for<0, 4>([&](auto G) {
        constexpr int g = G.value;
        wait_vm<12 - 4 * g>();
#pragma unroll
        for (int it = 4 * g; it < 4 * g + 4; ++it) {
          pin(wm[it]);
          const f32x2 s2 = {sv[it], sv[it]};
#pragma unroll
          for (int q = 0; q < 4; ++q) {
            const f32x2 w2 = {bflo(wm[it][q]), bfhi(wm[it][q])};
            acc[q] = it ? __builtin_elementwise_fma(s2, w2, acc[q]) : s2 * w2;
          }
        }
#pragma unroll
        for (int q = 0; q < 4; ++q) pin(acc[q]);
      });
      const float a0 = tred16(tred32(acc[0][0], acc[2][0]), tred32(acc[1][0], acc[3][0]));
      const float a1 = tred16(tred32(acc[0][1], acc[2][1]), tred32(acc[1][1], acc[3][1]));
      const float e0 = dpp_add<DPP_ROR8>(a0), e1 = dpp_add<DPP_ROR8>(a1);
      __builtin_amdgcn_raw_buffer_store_b32(__float_as_uint((lane & 8) ? e1 : e0), rw, O_MODP + (gk * NMOD + gcol * 64 + cp * 8 + r8) * 4, 0, SC1);
      if (urgent) {
        wait_vm<0>();
        if (lane < 8) st_flag(ws, O_FU + (lane * 14 + (gt >> 3)) * 128 + (gt & 7) * 8, tag);
        __builtin_amdgcn_s_sleep(AU_DELAY);
      }
    }
    STAMPE(1);
    {
      const unsigned vg = r8 * (F_ * 2) + ((cp ^ ((r8 >> 1) * 2)) * 16);
      const char* gs = (const char*)(wg + (size_t)(kq * 160 + wv * 40) * F_ + ug * 64);
      const char* us = (const char*)(wu + (size_t)(kq * 160 + wv * 40) * F_ + ug * 64);
      static_for<0, 5>([&](auto I) {
        dma16p<AG_OFF + I.value * 1024>(sbase + wv * 5120, vg, gs + I.value * (8 * F_ * 2));
        dma16p<AU_OFF + I.value * 1024>(sbase + wv * 5120, vg ^ 16, us + I.value * (8 * F_ * 2));
      });
    }
    wait_vm<10>();
    bar();
    if (tid < 8) st_flag(ws, O_FA0 + (tid * 12 + (j >> 3)) * 128 + (j & 7) * 8, tag);
    STAMPE(2);
    const int kg = lane >> 4, q4 = (lane & 15) >> 2, p4 = lane & 3;
    if (wv == 0) {
      const int po[1] = {O_FU + ((b & 7) * 14 + (lane < 50 ? lane >> 2 : 0)) * 128 + (lane & 3) * 16};
      poll<1, 1>(rw, po, tag);
      STAMPE(3);
      f32x4 mm[2];
      const int lo = lane < 40 ? lane * 16 : 0;
#pragma unroll
      for (int i = 0; i < 2; ++i) {
        mm[i] = f32x4{0.f, 0.f, 0.f, 0.f};
#pragma unroll
        for (int p = 0; p < MODP; ++p)
          mm[i] += __builtin_bit_cast(f32x4, __builtin_amdgcn_raw_buffer_load_b128(rw, lo, O_MODP + (p * NMOD + (3 + i) * D_ + kq * 160) * 4, SC1));
      }
      if (lane < 40) {
        *(u32x2*)(LS + lane * 4) = u32x2{pack_bf16(mm[0][0], mm[0][1]), pack_bf16(mm[0][2], mm[0][3])};
        *(f32x4*)(LC + lane * 4) = f32x4{rbf(1.f + rbf(mm[1][0])), rbf(1.f + rbf(mm[1][1])), rbf(1.f + rbf(mm[1][2])), rbf(1.f + rbf(mm[1][3]))};
      }
    }
    drain_bar();
    STAMPE(4);
    CPF_DONE();
    {
      const char* ds = (const char*)(wd + (size_t)(ks2 * 32 + wv * 8) * D_ + hf * 320);
      static_for<0, 5>([&](auto I) { dma16p<AD_OFF + I.value * 4096>(sbase + wv * 1024, r8 * (D_ * 2) + cp * 16, ds + I.value * 128); });
    }
    {
      const int rr = 4 * kg + q4;
      u32x2 lo[10], hi[10], sbl[5], sbh[5]; u32x4 cl[5], ch[5];
      unsigned ad[2];
#pragma unroll
      for (int t = 0; t < 2; ++t) ad[t] = sbase + (p4 >> 1) * AU_OFF + rr * 128 + (((wv * 2 + t) ^ (((rr >> 1) & 3) * 2 + (p4 >> 1))) * 16) + (p4 & 1) * 8;
      const unsigned ac = sbase + AM_OFF + kg * 16, as = sbase + AM_OFF + 640 + kg * 8;
      auto rd = [&](auto I) {
        constexpr int i = I.value;
        ds_b128<i * 128>(cl[i], ac); ds_b128<i * 128 + 64>(ch[i], ac);
        ds_tr<i * 4096>(lo[2 * i], ad[0]); ds_tr<i * 4096 + 2048>(hi[2 * i], ad[0]); ds_tr<i * 4096>(lo[2 * i + 1], ad[1]); ds_tr<i * 4096 + 2048>(hi[2 * i + 1], ad[1]);
        ds_b64<i * 64>(sbl[i], as); ds_b64<i * 64 + 32>(sbh[i], as);
      };
      rd(IC<0>{});
      const int src = ((lane & 48) + ((lane & 1) * 8 + ((lane & 15) >> 1))) * 4;
      const u32x4 ones = {0x3f803f80u, 0x3f803f80u, 0x3f803f80u, 0x3f803f80u};
      f32x4 aU[2] = {}, aW[2] = {};
      int so[2];
#pragma unroll
      for (int t = 0; t < 2; ++t) { const int T = ug * 8 + wv * 2 + t, sw3 = T / 3; so[t] = O_W1F + ((sw3 * W1_STEPS + kq * 5) * 3 + T - sw3 * 3) * 1024; }
      static_for<0, 5>([&](auto KS) {
        constexpr int ks = KS.value;
        wait_lgkm<0>();
        pin(cl[ks]); pin(ch[ks]); pin(sbl[ks]); pin(sbh[ks]);
        if constexpr (ks < 4) rd(IC<ks + 1>{});
        u32x4 pm[2];
#pragma unroll
        for (int t = 0; t < 2; ++t) {
          pin(lo[2 * ks + t]); pin(hi[2 * ks + t]);
          const u32x4 raw = cat(lo[2 * ks + t], hi[2 * ks + t]);
          u32x4 sc;
#pragma unroll
          for (int q = 0; q < 2; ++q) {
            sc[q] = pack_bf16(bflo(raw[q]) * __uint_as_float(cl[ks][2 * q]), bfhi(raw[q]) * __uint_as_float(cl[ks][2 * q + 1]));
            sc[2 + q] = pack_bf16(bflo(raw[2 + q]) * __uint_as_float(ch[ks][2 * q]), bfhi(raw[2 + q]) * __uint_as_float(ch[ks][2 * q + 1]));
          }
#pragma unroll
          for (int q = 0; q < 4; ++q) pm[t][q] = (u32)__builtin_amdgcn_ds_bpermute(src, sc[q]);
          aW[t] = mfma_b(raw, cat(sbl[ks], sbh[ks]), aW[t]);
          aU[t] = mfma_b(sc, ones, aU[t]);
        }
#pragma unroll
        for (int t = 0; t < 2; ++t) __builtin_amdgcn_raw_buffer_store_b128(pm[t], rw, l16, so[t] + ks * 3072, SC1);
      });
      const f32x4 su = (lane & 4) ? aU[1] : aU[0], sw = (lane & 4) ? aW[1] : aW[0];
      const float U = p4 == 0 ? su[0] : p4 == 1 ? su[1] : p4 == 2 ? su[2] : su[3];
      const float W = p4 == 0 ? sw[0] : p4 == 1 ? sw[1] : p4 == 2 ? sw[2] : sw[3];
      const float u1 = rbf(U), u2 = rbf(U - u1), u3 = U - u1 - u2;
      const float w1 = rbf(W), w2 = rbf(W - w1), w3 = W - w1 - w2;
      const int crow = 2 * (4 * (kg & 1) + p4) + (kg >> 1);
      __builtin_amdgcn_raw_buffer_store_b128(u32x4{pack_bf16(u1, w1), pack_bf16(u2, w2), pack_bf16(u3, w3), 0u}, rw,
                                             (lane & 8) ? WS_BYTES : ((lane & 4) ? so[1] : so[0]) + (20 - kq * 5) * 3072 + (kq * 16 + crow) * 16, 0, SC1);
    }
    STAMPE(5);
  } else {
    const int d = (b >> 1) * 8 + (b & 1) + 2 * wv;
    const int row = lane & 15, kg = lane >> 4;
    const int tt = b & 31, ct = b >> 5;
    const int ov = ((lane >> 2) * 2048 + (lane & 3)) * 16, os = (ct * 16 * 2048 + (tt * 8 + wv * 2) * 4) * 16;
    const int stg = b * 2 + (wv - 2);
    const bool xact = wv >= 2 && b < 128;
    u32 xx[5][8];
    u32 draw;
    unsigned aAddr, bA[8];
    const unsigned fl = sbase + wv * CH_BYTES;
    {
      const rsrc_t rk = mk_rsrc(kIn, S_ * D_ * 2), rv = mk_rsrc(vIn, S_ * D_ * 2);
      const int vo = ct * 256 + lane * 4, so = (tt * 64 + wv * 16) * 1280;
      const int sh0 = so ? so - 3 * 1280 : 0;
      u32 kk[19], vv[16];
      const u32x4 taps = *(const u32x4*)(shk + (ct * 64 + lane) * 8);
#pragma unroll
      for (int q = 0; q < 3; ++q) kk[q] = __builtin_amdgcn_raw_buffer_load_b32(rk, vo + q * 1280, sh0, NT);
#pragma unroll
      for (int q = 0; q < 8; ++q) kk[3 + q] = __builtin_amdgcn_raw_buffer_load_b32(rk, vo + (q & 1) * 1280, so + (q & 14) * 1280, NT);
#pragma unroll
      for (int q = 0; q < 8; ++q) vv[q] = __builtin_amdgcn_raw_buffer_load_b32(rv, vo + (q & 1) * 1280, so + (q & 14) * 1280, NT);
#pragma unroll
      for (int q = 8; q < 16; ++q) kk[3 + q] = __builtin_amdgcn_raw_buffer_load_b32(rk, vo + (q & 1) * 1280, so + (q & 14) * 1280, NT);
#pragma unroll
      for (int q = 8; q < 16; ++q) vv[q] = __builtin_amdgcn_raw_buffer_load_b32(rv, vo + (q & 1) * 1280, so + (q & 14) * 1280, NT);
      SCHED();
      SCHED();
      sld32(draw, (const u32*)ssmD + (d >> 1));
      aAddr = fl + (row & 3) * FC_BYTES + 2 * (64 * kg + (row & ~3));
      pin(aAddr);
#pragma unroll
      for (int e = 0; e < 8; ++e) { const int g = 32 + 2 * row + 1 - 8 * kg - e; bA[e] = sbase + CKV_OFF + 16 * ((4 * g + wv) ^ ((g >> 2) & 7)); pin(bA[e]); }
      *(u32x2*)(smem + CKV_OFF + tid * 8) = u32x2{0, 0};
      if (lane < 13) *(u32*)(smem + wv * CH_BYTES + (lane < 7 ? FC_BYTES + lane * 4 : 3 * FC_BYTES + (lane - 7) * 4)) = 0;
      SCHED();
      if (so == 0) { kk[0] = 0; kk[1] = 0; kk[2] = 0; }
      const f32x2 w0 = {bflo(taps[0]), bflo(taps[2])}, w1 = {bfhi(taps[0]), bfhi(taps[2])};
      const f32x2 w2 = {bflo(taps[1]), bflo(taps[3])}, w3 = {bfhi(taps[1]), bfhi(taps[3])};
#pragma unroll
      for (int r = 0; r < 2; ++r) {
        f32x2 kv[8];
#pragma unroll
        for (int q = 0; q < 8; ++q) {
          const int i = 8 * r + q;
          f32x2 a = f32x2{bflo(kk[i]), bfhi(kk[i])} * w3;
          a = __builtin_elementwise_fma(f32x2{bflo(kk[i + 1]), bfhi(kk[i + 1])}, w2, a);
          a = __builtin_elementwise_fma(f32x2{bflo(kk[i + 2]), bfhi(kk[i + 2])}, w1, a);
          a = __builtin_elementwise_fma(f32x2{bflo(kk[i + 3]), bfhi(kk[i + 3])}, w0, a);
          kv[q] = a * f32x2{bflo(vv[i]), bfhi(vv[i])};
        }
        u32x4 o0, o1;
#pragma unroll
        for (int t = 0; t < 4; ++t) {
          o0[t] = pack_bf16(kv[7 - 2 * t][0], kv[6 - 2 * t][0]);
          o1[t] = pack_bf16(kv[7 - 2 * t][1], kv[6 - 2 * t][1]);
        }
        if (r == 0 && X_EARLY) {
#pragma unroll
          for (int q = 11; q < 19; ++q) pin(kk[q]);
#pragma unroll
          for (int q = 8; q < 16; ++q) pin(vv[q]);
          if (xact) {
      const char* xp = (const char*)xIn + stg * 8 * 1280;
      static_for<0, 40>([&](auto I) { constexpr int t = I.value / 5, c = I.value % 5; gld32p<c * 256 + (t & 1) * 1280>(xx[c][t], lane * 4, xp + (t & 6) * 1280); });
          }
        }
        __builtin_amdgcn_raw_buffer_store_b128(o0, rw, ov + r * 64, O_KVG + os, SC1);
        __builtin_amdgcn_raw_buffer_store_b128(o1, rw, ov + r * 64, O_KVG + os + 16384, SC1);
      }
    }
    STAMPE(1);
    tag = next_tag(ep);
    {
      const char* fs = (const char*)(ssmk + (size_t)d * S_);
      dma16x4p<FC_BYTES + 28>(fl, l16, fs);
      dma16x4p<3 * FC_BYTES + 24>(fl, l16, fs);
    }
    if (xact && !X_EARLY) {
      const char* xp = (const char*)xIn + stg * 8 * 1280;
      static_for<0, 40>([&](auto I) { constexpr int t = I.value / 5, c = I.value % 5; gld32p<c * 256 + (t & 1) * 1280>(xx[c][t], lane * 4, xp + (t & 6) * 1280); });
      wait_vm<48>();
    } else {
      wait_vm<8>();
    }
    asm volatile("s_waitcnt lgkmcnt(0)\n\ts_barrier" ::: "memory");
    if (tid < 32) st_flag(ws, O_FK + ((b & ~31) + tid) * 256 + (b & 31) * 8, tag);
    STAMPE(2);
    int pa1[3] = {O_FX + ((b & 7) * 32 + (lane >> 2)) * 128 + (lane & 3) * 16, O_FX + ((b & 7) * 32 + 16 + (lane >> 2)) * 128 + (lane & 3) * 16,
                  O_FA0 + ((b & 7) * 12 + (lane < 48 ? lane >> 2 : 0)) * 128 + (lane & 3) * 16};
    const char* xgp = ws + O_XG + b * 16384 + wv * 4096;
    const char* stp = ws + O_ST1 + wv * 4096;
    const char* mdp = ws + O_MODP + d * 4;
    asm volatile("" : "+s"(xgp), "+s"(stp), "+s"(mdp));
    pin(pa1[0]); pin(pa1[1]); pin(pa1[2]);
    asm volatile("" : "+s"(draw));
    float Dv = (d & 1) ? bfhi(draw) : bflo(draw);
    unsigned ek = sbase + CKV_OFF + 16 * ((4 * (32 + 2 * row + (kg >> 1)) + wv) ^ ((row >> 1) & 7)) + ((kg & 1) ? 0 : 8);
    unsigned ex = (ek ^ 8) + (CX_OFF - CKV_OFF - 2048), es = sbase + CS_OFF + l16;
    unsigned mo = (((lane & 7) < MODP ? (lane & 7) : 0) * NMOD + ((lane >> 3) & 3) * D_) * 4;
    pin(Dv); pin(ek); pin(ex); pin(es); pin(mo);
    unsigned ko[2] = {(unsigned)(lane ^ (lane >> 4)) * 16, ((unsigned)(lane ^ (lane >> 4)) * 16) ^ 64};
    pin(ko[0]); pin(ko[1]);
    auto fcopies = [&]() {
      u32x4 m[5]; u32 lo[5], hi[5];
      const unsigned src = fl + FC_BYTES + l16, src4 = fl + FC_BYTES + 4096 + (lane & 1) * 16;
      static_for<0, 4>([&](auto I) { constexpr int i = I.value; ds_b128<i * 1024>(m[i], src); ds_b32<i * 1024>(lo[i], src - 4); ds_b32<i * 1024 + 16>(hi[i], src); });
      ds_b128<0>(m[4], src4); ds_b32<0>(lo[4], src4 - 4); ds_b32<16>(hi[4], src4);
      wait_lgkm<0>();
#pragma unroll
      for (int i = 0; i < 5; ++i) {
        pin(m[i]); pin(lo[i]); pin(hi[i]);
        if (i == 0 && lane == 0) lo[0] = 0;
        const u32 s1 = __builtin_amdgcn_alignbit(m[i][1], m[i][0], 16), s2 = __builtin_amdgcn_alignbit(m[i][2], m[i][1], 16), s3 = __builtin_amdgcn_alignbit(m[i][3], m[i][2], 16);
        char* dst = smem + wv * CH_BYTES + (i < 4 ? i * 1024 + l16 : 4096 + (lane & 1) * 16);
        *(u32x4*)dst = u32x4{__builtin_amdgcn_alignbit(m[i][0], lo[i], 16), s1, s2, s3};
        *(u32x4*)(dst + 2 * FC_BYTES) = u32x4{s1, s2, s3, __builtin_amdgcn_alignbit(hi[i], m[i][3], 16)};
      }
    };
    if (!xact) { if (wv == 0) wait_vm<1>(); else wait_vm<0>(); fcopies(); }
    if (xact) {
      wait_vm<0>();
      STAMPD(8);
#pragma unroll
      for (int c = 0; c < 5; ++c)
#pragma unroll
        for (int t = 0; t < 8; ++t) pin(xx[c][t]);
#pragma unroll
      for (int c = 0; c < 5; ++c) {
        u32x4 p0, p1;
#pragma unroll
        for (int t = 0; t < 4; ++t) {
          p0[t] = __builtin_amdgcn_perm(xx[c][2 * t + 1], xx[c][2 * t], 0x05040100u);
          p1[t] = __builtin_amdgcn_perm(xx[c][2 * t + 1], xx[c][2 * t], 0x07060302u);
        }
        const int so = O_XG + (c * 16 * 2048 + stg * 4) * 16;
        __builtin_amdgcn_raw_buffer_store_b128(p0, rw, ov, so, SC1);
        __builtin_amdgcn_raw_buffer_store_b128(p1, rw, ov, so + 16384, SC1);
      }
      const bf16x2 ones = {(__bf16)1.0f, (__bf16)1.0f};
      float s1[8], s2[8];
#pragma unroll
      for (int t = 0; t < 8; ++t) {
        s1[t] = 0.f; s2[t] = 0.f;
#pragma unroll
        for (int c = 0; c < 5; ++c) {
          const bf16x2 v = __builtin_bit_cast(bf16x2, xx[c][t]);
          s1[t] = __builtin_amdgcn_fdot2_f32_bf16(v, ones, s1[t], false);
          s2[t] = __builtin_amdgcn_fdot2_f32_bf16(v, v, s2[t], false);
        }
      }
      float q1[2], q2[2];
#pragma unroll
      for (int e = 0; e < 2; ++e) {
        q1[e] = dpp_add<DPP_ROR8>(tred16(tred32(s1[e], s1[4 + e]), tred32(s1[2 + e], s1[6 + e])));
        q2[e] = dpp_add<DPP_ROR8>(tred16(tred32(s2[e], s2[4 + e]), tred32(s2[2 + e], s2[6 + e])));
      }
      const float mean = sum8((lane & 8) ? q1[1] : q1[0]) * (1.f / D_);
      const float var = fmaxf(sum8((lane & 8) ? q2[1] : q2[0]) * (1.f / D_) - mean * mean, 0.f);
      const float sd = rbf(__builtin_amdgcn_sqrtf(rbf(rbf(var) + 1e-5f)));
      const float rs = __builtin_amdgcn_rcpf(sd);
      const int s = stg * 8 + (lane >> 3);
      const int nt = s >> 8, col = (s >> 4) & 15, k4 = (s >> 2) & 3, rr = s & 3;
      if ((lane & 7) == 0) __builtin_amdgcn_raw_buffer_store_b64(u32x2{__float_as_uint(rs), __float_as_uint(-rbf(mean) * rs)}, rw,
                                                                 O_ST1 + (((nt * 2 + (rr >> 1)) * 64 + k4 * 16 + col) * 4 + (rr & 1) * 2) * 4, 0, SC1);
      STAMPD(9);
      fcopies();
      wait_vm<0>();
      if (lane < 8) st_flag(ws, O_FX + (lane * 32 + (stg >> 3)) * 128 + (stg & 7) * 8, tag);
    } else if (wv < 2) {
      const int po[1] = {O_FK + b * 256 + wv * 128 + (lane & 7) * 16};
      poll<1, 0>(rw, po, tag);
      STAMPE(3);
      dma16x4<CKV_OFF + 2048>(sbase + wv * 8192, ko[0], ko[1], ws + O_KVG + b * 16384 + wv * 8192);
      dma16x4<CKV_OFF + 2048 + 4096>(sbase + wv * 8192, ko[0], ko[1], ws + O_KVG + b * 16384 + wv * 8192 + 4096);
      wait_vm<0>();
    }
    STAMPD(10);
    asm volatile("s_waitcnt lgkmcnt(0)\n\ts_barrier" ::: "memory");
    STAMPE(4);
    f32x4 acc[8];
    u32x2 ra[2][8][2]; u32x4 rb[2][8];
    u32x4 pf[3]; u32 mraw; u64 nm[6];
    auto load_n = [&](auto E, auto N) {
      constexpr int e = E.value, n = N.value, bf = e & 1;
      if constexpr (n < 2) ds_b64<16 * e + 8 * n>(ra[bf][0][n], aAddr);
      else if constexpr (n < 10) ds_b128<2048 * (n - 2)>(rb[bf][n - 2], bA[e]);
      else ds_b64<512 * ((n - 8) >> 1) + 16 * e + 8 * (n & 1)>(ra[bf][(n - 8) >> 1][n & 1], aAddr);
    };
    u32x2 kvw[8], xw[8]; u32x4 sA[8], sB[8];
    float T[8][4], Hh[8][4], c0, c1;
    auto kvop = [&](auto K) {
      constexpr int k = K.value, nt = k >> 3, r = k & 3;
      if constexpr ((k & 4) == 0) T[nt][r] = (r & 1) ? v_lo(kvw[nt][1 - (r >> 1)]) : v_hi(kvw[nt][1 - (r >> 1)]);
      else T[nt][r] = v_mul(T[nt][r], Dv);
    };
    auto xrd = [&](auto K) {
      constexpr int k = K.value, nt = k / 3;
      if constexpr (k % 3 == 0) ds_b64<2048 * nt>(xw[nt], ex);
      else if constexpr (k % 3 == 1) ds_b128<2048 * nt>(sA[nt], es);
      else ds_b128<2048 * nt + 1024>(sB[nt], es);
    };
    float xf[4];
    auto xop = [&](auto K) {
      constexpr int k = K.value, nt = k >> 4, ph = (k >> 2) & 3, r = k & 3;
      if constexpr (ph == 0) xf[r] = (r & 1) ? v_hi(xw[nt][r >> 1]) : v_lo(xw[nt][r >> 1]);
      else if constexpr (ph == 1) Hh[nt][r] = v_fma(xf[r], __uint_as_float((r < 2 ? sA[nt] : sB[nt])[2 * (r & 1)]), __uint_as_float((r < 2 ? sA[nt] : sB[nt])[2 * (r & 1) + 1]));
      else if constexpr (ph == 2) Hh[nt][r] = v_fma(Hh[nt][r], c1, c0);
      else T[nt][r] = v_fma(Hh[nt][r], T[nt][r], xf[r]);
    };
    static_for<0, 24>([&](auto N) { load_n(IC<7>{}, N); });
    wait_lgkm<14>();
    static_for<0, 8>([&](auto EE) {
      constexpr int e = 7 - EE.value, bf = e & 1;
      static_for<0, 8>([&](auto AA) {
        constexpr int a = AA.value;
        static_for<0, 8 - a>([&](auto BB) {
          constexpr int bb = BB.value, n = conv_gap(a, bb);
          if constexpr (e == 7 && n == 8) wait_lgkm<8>();
          mfma_<(e == 7 && a == 0)>(acc[a + bb], cat(ra[bf][a][0], ra[bf][a][1]), rb[bf][bb]);
          if constexpr (e > 0 && n < 24) load_n(IC<(e > 0 ? e - 1 : 0)>{}, IC<n>{});
          if constexpr (e == 7 && n >= 24 && n < 32) ds_b64<2048 * (n - 24)>(kvw[n - 24], ek);
          constexpr int g = 36 * (7 - e) + n;
          static_for<CS.rd[g], CS.rd[g + 1]>(xrd);
          if constexpr (e > 0 ? n == 34 : n == 12) wait_lgkm<0>();
          static_for<CS.op[g], CS.op[g + 1]>(xop);
          if constexpr ((e == 6 || e == 5) && n < 32) kvop(IC<(6 - e) * 32 + n>{});
          if constexpr (CV.kind[g] == 2) gld128<0>(pf[CV.idx[g]], pa1[CV.idx[g]], ws);
          if constexpr (CV.kind[g] == 3) { if constexpr (CV.idx[g] == 0) dma16<CX_OFF>(sbase + wv * 4096, ko[0], xgp); else dma16n<CV.idx[g] * 1024>(ko[CV.idx[g] & 1], xgp); }
          if constexpr (CV.kind[g] == 4) { if constexpr (CV.idx[g] == 0) dma16<CS_OFF>(sbase + wv * 4096, l16, stp); else dma16n<CV.idx[g] * 1024>(l16, stp); }
          if constexpr (CV.kind[g] == 5) gld32(mraw, mo, mdp);
          if constexpr (g == G_XCHK) { STAMPD(11); wait_vm<cv_younger(g, 2, 2)>(); }
          if constexpr (g >= G_XCHK && g < G_XCHK + 3) { constexpr int i = g - G_XCHK; nm[2 * i] = v_ne64(u32x2{pf[i][0], pf[i][1]}, tag); nm[2 * i + 1] = v_ne64(u32x2{pf[i][2], pf[i][3]}, tag); }
          if constexpr (g == G_XCHK + 3) {
            if (__builtin_expect(((nm[0] | nm[1]) | (nm[2] | nm[3]) | (nm[4] | nm[5])) != 0, 0)) poll<3, 1>(rw, pa1, tag);
            STAMPD(12);
          }
          if constexpr (e == CBE && n == 22) {
            STAMPD(13);
            wait_vm<cv_younger(g, 5, 0)>(); bar(); pin(mraw);
            STAMPD(14);
            const float mv = rbf(sum8((lane & 7) < MODP ? __uint_as_float(mraw) : 0.f));
            const float m_ga = rdl(mv, 16);
            c0 = rdl(mv, 0) * m_ga; c1 = rbf(1.f + rdl(mv, 8)) * m_ga;
            pin(c0); pin(c1);
          }
        });
      });
    });
    STAMPE(5);
    asm volatile("s_nop 7");
    static_for<0, 4>([&](auto NP) {
      constexpr int np = NP.value;
      static_for<(CS.op[288] > 32 * np ? CS.op[288] : 32 * np), 32 * np + 32>(xop);
      u32x4 o;
#pragma unroll
      for (int u = 0; u < 2; ++u) {
        const int nt = 2 * np + u;
        pin(acc[nt]);
        o[2 * u] = pack_bf16(fmaf(Hh[nt][0], acc[nt][0], T[nt][0]), fmaf(Hh[nt][1], acc[nt][1], T[nt][1]));
        o[2 * u + 1] = pack_bf16(fmaf(Hh[nt][2], acc[nt][2], T[nt][2]), fmaf(Hh[nt][3], acc[nt][3], T[nt][3]));
      }
      __builtin_amdgcn_raw_buffer_store_b128(o, rw, l16 + np * 1024, O_X2T + d * (TP_ * 2), SC1);
    });
  }
  const int kg = lane >> 4, c16 = lane & 15;
  const int kq0 = nc >> 2, r0 = (nc & 1) * 4 + wv, sgb = O_SG + (mt * 2 + ((nc >> 1) & 1)) * 8192;
  int sgo = (c16 >> 2) == kg ? sgb + (r0 * 4 + kq0) * 256 + c16 * 16 : WS_BYTES;
  __builtin_amdgcn_raw_buffer_store_b128(u32x4{0u, 0u, 0u, 0u}, rw, sgo, 0, SC1);
  asm volatile("" :: "v"(tch));
  u32x2 tag2 = {(u32)tag, (u32)(tag >> 32)};
  u32x4 gbr;
  pin(tag2);
  {
    const int fb = O_F2 + (b & 7) * 32 * 128;
    const int jw = (lane & 3) * 24 + ((nc * 12 + wv * 3 + ((lane >> 2) & 1) * 2) >> 3);
    const int pa[1] = {fb + (20 + (jw >> 3)) * 128 + (jw & 6) * 8};
    u32x4 pfa;
    gld128<0>(pfa, pa[0], ws);
    unsigned xoff[2];
#pragma unroll
    for (int p = 0; p < 2; ++p) {
      const int k = 4 * p + kg;
      xoff[p] = (k * TP_ + ((c16 ^ (2 * k)) * 8)) * 2 - p * 1024;
      pin(xoff[p]);
    }
    const char* wsrc = ws + O_W1F + (nc * 4 + wv) * W1_STEPS * 3072;
    const char* wq[4];
    unsigned xinc[4];
#pragma unroll
    for (int i = 0; i < 4; ++i) {
      wq[i] = wsrc + ((kq0 + i) & 3) * (5 * 3072) + 4096;
      xinc[i] = (i && kq0 + i == 4) ? (unsigned)(-19 * 32 * TP_ * 2) : 32 * TP_ * 2;
      asm volatile("" : "+s"(wq[i]), "+s"(xinc[i]));
    }
    unsigned xso = O_X2T + ((kq0 * 160 - 32 + 8 * wv) * TP_ + mt * 128) * 2;
    const u32x4 wsrd = {(u32)(uint64_t)ws, (u32)((uint64_t)ws >> 32) & 0xffffu, (u32)WS_BYTES, 0x00020000u};
    const unsigned ldsw = sbase + wv * 2048;
    unsigned sgl = sgb + wv * 2048 + l16;
    int hvo = kg < 3 ? ((3 * wv + kg) >> 2) * 1024 + ((3 * wv + kg) & 3) * 256 + c16 * 16 : WS_BYTES;
    const int hso = O_H2F + (mt * 8 * 48 + 3 * nc) * 1024;
    unsigned spa = sbase + G1_SP + r0 * 256 + c16 * 16;
    pin(sgl); pin(hvo); pin(sgo); pin(spa);
    u32x4 W[G1_R][3], WA_[WA > 0 ? 3 * WA : 1], WC[3], pfs[2], pfw, gran;
    int pw[1] = {O_FW + ((b & 7) * 12 + (lane < 48 ? lane >> 2 : 0)) * 128 + (lane & 3) * 16};
    unsigned xo = ((lane >> 3) * TP_ + (lane & 7) * 8) * 2;
    const char* xs = ws + O_X2T + (N * 80 * TP_ + M * 64) * 2;
    unsigned gbo = O_MODP + (((lane & 7) < MODP ? (lane & 7) : 0) * NMOD + 5 * D_ + N * 80 + wv * 20 + (lane < 40 ? lane >> 3 : 0) * 4) * 4;
    pin(pw[0]); pin(xo); pin(gbo);
    u32 hq[4];
    auto issue_w = [&](auto I) { constexpr int i = I.value; gld128<(((i % 15) & 7) - 4) * 1024>(W[(i / 3) % G1_R][i % 3], ((i % 15) & 8) ? l16b : l16, wq[i / 15]); };
    auto issue = [&](auto O) {
      constexpr int kind = G1S.kind[O.value], i = G1S.idx[O.value];
      if constexpr (kind == 0) {
        if constexpr (i & 1) xdma1(xoff[1], wsrd, xso);
        else xdma0<((i >> 1) % G1_SLOTS) * 8192>(ldsw, xoff[0], wsrd, xso, xinc[(i % 10) ? 0 : i / 10]);
      }
      else if constexpr (kind == 1) issue_w(IC<i>{});
      else if constexpr (kind == 2) issue_wd(IC<i>{});
      else if constexpr (kind == 3) {
        if constexpr (i < 2) dma16<XR_OFF + i * 4096>(sbase + wv * 1024, xo, xs + (size_t)(wv + 4 * i) * 8 * TP_ * 2);
        else if constexpr (i == 2) dma16<XR_OFF + 8192>(sbase + (wv & 1) * 1024, xo, xs + (size_t)((wv & 1) + 8) * 8 * TP_ * 2);
        else gld128<0>(gbr, gbo, ws);
      }
      else if constexpr (kind == 4) gld128<0>(pfw, pw[0], ws);
      else if constexpr (kind == 5) __builtin_amdgcn_raw_buffer_store_b128(gran, rw, sgo, 0, SC1);
      else if constexpr (kind == 6) gld128<i * 1024>(pfs[i], sgl, ws);
      else __builtin_amdgcn_raw_buffer_store_b128(u32x4{hq[0], hq[1], hq[2], hq[3]}, rw, hvo, hso + ((i + r0) & 7) * (48 * 1024), SC1);
    };
    unsigned xa[8];
#pragma unroll
    for (int jj = 0; jj < 8; ++jj) {
      const int rr = 4 * kg + (c16 >> 2);
      xa[jj] = sbase + rr * 256 + ((((jj + r0) & 7) ^ (rr & 7)) * 32) + (lane & 3) * 8;
      pin(xa[jj]);
    }
    u32x4 ones = {0x3f803f80u, 0x3f803f80u, 0x3f803f80u, 0x3f803f80u};
    pin(ones);
    int pc[2] = {fb + (lane >> 2) * 128 + (lane & 3) * 16, fb + (16 + (lane >> 2)) * 128 + (lane & 3) * 16};
    int f2o = lane < 8 ? O_F2 + (lane * 32 + (b >> 3)) * 128 + (b & 7) * 8 : WS_BYTES;
    pin(pc[0]); pin(pc[1]); pin(f2o);
    STAMPE(6);
    asm volatile("s_waitcnt vmcnt(1) lgkmcnt(0)\n\ts_barrier" ::: "memory");
    if (wv == 0) __builtin_amdgcn_raw_buffer_store_b64(tag2, rw, f2o, 0, SC1);
    STAMPE(7);
    if (bs >= CWG) {
      const int j = bs - CWG;
      u32x2 dl[5], dh[5];
      const unsigned add = sbase + AD_OFF + (8 * kg + (c16 >> 2)) * 128 + (lane & 3) * 8;
#pragma unroll
      for (int i = 0; i < 5; ++i) { const int tl = wv * 5 + i; const unsigned a2 = add + (tl >> 2) * 4096 + (tl & 3) * 32; ds_tr<0>(dl[i], a2); ds_tr<512>(dh[i], a2); }
      wait_lgkm<0>();
#pragma unroll
      for (int i = 0; i < 5; ++i) {
        pin(dl[i]); pin(dh[i]);
        __builtin_amdgcn_raw_buffer_store_b128(cat(dl[i], dh[i]), rw, l16, O_W2F + ((((j & 1) * 4 + wv) * 48 + (j >> 1)) * 5 + i) * 1024, SC1);
      }
      drain_bar();
      if (tid < 8) st_flag(ws, O_FW + (tid * 12 + (j >> 3)) * 128 + (j & 7) * 8, tag);
    }

    if (wv == 0) wait_vm<1>(); else wait_vm<0>();
    pin(pfa);
    if (!__all(tag_ok(pfa, tag))) poll<1, 2>(rw, pa, tag);
    static_for<0, 3 * G1_WPRE>(issue_w);
    static_for<0, 3>([&](auto I) { gld128a<I.value * 1024>(WC[I.value], l16, wsrc + 20 * 3072); });
    static_for<0, 3 * WA>([&](auto I) { constexpr int i = 3 * G1_WPRE + I.value; gld128a<(((i % 15) & 7) - 4) * 1024>(WA_[I.value], ((i % 15) & 8) ? l16b : l16, wq[i / 15]); });
    STAMPL(8);
    if (wv == 0) poll<2, 1>(rw, pc, tag);
    wait_vm<0>();
    bar();
    STAMPL(9);
    static_for<0, G1S.first[0]>(issue);
    f32x4 acc[8][3];
    u32x2 X[G1_LA + 1][2];
    auto xread = [&](auto T, auto H) {
      constexpr int t = T.value, h = H.value;
      ds_tr<(g1_ks(t) % G1_SLOTS) * 8192 + h * 4096>(X[t % (G1_LA + 1)][h], xa[g1_j(t)]);
    };
    f32x4 gram, sum1;
    u32 fs[4], fa; u64 sbad;
    unsigned spw = sbase + G1_SP + (2 * wv + (kg >> 1) + (kg & 1) * 8) * 256 + c16 * 16;
    pin(spw);
    auto fop = [&](auto K) {
      constexpr int k = K.value;
      if constexpr (k == 0) asm_and3(fa, pfs[0][1], pfs[0][3], pfs[1][1]);
      else if constexpr (k == 1) asm_and3(fa, fa, pfs[1][3], pfs[1][3]);
      else if constexpr (k == 2) asm_ne1(sbad, fa);
      else if constexpr (k == 4) v_swap32(fs[0], fs[1]);
      else if constexpr (k == 5) v_swap32(fs[2], fs[3]);
      else if constexpr (k == 6) asm_op2<0>(fs[0], fs[0], fs[1]);
      else if constexpr (k == 7) asm_op2<0>(fs[2], fs[2], fs[3]);
      else if constexpr (k == 8) v_swap16(fs[0], fs[2]);
      else if constexpr (k == 9) asm_op2<0>(fs[0], fs[0], fs[2]);
      else if constexpr (k == 10) asm_op1<0>(fs[0], fs[0]);
      else if constexpr (k == 11) asm_op1<1>(fs[1], fs[0]);
      else if constexpr (k == 12) v_swap16(fs[0], fs[1]);
      else if constexpr (k == 13) asm_op2<1>(fs[1], fs[0], fs[1]);
      else if constexpr (k == 14) asm_op1<2>(fs[1], fs[1]);
      else if constexpr (k == 15) asm_op1<3>(fs[1], fs[1]);
      else if constexpr (k == 16) asm_op1<4>(fs[1], fs[1]);
      else if constexpr (k == 17) asm_op1<3>(fs[1], fs[1]);
      else if constexpr (k == 18) asm_op1<5>(fs[1], fs[1]);
      else if constexpr (k == 19) asm_op2<2>(fs[0], fs[0], fs[1]);
      else if constexpr (k == 20) asm_op1<1>(fs[1], fs[0]);
      else if constexpr (k == 21) asm_op1<1>(fs[2], fs[0]);
      else if constexpr (k == 22) asm_op1<6>(fs[3], fs[0]);
      else if constexpr (k == 23) ds_w128u<0>(spw, u32x4{fs[0], fs[1], fs[2], fs[3]});
    };
    float eg[6]; u32x4 xc[2];
    auto eop = [&](auto J, auto K) {
      constexpr int j = J.value, k = K.value;
      if constexpr (k < 15) {
        if constexpr (k < 6) eg[k] = v_relu(acc[j][k >> 1][2 * (k & 1)]);
        else if constexpr (k < 12) eg[k - 6] = v_mul(eg[k - 6], acc[j][(k - 6) >> 1][2 * (k & 1) + 1]);
        else hq[k == 12 ? 1 : k == 13 ? 0 : 2] = v_cvt(eg[k == 12 ? 2 : k == 13 ? 0 : 4], eg[k == 12 ? 3 : k == 13 ? 1 : 5]);
      } else if constexpr (k == 15) v_swap32(hq[1], hq[3]);
      else if constexpr (k == 16) v_swap32(hq[0], hq[2]);
      else if constexpr (k == 17) v_swap16(hq[0], hq[1]);
      else v_swap16(hq[2], hq[3]);
    };
    hq[3] = 0;
    pin(hq[3]);
    wait_vm<G1S.first[0] - 2>();
    bar();
    STAMPL(10);
    static_for<0, G1_LA>([&](auto T) { xread(T, IC<0>{}); xread(T, IC<1>{}); });
    static_for<0, 160>([&](auto T) {
      constexpr int t = T.value, ks = g1_ks(t), jj = g1_j(t), tn = t + G1_LA, u = t & 3;
      if constexpr (t == 24) STAMPG(1);
      if constexpr (t == 48) STAMPG(2);
      if constexpr (t == 72) STAMPG(3);
      if constexpr (t == 96) STAMPG(4);
      if constexpr (t == 124) STAMPG(5);
      if constexpr (t == 126) STAMPG(6);
      if constexpr (!(t & 1)) wait_lgkm<2 * ((tn <= 160 ? tn : 160) - t - 2)>();
      if constexpr (t <= 124 && (t & 7) == 4) {
        static_assert(g1_need(t) < G1S.first[t], "not issued yet");
        wait_vm<G1S.first[t] - 1 - g1_need(t)>();
        bar();
      }
      if constexpr (t == T_WCHK) {
        if (__builtin_expect((v_ne64(u32x2{pfw[0], pfw[1]}, tag) | v_ne64(u32x2{pfw[2], pfw[3]}, tag)) != 0, 0)) poll<1, 1>(rw, pw, tag);
      }
      if constexpr (t == 35) {
        pin(gram); pin(sum1);
        const int c3 = lane & 3;
        gran = u32x4{__float_as_uint(sum1[0]), 1u, __float_as_uint(c3 == 0 ? gram[0] : c3 == 1 ? gram[1] : c3 == 2 ? gram[2] : gram[3]), 1u};
        pin(gran);
      }
      if constexpr (t == 111) {
        if (__builtin_expect(sbad != 0, 0)) {
#pragma unroll 1
          for (int spn = 0; spn < SPIN_CAP; ++spn) {
            pfs[0] = __builtin_amdgcn_raw_buffer_load_b128(rw, sgl, 0, SC1);
            pfs[1] = __builtin_amdgcn_raw_buffer_load_b128(rw, sgl + 1024, 0, SC1);
            if (__all((pfs[0][1] & pfs[0][3] & pfs[1][1] & pfs[1][3]) == 1u)) break;
            asm volatile("" ::: "memory");
          }
        }
        fs[0] = pfs[0][0]; fs[1] = pfs[1][0]; fs[2] = pfs[0][2]; fs[3] = pfs[1][2];
      }
      if constexpr (t == 123) wait_lgkm<0>();
      auto shadow = [&](auto S) {
        constexpr int sl = S.value;
        if constexpr (t >= 109 && t < 121 && sl < 2) fop(IC<2 * (t - 109) + sl>{});
        if constexpr (t >= 132) {
          constexpr int k = 4 * u + sl - 2;
          if constexpr (k >= 0 && k < 14) eop(IC<jj - 1>{}, IC<k>{});
        }
      };
      const u32x4 xb = cat(X[t % (G1_LA + 1)][0], X[t % (G1_LA + 1)][1]);
      if constexpr (ks >= G1_WPRE && ks < G1_WPRE + WA) mfma_a<false>(acc[jj][0], WA_[3 * (ks - G1_WPRE) + 0], xb); else mfma_<ks == 0>(acc[jj][0], W[ks % G1_R][0], xb);
      if constexpr (tn < 160) xread(IC<tn>{}, IC<0>{});
      shadow(IC<0>{});
      if constexpr (ks >= G1_WPRE && ks < G1_WPRE + WA) mfma_a<false>(acc[jj][1], WA_[3 * (ks - G1_WPRE) + 1], xb); else mfma_<ks == 0>(acc[jj][1], W[ks % G1_R][1], xb);
      if constexpr (tn < 160) xread(IC<tn>{}, IC<1>{});
      shadow(IC<1>{});
      if constexpr (ks >= G1_WPRE && ks < G1_WPRE + WA) mfma_a<false>(acc[jj][2], WA_[3 * (ks - G1_WPRE) + 2], xb); else mfma_<ks == 0>(acc[jj][2], W[ks % G1_R][2], xb);
      if constexpr (!(t >= 128 && u == 3)) static_for<G1S.first[t], G1S.first[t + 1]>(issue);
      if constexpr (t >= 126 && t < 158 && u == 2) ds_b128<((t - 126) >> 2) * 256>(xc[((t - 126) >> 2) & 1], spa);
      shadow(IC<2>{});
      shadow(IC<3>{});
      if constexpr (t < 40 && jj == 0) { mfma_<ks == 0>(gram, xb, xb); mfma_<ks == 0>(sum1, ones, xb); }
      if constexpr (t >= 128 && u == 3) {
        mfma_a<false>(acc[jj][0], WC[0], xc[jj & 1]);
        if constexpr (t >= 132) { eop(IC<jj - 1>{}, IC<14>{}); eop(IC<jj - 1>{}, IC<15>{}); }
        mfma_a<false>(acc[jj][1], WC[1], xc[jj & 1]);
        if constexpr (t >= 132) { eop(IC<jj - 1>{}, IC<16>{}); asm volatile("s_nop 0"); }
        mfma_a<false>(acc[jj][2], WC[2], xc[jj & 1]);
        if constexpr (t >= 132) { eop(IC<jj - 1>{}, IC<17>{}); eop(IC<jj - 1>{}, IC<18>{}); }
        static_for<G1S.first[t], G1S.first[t + 1]>(issue);
      }
    });
    STAMPL(11);
    asm volatile("s_nop 3");
    static_for<0, 15>([&](auto K) { eop(IC<7>{}, K); });
    eop(IC<7>{}, IC<15>{}); asm volatile("s_nop 0"); eop(IC<7>{}, IC<16>{}); asm volatile("s_nop 1"); eop(IC<7>{}, IC<17>{}); eop(IC<7>{}, IC<18>{});
    __builtin_amdgcn_raw_buffer_store_b128(u32x4{hq[0], hq[1], hq[2], hq[3]}, rw, hvo, hso + ((7 + r0) & 7) * (48 * 1024), SC1);
  }
  STAMPL(12);
  float rs2;
  {
    const u32 p = *(const u32*)(smem + G1_SP + ((nc & 4) + wv) * 256 + c16 * 16);
    const float rs = __builtin_amdgcn_rcpf(bfhi(p));
    rs2 = rs * rs;
    pin(rs2);
  }
  const char* hp[4];
  unsigned pwa[3];
#pragma unroll
  for (int jj = 0; jj < 4; ++jj) { hp[jj] = ws + O_H2F + ((M * 4 + ((jj + wv + 1) & 3)) * 48 + wv * 12) * 1024 + 4096; asm volatile("" : "+s"(hp[jj])); }
#pragma unroll
  for (int jj = 0; jj < 3; ++jj) { pwa[jj] = sbase + ((jj + wv + 1) & 3) * 15360 + jj * 5120 + l16; pin(pwa[jj]); }
  unsigned pra = sbase + wv * 15360 + l16, gba = sbase + XR_OFF + 10240 + kg * 16;
  unsigned xad = sbase + XR_OFF + (4 * kg + (c16 >> 2)) * 128 + wv * 32 + (lane & 3) * 8;
  int oo;
  {
    const int pos = M * 64 + wv * 16 + c16;
    const int l8 = (pos >> 3) & 63;
    const int tok = 256 * (2 * (pos >> 9) + ((pos >> 2) & 1)) + 16 * (l8 & 15) + 4 * (l8 >> 4) + (pos & 3);
    oo = (tok * D_ + N * 80 + 4 * kg) * 2;
  }
  pin(pra); pin(gba); pin(xad); pin(oo);
  int po3[1] = {O_F3 + (mt * 16 + nc) * 128 + wv * 32 + (lane & 1) * 16};
  int f3o = tid < 16 ? O_F3 + (mt * 16 + tid) * 128 + nc * 8 : WS_BYTES;
  pin(po3[0]); pin(f3o);
  {
    pin(gbr);
    const float g = sum8((lane & 7) < MODP ? __uint_as_float(gbr[0]) : 0.f), g2 = sum8((lane & 7) < MODP ? __uint_as_float(gbr[1]) : 0.f);
    const float g3 = sum8((lane & 7) < MODP ? __uint_as_float(gbr[2]) : 0.f), g4 = sum8((lane & 7) < MODP ? __uint_as_float(gbr[3]) : 0.f);
    if ((lane & 7) == 0 && lane < 40) *(f32x4*)(smem + XR_OFF + 10240 + wv * 80 + (lane >> 3) * 16) = f32x4{rbf(g), rbf(g2), rbf(g3), rbf(g4)};
  }
  drain_bar();
  if (wv == 0) __builtin_amdgcn_raw_buffer_store_b64(tag2, rw, f3o, 0, SC1);
  STAMPG(7);
  static_for<G1_WDN, 60>(issue_wd);
  poll<1, 0>(rw, po3, tag);
  STAMPL(13);
  u32x4 H[G2_R];
  auto issue_h = [&](auto O) { constexpr int o = O.value; gld128<(((o % 12) & 7) - 4) * 1024>(H[o % G2_R], ((o % 12) & 8) ? l16b : l16, hp[o / 12]); };
  static_for<0, G2_PRE>(issue_h);
  f32x4 acc[4][5], pt[3][5], gbv[5];
  u32x2 xw[5];
  float G[5][4], Tt[5][4];
  static_for<0, 240>([&](auto GG) {
    constexpr int g = GG.value, o = g / 5, jj = o / 12, ks = o % 12, i = g % 5, q = g % 60;
    constexpr int i0 = (G2_PRE + (g + 3) / 4 < 48) ? G2_PRE + (g + 3) / 4 : 48;
    static_assert(i0 > o && i0 < o + G2_R, "ring");
    if constexpr (i == 0) wait_vm<i0 - 1 - o>();
    mfma_a<ks == 0>(acc[jj][i], WD[ks * 5 + i], H[o % G2_R]);
    if constexpr ((g & 3) == 0 && i0 < 48) issue_h(IC<i0>{});
    if constexpr (jj >= 1 && q >= 5 && q < 15 && (q & 1)) ds_w128<((q - 5) / 2) * 1024>(pwa[jj - 1], acc[jj - 1][(q - 5) / 2]);
    if constexpr (g >= 81 && g < 86) ds_tr<(g - 81) * 2048>(xw[g - 81], xad);
    if constexpr (g >= 86 && g < 91) ds_f128<(g - 86) * 64>(gbv[g - 86], gba);
    if constexpr (g == 96) wait_lgkm<0>();
    if constexpr (g >= 97 && g < 117) {
      constexpr int k = g - 97;
      Tt[k >> 2][k & 3] = (k & 1) ? v_hi(xw[k >> 2][(k >> 1) & 1]) : v_lo(xw[k >> 2][(k >> 1) & 1]);
      G[k >> 2][k & 3] = v_mul(gbv[k >> 2][k & 3], rs2);
    }
    if constexpr (g == 198) { wait_lgkm<0>(); bar(); }
    if constexpr (g >= 199 && g < 214) ds_f128<(g - 199) * 1024>(pt[(g - 199) / 5][(g - 199) % 5], pra);
    if constexpr (g == 208) wait_lgkm<5>();
    if constexpr (g == 218) wait_lgkm<0>();
    if constexpr (g >= 209) {
      constexpr int k0 = g < 214 ? g - 209 : 5 + 2 * (g - 214), k1 = g < 214 ? k0 + 1 : k0 + 2;
      static_for<k0, (k1 < 60 ? k1 : 60)>([&](auto K) { constexpr int k = K.value; Tt[(k % 20) >> 2][k & 3] = v_fma(G[(k % 20) >> 2][k & 3], pt[k / 20][(k % 20) >> 2][k & 3], Tt[(k % 20) >> 2][k & 3]); });
    }
  });
  STAMPL(14);
  static_for<57, 60>([&](auto K) { constexpr int k = K.value; Tt[(k % 20) >> 2][k & 3] = v_fma(G[(k % 20) >> 2][k & 3], pt[k / 20][(k % 20) >> 2][k & 3], Tt[(k % 20) >> 2][k & 3]); });
  asm volatile("s_nop 2");
  const rsrc_t ro = mk_rsrc(out, S_ * D_ * 2);
#pragma unroll
  for (int i = 0; i < 5; ++i) {
    pin(acc[3][i]);
    __builtin_amdgcn_raw_buffer_store_b64(u32x2{pack_bf16(fmaf(G[i][0], acc[3][i][0], Tt[i][0]), fmaf(G[i][1], acc[3][i][1], Tt[i][1])),
                                                pack_bf16(fmaf(G[i][2], acc[3][i][2], Tt[i][2]), fmaf(G[i][3], acc[3][i][3], Tt[i][3]))}, ro, oo + i * 32, 0, 0);
  }
  if (b == 0 && tid == 0) st_flag(ws, O_EPOCH, tag);
  STAMP_END();
}

extern "C" void kernel_launch(void* const* d_in, const int* in_sizes, int n_in, void* d_out, int out_size,
                              void* d_ws, size_t ws_size, hipStream_t stream) {
  k_fused<<<256, 256, 0, stream>>>((const u16*)d_in[0], (const u16*)d_in[1], (const u16*)d_in[2], (const u16*)d_in[3],
                                   (const u16*)d_in[4], (const u16*)d_in[5], (const u16*)d_in[7], (const u16*)d_in[8],
                                   (const u16*)d_in[9], (const u16*)d_in[10], (const u16*)d_in[11], (u16*)d_out, (char*)d_ws);
}
```

```cpp
#define TOUCH
#include <hip/hip_runtime.h>
#include <stdint.h>

typedef unsigned short u16;
typedef unsigned int u32;
typedef __attribute__((ext_vector_type(2))) __bf16 bf16x2;
typedef __attribute__((ext_vector_type(4))) float f32x4;
typedef __attribute__((ext_vector_type(2))) float f32x2;
typedef __attribute__((ext_vector_type(4))) u32 u32x4;
typedef __attribute__((ext_vector_type(2))) u32 u32x2;

constexpr int S_ = 2048, D_ = 640, F_ = 1536;
constexpr int TP_ = S_ + 64;
constexpr int NMOD = 6 * D_;
constexpr int MODP = 5;
constexpr int CWG = 160;

__device__ __forceinline__ float bflo(u32 w) { return __uint_as_float(w << 16); }
__device__ __forceinline__ float bfhi(u32 w) { return __uint_as_float(w & 0xffff0000u); }
__device__ __forceinline__ u32 pack_bf16(float lo, float hi) {
  bf16x2 v; v[0] = (__bf16)lo; v[1] = (__bf16)hi; return __builtin_bit_cast(u32, v);
}
__device__ __forceinline__ float rbf(float x) { __bf16 b = (__bf16)x; return (float)b; }
#define SCHED() __builtin_amdgcn_sched_barrier(0)
template <int V> struct IC { static constexpr int value = V; };
template <int B, int E, class Fn> __device__ __forceinline__ void static_for(Fn&& f) {
  if constexpr (B < E) { f(IC<B>{}); static_for<B + 1, E>(f); }
}
__device__ __forceinline__ void mfma(f32x4& c, u32x4 a, u32x4 b) {
  asm volatile("v_mfma_f32_16x16x32_bf16 %0, %1, %2, %0" : "+v"(c) : "v"(a), "v"(b));
}
__device__ __forceinline__ void mfma0(f32x4& c, u32x4 a, u32x4 b) {
  asm volatile("v_mfma_f32_16x16x32_bf16 %0, %1, %2, 0" : "=&v"(c) : "v"(a), "v"(b));
}
template <bool FIRST> __device__ __forceinline__ void mfma_(f32x4& c, u32x4 a, u32x4 b) { if constexpr (FIRST) mfma0(c, a, b); else mfma(c, a, b); }
__device__ __forceinline__ u32 alignbit(u32 hi, u32 lo, u32 sh) {
  u32 d; asm volatile("v_alignbit_b32 %0, %1, %2, %3" : "=v"(d) : "v"(hi), "v"(lo), "v"(sh)); return d;
}
template <int OFF> __device__ __forceinline__ void ds_tr(u32x2& dst, unsigned addr) {
  asm volatile("ds_read_b64_tr_b16 %0, %1 offset:%2" : "=v"(dst) : "v"(addr), "i"(OFF) : "memory");
}
template <int OFF> __device__ __forceinline__ void ds_b32(u32& dst, unsigned addr) {
  asm volatile("ds_read_b32 %0, %1 offset:%2" : "=v"(dst) : "v"(addr), "i"(OFF) : "memory");
}
template <int OFF> __device__ __forceinline__ void ds_b64(u32x2& dst, unsigned addr) {
  asm volatile("ds_read_b64 %0, %1 offset:%2" : "=v"(dst) : "v"(addr), "i"(OFF) : "memory");
}
template <int OFF> __device__ __forceinline__ void ds_b128(u32x4& dst, unsigned addr) {
  asm volatile("ds_read_b128 %0, %1 offset:%2" : "=v"(dst) : "v"(addr), "i"(OFF) : "memory");
}
template <int OFF> __device__ __forceinline__ void ds_w128(unsigned addr, f32x4 v) {
  asm volatile("ds_write_b128 %0, %1 offset:%2" :: "v"(addr), "v"(v), "i"(OFF) : "memory");
}
template <int OFF> __device__ __forceinline__ void gld128(u32x4& dst, unsigned voff, const char* sb) {
  asm volatile("global_load_dwordx4 %0, %1, %2 offset:%3 sc1" : "=v"(dst) : "v"(voff), "s"(sb), "i"(OFF) : "memory");
}
__device__ __forceinline__ void gld32(u32& dst, unsigned voff, const char* sb) {
  asm volatile("global_load_dword %0, %1, %2 sc1" : "=v"(dst) : "v"(voff), "s"(sb) : "memory");
}
template <int LOFF> __device__ __forceinline__ void dma16(unsigned ldsb, unsigned voff, const char* sb) {
  asm volatile("s_add_u32 m0, %0, %3\n\ts_nop 0\n\tglobal_load_lds_dwordx4 %1, %2 sc1" :: "s"(ldsb), "v"(voff), "s"(sb), "i"(LOFF) : "memory", "scc");
}
template <int LOFF> __device__ __forceinline__ void dma16x4(unsigned ldsb, unsigned v0, unsigned v1, const char* sb) {
  asm volatile("s_add_u32 m0, %0, %4\n\ts_nop 0\n\tglobal_load_lds_dwordx4 %1, %3 sc1\n\tglobal_load_lds_dwordx4 %2, %3 offset:1024 sc1\n\t"
               "global_load_lds_dwordx4 %1, %3 offset:2048 sc1\n\tglobal_load_lds_dwordx4 %2, %3 offset:3072 sc1"
               :: "s"(ldsb), "v"(v0), "v"(v1), "s"(sb), "i"(LOFF) : "memory", "scc");
}
template <int N> __device__ __forceinline__ void wait_vm() { asm volatile("s_waitcnt vmcnt(%0)" :: "i"(N) : "memory"); }
template <int N> __device__ __forceinline__ void wait_lgkm() { asm volatile("s_waitcnt lgkmcnt(%0)" :: "i"(N) : "memory"); }
__device__ __forceinline__ void keep(u32x4 v) { asm volatile("" :: "v"(v)); }
__device__ __forceinline__ void real() { __builtin_amdgcn_s_setprio(0); }
__device__ __forceinline__ void bar() { asm volatile("s_barrier" ::: "memory"); }
template <class T> __device__ __forceinline__ void pin(T& a) { asm volatile("" : "+v"(a)); }
__device__ __forceinline__ u32x4 cat(u32x2 a, u32x2 b) { return u32x4{a[0], a[1], b[0], b[1]}; }

typedef __amdgpu_buffer_rsrc_t rsrc_t;
__device__ __forceinline__ rsrc_t mk_rsrc(const void* p, int bytes) { return __builtin_amdgcn_make_buffer_rsrc((void*)p, 0, bytes, 0x00020000); }
template <int CTRL> __device__ __forceinline__ float dpp_add(float x) {
  return x + __builtin_bit_cast(float, __builtin_amdgcn_mov_dpp(__builtin_bit_cast(int, x), CTRL, 0xf, 0xf, true));
}
constexpr int DPP_XOR1 = 0xB1, DPP_XOR2 = 0x4E, DPP_HMIRROR = 0x141, DPP_ROR8 = 0x128;
__device__ __forceinline__ float sum8(float x) { return dpp_add<DPP_HMIRROR>(dpp_add<DPP_XOR2>(dpp_add<DPP_XOR1>(x))); }
__device__ __forceinline__ float tred32(float a, float b) {
  auto r = __builtin_amdgcn_permlane32_swap(__float_as_uint(a), __float_as_uint(b), false, false);
  return __uint_as_float(r[0]) + __uint_as_float(r[1]);
}
__device__ __forceinline__ float tred16(float a, float b) {
  auto r = __builtin_amdgcn_permlane16_swap(__float_as_uint(a), __float_as_uint(b), false, false);
  return __uint_as_float(r[0]) + __uint_as_float(r[1]);
}
__device__ __forceinline__ float rdl(float x, int l) { return __int_as_float(__builtin_amdgcn_readlane(__float_as_int(x), l)); }

typedef unsigned long long u64;
#define GAS __attribute__((address_space(1)))
constexpr int SC1 = 16;
#define BS " sc1"
#define A0_DELAY 16
#define F_LATE 1
#define A1_DELAY 4
#define A3_DELAY 24
#define AU_DELAY 16
#define A_DELAY 2
#define FXE 7
#define PE 4
#define NT 0
#define NTS ""
template <int OFF> __device__ __forceinline__ void gld128b(u32x4& dst, unsigned voff, const char* sb) {
  asm volatile("global_load_dwordx4 %0, %1, %2 offset:%3" BS : "=v"(dst) : "v"(voff), "s"(sb), "i"(OFF) : "memory");
}
template <int OFF> __device__ __forceinline__ void gld128a(u32x4& dst, unsigned voff, const char* sb) {
  asm volatile("global_load_dwordx4 %0, %1, %2 offset:%3" BS : "=a"(dst) : "v"(voff), "s"(sb), "i"(OFF) : "memory");
}
template <bool FIRST> __device__ __forceinline__ void mfma_a(f32x4& c, u32x4 a, u32x4 b) {
  if constexpr (FIRST) asm volatile("v_mfma_f32_16x16x32_bf16 %0, %1, %2, 0" : "=&v"(c) : "a"(a), "v"(b));
  else asm volatile("v_mfma_f32_16x16x32_bf16 %0, %1, %2, %0" : "+v"(c) : "a"(a), "v"(b));
}
typedef __attribute__((ext_vector_type(8))) __bf16 bf16x8;
__device__ __forceinline__ f32x4 mfma_b(u32x4 a, u32x4 b, f32x4 c) {
  return __builtin_amdgcn_mfma_f32_16x16x32_bf16(__builtin_bit_cast(bf16x8, a), __builtin_bit_cast(bf16x8, b), c, 0, 0, 0);
}
template <int LOFF> __device__ __forceinline__ void dma16p(unsigned ldsb, unsigned voff, const char* sb) {
  asm volatile("s_add_u32 m0, %0, %3\n\ts_nop 0\n\tglobal_load_lds_dwordx4 %1, %2" NTS :: "s"(ldsb), "v"(voff), "s"(sb), "i"(LOFF) : "memory", "scc");
}
template <int LOFF> __device__ __forceinline__ void dma16x4p(unsigned ldsb, unsigned v0, const char* sb) {
  asm volatile("s_add_u32 m0, %0, %3\n\ts_nop 0\n\tglobal_load_lds_dwordx4 %1, %2" NTS "\n\tglobal_load_lds_dwordx4 %1, %2 offset:1024" NTS "\n\t"
               "global_load_lds_dwordx4 %1, %2 offset:2048" NTS "\n\tglobal_load_lds_dwordx4 %1, %2 offset:3072" NTS
               :: "s"(ldsb), "v"(v0), "s"(sb), "i"(LOFF) : "memory", "scc");
}

#undef DBGC
#define STAMP(I)
#define STAMP_DECL()
#define STAMP_END()

__device__ __forceinline__ void gld128p(u32x4& dst, unsigned voff, const char* sb) {
  asm volatile("global_load_dwordx4 %0, %1, %2" NTS : "=v"(dst) : "v"(voff), "s"(sb) : "memory");
}
template <int OFF> __device__ __forceinline__ void gld32p(u32& dst, unsigned voff, const char* sb) {
  asm volatile("global_load_dword %0, %1, %2 offset:%3" NTS : "=v"(dst) : "v"(voff), "s"(sb), "i"(OFF) : "memory");
}
template <int OFF> __device__ __forceinline__ void gld16p(u32& dst, unsigned voff, const char* sb) {
  asm volatile("global_load_ushort %0, %1, %2 offset:%3" : "=v"(dst) : "v"(voff), "s"(sb), "i"(OFF) : "memory");
}
#define CPF_DONE() asm volatile("" :: "v"(cpf))
#define X_POS 1
#define F_POS 1
template <int OFF> __device__ __forceinline__ void dma16n(unsigned voff, const char* sb) {
  asm volatile("global_load_lds_dwordx4 %0, %1 offset:%2 sc1" :: "v"(voff), "s"(sb), "i"(OFF) : "memory");
}
__device__ __forceinline__ u64 v_ne64(u32x2 v, u64 tag) { u64 m; asm volatile("v_cmp_ne_u64 %0, %1, %2" : "=s"(m) : "v"(v), "s"(tag)); return m; }
__device__ __forceinline__ float v_lo(u32 w) { float d; asm volatile("v_lshlrev_b32 %0, 16, %1" : "=v"(d) : "v"(w)); return d; }
__device__ __forceinline__ float v_hi(u32 w) { float d; asm volatile("v_and_b32 %0, 0xffff0000, %1" : "=v"(d) : "v"(w)); return d; }
__device__ __forceinline__ float v_fma(float a, float b, float c) { float d; asm volatile("v_fma_f32 %0, %1, %2, %3" : "=v"(d) : "v"(a), "v"(b), "v"(c)); return d; }
__device__ __forceinline__ float v_mul(float a, float b) { float d; asm volatile("v_mul_f32 %0, %1, %2" : "=v"(d) : "v"(a), "v"(b)); return d; }
__device__ __forceinline__ float v_add(float a, float b) { float d; asm volatile("v_add_f32 %0, %1, %2" : "=v"(d) : "v"(a), "v"(b)); return d; }
#define STAMPD(I)
#define STAMPL(I) STAMP(I)
__device__ __forceinline__ float v_relu(float a) { float d; asm volatile("v_max_i32 %0, 0, %1" : "=v"(d) : "v"(a)); return d; }
__device__ __forceinline__ u32 v_cvt(float a, float b) { u32 d; asm volatile("v_cvt_pk_bf16_f32 %0, %1, %2" : "=v"(d) : "v"(a), "v"(b)); return d; }
__device__ __forceinline__ u32 v_perm(u32 a, u32 sel) { u32 d; asm volatile("v_perm_b32 %0, %1, %1, %2" : "=v"(d) : "v"(a), "s"(sel)); return d; }
__device__ __forceinline__ void v_swap32(u32& a, u32& b) { asm volatile("v_permlane32_swap_b32 %0, %1" : "+v"(a), "+v"(b)); }
__device__ __forceinline__ void v_swap16(u32& a, u32& b) { asm volatile("v_permlane16_swap_b32 %0, %1" : "+v"(a), "+v"(b)); }
template <int OFF> __device__ __forceinline__ void ds_f128(f32x4& dst, unsigned addr) {
  asm volatile("ds_read_b128 %0, %1 offset:%2" : "=v"(dst) : "v"(addr), "i"(OFF) : "memory");
}
#define STAMPG(I)
#define STAMPE(I) STAMP(I)
__device__ __forceinline__ void asm_and3(u32& d, u32 a, u32 b, u32 c) { asm volatile("v_bitop3_b32 %0, %1, %2, %3 bitop3:0x80" : "=v"(d) : "v"(a), "v"(b), "v"(c)); }
__device__ __forceinline__ void asm_ne1(unsigned long long& m, u32 a) { asm volatile("v_cmp_ne_u32 %0, 1, %1" : "=s"(m) : "v"(a)); }
__device__ __forceinline__ void ds_w32(unsigned addr, u32 v) { asm volatile("ds_write_b32 %0, %1" :: "v"(addr), "v"(v) : "memory"); }
template <int OP> __device__ __forceinline__ void asm_op1(u32& d, u32 a) {
  if constexpr (OP == 0) asm volatile("v_mul_f32 %0, 0x3acccccd, %1" : "=v"(d) : "v"(a));
  else if constexpr (OP == 1) asm volatile("v_mov_b32 %0, %1" : "=v"(d) : "v"(a));
  else if constexpr (OP == 2) asm volatile("v_max_f32 %0, 0, %1" : "=v"(d) : "v"(a));
  else if constexpr (OP == 3) asm volatile("v_cvt_pk_bf16_f32 %0, 0, %1" : "=v"(d) : "v"(a));
  else if constexpr (OP == 4) asm volatile("v_add_f32 %0, 0x3727c5ac, %1" : "=v"(d) : "v"(a));
  else if constexpr (OP == 5) asm volatile("v_sqrt_f32 %0, %1" : "=v"(d) : "v"(a));
  else asm volatile("v_mov_b32 %0, 0" : "=v"(d));
}
template <int OP> __device__ __forceinline__ void asm_op2(u32& d, u32 a, u32 b) {
  if constexpr (OP == 0) asm volatile("v_add_f32 %0, %1, %2" : "=v"(d) : "v"(a), "v"(b));
  else if constexpr (OP == 1) asm volatile("v_fma_f32 %0, -%1, %1, %2" : "=v"(d) : "v"(a), "v"(b));
  else asm volatile("v_cvt_pk_bf16_f32 %0, -%1, %2" : "=v"(d) : "v"(a), "v"(b));
}
template <int LOFF> __device__ __forceinline__ void xdma0(unsigned ldsb, unsigned voff, u32x4 srd, unsigned& soff, unsigned inc) {
  asm volatile("s_add_u32 m0, %1, %5\n\ts_add_u32 %0, %0, %4\n\tbuffer_load_dwordx4 %2, %3, %0 offen" BS " lds"
               : "+s"(soff) : "s"(ldsb), "v"(voff), "s"(srd), "s"(inc), "i"(LOFF) : "memory", "scc");
}
__device__ __forceinline__ void xdma1(unsigned voff, u32x4 srd, unsigned soff) {
  asm volatile("buffer_load_dwordx4 %0, %1, %2 offen offset:1024" BS " lds" :: "v"(voff), "s"(srd), "s"(soff) : "memory");
}
template <int OFF> __device__ __forceinline__ void ds_w128u(unsigned addr, u32x4 v) {
  asm volatile("ds_write_b128 %0, %1 offset:%2" :: "v"(addr), "v"(v), "i"(OFF) : "memory");
}
constexpr int W1_STEPS = 21;
constexpr int O_EPOCH = 0;
constexpr int O_FK = 4096;
constexpr int O_FA0 = O_FK + 160 * 256;
constexpr int O_FW = O_FA0 + 8 * 12 * 128;
constexpr int O_FU = O_FW + 8 * 12 * 128;
constexpr int O_FX = O_FU + 8 * 14 * 128;
constexpr int O_F2 = O_FX + 8 * 32 * 128;
constexpr int O_F3 = O_F2 + 8 * 32 * 128;
constexpr int O_SG = O_F3 + 256 * 128;
constexpr int O_MODP = O_SG + 32 * 8192;
constexpr int O_ST1 = O_MODP + MODP * NMOD * 4;
constexpr int O_KVG = O_ST1 + 16384;
constexpr int O_XG = O_KVG + S_ * D_ * 2;
constexpr int O_X2T = O_XG + S_ * D_ * 2;
constexpr int O_W1F = O_X2T + TP_ * D_ * 2;
constexpr int O_W2F = O_W1F + 64 * W1_STEPS * 3 * 1024;
constexpr int O_H2F = O_W2F + 8 * 48 * 5 * 1024;
constexpr int WS_BYTES = O_H2F + S_ * F_ * 2;
static_assert(O_MODP % 256 == 0 && O_ST1 % 256 == 0 && O_KVG % 256 == 0 && O_X2T % 256 == 0 && O_W1F % 256 == 0, "alignment");

__device__ __forceinline__ void st128(u32x4 v, rsrc_t r, int vo, int so) {
  __builtin_amdgcn_raw_buffer_store_b128(v, r, vo, so, SC1);
  asm volatile("s_nop 1" :: "v"(v) : "memory");
}
constexpr int SPIN_CAP = 1 << 16;
constexpr unsigned CODE_BYTES = 37 * 1024;
__device__ __forceinline__ void st_flag(char* ws, int off, u64 tag) {
  __hip_atomic_store((GAS u64*)(ws + off), tag, __ATOMIC_RELAXED, __HIP_MEMORY_SCOPE_AGENT);
}
__device__ __forceinline__ bool tag_ok(u32x4 v, u64 tag) {
  const u32 lo = (u32)tag, hi = (u32)(tag >> 32);
  return ((v[0] ^ lo) | (v[1] ^ hi) | (v[2] ^ lo) | (v[3] ^ hi)) == 0;
}
template <int NL, int SLEEP> __device__ __forceinline__ void poll(rsrc_t rw, const int (&off)[NL], u64 tag) {
#pragma unroll 1
  for (int sp = 0; sp < SPIN_CAP; ++sp) {
    bool ok = true;
#pragma unroll
    for (int i = 0; i < NL; ++i) ok &= tag_ok(__builtin_amdgcn_raw_buffer_load_b128(rw, off[i], 0, SC1), tag);
    if (__all(ok)) break;
    if (SLEEP) __builtin_amdgcn_s_sleep(SLEEP);
    asm volatile("" ::: "memory");
  }
  asm volatile("" ::: "memory");
}
__device__ __forceinline__ u64 make_tag(u32x2 e) {
  return (((u64)(u32)__builtin_amdgcn_readfirstlane(e[1]) << 32) | (u32)__builtin_amdgcn_readfirstlane(e[0])) + 1;
}
__device__ __forceinline__ void drain_bar() { asm volatile("s_waitcnt vmcnt(0) lgkmcnt(0)\n\ts_barrier" ::: "memory"); }

constexpr int FC_BYTES = 2064 * 2;
constexpr int CH_BYTES = 4 * FC_BYTES;
constexpr int CKV_OFF = 66048;
constexpr int CX_OFF = CKV_OFF + 2048 + 16384, CS_OFF = CX_OFF + 16384;
constexpr int conv_gap(int a, int b) { int n = 0; for (int i = 0; i < a; ++i) n += 8 - i; return n + b; }
struct CSched { int rd[290]; int op[290]; };
constexpr int CBE = PE - 2;
constexpr int CW1 = 12, CW2 = CBE >= 2 ? 12 : (CBE == 1 ? 19 : 35);
constexpr CSched conv_make() {
  CSched S{};
  int rd = 0, op = 0, landed = 0;
  for (int g = 0; g < 288; ++g) {
    const int e = 7 - g / 36, n = g % 36;
    S.rd[g] = rd; S.op[g] = op;
    const bool open = e < CBE || (e == CBE && n >= 24);
    bool ds = e > 0 && n < 24;
    if (open && !ds && rd < 24 && (e == 0 || n < 33)) { ++rd; ds = true; }
    if (e > 0 ? n == 34 : (n == CW1 || n == CW2)) landed = rd;
    int k = ds ? 1 : 2;
    while (k-- > 0 && op < (landed / 3) * 16) ++op;
  }
  S.rd[288] = rd; S.op[288] = op;
  return S;
}
constexpr CSched CS = conv_make();
static_assert(CS.rd[288 - 36 + CW2] == 24, "reads must be out before the last wait");
#define XSP 2
#define XCH 8
constexpr int G_XCHK = 36 * (7 - PE) + XCH;
struct CVSched { int kind[290]; int idx[290]; };
constexpr CVSched cv_make() {
  CVSched S{};
  for (int i = 0; i < 3; ++i) { S.kind[G_XCHK - 44 + 4 * i] = 2; S.idx[G_XCHK - 44 + 4 * i] = i; }
  for (int i = 0; i < 8; ++i) { S.kind[G_XCHK + 4 + XSP * i] = 3 + i / 4; S.idx[G_XCHK + 4 + XSP * i] = i & 3; }
  S.kind[G_XCHK + 4 + XSP * 8] = 5;
  return S;
}
constexpr CVSched CV = cv_make();
constexpr int cv_younger(int g, int kind, int idx) {
  int n = -1;
  for (int i = 0; i <= g; ++i) { if (n >= 0 && CV.kind[i]) ++n; if (CV.kind[i] == kind && CV.idx[i] == idx) n = 0; }
  return n;
}
static_assert(G_XCHK > 44, "conv request schedule");
constexpr int G1_R = 7;
constexpr int G1_SLOTS = 8;
constexpr int G1_LA = 4;
constexpr int G1_SP = G1_SLOTS * 8192;
constexpr int G1_WPRE = 6;
constexpr int g1_ks(int t) { return t < 128 ? t >> 3 : 16 + (t & 3); }
constexpr int g1_j(int t) { return t < 128 ? t & 7 : (t - 128) >> 2; }
struct G1Sched { int kind[256]; int idx[256]; int first[162]; int n; };
#define T_SPOLL 85
#define XPRE 4
constexpr int T_SST = 37;
#define WA 6
#define PLATE 10
#define PSL 1
#define WD_G1 60
#define WD_TEND 156
constexpr int T_WPOLL = 1, T_WCHK = 21;
constexpr G1Sched g1_make() {
  G1Sched S{};
  int n = 0, x = 0, w = 3 * G1_WPRE, wd = 0, rs = 0, credit = 0;
  for (; x < 2 * XPRE; ++x) { S.kind[n] = 0; S.idx[n] = x; ++n; }
  for (int t = 0; t < 160; ++t) {
    S.first[t] = n;
    const int ks = t < 128 ? t >> 3 : 16;
    credit += 3;
    if (credit > 6) credit = 6;
    int kind = -1, idx = 0;
    const bool xok = x < 40 && (x >> 1) <= ks + ((t & 7) >= 4 ? 7 : 6), wok = w < 3 * (20 - WA) && w / 3 <= ks + G1_R - 1;
    const int alive = 3 + 3 * (ks < 20 - WA ? WA : 20 - ks);
    if (t == T_WPOLL) kind = 4;
    else if (t == T_SST) kind = 5;
    else if (t == T_SPOLL || t == T_SPOLL + 1) { kind = 6; idx = t - T_SPOLL; }
    else if (t >= 136 && (t & 3) == 0) { kind = 7; idx = (t - 136) >> 2; }
    else if (credit >= 4) {
      if (xok && (x >> 1) <= ks + 5 + (ks > 8) * 2 && (!wok || (x >> 1) < w / 3)) { kind = 0; idx = x++; }
      else if (wok) { kind = 1; idx = w++; }
      else if (rs < 4 && !(x & 1)) { kind = 3; idx = rs++; }
      else if (t > T_WCHK && t < WD_TEND && wd < WD_G1 && wd + alive < 64) { kind = 2; idx = wd++; }
      else if (xok) { kind = 0; idx = x++; }
    }
    if (kind >= 0) { S.kind[n] = kind; S.idx[n] = idx; ++n; credit -= 4; }
  }
  S.first[160] = n; S.n = n;
  return S;
}
constexpr G1Sched G1S = g1_make();
constexpr int g1_pos(int kind, int idx) { for (int i = 0; i < G1S.n; ++i) if (G1S.kind[i] == kind && G1S.idx[i] == idx) return i; return -1; }
constexpr int g1_wd_count() { int c = 0; for (int i = 0; i < G1S.n; ++i) c += G1S.kind[i] == 2; return c; }
constexpr int G1_WDN = g1_wd_count();
constexpr int imax(int a, int b) { return a > b ? a : b; }
constexpr int imin(int a, int b) { return a < b ? a : b; }
constexpr int g1_need(int t) {
  const int s = t == 124 ? 19 : (t + 4) / 8;
  int p = imax(g1_pos(0, 2 * s + 1), g1_pos(1, 3 * imin(s, 19 - WA) + 2));
  if (t == 108) p = imax(p, g1_pos(6, 1));
  if (t == T_WCHK - 1) p = imax(p, g1_pos(4, 0));
  return p;
}
static_assert(g1_pos(1, 3 * (20 - WA) - 1) >= 0 && g1_pos(0, 39) >= 0 && g1_pos(7, 5) >= 0, "schedule incomplete");
constexpr int G2_R = 28, G2_PRE = 16;
constexpr int XR_OFF = 69632;
constexpr int LDS_BYTES = 118 * 1024;
constexpr int AG_OFF = 0, AU_OFF = 20480, AD_OFF = 40960, AM_OFF = 61440;

__global__ __launch_bounds__(256, 1) void k_fused(const u16* __restrict__ xIn, const u16* __restrict__ kIn, const u16* __restrict__ vIn,
                                                  const u16* __restrict__ shk, const u16* __restrict__ ssmk, const u16* __restrict__ ssmD,
                                                  const u16* __restrict__ wg, const u16* __restrict__ wu, const u16* __restrict__ wd,
                                                  const u16* __restrict__ cIn, const u16* __restrict__ wmod,
                                                  u16* __restrict__ out, char* __restrict__ ws) {
  __shared__ __attribute__((aligned(16))) char smem[LDS_BYTES];
  STAMP_DECL();
  STAMP(0);
  u32 cpf = 0;
  if (blockIdx.x >= 240) {
    uint64_t pc; asm volatile("s_getpc_b64 %0" : "=s"(pc));
    const unsigned co = (((blockIdx.x >> 3) & 1) * 256 + threadIdx.x) * 128;
    asm volatile("global_load_dword %0, %1, %2" : "=v"(cpf) : "v"(co < CODE_BYTES ? co : 0u), "s"(pc & ~(uint64_t)127) : "memory");
  }
  asm volatile("" :: "s"(xIn), "s"(kIn), "s"(vIn), "s"(shk), "s"(ssmk), "s"(ssmD), "s"(wg), "s"(wu), "s"(wd), "s"(cIn), "s"(wmod), "s"(out), "s"(ws));
  u64 tag;
  const int tid = threadIdx.x, lane = tid & 63;
  const int wv = __builtin_amdgcn_readfirstlane(tid >> 6);
  const int b = blockIdx.x;
  const unsigned sbase = (unsigned)(size_t)smem;
  const unsigned l16 = lane * 16;
  const rsrc_t rw = mk_rsrc(ws, WS_BYTES);
#define TSTR 0
  u32 tch = 0;
  if (wv == 3) {
    constexpr int PER = (O_ST1 + 16384 - O_FK + 256 * 128 - 1) / (256 * 128);
    static_assert(PER <= 64, "one request per workgroup");
    asm volatile("global_load_dword %0, %1, %2" : "=v"(tch) : "v"(lane < PER || !TSTR ? O_FK + (b * PER + (lane < PER ? lane : 0)) * 128 : min((lane - PER) * TSTR + b * 128, WS_BYTES - 4)), "s"(ws) : "memory");
  }
  int bs = blockIdx.x;
  asm volatile("" : "+s"(bs));
  const int xcd = bs & 7, idx = bs >> 3;
  const int mt = (xcd & 1) * 8 + (idx & 7), nc = (xcd >> 1) * 4 + (idx >> 3);
  const int M = mt * 2 + ((nc >> 2) & 1), N = (nc >> 3) * 4 + (nc & 3);
  u32x4 WD[60];
  const char* wdp = ws + O_W2F + (N * 48 + wv * 12) * 5120 + 4096;
  const unsigned l16b = l16 + 8192;
  auto issue_wd = [&](auto I) { constexpr int i = I.value; gld128a<((i & 7) - 4) * 1024>(WD[i], (i & 8) ? l16b : l16, wdp + (i >> 4) * 16384); };
  auto clear_slot = [&]() {
    const int so = O_SG + (mt * 2 + ((nc >> 1) & 1)) * 8192 + (((nc & 1) * 4 + wv) * 4 + (nc >> 2)) * 256;
    __builtin_amdgcn_raw_buffer_store_b32(0u, rw, lane * 4, so, SC1);
  };
  if (b >= CWG) {
    const int j = b - CWG;
    const int r8 = lane >> 3, cp = lane & 7;
    const int ks2 = j >> 1, hf = j & 1;
    const int ug = j % 24, kq = j / 24;
    float* LC = (float*)(smem + AM_OFF);
    u16* LS = (u16*)(smem + AM_OFF + 640);
    const int gt = wv * 96 + j;
    const bool gact = gt < 300, urgent = gt < 100;
    const int gq = gt / MODP, gk = gt - gq * MODP, gcol = urgent ? 30 + gq : (gq < 50 ? gq - 20 : gq);
    u32x4 wm[16]; u32 cv[2];
    u32x2 ep = __builtin_amdgcn_raw_buffer_load_b64(rw, O_EPOCH, 0, 0);
    SCHED();
    if (!urgent) __builtin_amdgcn_s_sleep(A0_DELAY);
    if (gact) {
      static_for<0, 2>([&](auto I) { gld16p<I.value * 128>(cv[I.value], lane * 2, (const char*)(cIn + gk * 128)); });
      static_for<0, 16>([&](auto I) { gld128p(wm[I.value], r8 * (NMOD * 2) + cp * 16, (const char*)(wmod + (size_t)(gk * 128 + I.value * 8) * NMOD + gcol * 64)); });
    }
    if (gact) {
      wait_vm<16>();
      float sl[2], sv[16];
#pragma unroll
      for (int i = 0; i < 2; ++i) { pin(cv[i]); const float x = bflo(cv[i]); sl[i] = rbf(x * __builtin_amdgcn_rcpf(1.f + __expf(-x))); }
#pragma unroll
      for (int it = 0; it < 16; ++it) { sv[it] = __shfl(sl[it >> 3], (it & 7) * 8 + r8); pin(sv[it]); }
      f32x2 acc[4];
      static_for<0, 4>([&](auto G) {
        constexpr int g = G.value;
        wait_vm<12 - 4 * g>();
#pragma unroll
        for (int it = 4 * g; it < 4 * g + 4; ++it) {
          pin(wm[it]);
          const f32x2 s2 = {sv[it], sv[it]};
#pragma unroll
          for (int q = 0; q < 4; ++q) {
            const f32x2 w2 = {bflo(wm[it][q]), bfhi(wm[it][q])};
            acc[q] = it ? __builtin_elementwise_fma(s2, w2, acc[q]) : s2 * w2;
          }
        }
#pragma unroll
        for (int q = 0; q < 4; ++q) pin(acc[q]);
      });
      pin(ep);
      const float a0 = tred16(tred32(acc[0][0], acc[2][0]), tred32(acc[1][0], acc[3][0]));
      const float a1 = tred16(tred32(acc[0][1], acc[2][1]), tred32(acc[1][1], acc[3][1]));
      const float e0 = dpp_add<DPP_ROR8>(a0), e1 = dpp_add<DPP_ROR8>(a1);
      __builtin_amdgcn_raw_buffer_store_b32(__float_as_uint((lane & 8) ? e1 : e0), rw, O_MODP + (gk * NMOD + gcol * 64 + cp * 8 + r8) * 4, 0, SC1);
      if (urgent) {
        wait_vm<0>();
        if (lane < 8) st_flag(ws, O_FU + (lane * 14 + (gt >> 3)) * 128 + (gt & 7) * 8, make_tag(ep));
        __builtin_amdgcn_s_sleep(AU_DELAY);
      }
    } else {
      pin(ep);
    }
    tag = make_tag(ep);
    STAMPE(1);
    {
      const unsigned vg = r8 * (F_ * 2) + ((cp ^ ((r8 >> 1) * 2)) * 16);
      const char* gs = (const char*)(wg + (size_t)(kq * 160 + wv * 40) * F_ + ug * 64);
      const char* us = (const char*)(wu + (size_t)(kq * 160 + wv * 40) * F_ + ug * 64);
      static_for<0, 5>([&](auto I) {
        dma16p<AG_OFF + I.value * 1024>(sbase + wv * 5120, vg, gs + I.value * (8 * F_ * 2));
        dma16p<AU_OFF + I.value * 1024>(sbase + wv * 5120, vg ^ 16, us + I.value * (8 * F_ * 2));
      });
    }
    wait_vm<10>();
    bar();
    if (tid < 8) st_flag(ws, O_FA0 + (tid * 12 + (j >> 3)) * 128 + (j & 7) * 8, tag);
    STAMPE(2);
    const int kg = lane >> 4, q4 = (lane & 15) >> 2, p4 = lane & 3;
    if (wv == 0) {
      const int po[1] = {O_FU + ((b & 7) * 14 + (lane < 50 ? lane >> 2 : 0)) * 128 + (lane & 3) * 16};
      poll<1, 1>(rw, po, tag);
      STAMPE(3);
      f32x4 mm[2];
      const int lo = lane < 40 ? lane * 16 : 0;
#pragma unroll
      for (int i = 0; i < 2; ++i) {
        mm[i] = f32x4{0.f, 0.f, 0.f, 0.f};
#pragma unroll
        for (int p = 0; p < MODP; ++p)
          mm[i] += __builtin_bit_cast(f32x4, __builtin_amdgcn_raw_buffer_load_b128(rw, lo, O_MODP + (p * NMOD + (3 + i) * D_ + kq * 160) * 4, SC1));
      }
      if (lane < 40) {
        *(u32x2*)(LS + lane * 4) = u32x2{pack_bf16(mm[0][0], mm[0][1]), pack_bf16(mm[0][2], mm[0][3])};
        *(f32x4*)(LC + lane * 4) = f32x4{rbf(1.f + rbf(mm[1][0])), rbf(1.f + rbf(mm[1][1])), rbf(1.f + rbf(mm[1][2])), rbf(1.f + rbf(mm[1][3]))};
      }
    }
    drain_bar();
    STAMPE(4);
    CPF_DONE();
    clear_slot();
    {
      const char* ds = (const char*)(wd + (size_t)(ks2 * 32 + wv * 8) * D_ + hf * 320);
      static_for<0, 5>([&](auto I) { dma16p<AD_OFF + I.value * 4096>(sbase + wv * 1024, r8 * (D_ * 2) + cp * 16, ds + I.value * 128); });
    }
    {
      const int rr = 4 * kg + q4;
      u32x2 lo[10], hi[10], sbl[5], sbh[5]; u32x4 cl[5], ch[5];
      unsigned ad[2];
#pragma unroll
      for (int t = 0; t < 2; ++t) ad[t] = sbase + (p4 >> 1) * AU_OFF + rr * 128 + (((wv * 2 + t) ^ (((rr >> 1) & 3) * 2 + (p4 >> 1))) * 16) + (p4 & 1) * 8;
      const unsigned ac = sbase + AM_OFF + kg * 16, as = sbase + AM_OFF + 640 + kg * 8;
      auto rd = [&](auto I) {
        constexpr int i = I.value;
        ds_b128<i * 128>(cl[i], ac); ds_b128<i * 128 + 64>(ch[i], ac);
        ds_tr<i * 4096>(lo[2 * i], ad[0]); ds_tr<i * 4096 + 2048>(hi[2 * i], ad[0]); ds_tr<i * 4096>(lo[2 * i + 1], ad[1]); ds_tr<i * 4096 + 2048>(hi[2 * i + 1], ad[1]);
        ds_b64<i * 64>(sbl[i], as); ds_b64<i * 64 + 32>(sbh[i], as);
      };
      rd(IC<0>{});
      const int src = ((lane & 48) + ((lane & 1) * 8 + ((lane & 15) >> 1))) * 4;
      const u32x4 ones = {0x3f803f80u, 0x3f803f80u, 0x3f803f80u, 0x3f803f80u};
      f32x4 aU[2] = {}, aW[2] = {};
      int so[2];
#pragma unroll
      for (int t = 0; t < 2; ++t) { const int T = ug * 8 + wv * 2 + t, sw3 = T / 3; so[t] = O_W1F + ((sw3 * W1_STEPS + kq * 5) * 3 + T - sw3 * 3) * 1024; }
      static_for<0, 5>([&](auto KS) {
        constexpr int ks = KS.value;
        wait_lgkm<0>();
        pin(cl[ks]); pin(ch[ks]); pin(sbl[ks]); pin(sbh[ks]);
        if constexpr (ks < 4) rd(IC<ks + 1>{});
        u32x4 pm[2];
#pragma unroll
        for (int t = 0; t < 2; ++t) {
          pin(lo[2 * ks + t]); pin(hi[2 * ks + t]);
          const u32x4 raw = cat(lo[2 * ks + t], hi[2 * ks + t]);
          u32x4 sc;
#pragma unroll
          for (int q = 0; q < 2; ++q) {
            sc[q] = pack_bf16(bflo(raw[q]) * __uint_as_float(cl[ks][2 * q]), bfhi(raw[q]) * __uint_as_float(cl[ks][2 * q + 1]));
            sc[2 + q] = pack_bf16(bflo(raw[2 + q]) * __uint_as_float(ch[ks][2 * q]), bfhi(raw[2 + q]) * __uint_as_float(ch[ks][2 * q + 1]));
          }
#pragma unroll
          for (int q = 0; q < 4; ++q) pm[t][q] = (u32)__builtin_amdgcn_ds_bpermute(src, sc[q]);
          aW[t] = mfma_b(raw, cat(sbl[ks], sbh[ks]), aW[t]);
          aU[t] = mfma_b(sc, ones, aU[t]);
        }
#pragma unroll
        for (int t = 0; t < 2; ++t) st128(pm[t], rw, l16, so[t] + ks * 3072);
      });
      const f32x4 su = (lane & 4) ? aU[1] : aU[0], sw = (lane & 4) ? aW[1] : aW[0];
      const float U = p4 == 0 ? su[0] : p4 == 1 ? su[1] : p4 == 2 ? su[2] : su[3];
      const float W = p4 == 0 ? sw[0] : p4 == 1 ? sw[1] : p4 == 2 ? sw[2] : sw[3];
      const float u1 = rbf(U), u2 = rbf(U - u1), u3 = U - u1 - u2;
      const float w1 = rbf(W), w2 = rbf(W - w1), w3 = W - w1 - w2;
      const int crow = 2 * (4 * (kg & 1) + p4) + (kg >> 1);
      __builtin_amdgcn_raw_buffer_store_b128(u32x4{pack_bf16(u1, w1), pack_bf16(u2, w2), pack_bf16(u3, w3), 0u}, rw,
                                             (lane & 8) ? WS_BYTES : ((lane & 4) ? so[1] : so[0]) + (20 - kq * 5) * 3072 + (kq * 16 + crow) * 16, 0, SC1);
    }
    STAMPE(5);
  } else {
    const int d = (b >> 1) * 8 + (b & 1) + 2 * wv;
    const int row = lane & 15, kg = lane >> 4;
    const int tt = b & 31, ct = b >> 5;
    const int ov = ((lane >> 2) * 2048 + (lane & 3)) * 16, os = (ct * 16 * 2048 + (tt * 8 + wv * 2) * 4) * 16;
    const int stg = b * 2 + (wv - 2);
    const bool xact = wv >= 2 && b < 128;
    u32 xx[5][8];
    u32 draw; u32x2 ep;
    unsigned aAddr, bA[8];
    const unsigned fl = sbase + wv * CH_BYTES;
    {
      const rsrc_t rk = mk_rsrc(kIn, S_ * D_ * 2), rv = mk_rsrc(vIn, S_ * D_ * 2);
      const int vo = ct * 256 + lane * 4, so = (tt * 64 + wv * 16) * 1280;
      const int sh0 = so ? so - 3 * 1280 : 0;
      u32 kk[19], vv[16];
      const u32x4 taps = *(const u32x4*)(shk + (ct * 64 + lane) * 8);
#pragma unroll
      for (int q = 0; q < 3; ++q) kk[q] = __builtin_amdgcn_raw_buffer_load_b32(rk, vo + q * 1280, sh0, NT);
#pragma unroll
      for (int q = 0; q < 8; ++q) kk[3 + q] = __builtin_amdgcn_raw_buffer_load_b32(rk, vo + (q & 1) * 1280, so + (q & 14) * 1280, NT);
#pragma unroll
      for (int q = 0; q < 8; ++q) vv[q] = __builtin_amdgcn_raw_buffer_load_b32(rv, vo + (q & 1) * 1280, so + (q & 14) * 1280, NT);
#pragma unroll
      for (int q = 8; q < 16; ++q) kk[3 + q] = __builtin_amdgcn_raw_buffer_load_b32(rk, vo + (q & 1) * 1280, so + (q & 14) * 1280, NT);
#pragma unroll
      for (int q = 8; q < 16; ++q) vv[q] = __builtin_amdgcn_raw_buffer_load_b32(rv, vo + (q & 1) * 1280, so + (q & 14) * 1280, NT);
      SCHED();
      SCHED();
      if (F_POS == 0) {
        const char* fs = (const char*)(ssmk + (size_t)d * S_);
        dma16x4p<FC_BYTES + 28>(fl, l16, fs);
        dma16x4p<3 * FC_BYTES + 24>(fl, l16, fs);
      }
      aAddr = fl + (row & 3) * FC_BYTES + 2 * (64 * kg + (row & ~3));
      pin(aAddr);
#pragma unroll
      for (int e = 0; e < 8; ++e) { const int g = 32 + 2 * row + 1 - 8 * kg - e; bA[e] = sbase + CKV_OFF + 16 * ((4 * g + wv) ^ ((g >> 2) & 7)); pin(bA[e]); }
      ep = __builtin_amdgcn_raw_buffer_load_b64(rw, O_EPOCH, 0, 0);
      draw = __builtin_amdgcn_raw_buffer_load_b32(mk_rsrc(ssmD, D_ * 2), (d >> 1) * 4, 0, 0);
      *(u32x2*)(smem + CKV_OFF + tid * 8) = u32x2{0, 0};
      if (lane < 13) *(u32*)(smem + wv * CH_BYTES + (lane < 7 ? FC_BYTES + lane * 4 : 3 * FC_BYTES + (lane - 7) * 4)) = 0;
      SCHED();
      if (so == 0) { kk[0] = 0; kk[1] = 0; kk[2] = 0; }
      const f32x2 w0 = {bflo(taps[0]), bflo(taps[2])}, w1 = {bfhi(taps[0]), bfhi(taps[2])};
      const f32x2 w2 = {bflo(taps[1]), bflo(taps[3])}, w3 = {bfhi(taps[1]), bfhi(taps[3])};
#pragma unroll
      for (int r = 0; r < 2; ++r) {
        f32x2 kv[8];
#pragma unroll
        for (int q = 0; q < 8; ++q) {
          const int i = 8 * r + q;
          f32x2 a = f32x2{bflo(kk[i]), bfhi(kk[i])} * w3;
          a = __builtin_elementwise_fma(f32x2{bflo(kk[i + 1]), bfhi(kk[i + 1])}, w2, a);
          a = __builtin_elementwise_fma(f32x2{bflo(kk[i + 2]), bfhi(kk[i + 2])}, w1, a);
          a = __builtin_elementwise_fma(f32x2{bflo(kk[i + 3]), bfhi(kk[i + 3])}, w0, a);
          kv[q] = a * f32x2{bflo(vv[i]), bfhi(vv[i])};
        }
        u32x4 o0, o1;
#pragma unroll
        for (int t = 0; t < 4; ++t) {
          o0[t] = pack_bf16(kv[7 - 2 * t][0], kv[6 - 2 * t][0]);
          o1[t] = pack_bf16(kv[7 - 2 * t][1], kv[6 - 2 * t][1]);
        }
        st128(o0, rw, ov + r * 64, O_KVG + os);
        st128(o1, rw, ov + r * 64, O_KVG + os + 16384);
      }
    }
    STAMPE(1);
    pin(ep); pin(draw);
    tag = make_tag(ep);
    auto fload = [&]() {
      const char* fs = (const char*)(ssmk + (size_t)d * S_);
      dma16x4p<FC_BYTES + 28>(fl, l16, fs);
      dma16x4p<3 * FC_BYTES + 24>(fl, l16, fs);
    };
    auto xload = [&]() {
      const char* xp = (const char*)xIn + stg * 8 * 1280;
      static_for<0, 40>([&](auto I) { constexpr int t = I.value / 5, c = I.value % 5; gld32p<c * 256 + (t & 1) * 1280>(xx[c][t], lane * 4, xp + (t & 6) * 1280); });
    };
    if (F_POS == 1) fload();
    if (xact && X_POS == 1) {
      xload();
      wait_vm<40 + (F_POS == 1 ? 8 : 0)>();
    } else {
      wait_vm<(F_POS == 1 ? 8 : 0)>();
    }
    asm volatile("s_waitcnt lgkmcnt(0)\n\ts_barrier" ::: "memory");
    if (tid < 32) st_flag(ws, O_FK + ((b & ~31) + tid) * 256 + (b & 31) * 8, tag);
    if (xact && X_POS == 2) xload();
    if (F_POS == 2) fload();
    STAMPE(2);
    int pa1[3] = {O_FX + ((b & 7) * 32 + (lane >> 2)) * 128 + (lane & 3) * 16, O_FX + ((b & 7) * 32 + 16 + (lane >> 2)) * 128 + (lane & 3) * 16,
                  O_FA0 + ((b & 7) * 12 + (lane < 48 ? lane >> 2 : 0)) * 128 + (lane & 3) * 16};
    const char* xgp = ws + O_XG + b * 16384 + wv * 4096;
    const char* stp = ws + O_ST1 + wv * 4096;
    const char* mdp = ws + O_MODP + d * 4;
    asm volatile("" : "+s"(xgp), "+s"(stp), "+s"(mdp));
    pin(pa1[0]); pin(pa1[1]); pin(pa1[2]);
    float Dv = (d & 1) ? bfhi(draw) : bflo(draw);
    unsigned ek = sbase + CKV_OFF + 16 * ((4 * (32 + 2 * row + (kg >> 1)) + wv) ^ ((row >> 1) & 7)) + ((kg & 1) ? 0 : 8);
    unsigned ex = (ek ^ 8) + (CX_OFF - CKV_OFF - 2048), es = sbase + CS_OFF + l16;
    unsigned mo = (((lane & 7) < MODP ? (lane & 7) : 0) * NMOD + ((lane >> 3) & 3) * D_) * 4;
    pin(Dv); pin(ek); pin(ex); pin(es); pin(mo);
    unsigned ko[2] = {(unsigned)(lane ^ (lane >> 4)) * 16, ((unsigned)(lane ^ (lane >> 4)) * 16) ^ 64};
    pin(ko[0]); pin(ko[1]);
    auto fcopies = [&]() {
      u32x4 m[5]; u32 lo[5], hi[5];
      const unsigned src = fl + FC_BYTES + l16, src4 = fl + FC_BYTES + 4096 + (lane & 1) * 16;
      static_for<0, 4>([&](auto I) { constexpr int i = I.value; ds_b128<i * 1024>(m[i], src); ds_b32<i * 1024>(lo[i], src - 4); ds_b32<i * 1024 + 16>(hi[i], src); });
      ds_b128<0>(m[4], src4); ds_b32<0>(lo[4], src4 - 4); ds_b32<16>(hi[4], src4);
      wait_lgkm<0>();
#pragma unroll
      for (int i = 0; i < 5; ++i) {
        pin(m[i]); pin(lo[i]); pin(hi[i]);
        if (i == 0 && lane == 0) lo[0] = 0;
        const u32 s1 = __builtin_amdgcn_alignbit(m[i][1], m[i][0], 16), s2 = __builtin_amdgcn_alignbit(m[i][2], m[i][1], 16), s3 = __builtin_amdgcn_alignbit(m[i][3], m[i][2], 16);
        char* dst = smem + wv * CH_BYTES + (i < 4 ? i * 1024 + l16 : 4096 + (lane & 1) * 16);
        *(u32x4*)dst = u32x4{__builtin_amdgcn_alignbit(m[i][0], lo[i], 16), s1, s2, s3};
        *(u32x4*)(dst + 2 * FC_BYTES) = u32x4{s1, s2, s3, __builtin_amdgcn_alignbit(hi[i], m[i][3], 16)};
      }
    };
    if (!xact) { if (wv == 0 && F_POS != 2) wait_vm<1>(); else wait_vm<0>(); fcopies(); }
    if (xact) {
      wait_vm<(F_POS == 2 ? 8 : 0)>();
      STAMPD(8);
#pragma unroll
      for (int c = 0; c < 5; ++c)
#pragma unroll
        for (int t = 0; t < 8; ++t) pin(xx[c][t]);
#pragma unroll
      for (int c = 0; c < 5; ++c) {
        u32x4 p0, p1;
#pragma unroll
        for (int t = 0; t < 4; ++t) {
          p0[t] = __builtin_amdgcn_perm(xx[c][2 * t + 1], xx[c][2 * t], 0x05040100u);
          p1[t] = __builtin_amdgcn_perm(xx[c][2 * t + 1], xx[c][2 * t], 0x07060302u);
        }
        const int so = O_XG + (c * 16 * 2048 + stg * 4) * 16;
        st128(p0, rw, ov, so);
        st128(p1, rw, ov, so + 16384);
      }
      const bf16x2 ones = {(__bf16)1.0f, (__bf16)1.0f};
      float s1[8], s2[8];
#pragma unroll
      for (int t = 0; t < 8; ++t) {
        s1[t] = 0.f; s2[t] = 0.f;
#pragma unroll
        for (int c = 0; c < 5; ++c) {
          const bf16x2 v = __builtin_bit_cast(bf16x2, xx[c][t]);
          s1[t] = __builtin_amdgcn_fdot2_f32_bf16(v, ones, s1[t], false);
          s2[t] = __builtin_amdgcn_fdot2_f32_bf16(v, v, s2[t], false);
        }
      }
      float q1[2], q2[2];
#pragma unroll
      for (int e = 0; e < 2; ++e) {
        q1[e] = dpp_add<DPP_ROR8>(tred16(tred32(s1[e], s1[4 + e]), tred32(s1[2 + e], s1[6 + e])));
        q2[e] = dpp_add<DPP_ROR8>(tred16(tred32(s2[e], s2[4 + e]), tred32(s2[2 + e], s2[6 + e])));
      }
      const float mean = sum8((lane & 8) ? q1[1] : q1[0]) * (1.f / D_);
      const float var = fmaxf(sum8((lane & 8) ? q2[1] : q2[0]) * (1.f / D_) - mean * mean, 0.f);
      const float sd = rbf(__builtin_amdgcn_sqrtf(rbf(rbf(var) + 1e-5f)));
      const float rs = __builtin_amdgcn_rcpf(sd);
      const int s = stg * 8 + (lane >> 3);
      const int nt = s >> 8, col = (s >> 4) & 15, k4 = (s >> 2) & 3, rr = s & 3;
      if ((lane & 7) == 0) __builtin_amdgcn_raw_buffer_store_b64(u32x2{__float_as_uint(rs), __float_as_uint(-rbf(mean) * rs)}, rw,
                                                                 O_ST1 + (((nt * 2 + (rr >> 1)) * 64 + k4 * 16 + col) * 4 + (rr & 1) * 2) * 4, 0, SC1);
      STAMPD(9);
      if (F_POS == 2) wait_vm<11>();
      fcopies();
      wait_vm<0>();
      if (lane < 8) st_flag(ws, O_FX + (lane * 32 + (stg >> 3)) * 128 + (stg & 7) * 8, tag);
    } else if (wv < 2) {
      const int po[1] = {O_FK + b * 256 + wv * 128 + (lane & 7) * 16};
      poll<1, 0>(rw, po, tag);
      STAMPE(3);
      dma16x4<CKV_OFF + 2048>(sbase + wv * 8192, ko[0], ko[1], ws + O_KVG + b * 16384 + wv * 8192);
      dma16x4<CKV_OFF + 2048 + 4096>(sbase + wv * 8192, ko[0], ko[1], ws + O_KVG + b * 16384 + wv * 8192 + 4096);
      wait_vm<0>();
    }
    STAMPD(10);
    asm volatile("s_waitcnt lgkmcnt(0)\n\ts_barrier" ::: "memory");
    STAMPE(4);
    f32x4 acc[8];
    u32x2 ra[2][8][2]; u32x4 rb[2][8];
    u32x4 pf[3]; u32 mraw; u64 nm[6];
    auto load_n = [&](auto E, auto N) {
      constexpr int e = E.value, n = N.value, bf = e & 1;
      if constexpr (n < 2) ds_b64<16 * e + 8 * n>(ra[bf][0][n], aAddr);
      else if constexpr (n < 10) ds_b128<2048 * (n - 2)>(rb[bf][n - 2], bA[e]);
      else ds_b64<512 * ((n - 8) >> 1) + 16 * e + 8 * (n & 1)>(ra[bf][(n - 8) >> 1][n & 1], aAddr);
    };
    u32x2 kvw[8], xw[8]; u32x4 sA[8], sB[8];
    float T[8][4], Hh[8][4], c0, c1;
    auto kvop = [&](auto K) {
      constexpr int k = K.value, nt = k >> 3, r = k & 3;
      if constexpr ((k & 4) == 0) T[nt][r] = (r & 1) ? v_lo(kvw[nt][1 - (r >> 1)]) : v_hi(kvw[nt][1 - (r >> 1)]);
      else T[nt][r] = v_mul(T[nt][r], Dv);
    };
    auto xrd = [&](auto K) {
      constexpr int k = K.value, nt = k / 3;
      if constexpr (k % 3 == 0) ds_b64<2048 * nt>(xw[nt], ex);
      else if constexpr (k % 3 == 1) ds_b128<2048 * nt>(sA[nt], es);
      else ds_b128<2048 * nt + 1024>(sB[nt], es);
    };
    float xf[4];
    auto xop = [&](auto K) {
      constexpr int k = K.value, nt = k >> 4, ph = (k >> 2) & 3, r = k & 3;
      if constexpr (ph == 0) xf[r] = (r & 1) ? v_hi(xw[nt][r >> 1]) : v_lo(xw[nt][r >> 1]);
      else if constexpr (ph == 1) Hh[nt][r] = v_fma(xf[r], __uint_as_float((r < 2 ? sA[nt] : sB[nt])[2 * (r & 1)]), __uint_as_float((r < 2 ? sA[nt] : sB[nt])[2 * (r & 1) + 1]));
      else if constexpr (ph == 2) Hh[nt][r] = v_fma(Hh[nt][r], c1, c0);
      else T[nt][r] = v_fma(Hh[nt][r], T[nt][r], xf[r]);
    };
    static_for<0, 24>([&](auto N) { load_n(IC<7>{}, N); });
    clear_slot();
    wait_lgkm<14>();
    static_for<0, 8>([&](auto EE) {
      constexpr int e = 7 - EE.value, bf = e & 1;
      static_for<0, 8>([&](auto AA) {
        constexpr int a = AA.value;
        static_for<0, 8 - a>([&](auto BB) {
          constexpr int bb = BB.value, n = conv_gap(a, bb);
          if constexpr (e == 7 && n == 8) wait_lgkm<8>();
          mfma_<(e == 7 && a == 0)>(acc[a + bb], cat(ra[bf][a][0], ra[bf][a][1]), rb[bf][bb]);
          if constexpr (e > 0 && n < 24) load_n(IC<(e > 0 ? e - 1 : 0)>{}, IC<n>{});
          if constexpr (e == 7 && n >= 24 && n < 32) ds_b64<2048 * (n - 24)>(kvw[n - 24], ek);
          constexpr int g = 36 * (7 - e) + n;
          static_for<CS.rd[g], CS.rd[g + 1]>(xrd);
          if constexpr (e > 0 ? n == 34 : (n == CW1 || n == CW2)) wait_lgkm<0>();
          static_for<CS.op[g], CS.op[g + 1]>(xop);
          if constexpr ((e == 6 || e == 5) && n < 32) kvop(IC<(6 - e) * 32 + n>{});
          if constexpr (CV.kind[g] == 2) gld128<0>(pf[CV.idx[g]], pa1[CV.idx[g]], ws);
          if constexpr (CV.kind[g] == 3) { if constexpr (CV.idx[g] == 0) dma16<CX_OFF>(sbase + wv * 4096, ko[0], xgp); else dma16n<CV.idx[g] * 1024>(ko[CV.idx[g] & 1], xgp); }
          if constexpr (CV.kind[g] == 4) { if constexpr (CV.idx[g] == 0) dma16<CS_OFF>(sbase + wv * 4096, l16, stp); else dma16n<CV.idx[g] * 1024>(l16, stp); }
          if constexpr (CV.kind[g] == 5) gld32(mraw, mo, mdp);
          if constexpr (g == G_XCHK) { STAMPD(11); wait_vm<cv_younger(g, 2, 2)>(); }
          if constexpr (g >= G_XCHK && g < G_XCHK + 3) { constexpr int i = g - G_XCHK; nm[2 * i] = v_ne64(u32x2{pf[i][0], pf[i][1]}, tag); nm[2 * i + 1] = v_ne64(u32x2{pf[i][2], pf[i][3]}, tag); }
          if constexpr (g == G_XCHK + 3) {
            if (__builtin_expect(((nm[0] | nm[1]) | (nm[2] | nm[3]) | (nm[4] | nm[5])) != 0, 0)) poll<3, 1>(rw, pa1, tag);
            STAMPD(12);
          }
          if constexpr (e == CBE && n == 22) {
            STAMPD(13);
            wait_vm<cv_younger(g, 5, 0)>(); bar(); pin(mraw);
            STAMPD(14);
            const float mv = rbf(sum8((lane & 7) < MODP ? __uint_as_float(mraw) : 0.f));
            const float m_ga = rdl(mv, 16);
            c0 = rdl(mv, 0) * m_ga; c1 = rbf(1.f + rdl(mv, 8)) * m_ga;
            pin(c0); pin(c1);
          }
        });
      });
    });
    STAMPE(5);
    asm volatile("s_nop 7");
    static_for<0, 4>([&](auto NP) {
      constexpr int np = NP.value;
      static_for<(CS.op[288] > 32 * np ? CS.op[288] : 32 * np), 32 * np + 32>(xop);
      u32x4 o;
#pragma unroll
      for (int u = 0; u < 2; ++u) {
        const int nt = 2 * np + u;
        pin(acc[nt]);
        o[2 * u] = pack_bf16(fmaf(Hh[nt][0], acc[nt][0], T[nt][0]), fmaf(Hh[nt][1], acc[nt][1], T[nt][1]));
        o[2 * u + 1] = pack_bf16(fmaf(Hh[nt][2], acc[nt][2], T[nt][2]), fmaf(Hh[nt][3], acc[nt][3], T[nt][3]));
      }
      st128(o, rw, l16 + np * 1024, O_X2T + d * (TP_ * 2));
    });
  }
  const int kg = lane >> 4, c16 = lane & 15;
  const int kq0 = nc >> 2, r0 = (nc & 1) * 4 + wv, sgb = O_SG + (mt * 2 + ((nc >> 1) & 1)) * 8192;
  asm volatile("" :: "v"(tch));
  u32x2 tag2 = {(u32)tag, (u32)(tag >> 32)};
  u32x4 gbr;
  pin(tag2);
  {
    const int fb = O_F2 + (b & 7) * 32 * 128;
    const int jw = (lane & 3) * 24 + ((nc * 12 + wv * 3 + ((lane >> 2) & 1) * 2) >> 3);
    const int pa[1] = {fb + (20 + (jw >> 3)) * 128 + (jw & 6) * 8};
    u32x4 pfa;
    gld128<0>(pfa, pa[0], ws);
    const char* wsrc = ws + O_W1F + (nc * 4 + wv) * W1_STEPS * 3072;
    const char* wq[4];
    unsigned xinc[4];
#pragma unroll
    for (int i = 0; i < 4; ++i) {
      wq[i] = wsrc + ((kq0 + i) & 3) * (5 * 3072) + 4096;
      xinc[i] = (i && kq0 + i == 4) ? (unsigned)(-19 * 32 * TP_ * 2) : 32 * TP_ * 2;
      asm volatile("" : "+s"(wq[i]), "+s"(xinc[i]));
    }
    u32x4 W[G1_R][3], WA_[WA > 0 ? 3 * WA : 1], WC[3], pfs[2], pfw, gran;
    auto issue_w = [&](auto I) { constexpr int i = I.value; gld128b<(((i % 15) & 7) - 4) * 1024>(W[(i / 3) % G1_R][i % 3], ((i % 15) & 8) ? l16b : l16, wq[i / 15]); };
    int pc[2] = {fb + (lane >> 2) * 128 + (lane & 3) * 16, fb + (16 + (lane >> 2)) * 128 + (lane & 3) * 16};
    int f2o = lane < 8 ? O_F2 + (lane * 32 + (b >> 3)) * 128 + (b & 7) * 8 : WS_BYTES;
    pin(pc[0]); pin(pc[1]); pin(f2o);
    STAMPE(6);
    asm volatile("s_waitcnt vmcnt(1) lgkmcnt(0)\n\ts_barrier" ::: "memory");
    if (wv == 0) __builtin_amdgcn_raw_buffer_store_b64(tag2, rw, f2o, 0, SC1);
    STAMPE(7);
    if (bs >= CWG) {
      const int j = bs - CWG;
      u32x2 dl[5], dh[5];
      const unsigned add = sbase + AD_OFF + (8 * kg + (c16 >> 2)) * 128 + (lane & 3) * 8;
#pragma unroll
      for (int i = 0; i < 5; ++i) { const int tl = wv * 5 + i; const unsigned a2 = add + (tl >> 2) * 4096 + (tl & 3) * 32; ds_tr<0>(dl[i], a2); ds_tr<512>(dh[i], a2); }
      wait_lgkm<0>();
#pragma unroll
      for (int i = 0; i < 5; ++i) {
        pin(dl[i]); pin(dh[i]);
        __builtin_amdgcn_raw_buffer_store_b128(cat(dl[i], dh[i]), rw, l16, O_W2F + ((((j & 1) * 4 + wv) * 48 + (j >> 1)) * 5 + i) * 1024, SC1);
      }
      drain_bar();
      if (tid < 8) st_flag(ws, O_FW + (tid * 12 + (j >> 3)) * 128 + (j & 7) * 8, tag);
    }

    if (wv == 0) wait_vm<1>(); else wait_vm<0>();
    pin(pfa);
    if (!__all(tag_ok(pfa, tag))) poll<1, 2>(rw, pa, tag);
    auto issue_a = [&](auto I) { constexpr int i = 3 * (20 - WA) + I.value; gld128a<(((i % 15) & 7) - 4) * 1024>(WA_[I.value], ((i % 15) & 8) ? l16b : l16, wq[i / 15]); };
    static_for<0, 3 * G1_WPRE>(issue_w);
    unsigned xoff[2];
#pragma unroll
    for (int p = 0; p < 2; ++p) {
      const int k = 4 * p + kg;
      xoff[p] = (k * TP_ + ((c16 ^ (2 * k)) * 8)) * 2 - p * 1024;
      pin(xoff[p]);
    }
    unsigned xso = O_X2T + ((kq0 * 160 - 32 + 8 * wv) * TP_ + mt * 128) * 2;
    const u32x4 wsrd = {(u32)(uint64_t)ws, (u32)((uint64_t)ws >> 32) & 0xffffu, (u32)WS_BYTES, 0x00020000u};
    const unsigned ldsw = sbase + wv * 2048;
    unsigned sgl = sgb + wv * 2048 + l16;
    int hvo = kg < 3 ? ((3 * wv + kg) >> 2) * 1024 + ((3 * wv + kg) & 3) * 256 + c16 * 16 : WS_BYTES;
    const int hso = O_H2F + (mt * 8 * 48 + 3 * nc) * 1024;
    unsigned spa = sbase + G1_SP + r0 * 256 + c16 * 16;
    int sgo = (c16 >> 2) == kg ? sgb + (r0 * 4 + kq0) * 256 + c16 * 16 : WS_BYTES;
    pin(sgl); pin(hvo); pin(sgo); pin(spa);
    int pw[1] = {O_FW + ((b & 7) * 12 + (lane < 48 ? lane >> 2 : 0)) * 128 + (lane & 3) * 16};
    unsigned xo = ((lane >> 3) * TP_ + (lane & 7) * 8) * 2;
    const char* xs = ws + O_X2T + (N * 80 * TP_ + M * 64) * 2;
    unsigned gbo = O_MODP + (((lane & 7) < MODP ? (lane & 7) : 0) * NMOD + 5 * D_ + N * 80 + wv * 20 + (lane < 40 ? lane >> 3 : 0) * 4) * 4;
    pin(pw[0]); pin(xo); pin(gbo);
    u32 hq[4]; u32x4 hs;
    int hto[8];
#pragma unroll
    for (int i = 0; i < 8; ++i) { hto[i] = hso + ((i + r0) & 7) * (48 * 1024); asm volatile("" : "+s"(hto[i])); }
    auto issue = [&](auto O) {
      constexpr int kind = G1S.kind[O.value], i = G1S.idx[O.value];
      if constexpr (kind == 0) {
        if constexpr (i & 1) xdma1(xoff[1], wsrd, xso);
        else xdma0<((i >> 1) % G1_SLOTS) * 8192>(ldsw, xoff[0], wsrd, xso, xinc[(i % 10) ? 0 : i / 10]);
      }
      else if constexpr (kind == 1) issue_w(IC<i>{});
      else if constexpr (kind == 2) issue_wd(IC<i>{});
      else if constexpr (kind == 3) {
        if constexpr (i < 2) dma16<XR_OFF + i * 4096>(sbase + wv * 1024, xo, xs + (size_t)(wv + 4 * i) * 8 * TP_ * 2);
        else if constexpr (i == 2) dma16<XR_OFF + 8192>(sbase + (wv & 1) * 1024, xo, xs + (size_t)((wv & 1) + 8) * 8 * TP_ * 2);
        else gld128<0>(gbr, gbo, ws);
      }
      else if constexpr (kind == 4) gld128<0>(pfw, pw[0], ws);
      else if constexpr (kind == 5) __builtin_amdgcn_raw_buffer_store_b128(gran, rw, sgo, 0, SC1);
      else if constexpr (kind == 6) gld128<i * 1024>(pfs[i], sgl, ws);
      else { hs = u32x4{hq[0], hq[1], hq[2], hq[3]}; __builtin_amdgcn_raw_buffer_store_b128(hs, rw, hvo, hto[i], SC1); }
    };
    unsigned xa[8];
#pragma unroll
    for (int jj = 0; jj < 8; ++jj) {
      const int rr = 4 * kg + (c16 >> 2);
      xa[jj] = sbase + rr * 256 + ((((jj + r0) & 7) ^ (rr & 7)) * 32) + (lane & 3) * 8;
      pin(xa[jj]);
    }
    u32x4 ones = {0x3f803f80u, 0x3f803f80u, 0x3f803f80u, 0x3f803f80u};
    pin(ones);
    static_for<0, 3>([&](auto I) { gld128a<I.value * 1024>(WC[I.value], l16, wsrc + 20 * 3072); });
    static_for<0, 3 * WA - PLATE>(issue_a);
    if (wv == 0) { gld128<0>(pfs[0], pc[0], ws); gld128<0>(pfs[1], pc[1], ws); }
    static_for<3 * WA - PLATE, 3 * WA>(issue_a);
    STAMPL(8);
    if (wv == 0) {
      wait_vm<PLATE>();
      pin(pfs[0]); pin(pfs[1]);
      if (!__all(tag_ok(pfs[0], tag) && tag_ok(pfs[1], tag))) poll<2, PSL>(rw, pc, tag);
    }
    bar();
    STAMPL(9);
    static_for<0, G1S.first[0]>(issue);
    f32x4 acc[8][3];
    u32x2 X[G1_LA + 1][2];
    auto xread = [&](auto T, auto H) {
      constexpr int t = T.value, h = H.value;
      ds_tr<(g1_ks(t) % G1_SLOTS) * 8192 + h * 4096>(X[t % (G1_LA + 1)][h], xa[g1_j(t)]);
    };
    f32x4 gram, sum1;
    u32 fs[4], fa; u64 sbad;
    unsigned spw = sbase + G1_SP + (2 * wv + (kg >> 1) + (kg & 1) * 8) * 256 + c16 * 16;
    pin(spw);
    auto fop = [&](auto K) {
      constexpr int k = K.value;
      if constexpr (k == 0) asm_and3(fa, pfs[0][1], pfs[0][3], pfs[1][1]);
      else if constexpr (k == 1) asm_and3(fa, fa, pfs[1][3], pfs[1][3]);
      else if constexpr (k == 2) asm_ne1(sbad, fa);
      else if constexpr (k == 4) v_swap32(fs[0], fs[1]);
      else if constexpr (k == 5) v_swap32(fs[2], fs[3]);
      else if constexpr (k == 6) asm_op2<0>(fs[0], fs[0], fs[1]);
      else if constexpr (k == 7) asm_op2<0>(fs[2], fs[2], fs[3]);
      else if constexpr (k == 8) v_swap16(fs[0], fs[2]);
      else if constexpr (k == 9) asm_op2<0>(fs[0], fs[0], fs[2]);
      else if constexpr (k == 10) asm_op1<0>(fs[0], fs[0]);
      else if constexpr (k == 11) asm_op1<1>(fs[1], fs[0]);
      else if constexpr (k == 12) v_swap16(fs[0], fs[1]);
      else if constexpr (k == 13) asm_op2<1>(fs[1], fs[0], fs[1]);
      else if constexpr (k == 14) asm_op1<2>(fs[1], fs[1]);
      else if constexpr (k == 15) asm_op1<3>(fs[1], fs[1]);
      else if constexpr (k == 16) asm_op1<4>(fs[1], fs[1]);
      else if constexpr (k == 17) asm_op1<3>(fs[1], fs[1]);
      else if constexpr (k == 18) asm_op1<5>(fs[1], fs[1]);
      else if constexpr (k == 19) asm_op2<2>(fs[0], fs[0], fs[1]);
      else if constexpr (k == 20) asm_op1<1>(fs[1], fs[0]);
      else if constexpr (k == 21) asm_op1<1>(fs[2], fs[0]);
      else if constexpr (k == 22) asm_op1<6>(fs[3], fs[0]);
      else if constexpr (k == 23) ds_w128u<0>(spw, u32x4{fs[0], fs[1], fs[2], fs[3]});
    };
    float eg[6]; u32x4 xc[2];
    auto eop = [&](auto J, auto K) {
      constexpr int j = J.value, k = K.value;
      if constexpr (k < 15) {
        if constexpr (k < 6) eg[k] = v_relu(acc[j][k >> 1][2 * (k & 1)]);
        else if constexpr (k < 12) eg[k - 6] = v_mul(eg[k - 6], acc[j][(k - 6) >> 1][2 * (k & 1) + 1]);
        else hq[k == 12 ? 1 : k == 13 ? 0 : 2] = v_cvt(eg[k == 12 ? 2 : k == 13 ? 0 : 4], eg[k == 12 ? 3 : k == 13 ? 1 : 5]);
      } else if constexpr (k == 15) v_swap32(hq[1], hq[3]);
      else if constexpr (k == 16) v_swap32(hq[0], hq[2]);
      else if constexpr (k == 17) v_swap16(hq[0], hq[1]);
      else v_swap16(hq[2], hq[3]);
    };
    hq[3] = 0;
    pin(hq[3]);
    wait_vm<G1S.first[0] - 2>();
    bar();
    STAMPL(10);
    static_for<0, G1_LA>([&](auto T) { xread(T, IC<0>{}); xread(T, IC<1>{}); });
    static_for<0, 160>([&](auto T) {
      constexpr int t = T.value, ks = g1_ks(t), jj = g1_j(t), tn = t + G1_LA, u = t & 3;
      if constexpr (t == 24) STAMPG(1);
      if constexpr (t == 48) STAMPG(2);
      if constexpr (t == 72) STAMPG(3);
      if constexpr (t == 96) STAMPG(4);
      if constexpr (t == 124) STAMPG(5);
      if constexpr (t == 126) STAMPG(6);
      if constexpr (!(t & 1)) wait_lgkm<2 * ((tn <= 160 ? tn : 160) - t - 2)>();
      if constexpr (t <= 124 && (t & 7) == 4) {
        static_assert(g1_need(t) < G1S.first[t], "not issued yet");
        wait_vm<G1S.first[t] - 1 - g1_need(t)>();
        bar();
      }
      if constexpr (t == T_WCHK) {
        if (__builtin_expect((v_ne64(u32x2{pfw[0], pfw[1]}, tag) | v_ne64(u32x2{pfw[2], pfw[3]}, tag)) != 0, 0)) poll<1, 1>(rw, pw, tag);
      }
      if constexpr (t == 35) {
        pin(gram); pin(sum1);
        const int c3 = lane & 3;
        gran = u32x4{__float_as_uint(sum1[0]), 1u, __float_as_uint(c3 == 0 ? gram[0] : c3 == 1 ? gram[1] : c3 == 2 ? gram[2] : gram[3]), 1u};
        pin(gran);
      }
      if constexpr (t == 111) {
        if (__builtin_expect(sbad != 0, 0)) {
#pragma unroll 1
          for (int spn = 0; spn < SPIN_CAP; ++spn) {
            pfs[0] = __builtin_amdgcn_raw_buffer_load_b128(rw, sgl, 0, SC1);
            pfs[1] = __builtin_amdgcn_raw_buffer_load_b128(rw, sgl + 1024, 0, SC1);
            if (__all((pfs[0][1] & pfs[0][3] & pfs[1][1] & pfs[1][3]) == 1u)) break;
            asm volatile("" ::: "memory");
          }
        }
        fs[0] = pfs[0][0]; fs[1] = pfs[1][0]; fs[2] = pfs[0][2]; fs[3] = pfs[1][2];
      }
      if constexpr (t == 123) wait_lgkm<0>();
      auto ep = [&](auto A, auto B) { if constexpr (t >= 132) static_for<A.value, B.value>([&](auto K) { eop(IC<jj - 1>{}, K); }); };
      constexpr int k0 = u == 0 ? 0 : 4 * u - 3;
      const u32x4 xb = cat(X[t % (G1_LA + 1)][0], X[t % (G1_LA + 1)][1]);
      if constexpr (ks >= 20 - WA) mfma_a<false>(acc[jj][0], WA_[3 * (ks - (20 - WA)) + 0], xb); else mfma_<ks == 0>(acc[jj][0], W[ks % G1_R][0], xb);
      if constexpr (tn < 160) xread(IC<tn>{}, IC<0>{});
      if constexpr (t >= 109 && t < 121) fop(IC<2 * (t - 109)>{});
      if constexpr (u > 0) ep(IC<k0>{}, IC<k0 + 1>{});
      if constexpr (t >= 136 && u == 1) keep(hs);
      if constexpr (t >= 136 && u == 0) eop(IC<(t >= 136 ? jj - 2 : 0)>{}, IC<18>{});
      if constexpr (ks >= 20 - WA) mfma_a<false>(acc[jj][1], WA_[3 * (ks - (20 - WA)) + 1], xb); else mfma_<ks == 0>(acc[jj][1], W[ks % G1_R][1], xb);
      if constexpr (tn < 160) xread(IC<tn>{}, IC<1>{});
      if constexpr (t >= 109 && t < 121) fop(IC<2 * (t - 109) + 1>{});
      if constexpr (u > 0) ep(IC<k0 + 1>{}, IC<k0 + 2>{}); else ep(IC<0>{}, IC<1>{});
      if constexpr (ks >= 20 - WA) mfma_a<false>(acc[jj][2], WA_[3 * (ks - (20 - WA)) + 2], xb); else mfma_<ks == 0>(acc[jj][2], W[ks % G1_R][2], xb);
      if constexpr (t >= 128 && (u > 0 || t < 136)) real();
      static_for<G1S.first[t], G1S.first[t + 1]>(issue);
      if constexpr (u > 0) ep(IC<k0 + 2>{}, IC<k0 + 4>{});
      if constexpr (t < 40 && jj == 0) { mfma_<ks == 0>(gram, xb, xb); mfma_<ks == 0>(sum1, ones, xb); }
      if constexpr (t >= 128 && u == 3) {
        mfma_a<false>(acc[jj][0], WC[0], xc[jj & 1]);
        real();
        ep(IC<13>{}, IC<15>{});
        mfma_a<false>(acc[jj][1], WC[1], xc[jj & 1]);
        real();
        ep(IC<15>{}, IC<17>{});
        mfma_a<false>(acc[jj][2], WC[2], xc[jj & 1]);
        real();
        ep(IC<17>{}, IC<18>{});
      }
      if constexpr (t >= 124 && t < 156 && u == 0) ds_b128<((t - 124) >> 2) * 256>(xc[((t - 124) >> 2) & 1], spa);
    });
    STAMPL(11);
    eop(IC<6>{}, IC<18>{});
    __builtin_amdgcn_raw_buffer_store_b128(u32x4{hq[0], hq[1], hq[2], hq[3]}, rw, hvo, hto[6], SC1);
    asm volatile("s_nop 3");
    static_for<0, 15>([&](auto K) { eop(IC<7>{}, K); });
    eop(IC<7>{}, IC<15>{}); asm volatile("s_nop 0"); eop(IC<7>{}, IC<16>{}); asm volatile("s_nop 1"); eop(IC<7>{}, IC<17>{}); eop(IC<7>{}, IC<18>{});
    __builtin_amdgcn_raw_buffer_store_b128(u32x4{hq[0], hq[1], hq[2], hq[3]}, rw, hvo, hto[7], SC1);
  }
  STAMPL(12);
  float rs2;
  {
    const u32 p = *(const u32*)(smem + G1_SP + ((nc & 4) + wv) * 256 + c16 * 16);
    const float rs = __builtin_amdgcn_rcpf(bfhi(p));
    rs2 = rs * rs;
    pin(rs2);
  }
  const char* hp[4];
  unsigned pwa[3];
#pragma unroll
  for (int jj = 0; jj < 4; ++jj) { hp[jj] = ws + O_H2F + ((M * 4 + ((jj + wv + 1) & 3)) * 48 + wv * 12) * 1024 + 4096; asm volatile("" : "+s"(hp[jj])); }
#pragma unroll
  for (int jj = 0; jj < 3; ++jj) { pwa[jj] = sbase + ((jj + wv + 1) & 3) * 15360 + jj * 5120 + l16; pin(pwa[jj]); }
  unsigned pra = sbase + wv * 15360 + l16, gba = sbase + XR_OFF + 10240 + kg * 16;
  unsigned xad = sbase + XR_OFF + (4 * kg + (c16 >> 2)) * 128 + wv * 32 + (lane & 3) * 8;
  int oo;
  {
    const int pos = M * 64 + wv * 16 + c16;
    const int l8 = (pos >> 3) & 63;
    const int tok = 256 * (2 * (pos >> 9) + ((pos >> 2) & 1)) + 16 * (l8 & 15) + 4 * (l8 >> 4) + (pos & 3);
    oo = (tok * D_ + N * 80 + 4 * kg) * 2;
  }
  pin(pra); pin(gba); pin(xad); pin(oo);
  int po3[1] = {O_F3 + (mt * 16 + nc) * 128 + wv * 32 + (lane & 1) * 16};
  int f3o = tid < 16 ? O_F3 + (mt * 16 + tid) * 128 + nc * 8 : WS_BYTES;
  pin(po3[0]); pin(f3o);
  {
    pin(gbr);
    const float g = sum8((lane & 7) < MODP ? __uint_as_float(gbr[0]) : 0.f), g2 = sum8((lane & 7) < MODP ? __uint_as_float(gbr[1]) : 0.f);
    const float g3 = sum8((lane & 7) < MODP ? __uint_as_float(gbr[2]) : 0.f), g4 = sum8((lane & 7) < MODP ? __uint_as_float(gbr[3]) : 0.f);
    if ((lane & 7) == 0 && lane < 40) *(f32x4*)(smem + XR_OFF + 10240 + wv * 80 + (lane >> 3) * 16) = f32x4{rbf(g), rbf(g2), rbf(g3), rbf(g4)};
  }
  drain_bar();
  if (wv == 0) __builtin_amdgcn_raw_buffer_store_b64(tag2, rw, f3o, 0, SC1);
  STAMPG(7);
  static_for<G1_WDN, 60>(issue_wd);
  poll<1, 0>(rw, po3, tag);
  STAMPL(13);
  u32x4 H[G2_R];
  auto issue_h = [&](auto O) { constexpr int o = O.value; gld128b<(((o % 12) & 7) - 4) * 1024>(H[o % G2_R], ((o % 12) & 8) ? l16b : l16, hp[o / 12]); };
  static_for<0, G2_PRE>(issue_h);
  f32x4 acc[4][5], pt[3][5], gbv[5];
  u32x2 xw[5];
  float G[5][4], Tt[5][4];
  static_for<0, 240>([&](auto GG) {
    constexpr int g = GG.value, o = g / 5, jj = o / 12, ks = o % 12, i = g % 5, q = g % 60;
    constexpr int i0 = (G2_PRE + (g + 3) / 4 < 48) ? G2_PRE + (g + 3) / 4 : 48;
    static_assert(i0 > o && i0 < o + G2_R, "ring");
    if constexpr (i == 0) wait_vm<i0 - 1 - o>();
    mfma_a<ks == 0>(acc[jj][i], WD[ks * 5 + i], H[o % G2_R]);
    if constexpr ((g & 3) == 0 && i0 < 48) issue_h(IC<i0>{});
    if constexpr (jj >= 1 && q >= 5 && q < 15 && (q & 1)) ds_w128<((q - 5) / 2) * 1024>(pwa[jj - 1], acc[jj - 1][(q - 5) / 2]);
    if constexpr (g >= 81 && g < 86) ds_tr<(g - 81) * 2048>(xw[g - 81], xad);
    if constexpr (g >= 86 && g < 91) ds_f128<(g - 86) * 64>(gbv[g - 86], gba);
    if constexpr (g == 96) wait_lgkm<0>();
    if constexpr (g >= 97 && g < 117) {
      constexpr int k = g - 97;
      Tt[k >> 2][k & 3] = (k & 1) ? v_hi(xw[k >> 2][(k >> 1) & 1]) : v_lo(xw[k >> 2][(k >> 1) & 1]);
      G[k >> 2][k & 3] = v_mul(gbv[k >> 2][k & 3], rs2);
    }
    if constexpr (g == 198) { wait_lgkm<0>(); bar(); }
    if constexpr (g >= 199 && g < 214) ds_f128<(g - 199) * 1024>(pt[(g - 199) / 5][(g - 199) % 5], pra);
    if constexpr (g == 208) wait_lgkm<5>();
    if constexpr (g == 218) wait_lgkm<0>();
    if constexpr (g >= 209) {
      constexpr int k0 = g < 214 ? g - 209 : 5 + 2 * (g - 214), k1 = g < 214 ? k0 + 1 : k0 + 2;
      static_for<k0, (k1 < 60 ? k1 : 60)>([&](auto K) { constexpr int k = K.value; Tt[(k % 20) >> 2][k & 3] = v_fma(G[(k % 20) >> 2][k & 3], pt[k / 20][(k % 20) >> 2][k & 3], Tt[(k % 20) >> 2][k & 3]); });
    }
  });
  STAMPL(14);
  static_for<57, 60>([&](auto K) { constexpr int k = K.value; Tt[(k % 20) >> 2][k & 3] = v_fma(G[(k % 20) >> 2][k & 3], pt[k / 20][(k % 20) >> 2][k & 3], Tt[(k % 20) >> 2][k & 3]); });
  asm volatile("s_nop 2");
  const rsrc_t ro = mk_rsrc(out, S_ * D_ * 2);
#pragma unroll
  for (int i = 0; i < 5; ++i) {
    pin(acc[3][i]);
    __builtin_amdgcn_raw_buffer_store_b64(u32x2{pack_bf16(fmaf(G[i][0], acc[3][i][0], Tt[i][0]), fmaf(G[i][1], acc[3][i][1], Tt[i][1])),
                                                pack_bf16(fmaf(G[i][2], acc[3][i][2], Tt[i][2]), fmaf(G[i][3], acc[3][i][3], Tt[i][3]))}, ro, oo + i * 32, 0, 0);
  }
  if (b == 0 && tid == 0) st_flag(ws, O_EPOCH, tag);
  STAMP_END();
}

extern "C" void kernel_launch(void* const* d_in, const int* in_sizes, int n_in, void* d_out, int out_size,
                              void* d_ws, size_t ws_size, hipStream_t stream) {
  k_fused<<<256, 256, 0, stream>>>((const u16*)d_in[0], (const u16*)d_in[1], (const u16*)d_in[2], (const u16*)d_in[3],
                                   (const u16*)d_in[4], (const u16*)d_in[5], (const u16*)d_in[7], (const u16*)d_in[8],
                                   (const u16*)d_in[9], (const u16*)d_in[10], (const u16*)d_in[11], (u16*)d_out, (char*)d_ws);
}
```

```cpp
#define TOUCH
#include <hip/hip_runtime.h>
#include <stdint.h>

typedef unsigned short u16;
typedef unsigned int u32;
typedef __attribute__((ext_vector_type(2))) __bf16 bf16x2;
typedef __attribute__((ext_vector_type(4))) float f32x4;
typedef __attribute__((ext_vector_type(2))) float f32x2;
typedef __attribute__((ext_vector_type(4))) u32 u32x4;
typedef __attribute__((ext_vector_type(2))) u32 u32x2;

constexpr int S_ = 2048, D_ = 640, F_ = 1536;
constexpr int TP_ = S_ + 64;
constexpr int NMOD = 6 * D_;
constexpr int MODP = 5;
constexpr int CWG = 160;

__device__ __forceinline__ float bflo(u32 w) { return __uint_as_float(w << 16); }
__device__ __forceinline__ float bfhi(u32 w) { return __uint_as_float(w & 0xffff0000u); }
__device__ __forceinline__ u32 pack_bf16(float lo, float hi) {
  bf16x2 v; v[0] = (__bf16)lo; v[1] = (__bf16)hi; return __builtin_bit_cast(u32, v);
}
__device__ __forceinline__ float rbf(float x) { __bf16 b = (__bf16)x; return (float)b; }
#define SCHED() __builtin_amdgcn_sched_barrier(0)
template <int V> struct IC { static constexpr int value = V; };
template <int B, int E, class Fn> __device__ __forceinline__ void static_for(Fn&& f) {
  if constexpr (B < E) { f(IC<B>{}); static_for<B + 1, E>(f); }
}
__device__ __forceinline__ void mfma(f32x4& c, u32x4 a, u32x4 b) {
  asm volatile("v_mfma_f32_16x16x32_bf16 %0, %1, %2, %0" : "+v"(c) : "v"(a), "v"(b));
}
__device__ __forceinline__ void mfma0(f32x4& c, u32x4 a, u32x4 b) {
  asm volatile("v_mfma_f32_16x16x32_bf16 %0, %1, %2, 0" : "=&v"(c) : "v"(a), "v"(b));
}
template <bool FIRST> __device__ __forceinline__ void mfma_(f32x4& c, u32x4 a, u32x4 b) { if constexpr (FIRST) mfma0(c, a, b); else mfma(c, a, b); }
__device__ __forceinline__ u32 alignbit(u32 hi, u32 lo, u32 sh) {
  u32 d; asm volatile("v_alignbit_b32 %0, %1, %2, %3" : "=v"(d) : "v"(hi), "v"(lo), "v"(sh)); return d;
}
template <int OFF> __device__ __forceinline__ void ds_tr(u32x2& dst, unsigned addr) {
  asm volatile("ds_read_b64_tr_b16 %0, %1 offset:%2" : "=v"(dst) : "v"(addr), "i"(OFF) : "memory");
}
template <int OFF> __device__ __forceinline__ void ds_b32(u32& dst, unsigned addr) {
  asm volatile("ds_read_b32 %0, %1 offset:%2" : "=v"(dst) : "v"(addr), "i"(OFF) : "memory");
}
template <int OFF> __device__ __forceinline__ void ds_b64(u32x2& dst, unsigned addr) {
  asm volatile("ds_read_b64 %0, %1 offset:%2" : "=v"(dst) : "v"(addr), "i"(OFF) : "memory");
}
template <int OFF> __device__ __forceinline__ void ds_b128(u32x4& dst, unsigned addr) {
  asm volatile("ds_read_b128 %0, %1 offset:%2" : "=v"(dst) : "v"(addr), "i"(OFF) : "memory");
}
template <int OFF> __device__ __forceinline__ void ds_w128(unsigned addr, f32x4 v) {
  asm volatile("ds_write_b128 %0, %1 offset:%2" :: "v"(addr), "v"(v), "i"(OFF) : "memory");
}
template <int OFF> __device__ __forceinline__ void gld128(u32x4& dst, unsigned voff, const char* sb) {
  asm volatile("global_load_dwordx4 %0, %1, %2 offset:%3 sc1" : "=v"(dst) : "v"(voff), "s"(sb), "i"(OFF) : "memory");
}
__device__ __forceinline__ void gld32(u32& dst, unsigned voff, const char* sb) {
  asm volatile("global_load_dword %0, %1, %2 sc1" : "=v"(dst) : "v"(voff), "s"(sb) : "memory");
}
template <int LOFF> __device__ __forceinline__ void dma16(unsigned ldsb, unsigned voff, const char* sb) {
  asm volatile("s_add_u32 m0, %0, %3\n\ts_nop 0\n\tglobal_load_lds_dwordx4 %1, %2 sc1" :: "s"(ldsb), "v"(voff), "s"(sb), "i"(LOFF) : "memory", "scc");
}
template <int LOFF> __device__ __forceinline__ void dma16x4(unsigned ldsb, unsigned v0, unsigned v1, const char* sb) {
  asm volatile("s_add_u32 m0, %0, %4\n\ts_nop 0\n\tglobal_load_lds_dwordx4 %1, %3 sc1\n\tglobal_load_lds_dwordx4 %2, %3 offset:1024 sc1\n\t"
               "global_load_lds_dwordx4 %1, %3 offset:2048 sc1\n\tglobal_load_lds_dwordx4 %2, %3 offset:3072 sc1"
               :: "s"(ldsb), "v"(v0), "v"(v1), "s"(sb), "i"(LOFF) : "memory", "scc");
}
template <int N> __device__ __forceinline__ void wait_vm() { asm volatile("s_waitcnt vmcnt(%0)" :: "i"(N) : "memory"); }
template <int N> __device__ __forceinline__ void wait_lgkm() { asm volatile("s_waitcnt lgkmcnt(%0)" :: "i"(N) : "memory"); }
__device__ __forceinline__ void keep(u32x4 v) { asm volatile("" :: "v"(v)); }
__device__ __forceinline__ void real() { __builtin_amdgcn_s_setprio(0); }
__device__ __forceinline__ void bar() { asm volatile("s_barrier" ::: "memory"); }
template <class T> __device__ __forceinline__ void pin(T& a) { asm volatile("" : "+v"(a)); }
__device__ __forceinline__ u32x4 cat(u32x2 a, u32x2 b) { return u32x4{a[0], a[1], b[0], b[1]}; }

typedef __amdgpu_buffer_rsrc_t rsrc_t;
__device__ __forceinline__ rsrc_t mk_rsrc(const void* p, int bytes) { return __builtin_amdgcn_make_buffer_rsrc((void*)p, 0, bytes, 0x00020000); }
template <int CTRL> __device__ __forceinline__ float dpp_add(float x) {
  return x + __builtin_bit_cast(float, __builtin_amdgcn_mov_dpp(__builtin_bit_cast(int, x), CTRL, 0xf, 0xf, true));
}
constexpr int DPP_XOR1 = 0xB1, DPP_XOR2 = 0x4E, DPP_HMIRROR = 0x141, DPP_ROR8 = 0x128;
__device__ __forceinline__ float sum8(float x) { return dpp_add<DPP_HMIRROR>(dpp_add<DPP_XOR2>(dpp_add<DPP_XOR1>(x))); }
__device__ __forceinline__ float tred32(float a, float b) {
  auto r = __builtin_amdgcn_permlane32_swap(__float_as_uint(a), __float_as_uint(b), false, false);
  return __uint_as_float(r[0]) + __uint_as_float(r[1]);
}
__device__ __forceinline__ float tred16(float a, float b) {
  auto r = __builtin_amdgcn_permlane16_swap(__float_as_uint(a), __float_as_uint(b), false, false);
  return __uint_as_float(r[0]) + __uint_as_float(r[1]);
}
__device__ __forceinline__ float rdl(float x, int l) { return __int_as_float(__builtin_amdgcn_readlane(__float_as_int(x), l)); }

typedef unsigned long long u64;
#define GAS __attribute__((address_space(1)))
constexpr int SC1 = 16;
#define BS " sc1"
#define A0_DELAY 16
#define F_LATE 1
#define A1_DELAY 4
#define A3_DELAY 24
#define AU_DELAY 16
#define A_DELAY 2
#define FXE 7
#define PE 4
#define NT 0
#define NTS ""
template <int OFF> __device__ __forceinline__ void gld128b(u32x4& dst, unsigned voff, const char* sb) {
  asm volatile("global_load_dwordx4 %0, %1, %2 offset:%3" BS : "=v"(dst) : "v"(voff), "s"(sb), "i"(OFF) : "memory");
}
template <int OFF> __device__ __forceinline__ void gld128a(u32x4& dst, unsigned voff, const char* sb) {
  asm volatile("global_load_dwordx4 %0, %1, %2 offset:%3" BS : "=a"(dst) : "v"(voff), "s"(sb), "i"(OFF) : "memory");
}
template <bool FIRST> __device__ __forceinline__ void mfma_a(f32x4& c, u32x4 a, u32x4 b) {
  if constexpr (FIRST) asm volatile("v_mfma_f32_16x16x32_bf16 %0, %1, %2, 0" : "=&v"(c) : "a"(a), "v"(b));
  else asm volatile("v_mfma_f32_16x16x32_bf16 %0, %1, %2, %0" : "+v"(c) : "a"(a), "v"(b));
}
typedef __attribute__((ext_vector_type(8))) __bf16 bf16x8;
__device__ __forceinline__ f32x4 mfma_b(u32x4 a, u32x4 b, f32x4 c) {
  return __builtin_amdgcn_mfma_f32_16x16x32_bf16(__builtin_bit_cast(bf16x8, a), __builtin_bit_cast(bf16x8, b), c, 0, 0, 0);
}
template <int LOFF> __device__ __forceinline__ void dma16p(unsigned ldsb, unsigned voff, const char* sb) {
  asm volatile("s_add_u32 m0, %0, %3\n\ts_nop 0\n\tglobal_load_lds_dwordx4 %1, %2" NTS :: "s"(ldsb), "v"(voff), "s"(sb), "i"(LOFF) : "memory", "scc");
}
template <int LOFF> __device__ __forceinline__ void dma16x4p(unsigned ldsb, unsigned v0, const char* sb) {
  asm volatile("s_add_u32 m0, %0, %3\n\ts_nop 0\n\tglobal_load_lds_dwordx4 %1, %2" NTS "\n\tglobal_load_lds_dwordx4 %1, %2 offset:1024" NTS "\n\t"
               "global_load_lds_dwordx4 %1, %2 offset:2048" NTS "\n\tglobal_load_lds_dwordx4 %1, %2 offset:3072" NTS
               :: "s"(ldsb), "v"(v0), "s"(sb), "i"(LOFF) : "memory", "scc");
}

#undef DBGC
#define STAMP(I)
#define STAMP_DECL()
#define STAMP_END()

__device__ __forceinline__ void gld128p(u32x4& dst, unsigned voff, const char* sb) {
  asm volatile("global_load_dwordx4 %0, %1, %2" NTS : "=v"(dst) : "v"(voff), "s"(sb) : "memory");
}
template <int OFF> __device__ __forceinline__ void gld32p(u32& dst, unsigned voff, const char* sb) {
  asm volatile("global_load_dword %0, %1, %2 offset:%3" NTS : "=v"(dst) : "v"(voff), "s"(sb), "i"(OFF) : "memory");
}
template <int OFF> __device__ __forceinline__ void gld16p(u32& dst, unsigned voff, const char* sb) {
  asm volatile("global_load_ushort %0, %1, %2 offset:%3" : "=v"(dst) : "v"(voff), "s"(sb), "i"(OFF) : "memory");
}
#define CPF_DONE() asm volatile("" :: "v"(cpf))
#define X_POS 1
#define F_POS 1
template <int OFF> __device__ __forceinline__ void dma16n(unsigned voff, const char* sb) {
  asm volatile("global_load_lds_dwordx4 %0, %1 offset:%2 sc1" :: "v"(voff), "s"(sb), "i"(OFF) : "memory");
}
__device__ __forceinline__ u64 v_ne64(u32x2 v, u64 tag) { u64 m; asm volatile("v_cmp_ne_u64 %0, %1, %2" : "=s"(m) : "v"(v), "s"(tag)); return m; }
__device__ __forceinline__ float v_lo(u32 w) { float d; asm volatile("v_lshlrev_b32 %0, 16, %1" : "=v"(d) : "v"(w)); return d; }
__device__ __forceinline__ float v_hi(u32 w) { float d; asm volatile("v_and_b32 %0, 0xffff0000, %1" : "=v"(d) : "v"(w)); return d; }
__device__ __forceinline__ float v_fma(float a, float b, float c) { float d; asm volatile("v_fma_f32 %0, %1, %2, %3" : "=v"(d) : "v"(a), "v"(b), "v"(c)); return d; }
__device__ __forceinline__ float v_mul(float a, float b) { float d; asm volatile("v_mul_f32 %0, %1, %2" : "=v"(d) : "v"(a), "v"(b)); return d; }
__device__ __forceinline__ float v_add(float a, float b) { float d; asm volatile("v_add_f32 %0, %1, %2" : "=v"(d) : "v"(a), "v"(b)); return d; }
#define STAMPD(I)
#define STAMPL(I) STAMP(I)
__device__ __forceinline__ float v_relu(float a) { float d; asm volatile("v_max_i32 %0, 0, %1" : "=v"(d) : "v"(a)); return d; }
__device__ __forceinline__ u32 v_cvt(float a, float b) { u32 d; asm volatile("v_cvt_pk_bf16_f32 %0, %1, %2" : "=v"(d) : "v"(a), "v"(b)); return d; }
__device__ __forceinline__ u32 v_perm(u32 a, u32 sel) { u32 d; asm volatile("v_perm_b32 %0, %1, %1, %2" : "=v"(d) : "v"(a), "s"(sel)); return d; }
__device__ __forceinline__ void v_swap32(u32& a, u32& b) { asm volatile("v_permlane32_swap_b32 %0, %1" : "+v"(a), "+v"(b)); }
__device__ __forceinline__ void v_swap16(u32& a, u32& b) { asm volatile("v_permlane16_swap_b32 %0, %1" : "+v"(a), "+v"(b)); }
template <int OFF> __device__ __forceinline__ void ds_f128(f32x4& dst, unsigned addr) {
  asm volatile("ds_read_b128 %0, %1 offset:%2" : "=v"(dst) : "v"(addr), "i"(OFF) : "memory");
}
#define STAMPG(I)
#define STAMPE(I) STAMP(I)
__device__ __forceinline__ void asm_and3(u32& d, u32 a, u32 b, u32 c) { asm volatile("v_bitop3_b32 %0, %1, %2, %3 bitop3:0x80" : "=v"(d) : "v"(a), "v"(b), "v"(c)); }
__device__ __forceinline__ void asm_ne1(unsigned long long& m, u32 a) { asm volatile("v_cmp_ne_u32 %0, 1, %1" : "=s"(m) : "v"(a)); }
__device__ __forceinline__ void ds_w32(unsigned addr, u32 v) { asm volatile("ds_write_b32 %0, %1" :: "v"(addr), "v"(v) : "memory"); }
template <int OP> __device__ __forceinline__ void asm_op1(u32& d, u32 a) {
  if constexpr (OP == 0) asm volatile("v_mul_f32 %0, 0x3acccccd, %1" : "=v"(d) : "v"(a));
  else if constexpr (OP == 1) asm volatile("v_mov_b32 %0, %1" : "=v"(d) : "v"(a));
  else if constexpr (OP == 2) asm volatile("v_max_f32 %0, 0, %1" : "=v"(d) : "v"(a));
  else if constexpr (OP == 3) asm volatile("v_cvt_pk_bf16_f32 %0, 0, %1" : "=v"(d) : "v"(a));
  else if constexpr (OP == 4) asm volatile("v_add_f32 %0, 0x3727c5ac, %1" : "=v"(d) : "v"(a));
  else if constexpr (OP == 5) asm volatile("v_sqrt_f32 %0, %1" : "=v"(d) : "v"(a));
  else asm volatile("v_mov_b32 %0, 0" : "=v"(d));
}
template <int OP> __device__ __forceinline__ void asm_op2(u32& d, u32 a, u32 b) {
  if constexpr (OP == 0) asm volatile("v_add_f32 %0, %1, %2" : "=v"(d) : "v"(a), "v"(b));
  else if constexpr (OP == 1) asm volatile("v_fma_f32 %0, -%1, %1, %2" : "=v"(d) : "v"(a), "v"(b));
  else asm volatile("v_cvt_pk_bf16_f32 %0, -%1, %2" : "=v"(d) : "v"(a), "v"(b));
}
template <int LOFF> __device__ __forceinline__ void xdma0(unsigned ldsb, unsigned voff, u32x4 srd, unsigned& soff, unsigned inc) {
  asm volatile("s_add_u32 m0, %1, %5\n\ts_add_u32 %0, %0, %4\n\tbuffer_load_dwordx4 %2, %3, %0 offen" BS " lds"
               : "+s"(soff) : "s"(ldsb), "v"(voff), "s"(srd), "s"(inc), "i"(LOFF) : "memory", "scc");
}
__device__ __forceinline__ void xdma1(unsigned voff, u32x4 srd, unsigned soff) {
  asm volatile("buffer_load_dwordx4 %0, %1, %2 offen offset:1024" BS " lds" :: "v"(voff), "s"(srd), "s"(soff) : "memory");
}
template <int OFF> __device__ __forceinline__ void ds_w128u(unsigned addr, u32x4 v) {
  asm volatile("ds_write_b128 %0, %1 offset:%2" :: "v"(addr), "v"(v), "i"(OFF) : "memory");
}
__device__ __forceinline__ float v_rcp(float a) { float d; asm volatile("v_rcp_f32 %0, %1" : "=v"(d) : "v"(a)); return d; }
template <int OFF> __device__ __forceinline__ void ds_hi16(float& dst, unsigned addr) {
  asm volatile("ds_read_u16_d16_hi %0, %1 offset:%2" : "+v"(dst) : "v"(addr), "i"(OFF) : "memory");
}
__device__ __forceinline__ unsigned v_addc(unsigned a, unsigned c) { unsigned d; asm volatile("v_add_u32 %0, %2, %1" : "=v"(d) : "v"(a), "s"(c)); return d; }
__device__ __forceinline__ void to_acc(u32x4& d, u32x4 v) {
  asm volatile("" : "+v"(v));
  asm volatile("" : "=a"(d) : "0"(v));
}
constexpr int W1_STEPS = 21;
constexpr int O_EPOCH = 0;
constexpr int O_FK = 4096;
constexpr int O_FA0 = O_FK + 160 * 256;
constexpr int O_FW = O_FA0 + 8 * 12 * 128;
constexpr int O_FU = O_FW + 8 * 12 * 128;
constexpr int O_FX = O_FU + 8 * 14 * 128;
constexpr int O_F2 = O_FX + 8 * 32 * 128;
constexpr int O_F3 = O_F2 + 8 * 32 * 128;
constexpr int O_SG = O_F3 + 256 * 512;
constexpr int O_MODP = O_SG + 32 * 8192;
constexpr int O_ST1 = O_MODP + MODP * NMOD * 4;
constexpr int O_KVG = O_ST1 + 16384;
constexpr int O_XG = O_KVG + S_ * D_ * 2;
constexpr int O_X2T = O_XG + S_ * D_ * 2;
constexpr int O_W1F = O_X2T + TP_ * D_ * 2;
constexpr int O_W2F = O_W1F + 64 * W1_STEPS * 3 * 1024;
constexpr int O_H2F = O_W2F + 8 * 48 * 5 * 1024;
constexpr int WS_BYTES = O_H2F + S_ * F_ * 2;
static_assert(O_MODP % 256 == 0 && O_ST1 % 256 == 0 && O_KVG % 256 == 0 && O_X2T % 256 == 0 && O_W1F % 256 == 0, "alignment");

__device__ __forceinline__ void st128(u32x4 v, rsrc_t r, int vo, int so) {
  __builtin_amdgcn_raw_buffer_store_b128(v, r, vo, so, SC1);
  asm volatile("s_nop 1" :: "v"(v) : "memory");
}
constexpr int SPIN_CAP = 1 << 16;
constexpr unsigned CODE_BYTES = 39 * 1024;
__device__ __forceinline__ void st_flag(char* ws, int off, u64 tag) {
  __hip_atomic_store((GAS u64*)(ws + off), tag, __ATOMIC_RELAXED, __HIP_MEMORY_SCOPE_AGENT);
}
__device__ __forceinline__ bool tag_ok(u32x4 v, u64 tag) {
  const u32 lo = (u32)tag, hi = (u32)(tag >> 32);
  return ((v[0] ^ lo) | (v[1] ^ hi) | (v[2] ^ lo) | (v[3] ^ hi)) == 0;
}
template <int NL, int SLEEP> __device__ __forceinline__ void poll(rsrc_t rw, const int (&off)[NL], u64 tag) {
#pragma unroll 1
  for (int sp = 0; sp < SPIN_CAP; ++sp) {
    bool ok = true;
#pragma unroll
    for (int i = 0; i < NL; ++i) ok &= tag_ok(__builtin_amdgcn_raw_buffer_load_b128(rw, off[i], 0, SC1), tag);
    if (__all(ok)) break;
    if (SLEEP) __builtin_amdgcn_s_sleep(SLEEP);
    asm volatile("" ::: "memory");
  }
  asm volatile("" ::: "memory");
}
__device__ __forceinline__ u64 make_tag(u32x2 e) {
  return (((u64)(u32)__builtin_amdgcn_readfirstlane(e[1]) << 32) | (u32)__builtin_amdgcn_readfirstlane(e[0])) + 1;
}
__device__ __forceinline__ void drain_bar() { asm volatile("s_waitcnt vmcnt(0) lgkmcnt(0)\n\ts_barrier" ::: "memory"); }

constexpr int FC_BYTES = 2064 * 2;
constexpr int CH_BYTES = 4 * FC_BYTES;
constexpr int CKV_OFF = 66048;
constexpr int CX_OFF = CKV_OFF + 2048 + 16384, CS_OFF = CX_OFF + 16384;
constexpr int conv_gap(int a, int b) { int n = 0; for (int i = 0; i < a; ++i) n += 8 - i; return n + b; }
struct CSched { int rd[290]; int op[290]; };
constexpr int CBE = PE - 2;
constexpr int CW1 = 12, CW2 = CBE >= 2 ? 12 : (CBE == 1 ? 19 : 35);
constexpr CSched conv_make() {
  CSched S{};
  int rd = 0, op = 0, landed = 0;
  for (int g = 0; g < 288; ++g) {
    const int e = 7 - g / 36, n = g % 36;
    S.rd[g] = rd; S.op[g] = op;
    const bool open = e < CBE || (e == CBE && n >= 24);
    bool ds = e > 0 && n < 24;
    if (open && !ds && rd < 24 && (e == 0 || n < 33)) { ++rd; ds = true; }
    if (e > 0 ? n == 34 : (n == CW1 || n == CW2)) landed = rd;
    int k = ds ? 1 : 2;
    while (k-- > 0 && op < (landed / 3) * 16) ++op;
  }
  S.rd[288] = rd; S.op[288] = op;
  return S;
}
constexpr CSched CS = conv_make();
static_assert(CS.rd[288 - 36 + CW2] == 24, "reads must be out before the last wait");
#define XSP 2
#define XCH 8
constexpr int G_XCHK = 36 * (7 - PE) + XCH;
struct CVSched { int kind[290]; int idx[290]; };
constexpr CVSched cv_make() {
  CVSched S{};
  for (int i = 0; i < 3; ++i) { S.kind[G_XCHK - 44 + 4 * i] = 2; S.idx[G_XCHK - 44 + 4 * i] = i; }
  for (int i = 0; i < 8; ++i) { S.kind[G_XCHK + 4 + XSP * i] = 3 + i / 4; S.idx[G_XCHK + 4 + XSP * i] = i & 3; }
  S.kind[G_XCHK + 4 + XSP * 8] = 5;
  return S;
}
constexpr CVSched CV = cv_make();
constexpr int cv_younger(int g, int kind, int idx) {
  int n = -1;
  for (int i = 0; i <= g; ++i) { if (n >= 0 && CV.kind[i]) ++n; if (CV.kind[i] == kind && CV.idx[i] == idx) n = 0; }
  return n;
}
static_assert(G_XCHK > 44, "conv request schedule");
constexpr int G1_R = 6;
constexpr int G1_RS = 7;
constexpr int G1_LA = 4;
constexpr int G1_SP = 15 * 8192;
constexpr int G1_WPRE = 5;
constexpr int g1_ord(int g) { return (g >> 1) + 4 * (g & 1); }
constexpr int g1_ks(int t) { return t < 96 ? t >> 3 : 12 + (t & 7); }
constexpr int g1_j(int t) { return t < 96 ? t & 7 : g1_ord((t - 96) >> 3); }
constexpr int g1_slot(int ks) { return ks < 12 ? ks % G1_RS : G1_RS + ks - 12; }
struct GSched { int kind[320]; int idx[320]; int first[162]; int mid; int first2[242]; int n; };
#define T_SPOLL 66
#define WA 24
#define PLATE 16
#define PSL 1
#define CR1 12
#define CR2 14
#define WDN1 60
#define T_DV 112
#define T_DEND 157
#define T_P0 147
#define DPRE 0
#define G2PRE 8
#define G_P1 2
#define G_C1 24
#define G_P2 30
#define G_C2 68
#define G_F3 12
#define G_P3 84
#define G_C3 124
#define FINW 26
#define HRATE 3
#define T_F2 159
#define T_SETUP 135
#define T_F0 128
#define T_F1 144
#define T_C0 156
constexpr int XPRE = 5, T_SST = 23, T_WPOLL = 14, T_WCHK = 37;
#define G2R 22
constexpr int G2_R = G2R, G2_ST = 58;
constexpr GSched g_make() {
  GSched S{};
  int n = 0, x = 0, w = 3 * G1_WPRE, wa = WA, wd = 0, rs = 0, h = 0, credit = 0;
  auto put = [&](int k, int i) { S.kind[n] = k; S.idx[n] = i; ++n; };
  for (; x < 2 * XPRE; ++x) put(0, x);
  for (int t = 0; t < 160; ++t) {
    S.first[t] = n;
    const int ks = t >> 3;
    credit += t < 96 ? CR1 : CR2;
    if (credit > 28) credit = 28;
    const bool xok = x < 40 && ((x >> 1) >= 12 || (x >> 1) <= ks + G1_RS - ((t & 7) >= 4 ? 1 : 2)), wok = w < 36 && w / 3 <= ks + G1_R - 1;
    const int n0 = n;
    if (t == T_WPOLL) put(4, 0);
    else if (t == T_SST) put(5, 0);
    else if (t == T_SPOLL || t == T_SPOLL + 1) put(6, t - T_SPOLL);
    else if (t >= 109 && (t & 7) == 5) put(7, (t - 109) >> 3);
    else if (t == T_F0 || t == T_F1 || t == T_F2) put(9, t == T_F0 ? 0 : t == T_F1 ? 1 : 2);
    else if (t == T_P0) put(10, 0);
    else if (t >= T_C0) put(11, h++);
    else if (credit >= 16) {
      if (xok && (!wok || (x >> 1) < w / 3)) put(0, x++);
      else if (wok) put(1, w++);
      else if (wa < 24) put(8, wa++);
      else if (rs < 4 && !(x & 1)) put(3, rs++);
      else if (t > T_WCHK && wd < (t < T_DV ? 37 : WDN1) && t < T_DEND) put(2, wd++);
    }
    if (n > n0) credit -= 16;
  }
  S.first[160] = n;
  put(7, 7);
  for (int i = 0; i < DPRE && wd < 60; ++i) put(2, wd++);
  S.mid = n;
  auto next = [&](int g) {
    const int hmax = g <= G_C1 ? 12 : (g <= G_C2 ? 24 : (g <= G_C3 ? 36 : 48));
    if (wd < 60 && (wd / 5 < h || h >= 12)) put(2, wd++);
    else if (h < hmax && h < g / 5 + G2_R - 1) put(11, h++);
  };
  for (int i = 0; i < G2PRE; ++i) next(0);
  for (int g = 0; g < 240; ++g) {
    S.first2[g] = n;
    if (g == G_F3) put(9, 3);
    else if (g == G_P1) put(10, 1);
    else if (g == G_P2) put(10, 2);
    else if (g == G_P3) put(10, 3);
    else if (g > 60 && g % 60 == G2_ST) put(12, g / 60 - 1);
    else if (wd < 60 ? (g & 1) : g % HRATE == 1) next(g);
  }
  S.first2[240] = n; S.n = n;
  return S;
}
constexpr GSched GS = g_make();
constexpr int g_pos(int kind, int idx) { for (int i = 0; i < GS.n; ++i) if (GS.kind[i] == kind && GS.idx[i] == idx) return i; return -1; }
constexpr int imax(int a, int b) { return a > b ? a : b; }
constexpr int g1_need(int t) {
  if (t == T_F0) return g_pos(7, 1);
  if (t == T_F1) return g_pos(7, 3);
  if (t == T_F2) return g_pos(7, 5);
  if (t == 92) return imax(g_pos(0, 31), g_pos(8, 23));
  if (t == 96) return g_pos(0, 39);
  if (t == 100) return 0;
  if (t > 92 || (t & 7) != 4) return -1;
  const int s = (t + 4) / 8;
  int p = imax(g_pos(0, 2 * s + 1), g_pos(1, 3 * s + 2));
  if (t == 12) p = imax(p, g_pos(0, 9));
  if (t == 84) p = imax(p, g_pos(6, 1));
  if (t == T_WCHK - 1) p = imax(p, g_pos(4, 0));
  return p;
}
constexpr int g_lastdv() { int p = -1; for (int i = 37; i < 60; ++i) if (g_pos(2, i) < GS.first[160]) p = g_pos(2, i); return p; }
constexpr int g2_need(int o) { return o < 12 ? imax(g_pos(11, o), g_pos(2, 5 * o + 4)) : g_pos(11, o); }
static_assert(g_pos(1, 35) >= 0 && g_pos(0, 39) >= 0 && g_pos(2, 59) >= 0 && g_pos(11, 47) >= 0 && g_pos(3, 3) >= 0, "schedule incomplete");
constexpr int XR_OFF = G1_SP + 2048;
constexpr int GB_OFF = XR_OFF + 10400;
constexpr int LDS_BYTES = 134 * 1024;
static_assert(GB_OFF + 320 <= LDS_BYTES, "LDS");
constexpr int AG_OFF = 0, AU_OFF = 20480, AD_OFF = 40960, AM_OFF = 61440;

__global__ __launch_bounds__(256, 1) void k_fused(const u16* __restrict__ xIn, const u16* __restrict__ kIn, const u16* __restrict__ vIn,
                                                  const u16* __restrict__ shk, const u16* __restrict__ ssmk, const u16* __restrict__ ssmD,
                                                  const u16* __restrict__ wg, const u16* __restrict__ wu, const u16* __restrict__ wd,
                                                  const u16* __restrict__ cIn, const u16* __restrict__ wmod,
                                                  u16* __restrict__ out, char* __restrict__ ws) {
  __shared__ __attribute__((aligned(16))) char smem[LDS_BYTES];
  STAMP_DECL();
  STAMP(0);
  u32 cpf = 0;
  if (blockIdx.x >= 240) {
    uint64_t pc; asm volatile("s_getpc_b64 %0" : "=s"(pc));
    const unsigned co = (((blockIdx.x >> 3) & 1) * 256 + threadIdx.x) * 128;
    asm volatile("global_load_dword %0, %1, %2" : "=v"(cpf) : "v"(co < CODE_BYTES ? co : 0u), "s"(pc & ~(uint64_t)127) : "memory");
  }
  asm volatile("" :: "s"(xIn), "s"(kIn), "s"(vIn), "s"(shk), "s"(ssmk), "s"(ssmD), "s"(wg), "s"(wu), "s"(wd), "s"(cIn), "s"(wmod), "s"(out), "s"(ws));
  u64 tag;
  const int tid = threadIdx.x, lane = tid & 63;
  const int wv = __builtin_amdgcn_readfirstlane(tid >> 6);
  const int b = blockIdx.x;
  const unsigned sbase = (unsigned)(size_t)smem;
  const unsigned l16 = lane * 16;
  const rsrc_t rw = mk_rsrc(ws, WS_BYTES);
#define TSTR 0
  u32 tch = 0;
  if (wv == 3) {
    constexpr int PER = (O_ST1 + 16384 - O_FK + 256 * 128 - 1) / (256 * 128);
    static_assert(PER <= 64, "one request per workgroup");
    asm volatile("global_load_dword %0, %1, %2" : "=v"(tch) : "v"(lane < PER || !TSTR ? O_FK + (b * PER + (lane < PER ? lane : 0)) * 128 : min((lane - PER) * TSTR + b * 128, WS_BYTES - 4)), "s"(ws) : "memory");
  }
  int bs = blockIdx.x;
  asm volatile("" : "+s"(bs));
  const int xcd = bs & 7, idx = bs >> 3;
  const int mt = (xcd & 1) * 8 + (idx & 7), nc = (xcd >> 1) * 4 + (idx >> 3);
  const int M = mt * 2 + ((nc >> 2) & 1), N = (nc >> 3) * 4 + (nc & 3);
  u32x4 WD[60];
  const char* wdp = ws + O_W2F + (N * 48 + wv * 12) * 5120 + 4096;
  const unsigned l16b = l16 + 8192;
  auto issue_wd = [&](auto I) { constexpr int i = I.value; gld128a<((i & 7) - 4) * 1024>(WD[i], (i & 8) ? l16b : l16, wdp + (i >> 4) * 16384); };
  auto clear_slot = [&]() {
    const int so = O_SG + (mt * 2 + ((nc >> 1) & 1)) * 8192 + (((nc & 1) * 4 + wv) * 4 + (nc >> 2)) * 256;
    __builtin_amdgcn_raw_buffer_store_b32(0u, rw, lane * 4, so, SC1);
  };
  if (b >= CWG) {
    const int j = b - CWG;
    const int r8 = lane >> 3, cp = lane & 7;
    const int ks2 = j >> 1, hf = j & 1;
    const int ug = j % 24, kq = j / 24;
    float* LC = (float*)(smem + AM_OFF);
    u16* LS = (u16*)(smem + AM_OFF + 640);
    const int gt = wv * 96 + j;
    const bool gact = gt < 300, urgent = gt < 100;
    const int gq = gt / MODP, gk = gt - gq * MODP, gcol = urgent ? 30 + gq : (gq < 50 ? gq - 20 : gq);
    u32x4 wm[16]; u32 cv[2];
    u32x2 ep = __builtin_amdgcn_raw_buffer_load_b64(rw, O_EPOCH, 0, 0);
    SCHED();
    if (!urgent) __builtin_amdgcn_s_sleep(A0_DELAY);
    if (gact) {
      static_for<0, 2>([&](auto I) { gld16p<I.value * 128>(cv[I.value], lane * 2, (const char*)(cIn + gk * 128)); });
      static_for<0, 16>([&](auto I) { gld128p(wm[I.value], r8 * (NMOD * 2) + cp * 16, (const char*)(wmod + (size_t)(gk * 128 + I.value * 8) * NMOD + gcol * 64)); });
    }
    if (gact) {
      wait_vm<16>();
      float sl[2], sv[16];
#pragma unroll
      for (int i = 0; i < 2; ++i) { pin(cv[i]); const float x = bflo(cv[i]); sl[i] = rbf(x * __builtin_amdgcn_rcpf(1.f + __expf(-x))); }
#pragma unroll
      for (int it = 0; it < 16; ++it) { sv[it] = __shfl(sl[it >> 3], (it & 7) * 8 + r8); pin(sv[it]); }
      f32x2 acc[4];
      static_for<0, 4>([&](auto G) {
        constexpr int g = G.value;
        wait_vm<12 - 4 * g>();
#pragma unroll
        for (int it = 4 * g; it < 4 * g + 4; ++it) {
          pin(wm[it]);
          const f32x2 s2 = {sv[it], sv[it]};
#pragma unroll
          for (int q = 0; q < 4; ++q) {
            const f32x2 w2 = {bflo(wm[it][q]), bfhi(wm[it][q])};
            acc[q] = it ? __builtin_elementwise_fma(s2, w2, acc[q]) : s2 * w2;
          }
        }
#pragma unroll
        for (int q = 0; q < 4; ++q) pin(acc[q]);
      });
      pin(ep);
      const float a0 = tred16(tred32(acc[0][0], acc[2][0]), tred32(acc[1][0], acc[3][0]));
      const float a1 = tred16(tred32(acc[0][1], acc[2][1]), tred32(acc[1][1], acc[3][1]));
      const float e0 = dpp_add<DPP_ROR8>(a0), e1 = dpp_add<DPP_ROR8>(a1);
      __builtin_amdgcn_raw_buffer_store_b32(__float_as_uint((lane & 8) ? e1 : e0), rw, O_MODP + (gk * NMOD + gcol * 64 + cp * 8 + r8) * 4, 0, SC1);
      if (urgent) {
        wait_vm<0>();
        if (lane < 8) st_flag(ws, O_FU + (lane * 14 + (gt >> 3)) * 128 + (gt & 7) * 8, make_tag(ep));
        __builtin_amdgcn_s_sleep(AU_DELAY);
      }
    } else {
      pin(ep);
    }
    tag = make_tag(ep);
    STAMPE(1);
    {
      const unsigned vg = r8 * (F_ * 2) + ((cp ^ ((r8 >> 1) * 2)) * 16);
      const char* gs = (const char*)(wg + (size_t)(kq * 160 + wv * 40) * F_ + ug * 64);
      const char* us = (const char*)(wu + (size_t)(kq * 160 + wv * 40) * F_ + ug * 64);
      static_for<0, 5>([&](auto I) {
        dma16p<AG_OFF + I.value * 1024>(sbase + wv * 5120, vg, gs + I.value * (8 * F_ * 2));
        dma16p<AU_OFF + I.value * 1024>(sbase + wv * 5120, vg ^ 16, us + I.value * (8 * F_ * 2));
      });
    }
    wait_vm<10>();
    bar();
    if (tid < 8) st_flag(ws, O_FA0 + (tid * 12 + (j >> 3)) * 128 + (j & 7) * 8, tag);
    STAMPE(2);
    const int kg = lane >> 4, q4 = (lane & 15) >> 2, p4 = lane & 3;
    if (wv == 0) {
      const int po[1] = {O_FU + ((b & 7) * 14 + (lane < 50 ? lane >> 2 : 0)) * 128 + (lane & 3) * 16};
      poll<1, 1>(rw, po, tag);
      STAMPE(3);
      f32x4 mm[2];
      const int lo = lane < 40 ? lane * 16 : 0;
#pragma unroll
      for (int i = 0; i < 2; ++i) {
        mm[i] = f32x4{0.f, 0.f, 0.f, 0.f};
#pragma unroll
        for (int p = 0; p < MODP; ++p)
          mm[i] += __builtin_bit_cast(f32x4, __builtin_amdgcn_raw_buffer_load_b128(rw, lo, O_MODP + (p * NMOD + (3 + i) * D_ + kq * 160) * 4, SC1));
      }
      if (lane < 40) {
        *(u32x2*)(LS + lane * 4) = u32x2{pack_bf16(mm[0][0], mm[0][1]), pack_bf16(mm[0][2], mm[0][3])};
        *(f32x4*)(LC + lane * 4) = f32x4{rbf(1.f + rbf(mm[1][0])), rbf(1.f + rbf(mm[1][1])), rbf(1.f + rbf(mm[1][2])), rbf(1.f + rbf(mm[1][3]))};
      }
    }
    drain_bar();
    STAMPE(4);
    CPF_DONE();
    clear_slot();
    {
      const char* ds = (const char*)(wd + (size_t)(ks2 * 32 + wv * 8) * D_ + hf * 320);
      static_for<0, 5>([&](auto I) { dma16p<AD_OFF + I.value * 4096>(sbase + wv * 1024, r8 * (D_ * 2) + cp * 16, ds + I.value * 128); });
    }
    {
      const int rr = 4 * kg + q4;
      u32x2 lo[10], hi[10], sbl[5], sbh[5]; u32x4 cl[5], ch[5];
      unsigned ad[2];
#pragma unroll
      for (int t = 0; t < 2; ++t) ad[t] = sbase + (p4 >> 1) * AU_OFF + rr * 128 + (((wv * 2 + t) ^ (((rr >> 1) & 3) * 2 + (p4 >> 1))) * 16) + (p4 & 1) * 8;
      const unsigned ac = sbase + AM_OFF + kg * 16, as = sbase + AM_OFF + 640 + kg * 8;
      auto rd = [&](auto I) {
        constexpr int i = I.value;
        ds_b128<i * 128>(cl[i], ac); ds_b128<i * 128 + 64>(ch[i], ac);
        ds_tr<i * 4096>(lo[2 * i], ad[0]); ds_tr<i * 4096 + 2048>(hi[2 * i], ad[0]); ds_tr<i * 4096>(lo[2 * i + 1], ad[1]); ds_tr<i * 4096 + 2048>(hi[2 * i + 1], ad[1]);
        ds_b64<i * 64>(sbl[i], as); ds_b64<i * 64 + 32>(sbh[i], as);
      };
      rd(IC<0>{});
      const int src = ((lane & 48) + ((lane & 1) * 8 + ((lane & 15) >> 1))) * 4;
      const u32x4 ones = {0x3f803f80u, 0x3f803f80u, 0x3f803f80u, 0x3f803f80u};
      f32x4 aU[2] = {}, aW[2] = {};
      int so[2];
#pragma unroll
      for (int t = 0; t < 2; ++t) { const int T = ug * 8 + wv * 2 + t, sw3 = T / 3; so[t] = O_W1F + ((sw3 * W1_STEPS + kq * 5) * 3 + T - sw3 * 3) * 1024; }
      static_for<0, 5>([&](auto KS) {
        constexpr int ks = KS.value;
        wait_lgkm<0>();
        pin(cl[ks]); pin(ch[ks]); pin(sbl[ks]); pin(sbh[ks]);
        if constexpr (ks < 4) rd(IC<ks + 1>{});
        u32x4 pm[2];
#pragma unroll
        for (int t = 0; t < 2; ++t) {
          pin(lo[2 * ks + t]); pin(hi[2 * ks + t]);
          const u32x4 raw = cat(lo[2 * ks + t], hi[2 * ks + t]);
          u32x4 sc;
#pragma unroll
          for (int q = 0; q < 2; ++q) {
            sc[q] = pack_bf16(bflo(raw[q]) * __uint_as_float(cl[ks][2 * q]), bfhi(raw[q]) * __uint_as_float(cl[ks][2 * q + 1]));
            sc[2 + q] = pack_bf16(bflo(raw[2 + q]) * __uint_as_float(ch[ks][2 * q]), bfhi(raw[2 + q]) * __uint_as_float(ch[ks][2 * q + 1]));
          }
#pragma unroll
          for (int q = 0; q < 4; ++q) pm[t][q] = (u32)__builtin_amdgcn_ds_bpermute(src, sc[q]);
          aW[t] = mfma_b(raw, cat(sbl[ks], sbh[ks]), aW[t]);
          aU[t] = mfma_b(sc, ones, aU[t]);
        }
#pragma unroll
        for (int t = 0; t < 2; ++t) st128(pm[t], rw, l16, so[t] + ks * 3072);
      });
      const f32x4 su = (lane & 4) ? aU[1] : aU[0], sw = (lane & 4) ? aW[1] : aW[0];
      const float U = p4 == 0 ? su[0] : p4 == 1 ? su[1] : p4 == 2 ? su[2] : su[3];
      const float W = p4 == 0 ? sw[0] : p4 == 1 ? sw[1] : p4 == 2 ? sw[2] : sw[3];
      const float u1 = rbf(U), u2 = rbf(U - u1), u3 = U - u1 - u2;
      const float w1 = rbf(W), w2 = rbf(W - w1), w3 = W - w1 - w2;
      const int crow = 2 * (4 * (kg & 1) + p4) + (kg >> 1);
      __builtin_amdgcn_raw_buffer_store_b128(u32x4{pack_bf16(u1, w1), pack_bf16(u2, w2), pack_bf16(u3, w3), 0u}, rw,
                                             (lane & 8) ? WS_BYTES : ((lane & 4) ? so[1] : so[0]) + (20 - kq * 5) * 3072 + (kq * 16 + crow) * 16, 0, SC1);
    }
    STAMPE(5);
  } else {
    const int d = (b >> 1) * 8 + (b & 1) + 2 * wv;
    const int row = lane & 15, kg = lane >> 4;
    const int tt = b & 31, ct = b >> 5;
    const int ov = ((lane >> 2) * 2048 + (lane & 3)) * 16, os = (ct * 16 * 2048 + (tt * 8 + wv * 2) * 4) * 16;
    const int stg = b * 2 + (wv - 2);
    const bool xact = wv >= 2 && b < 128;
    u32 xx[5][8];
    u32 draw; u32x2 ep;
    unsigned aAddr, bA[8];
    const unsigned fl = sbase + wv * CH_BYTES;
    {
      const rsrc_t rk = mk_rsrc(kIn, S_ * D_ * 2), rv = mk_rsrc(vIn, S_ * D_ * 2);
      const int vo = ct * 256 + lane * 4, so = (tt * 64 + wv * 16) * 1280;
      const int sh0 = so ? so - 3 * 1280 : 0;
      u32 kk[19], vv[16];
      const u32x4 taps = *(const u32x4*)(shk + (ct * 64 + lane) * 8);
#pragma unroll
      for (int q = 0; q < 3; ++q) kk[q] = __builtin_amdgcn_raw_buffer_load_b32(rk, vo + q * 1280, sh0, NT);
#pragma unroll
      for (int q = 0; q < 8; ++q) kk[3 + q] = __builtin_amdgcn_raw_buffer_load_b32(rk, vo + (q & 1) * 1280, so + (q & 14) * 1280, NT);
#pragma unroll
      for (int q = 0; q < 8; ++q) vv[q] = __builtin_amdgcn_raw_buffer_load_b32(rv, vo + (q & 1) * 1280, so + (q & 14) * 1280, NT);
#pragma unroll
      for (int q = 8; q < 16; ++q) kk[3 + q] = __builtin_amdgcn_raw_buffer_load_b32(rk, vo + (q & 1) * 1280, so + (q & 14) * 1280, NT);
#pragma unroll
      for (int q = 8; q < 16; ++q) vv[q] = __builtin_amdgcn_raw_buffer_load_b32(rv, vo + (q & 1) * 1280, so + (q & 14) * 1280, NT);
      SCHED();
      SCHED();
      if (F_POS == 0) {
        const char* fs = (const char*)(ssmk + (size_t)d * S_);
        dma16x4p<FC_BYTES + 28>(fl, l16, fs);
        dma16x4p<3 * FC_BYTES + 24>(fl, l16, fs);
      }
      aAddr = fl + (row & 3) * FC_BYTES + 2 * (64 * kg + (row & ~3));
      pin(aAddr);
#pragma unroll
      for (int e = 0; e < 8; ++e) { const int g = 32 + 2 * row + 1 - 8 * kg - e; bA[e] = sbase + CKV_OFF + 16 * ((4 * g + wv) ^ ((g >> 2) & 7)); pin(bA[e]); }
      ep = __builtin_amdgcn_raw_buffer_load_b64(rw, O_EPOCH, 0, 0);
      draw = __builtin_amdgcn_raw_buffer_load_b32(mk_rsrc(ssmD, D_ * 2), (d >> 1) * 4, 0, 0);
      *(u32x2*)(smem + CKV_OFF + tid * 8) = u32x2{0, 0};
      if (lane < 13) *(u32*)(smem + wv * CH_BYTES + (lane < 7 ? FC_BYTES + lane * 4 : 3 * FC_BYTES + (lane - 7) * 4)) = 0;
      SCHED();
      if (so == 0) { kk[0] = 0; kk[1] = 0; kk[2] = 0; }
      const f32x2 w0 = {bflo(taps[0]), bflo(taps[2])}, w1 = {bfhi(taps[0]), bfhi(taps[2])};
      const f32x2 w2 = {bflo(taps[1]), bflo(taps[3])}, w3 = {bfhi(taps[1]), bfhi(taps[3])};
#pragma unroll
      for (int r = 0; r < 2; ++r) {
        f32x2 kv[8];
#pragma unroll
        for (int q = 0; q < 8; ++q) {
          const int i = 8 * r + q;
          f32x2 a = f32x2{bflo(kk[i]), bfhi(kk[i])} * w3;
          a = __builtin_elementwise_fma(f32x2{bflo(kk[i + 1]), bfhi(kk[i + 1])}, w2, a);
          a = __builtin_elementwise_fma(f32x2{bflo(kk[i + 2]), bfhi(kk[i + 2])}, w1, a);
          a = __builtin_elementwise_fma(f32x2{bflo(kk[i + 3]), bfhi(kk[i + 3])}, w0, a);
          kv[q] = a * f32x2{bflo(vv[i]), bfhi(vv[i])};
        }
        u32x4 o0, o1;
#pragma unroll
        for (int t = 0; t < 4; ++t) {
          o0[t] = pack_bf16(kv[7 - 2 * t][0], kv[6 - 2 * t][0]);
          o1[t] = pack_bf16(kv[7 - 2 * t][1], kv[6 - 2 * t][1]);
        }
        st128(o0, rw, ov + r * 64, O_KVG + os);
        st128(o1, rw, ov + r * 64, O_KVG + os + 16384);
      }
    }
    STAMPE(1);
    pin(ep); pin(draw);
    tag = make_tag(ep);
    auto fload = [&]() {
      const char* fs = (const char*)(ssmk + (size_t)d * S_);
      dma16x4p<FC_BYTES + 28>(fl, l16, fs);
      dma16x4p<3 * FC_BYTES + 24>(fl, l16, fs);
    };
    auto xload = [&]() {
      const char* xp = (const char*)xIn + stg * 8 * 1280;
      static_for<0, 40>([&](auto I) { constexpr int t = I.value / 5, c = I.value % 5; gld32p<c * 256 + (t & 1) * 1280>(xx[c][t], lane * 4, xp + (t & 6) * 1280); });
    };
    if (F_POS == 1) fload();
    if (xact && X_POS == 1) {
      xload();
      wait_vm<40 + (F_POS == 1 ? 8 : 0)>();
    } else {
      wait_vm<(F_POS == 1 ? 8 : 0)>();
    }
    asm volatile("s_waitcnt lgkmcnt(0)\n\ts_barrier" ::: "memory");
    if (tid < 32) st_flag(ws, O_FK + ((b & ~31) + tid) * 256 + (b & 31) * 8, tag);
    if (xact && X_POS == 2) xload();
    if (F_POS == 2) fload();
    STAMPE(2);
    int pa1[3] = {O_FX + ((b & 7) * 32 + (lane >> 2)) * 128 + (lane & 3) * 16, O_FX + ((b & 7) * 32 + 16 + (lane >> 2)) * 128 + (lane & 3) * 16,
                  O_FA0 + ((b & 7) * 12 + (lane < 48 ? lane >> 2 : 0)) * 128 + (lane & 3) * 16};
    const char* xgp = ws + O_XG + b * 16384 + wv * 4096;
    const char* stp = ws + O_ST1 + wv * 4096;
    const char* mdp = ws + O_MODP + d * 4;
    asm volatile("" : "+s"(xgp), "+s"(stp), "+s"(mdp));
    pin(pa1[0]); pin(pa1[1]); pin(pa1[2]);
    float Dv = (d & 1) ? bfhi(draw) : bflo(draw);
    unsigned ek = sbase + CKV_OFF + 16 * ((4 * (32 + 2 * row + (kg >> 1)) + wv) ^ ((row >> 1) & 7)) + ((kg & 1) ? 0 : 8);
    unsigned ex = (ek ^ 8) + (CX_OFF - CKV_OFF - 2048), es = sbase + CS_OFF + l16;
    unsigned mo = (((lane & 7) < MODP ? (lane & 7) : 0) * NMOD + ((lane >> 3) & 3) * D_) * 4;
    pin(Dv); pin(ek); pin(ex); pin(es); pin(mo);
    unsigned ko[2] = {(unsigned)(lane ^ (lane >> 4)) * 16, ((unsigned)(lane ^ (lane >> 4)) * 16) ^ 64};
    pin(ko[0]); pin(ko[1]);
    auto fcopies = [&]() {
      u32x4 m[5]; u32 lo[5], hi[5];
      const unsigned src = fl + FC_BYTES + l16, src4 = fl + FC_BYTES + 4096 + (lane & 1) * 16;
      static_for<0, 4>([&](auto I) { constexpr int i = I.value; ds_b128<i * 1024>(m[i], src); ds_b32<i * 1024>(lo[i], src - 4); ds_b32<i * 1024 + 16>(hi[i], src); });
      ds_b128<0>(m[4], src4); ds_b32<0>(lo[4], src4 - 4); ds_b32<16>(hi[4], src4);
      wait_lgkm<0>();
#pragma unroll
      for (int i = 0; i < 5; ++i) {
        pin(m[i]); pin(lo[i]); pin(hi[i]);
        if (i == 0 && lane == 0) lo[0] = 0;
        const u32 s1 = __builtin_amdgcn_alignbit(m[i][1], m[i][0], 16), s2 = __builtin_amdgcn_alignbit(m[i][2], m[i][1], 16), s3 = __builtin_amdgcn_alignbit(m[i][3], m[i][2], 16);
        char* dst = smem + wv * CH_BYTES + (i < 4 ? i * 1024 + l16 : 4096 + (lane & 1) * 16);
        *(u32x4*)dst = u32x4{__builtin_amdgcn_alignbit(m[i][0], lo[i], 16), s1, s2, s3};
        *(u32x4*)(dst + 2 * FC_BYTES) = u32x4{s1, s2, s3, __builtin_amdgcn_alignbit(hi[i], m[i][3], 16)};
      }
    };
    if (!xact) { if (wv == 0 && F_POS != 2) wait_vm<1>(); else wait_vm<0>(); fcopies(); }
    if (xact) {
      wait_vm<(F_POS == 2 ? 8 : 0)>();
      STAMPD(8);
#pragma unroll
      for (int c = 0; c < 5; ++c)
#pragma unroll
        for (int t = 0; t < 8; ++t) pin(xx[c][t]);
#pragma unroll
      for (int c = 0; c < 5; ++c) {
        u32x4 p0, p1;
#pragma unroll
        for (int t = 0; t < 4; ++t) {
          p0[t] = __builtin_amdgcn_perm(xx[c][2 * t + 1], xx[c][2 * t], 0x05040100u);
          p1[t] = __builtin_amdgcn_perm(xx[c][2 * t + 1], xx[c][2 * t], 0x07060302u);
        }
        const int so = O_XG + (c * 16 * 2048 + stg * 4) * 16;
        st128(p0, rw, ov, so);
        st128(p1, rw, ov, so + 16384);
      }
      const bf16x2 ones = {(__bf16)1.0f, (__bf16)1.0f};
      float s1[8], s2[8];
#pragma unroll
      for (int t = 0; t < 8; ++t) {
        s1[t] = 0.f; s2[t] = 0.f;
#pragma unroll
        for (int c = 0; c < 5; ++c) {
          const bf16x2 v = __builtin_bit_cast(bf16x2, xx[c][t]);
          s1[t] = __builtin_amdgcn_fdot2_f32_bf16(v, ones, s1[t], false);
          s2[t] = __builtin_amdgcn_fdot2_f32_bf16(v, v, s2[t], false);
        }
      }
      float q1[2], q2[2];
#pragma unroll
      for (int e = 0; e < 2; ++e) {
        q1[e] = dpp_add<DPP_ROR8>(tred16(tred32(s1[e], s1[4 + e]), tred32(s1[2 + e], s1[6 + e])));
        q2[e] = dpp_add<DPP_ROR8>(tred16(tred32(s2[e], s2[4 + e]), tred32(s2[2 + e], s2[6 + e])));
      }
      const float mean = sum8((lane & 8) ? q1[1] : q1[0]) * (1.f / D_);
      const float var = fmaxf(sum8((lane & 8) ? q2[1] : q2[0]) * (1.f / D_) - mean * mean, 0.f);
      const float sd = rbf(__builtin_amdgcn_sqrtf(rbf(rbf(var) + 1e-5f)));
      const float rs = __builtin_amdgcn_rcpf(sd);
      const int s = stg * 8 + (lane >> 3);
      const int nt = s >> 8, col = (s >> 4) & 15, k4 = (s >> 2) & 3, rr = s & 3;
      if ((lane & 7) == 0) __builtin_amdgcn_raw_buffer_store_b64(u32x2{__float_as_uint(rs), __float_as_uint(-rbf(mean) * rs)}, rw,
                                                                 O_ST1 + (((nt * 2 + (rr >> 1)) * 64 + k4 * 16 + col) * 4 + (rr & 1) * 2) * 4, 0, SC1);
      STAMPD(9);
      if (F_POS == 2) wait_vm<11>();
      fcopies();
      wait_vm<0>();
      if (lane < 8) st_flag(ws, O_FX + (lane * 32 + (stg >> 3)) * 128 + (stg & 7) * 8, tag);
    } else if (wv < 2) {
      const int po[1] = {O_FK + b * 256 + wv * 128 + (lane & 7) * 16};
      poll<1, 0>(rw, po, tag);
      STAMPE(3);
      dma16x4<CKV_OFF + 2048>(sbase + wv * 8192, ko[0], ko[1], ws + O_KVG + b * 16384 + wv * 8192);
      dma16x4<CKV_OFF + 2048 + 4096>(sbase + wv * 8192, ko[0], ko[1], ws + O_KVG + b * 16384 + wv * 8192 + 4096);
      wait_vm<0>();
    }
    STAMPD(10);
    asm volatile("s_waitcnt lgkmcnt(0)\n\ts_barrier" ::: "memory");
    STAMPE(4);
    f32x4 acc[8];
    u32x2 ra[2][8][2]; u32x4 rb[2][8];
    u32x4 pf[3]; u32 mraw; u64 nm[6];
    auto load_n = [&](auto E, auto N) {
      constexpr int e = E.value, n = N.value, bf = e & 1;
      if constexpr (n < 2) ds_b64<16 * e + 8 * n>(ra[bf][0][n], aAddr);
      else if constexpr (n < 10) ds_b128<2048 * (n - 2)>(rb[bf][n - 2], bA[e]);
      else ds_b64<512 * ((n - 8) >> 1) + 16 * e + 8 * (n & 1)>(ra[bf][(n - 8) >> 1][n & 1], aAddr);
    };
    u32x2 kvw[8], xw[8]; u32x4 sA[8], sB[8];
    float T[8][4], Hh[8][4], c0, c1;
    auto kvop = [&](auto K) {
      constexpr int k = K.value, nt = k >> 3, r = k & 3;
      if constexpr ((k & 4) == 0) T[nt][r] = (r & 1) ? v_lo(kvw[nt][1 - (r >> 1)]) : v_hi(kvw[nt][1 - (r >> 1)]);
      else T[nt][r] = v_mul(T[nt][r], Dv);
    };
    auto xrd = [&](auto K) {
      constexpr int k = K.value, nt = k / 3;
      if constexpr (k % 3 == 0) ds_b64<2048 * nt>(xw[nt], ex);
      else if constexpr (k % 3 == 1) ds_b128<2048 * nt>(sA[nt], es);
      else ds_b128<2048 * nt + 1024>(sB[nt], es);
    };
    float xf[4];
    auto xop = [&](auto K) {
      constexpr int k = K.value, nt = k >> 4, ph = (k >> 2) & 3, r = k & 3;
      if constexpr (ph == 0) xf[r] = (r & 1) ? v_hi(xw[nt][r >> 1]) : v_lo(xw[nt][r >> 1]);
      else if constexpr (ph == 1) Hh[nt][r] = v_fma(xf[r], __uint_as_float((r < 2 ? sA[nt] : sB[nt])[2 * (r & 1)]), __uint_as_float((r < 2 ? sA[nt] : sB[nt])[2 * (r & 1) + 1]));
      else if constexpr (ph == 2) Hh[nt][r] = v_fma(Hh[nt][r], c1, c0);
      else T[nt][r] = v_fma(Hh[nt][r], T[nt][r], xf[r]);
    };
    static_for<0, 24>([&](auto N) { load_n(IC<7>{}, N); });
    clear_slot();
    wait_lgkm<14>();
    static_for<0, 8>([&](auto EE) {
      constexpr int e = 7 - EE.value, bf = e & 1;
      static_for<0, 8>([&](auto AA) {
        constexpr int a = AA.value;
        static_for<0, 8 - a>([&](auto BB) {
          constexpr int bb = BB.value, n = conv_gap(a, bb);
          if constexpr (e == 7 && n == 8) wait_lgkm<8>();
          mfma_<(e == 7 && a == 0)>(acc[a + bb], cat(ra[bf][a][0], ra[bf][a][1]), rb[bf][bb]);
          if constexpr (e > 0 && n < 24) load_n(IC<(e > 0 ? e - 1 : 0)>{}, IC<n>{});
          if constexpr (e == 7 && n >= 24 && n < 32) ds_b64<2048 * (n - 24)>(kvw[n - 24], ek);
          constexpr int g = 36 * (7 - e) + n;
          static_for<CS.rd[g], CS.rd[g + 1]>(xrd);
          if constexpr (e > 0 ? n == 34 : (n == CW1 || n == CW2)) wait_lgkm<0>();
          static_for<CS.op[g], CS.op[g + 1]>(xop);
          if constexpr ((e == 6 || e == 5) && n < 32) kvop(IC<(6 - e) * 32 + n>{});
          if constexpr (CV.kind[g] == 2) gld128<0>(pf[CV.idx[g]], pa1[CV.idx[g]], ws);
          if constexpr (CV.kind[g] == 3) { if constexpr (CV.idx[g] == 0) dma16<CX_OFF>(sbase + wv * 4096, ko[0], xgp); else dma16n<CV.idx[g] * 1024>(ko[CV.idx[g] & 1], xgp); }
          if constexpr (CV.kind[g] == 4) { if constexpr (CV.idx[g] == 0) dma16<CS_OFF>(sbase + wv * 4096, l16, stp); else dma16n<CV.idx[g] * 1024>(l16, stp); }
          if constexpr (CV.kind[g] == 5) gld32(mraw, mo, mdp);
          if constexpr (g == G_XCHK) { STAMPD(11); wait_vm<cv_younger(g, 2, 2)>(); }
          if constexpr (g >= G_XCHK && g < G_XCHK + 3) { constexpr int i = g - G_XCHK; nm[2 * i] = v_ne64(u32x2{pf[i][0], pf[i][1]}, tag); nm[2 * i + 1] = v_ne64(u32x2{pf[i][2], pf[i][3]}, tag); }
          if constexpr (g == G_XCHK + 3) {
            if (__builtin_expect(((nm[0] | nm[1]) | (nm[2] | nm[3]) | (nm[4] | nm[5])) != 0, 0)) poll<3, 1>(rw, pa1, tag);
            STAMPD(12);
          }
          if constexpr (e == CBE && n == 22) {
            STAMPD(13);
            wait_vm<cv_younger(g, 5, 0)>(); bar(); pin(mraw);
            STAMPD(14);
            const float mv = rbf(sum8((lane & 7) < MODP ? __uint_as_float(mraw) : 0.f));
            const float m_ga = rdl(mv, 16);
            c0 = rdl(mv, 0) * m_ga; c1 = rbf(1.f + rdl(mv, 8)) * m_ga;
            pin(c0); pin(c1);
          }
        });
      });
    });
    STAMPE(5);
    asm volatile("s_nop 7");
    static_for<0, 4>([&](auto NP) {
      constexpr int np = NP.value;
      static_for<(CS.op[288] > 32 * np ? CS.op[288] : 32 * np), 32 * np + 32>(xop);
      u32x4 o;
#pragma unroll
      for (int u = 0; u < 2; ++u) {
        const int nt = 2 * np + u;
        pin(acc[nt]);
        o[2 * u] = pack_bf16(fmaf(Hh[nt][0], acc[nt][0], T[nt][0]), fmaf(Hh[nt][1], acc[nt][1], T[nt][1]));
        o[2 * u + 1] = pack_bf16(fmaf(Hh[nt][2], acc[nt][2], T[nt][2]), fmaf(Hh[nt][3], acc[nt][3], T[nt][3]));
      }
      st128(o, rw, l16 + np * 1024, O_X2T + d * (TP_ * 2));
    });
  }
  const int kg = lane >> 4, c16 = lane & 15;
  const int kq0 = nc >> 2, r0 = (nc & 1) * 4 + wv, sgb = O_SG + (mt * 2 + ((nc >> 1) & 1)) * 8192;
  asm volatile("" :: "v"(tch));
  u32x2 tag2 = {(u32)tag, (u32)(tag >> 32)};
  u32x4 gbr;
  pin(tag2);
  {
    const int fb = O_F2 + (b & 7) * 32 * 128;
    const int jw = (lane & 3) * 24 + ((nc * 12 + wv * 3 + ((lane >> 2) & 1) * 2) >> 3);
    const int pa[1] = {fb + (20 + (jw >> 3)) * 128 + (jw & 6) * 8};
    u32x4 pfa;
    gld128<0>(pfa, pa[0], ws);
    const char* wsrc = ws + O_W1F + (nc * 4 + wv) * W1_STEPS * 3072;
    const char* wq[4];
    unsigned xinc[4];
#pragma unroll
    for (int i = 0; i < 4; ++i) {
      wq[i] = wsrc + ((kq0 + i) & 3) * (5 * 3072) + 4096;
      xinc[i] = (i && kq0 + i == 4) ? (unsigned)(-19 * 32 * TP_ * 2) : 32 * TP_ * 2;
      asm volatile("" : "+s"(wq[i]), "+s"(xinc[i]));
    }
    u32x4 W[G1_R][3], WA_[24], WC[3], pfs[2], pfw, gran;
    auto issue_w = [&](auto I) { constexpr int i = I.value; gld128b<(((i % 15) & 7) - 4) * 1024>(W[(i / 3) % G1_R][i % 3], ((i % 15) & 8) ? l16b : l16, wq[i / 15]); };
    int pc[2] = {fb + (lane >> 2) * 128 + (lane & 3) * 16, fb + (16 + (lane >> 2)) * 128 + (lane & 3) * 16};
    int f2o = lane < 8 ? O_F2 + (lane * 32 + (b >> 3)) * 128 + (b & 7) * 8 : WS_BYTES;
    pin(pc[0]); pin(pc[1]); pin(f2o);
    STAMPE(6);
    asm volatile("s_waitcnt vmcnt(1) lgkmcnt(0)\n\ts_barrier" ::: "memory");
    if (wv == 0) __builtin_amdgcn_raw_buffer_store_b64(tag2, rw, f2o, 0, SC1);
    STAMPE(7);
    if (bs >= CWG) {
      const int j = bs - CWG;
      u32x2 dl[5], dh[5];
      const unsigned add = sbase + AD_OFF + (8 * kg + (c16 >> 2)) * 128 + (lane & 3) * 8;
#pragma unroll
      for (int i = 0; i < 5; ++i) { const int tl = wv * 5 + i; const unsigned a2 = add + (tl >> 2) * 4096 + (tl & 3) * 32; ds_tr<0>(dl[i], a2); ds_tr<512>(dh[i], a2); }
      wait_lgkm<0>();
#pragma unroll
      for (int i = 0; i < 5; ++i) {
        pin(dl[i]); pin(dh[i]);
        __builtin_amdgcn_raw_buffer_store_b128(cat(dl[i], dh[i]), rw, l16, O_W2F + ((((j & 1) * 4 + wv) * 48 + (j >> 1)) * 5 + i) * 1024, SC1);
      }
      drain_bar();
      if (tid < 8) st_flag(ws, O_FW + (tid * 12 + (j >> 3)) * 128 + (j & 7) * 8, tag);
    }

    if (wv == 0) wait_vm<1>(); else wait_vm<0>();
    pin(pfa);
    if (!__all(tag_ok(pfa, tag))) poll<1, 2>(rw, pa, tag);
    auto issue_a = [&](auto I) { constexpr int i = 36 + I.value; gld128a<(((i % 15) & 7) - 4) * 1024>(WA_[I.value], ((i % 15) & 8) ? l16b : l16, wq[i / 15]); };
    static_for<0, 3 * G1_WPRE>(issue_w);
    unsigned xoff[2];
#pragma unroll
    for (int p = 0; p < 2; ++p) {
      const int k = 4 * p + kg;
      xoff[p] = (k * TP_ + ((c16 ^ (2 * k)) * 8)) * 2 - p * 1024;
      pin(xoff[p]);
    }
    unsigned xso = O_X2T + ((kq0 * 160 - 32 + 8 * wv) * TP_ + mt * 128) * 2;
    const u32x4 wsrd = {(u32)(uint64_t)ws, (u32)((uint64_t)ws >> 32) & 0xffffu, (u32)WS_BYTES, 0x00020000u};
    const unsigned ldsw = sbase + wv * 2048;
    unsigned sgl = sgb + wv * 2048 + l16;
    int hvo = kg < 3 ? ((3 * wv + kg) >> 2) * 1024 + ((3 * wv + kg) & 3) * 256 + c16 * 16 : WS_BYTES;
    const int hso = O_H2F + (mt * 8 * 48 + 3 * nc) * 1024;
    unsigned spa = sbase + G1_SP + c16 * 16;
    int sgo = (c16 >> 2) == kg ? sgb + (r0 * 4 + kq0) * 256 + c16 * 16 : WS_BYTES;
    pin(sgl); pin(hvo); pin(sgo); pin(spa);
    int pw[1] = {O_FW + ((b & 7) * 12 + (lane < 48 ? lane >> 2 : 0)) * 128 + (lane & 3) * 16};
    unsigned xo = ((lane >> 3) * TP_ + (lane & 7) * 8) * 2;
    const char* xs = ws + O_X2T + (N * 80 * TP_ + M * 64) * 2;
    unsigned gbo = O_MODP + (((lane & 7) < MODP ? (lane & 7) : 0) * NMOD + 5 * D_ + N * 80 + wv * 20 + (lane < 40 ? lane >> 3 : 0) * 4) * 4;
    pin(pw[0]); pin(xo); pin(gbo);
    u32 hq[4]; u32x4 hs, H[G2_R], WV[23], pf3, ob;
    int po3 = O_F3 + (mt * 16 + nc) * 512 + wv * 32 + (lane & 1) * 16;
    int f3o = tid < 16 ? O_F3 + (mt * 16 + tid) * 512 + nc * 8 : WS_BYTES;
    int oo;
    pin(po3); pin(f3o);
    const char* hp = ws + O_H2F + (M * 4 * 48 + wv * 12) * 1024 + 4096;
    const rsrc_t ro = mk_rsrc(out, S_ * D_ * 2);
    auto issue = [&](auto O) {
      constexpr int kind = GS.kind[O.value], i = GS.idx[O.value];
      if constexpr (kind == 0) {
        if constexpr (i & 1) xdma1(xoff[1], wsrd, xso);
        else xdma0<g1_slot(i >> 1) * 8192>(ldsw, xoff[0], wsrd, xso, xinc[(i % 10) ? 0 : i / 10]);
      }
      else if constexpr (kind == 1) issue_w(IC<i>{});
      else if constexpr (kind == 2) {
        if constexpr (i >= 37 && O.value < GS.first[160]) gld128b<((i & 7) - 4) * 1024>(WV[i - 37], (i & 8) ? l16b : l16, wdp + (i >> 4) * 16384);
        else issue_wd(IC<i>{});
      }
      else if constexpr (kind == 3) {
        if constexpr (i < 2) dma16<XR_OFF + i * 4160>(sbase + wv * 1040, xo, xs + (size_t)(wv + 4 * i) * 8 * TP_ * 2);
        else if constexpr (i == 2) dma16<XR_OFF + 8320>(sbase + (wv & 1) * 1040, xo, xs + (size_t)((wv & 1) + 8) * 8 * TP_ * 2);
        else gld128<0>(gbr, gbo, ws);
      }
      else if constexpr (kind == 4) gld128<0>(pfw, pw[0], ws);
      else if constexpr (kind == 5) __builtin_amdgcn_raw_buffer_store_b128(gran, rw, sgo, 0, SC1);
      else if constexpr (kind == 6) gld128<i * 1024>(pfs[i], sgl, ws);
      else if constexpr (kind == 7) { hs = u32x4{hq[0], hq[1], hq[2], hq[3]}; __builtin_amdgcn_raw_buffer_store_b128(hs, rw, hvo, hso + g1_ord(i) * (48 * 1024), SC1); }
      else if constexpr (kind == 8) issue_a(IC<i>{});
      else if constexpr (kind == 9) __builtin_amdgcn_raw_buffer_store_b64(tag2, rw, f3o + i * 128, 0, SC1);
      else if constexpr (kind == 10) gld128<i * 128>(pf3, po3, ws);
      else if constexpr (kind == 11) gld128b<(((i % 12) & 7) - 4) * 1024>(H[i % G2_R], ((i % 12) & 8) ? l16b : l16, hp + (i / 12) * (48 * 1024));
      else __builtin_amdgcn_raw_buffer_store_b128(ob, ro, oo, i * (32 * D_ * 2), 0);
    };
    auto check3 = [&](auto ST) {
      if (__builtin_expect((v_ne64(u32x2{pf3[0], pf3[1]}, tag) | v_ne64(u32x2{pf3[2], pf3[3]}, tag)) != 0, 0)) {
        const int po[1] = {po3 + ST.value * 128};
        poll<1, 0>(rw, po, tag);
      }
    };
    unsigned xa[9], xm[8];
#pragma unroll
    for (int jj = 0; jj < 9; ++jj) {
      const int rr = 4 * kg + (c16 >> 2);
      xa[jj] = sbase + rr * 256 + (((jj < 8 ? jj : r0) ^ (rr & 7)) * 32) + (lane & 3) * 8;
      pin(xa[jj]);
    }
    u32x4 ones = {0x3f803f80u, 0x3f803f80u, 0x3f803f80u, 0x3f803f80u};
    pin(ones);
    static_for<0, 3>([&](auto I) { gld128a<I.value * 1024>(WC[I.value], l16, wsrc + 20 * 3072); });
    static_for<0, WA - PLATE>(issue_a);
    if (wv == 0) { gld128<0>(pfs[0], pc[0], ws); gld128<0>(pfs[1], pc[1], ws); }
    static_for<WA - PLATE, WA>(issue_a);
    STAMPL(8);
    if (wv == 0) {
      wait_vm<PLATE>();
      pin(pfs[0]); pin(pfs[1]);
      if (!__all(tag_ok(pfs[0], tag) && tag_ok(pfs[1], tag))) poll<2, PSL>(rw, pc, tag);
    }
    bar();
    STAMPL(9);
    static_for<0, GS.first[0]>(issue);
    f32x4 acc[8][3];
    u32x2 X[G1_LA + 1][2], XS[5][2];
    auto xread = [&](auto T, auto HH) {
      constexpr int t = T.value, h = HH.value;
      if constexpr (t < 96) ds_tr<g1_slot(g1_ks(t)) * 8192 + h * 4096>(X[t % (G1_LA + 1)][h], xa[g1_j(t)]);
      else ds_tr<(g1_ks(t) - 12) * 8192 + h * 4096>(X[t % (G1_LA + 1)][h], xm[g1_j(t)]);
    };
    f32x4 gram, sum1;
    u32 fs[4], fa; u64 sbad;
    unsigned spw = sbase + G1_SP + (2 * wv + (kg >> 1)) * 256 + c16 * 16;
    pin(spw);
    auto fop = [&](auto K) {
      constexpr int k = K.value;
      if constexpr (k == 0) asm_and3(fa, pfs[0][1], pfs[0][3], pfs[1][1]);
      else if constexpr (k == 1) asm_and3(fa, fa, pfs[1][3], pfs[1][3]);
      else if constexpr (k == 2) asm_ne1(sbad, fa);
      else if constexpr (k == 4) v_swap32(fs[0], fs[1]);
      else if constexpr (k == 5) v_swap32(fs[2], fs[3]);
      else if constexpr (k == 6) asm_op2<0>(fs[0], fs[0], fs[1]);
      else if constexpr (k == 7) asm_op2<0>(fs[2], fs[2], fs[3]);
      else if constexpr (k == 8) v_swap16(fs[0], fs[2]);
      else if constexpr (k == 9) asm_op2<0>(fs[0], fs[0], fs[2]);
      else if constexpr (k == 10) asm_op1<0>(fs[0], fs[0]);
      else if constexpr (k == 11) asm_op1<1>(fs[1], fs[0]);
      else if constexpr (k == 12) v_swap16(fs[0], fs[1]);
      else if constexpr (k == 13) asm_op2<1>(fs[1], fs[0], fs[1]);
      else if constexpr (k == 14) asm_op1<2>(fs[1], fs[1]);
      else if constexpr (k == 15) asm_op1<3>(fs[1], fs[1]);
      else if constexpr (k == 16) asm_op1<4>(fs[1], fs[1]);
      else if constexpr (k == 17) asm_op1<3>(fs[1], fs[1]);
      else if constexpr (k == 18) asm_op1<5>(fs[1], fs[1]);
      else if constexpr (k == 19) asm_op2<2>(fs[0], fs[0], fs[1]);
      else if constexpr (k == 20) asm_op1<1>(fs[1], fs[0]);
      else if constexpr (k == 21) asm_op1<1>(fs[2], fs[0]);
      else if constexpr (k == 22) asm_op1<6>(fs[3], fs[0]);
      else if constexpr (k == 23) ds_w128u<0>(spw, u32x4{fs[0], fs[1], fs[2], fs[3]});
    };
    float eg[6]; u32x4 xc;
    auto eop = [&](auto J, auto K) {
      constexpr int j = J.value, k = K.value;
      if constexpr (k < 15) {
        if constexpr (k < 6) eg[k] = v_relu(acc[j][k >> 1][2 * (k & 1)]);
        else if constexpr (k < 12) eg[k - 6] = v_mul(eg[k - 6], acc[j][(k - 6) >> 1][2 * (k & 1) + 1]);
        else hq[k == 12 ? 1 : k == 13 ? 0 : 2] = v_cvt(eg[k == 12 ? 2 : k == 13 ? 0 : 4], eg[k == 12 ? 3 : k == 13 ? 1 : 5]);
      } else if constexpr (k == 15) v_swap32(hq[1], hq[3]);
      else if constexpr (k == 16) v_swap32(hq[0], hq[2]);
      else if constexpr (k == 17) v_swap16(hq[0], hq[1]);
      else v_swap16(hq[2], hq[3]);
    };
    unsigned pwa, pra, gba, xad, sda;
    float xr[8], sdv, G[8], S[8], rs2;
    hq[3] = 0;
    pin(hq[3]);
    wait_vm<GS.first[0] - 2>();
    bar();
    STAMPL(10);
    static_for<0, G1_LA>([&](auto T) { xread(T, IC<0>{}); xread(T, IC<1>{}); });
    static_for<0, 160>([&](auto T) {
      constexpr int t = T.value, ks = g1_ks(t), jj = g1_j(t), tn = t + G1_LA, u = t & 7;
      if constexpr (t == 24) STAMPG(1);
      if constexpr (t == 48) STAMPG(2);
      if constexpr (t == 72) STAMPG(3);
      if constexpr (t == 98) STAMPG(4);
      if constexpr (t == 128) STAMPG(5);
      if constexpr (!(t & 1)) wait_lgkm<2 * ((tn <= 160 ? tn : 160) - t - 2)>();
      if constexpr (g1_need(t) >= 0) {
        static_assert(g1_need(t) < GS.first[t], "not issued yet");
        if constexpr (GS.first[t] - 1 - g1_need(t) < 63) wait_vm<GS.first[t] - 1 - g1_need(t)>();
        bar();
      }
      if constexpr (t == T_WCHK) {
        if (__builtin_expect((v_ne64(u32x2{pfw[0], pfw[1]}, tag) | v_ne64(u32x2{pfw[2], pfw[3]}, tag)) != 0, 0)) poll<1, 1>(rw, pw, tag);
      }
      if constexpr (t == T_C0) { wait_vm<GS.first[t] - 1 - g_pos(10, 0)>(); check3(IC<0>{}); }
      if constexpr (t == T_SETUP) {
        static_assert(g_pos(3, 3) < g1_need(T_F0) && T_SETUP > T_F0, "gate partials landed");
        {
          pin(gbr);
          const float g = sum8((lane & 7) < MODP ? __uint_as_float(gbr[0]) : 0.f), g2 = sum8((lane & 7) < MODP ? __uint_as_float(gbr[1]) : 0.f);
          const float g3 = sum8((lane & 7) < MODP ? __uint_as_float(gbr[2]) : 0.f), g4 = sum8((lane & 7) < MODP ? __uint_as_float(gbr[3]) : 0.f);
          if ((lane & 7) == 0 && lane < 40) *(f32x4*)(smem + GB_OFF + wv * 80 + (lane >> 3) * 16) = f32x4{rbf(g), rbf(g2), rbf(g3), rbf(g4)};
        }
        const int lf = lane < 40 ? lane : 0, ft = 4 * wv + lf / 10, fc = lf % 10;
        pwa = sbase + wv * 5440 + l16 + (kg >> 1) * 32;
        pra = sbase + fc * 544 + ft * 16; gba = sbase + GB_OFF + fc * 32;
        xad = sbase + XR_OFF + fc * 1040 + ft * 2; sda = sbase + G1_SP + ((nc & 4) * 16 + ft) * 16 + 2;
        {
          const int pos = M * 64 + ft, l8 = (pos >> 3) & 63;
          const int tok = 256 * (2 * (pos >> 9) + ((pos >> 2) & 1)) + 16 * (l8 & 15) + 4 * (l8 >> 4) + (pos & 3);
          oo = lane < 40 ? (tok * D_ + N * 80 + 8 * fc) * 2 : S_ * D_ * 2;
        }
        sdv = 0.f;
#pragma unroll
        for (int e = 0; e < 8; ++e) { xr[e] = 0.f; pin(xr[e]); }
        pin(pwa); pin(pra); pin(gba); pin(xad); pin(sda); pin(oo); pin(sdv);
      }
      if constexpr (t == T_SST - 2) {
        pin(gram); pin(sum1);
        const int c3 = lane & 3;
        gran = u32x4{__float_as_uint(sum1[0]), 1u, __float_as_uint(c3 == 0 ? gram[0] : c3 == 1 ? gram[1] : c3 == 2 ? gram[2] : gram[3]), 1u};
        pin(gran);
      }
      if constexpr (t == 86) {
        if (__builtin_expect(sbad != 0, 0)) {
#pragma unroll 1
          for (int spn = 0; spn < SPIN_CAP; ++spn) {
            pfs[0] = __builtin_amdgcn_raw_buffer_load_b128(rw, sgl, 0, SC1);
            pfs[1] = __builtin_amdgcn_raw_buffer_load_b128(rw, sgl + 1024, 0, SC1);
            if (__all((pfs[0][1] & pfs[0][3] & pfs[1][1] & pfs[1][3]) == 1u)) break;
            asm volatile("" ::: "memory");
          }
        }
        fs[0] = pfs[0][0]; fs[1] = pfs[1][0]; fs[2] = pfs[0][2]; fs[3] = pfs[1][2];
      }
      auto ep = [&](auto A, auto B) { if constexpr (t >= 104) static_for<A.value, (B.value < 19 ? B.value : 19)>([&](auto K) { eop(IC<g1_j(t - 8)>{}, K); }); };
      constexpr int k0 = u == 0 ? 0 : 4 * u - 3;
      constexpr int sr = t == 0 ? 0 : t == 4 ? 1 : (t >= 12 && t < 15) ? t - 10 : -1, sm = t == 4 ? 0 : t == 8 ? 1 : (t >= 16 && t < 19) ? t - 14 : -1;
      const u32x4 xb = cat(X[t % (G1_LA + 1)][0], X[t % (G1_LA + 1)][1]);
      if constexpr (ks >= 12) mfma_a<false>(acc[jj][0], WA_[3 * (ks - 12) + 0], xb); else mfma_<ks == 0>(acc[jj][0], W[ks % G1_R][0], xb);
      if constexpr (tn < 160) xread(IC<tn>{}, IC<0>{});
      if constexpr (t >= 84 && t < 96) fop(IC<2 * (t - 84)>{});
      if constexpr (u > 0) ep(IC<k0>{}, IC<k0 + 1>{});
      if constexpr (ks >= 12) mfma_a<false>(acc[jj][1], WA_[3 * (ks - 12) + 1], xb); else mfma_<ks == 0>(acc[jj][1], W[ks % G1_R][1], xb);
      if constexpr (tn < 160) xread(IC<tn>{}, IC<1>{});
      if constexpr (t >= 84 && t < 96) fop(IC<2 * (t - 84) + 1>{});
      if constexpr (u > 0) ep(IC<k0 + 1>{}, IC<k0 + 2>{}); else ep(IC<0>{}, IC<1>{});
      if constexpr (ks >= 12) mfma_a<false>(acc[jj][2], WA_[3 * (ks - 12) + 2], xb); else mfma_<ks == 0>(acc[jj][2], W[ks % G1_R][2], xb);
      if constexpr (t >= 96) real();
      static_for<GS.first[t], GS.first[t + 1]>(issue);
      if constexpr (u > 0) ep(IC<k0 + 2>{}, IC<k0 + 4>{});
      if constexpr (t >= 84 && t < 92) xm[t - 84] = v_addc(xa[t - 84], G1_RS * 8192);
      if constexpr (sm >= 0) { mfma_<sm == 0>(gram, cat(XS[sm][0], XS[sm][1]), cat(XS[sm][0], XS[sm][1])); mfma_<sm == 0>(sum1, ones, cat(XS[sm][0], XS[sm][1])); }
      if constexpr (sr >= 0) { ds_tr<sr * 8192>(XS[sr][0], xa[8]); ds_tr<sr * 8192 + 4096>(XS[sr][1], xa[8]); }
      if constexpr (t >= 96 && u == (t < 104 ? 4 : 0)) ds_b128<jj * 256>(xc, spa);
      if constexpr (t >= 96 && u == 7) {
        if constexpr (t < 104) wait_lgkm<6>();
        mfma_a<false>(acc[jj][0], WC[0], xc);
        mfma_a<false>(acc[jj][1], WC[1], xc);
        mfma_a<false>(acc[jj][2], WC[2], xc);
      }
    });
    STAMPL(11);
    asm volatile("s_nop 5");
    static_for<0, 15>([&](auto K) { eop(IC<7>{}, K); });
    eop(IC<7>{}, IC<15>{}); asm volatile("s_nop 0"); eop(IC<7>{}, IC<16>{}); asm volatile("s_nop 1"); eop(IC<7>{}, IC<17>{}); eop(IC<7>{}, IC<18>{});
    static_for<GS.first[160], GS.mid>(issue);
    STAMPL(12);
    if constexpr (T_C0 >= 160) { wait_vm<GS.mid - 1 - g_pos(10, 0)>(); check3(IC<0>{}); }
    static_for<GS.mid, GS.first2[0]>(issue);
    if constexpr (g_lastdv() >= 0) wait_vm<GS.first2[0] - 1 - g_lastdv()>();
    static_for<37, 60>([&](auto I) { if constexpr (g_pos(2, I.value) < GS.first[160]) to_acc(WD[I.value], WV[I.value - 37]); });
    f32x4 gbv[2], ac2[2][5], pt[8];
    auto pre = [&](auto J, auto K) {
      constexpr int j = J.value, k = K.value;
      if constexpr (k == 0) ds_hi16<j * 256>(sdv, sda);
      else if constexpr (k < 9) ds_hi16<(k - 1) * 128 + j * 32>(xr[k - 1], xad);
      else if constexpr (k == 9) rs2 = v_rcp(sdv);
      else if constexpr (k == 10) rs2 = v_mul(rs2, rs2);
      else G[k - 11] = v_mul(gbv[(k - 11) >> 2][(k - 11) & 3], rs2);
    };
    auto fin = [&](auto K) {
      constexpr int k = K.value, e = k & 7;
      if constexpr (k < 8) S[e] = v_add(pt[e >> 2][e & 3], pt[2 + (e >> 2)][e & 3]);
      else if constexpr (k < 24) S[e] = v_add(S[e], pt[2 * (k >> 3) + 2 + (e >> 2)][e & 3]);
      else if constexpr (k < 32) S[e] = v_fma(G[e], S[e], xr[e]);
      else ob[k - 32] = v_cvt(S[2 * (k - 32)], S[2 * (k - 32) + 1]);
    };
    auto prd = [&](auto J, auto K) { ds_f128<(J.value & 1) * 21760 + (K.value >> 1) * 5440 + (K.value & 1) * 256>(pt[K.value], pra); };
    STAMPL(13);
    static_for<0, 240>([&](auto GG) {
      constexpr int g = GG.value, o = g / 5, jj = o / 12, ks = o % 12, i = g % 5, q = g % 60;
      if constexpr (g == 60) STAMPG(6);
      if constexpr (g == 120) STAMPG(7);
      if constexpr (i == 0) { static_assert(g2_need(o) < GS.first2[g], "not issued yet"); wait_vm<GS.first2[g] - 1 - g2_need(o)>(); }
      if constexpr (g == G_F3) { wait_vm<GS.first2[g] - 1 - g_pos(7, 7)>(); bar(); }
      if constexpr (g == G_C1) { wait_vm<GS.first2[g] - 1 - g_pos(10, 1)>(); check3(IC<1>{}); }
      if constexpr (g == G_C2) { wait_vm<GS.first2[g] - 1 - g_pos(10, 2)>(); check3(IC<2>{}); }
      if constexpr (g == G_C3) { wait_vm<GS.first2[g] - 1 - g_pos(10, 3)>(); check3(IC<3>{}); }
      mfma_a<ks == 0>(ac2[jj & 1][i], WD[ks * 5 + i], H[o % G2_R]);
      if constexpr (i == 4) real();
      static_for<GS.first2[g], GS.first2[g + 1]>(issue);
      if constexpr (g == 30 || g == 32) ds_f128<(g - 30) * 8>(gbv[(g - 30) / 2], gba);
      if constexpr (jj >= 1) {
        if constexpr (q >= 5 && q < 15 && (q & 1)) ds_w128<((jj - 1) & 1) * 21760 + ((q - 5) / 2) * 1088>(pwa, ac2[(jj - 1) & 1][(q - 5) / 2]);
        if constexpr (q == 18) { wait_lgkm<0>(); bar(); }
        if constexpr (q >= 19 && q < 27) prd(IC<jj - 1>{}, IC<q - 19>{});
        if constexpr (q == 30) wait_lgkm<0>();
        if constexpr (q >= 31 && q < 31 + FINW) static_for<(36 * (q - 31) + FINW - 1) / FINW, (36 * (q - 30) + FINW - 1) / FINW>(fin);
      }
      if constexpr (jj >= 1 && q < 18 && !(q & 1)) pre(IC<jj - 1>{}, IC<q / 2>{});
      if constexpr (jj >= 1 && q >= 19 && q < 29) pre(IC<jj - 1>{}, IC<q - 10>{});
    });
    STAMPL(14);
    static_for<0, 9>([&](auto K) { pre(IC<3>{}, K); });
    static_for<0, 5>([&](auto I) { ds_w128<21760 + I.value * 1088>(pwa, ac2[1][I.value]); });
    wait_lgkm<5>();
    pre(IC<3>{}, IC<9>{}); asm volatile("s_nop 0");
    static_for<10, 19>([&](auto K) { pre(IC<3>{}, K); });
    wait_lgkm<0>(); bar();
    static_for<0, 8>([&](auto K) { prd(IC<3>{}, K); });
    wait_lgkm<4>();
    static_for<0, 8>(fin);
    wait_lgkm<0>();
    static_for<8, 36>(fin);
    __builtin_amdgcn_raw_buffer_store_b128(ob, ro, oo, 3 * (32 * D_ * 2), 0);
  }
  if (b == 0 && tid == 0) st_flag(ws, O_EPOCH, tag);
  STAMP_END();
}

extern "C" void kernel_launch(void* const* d_in, const int* in_sizes, int n_in, void* d_out, int out_size,
                              void* d_ws, size_t ws_size, hipStream_t stream) {
  k_fused<<<256, 256, 0, stream>>>((const u16*)d_in[0], (const u16*)d_in[1], (const u16*)d_in[2], (const u16*)d_in[3],
                                   (const u16*)d_in[4], (const u16*)d_in[5], (const u16*)d_in[7], (const u16*)d_in[8],
                                   (const u16*)d_in[9], (const u16*)d_in[10], (const u16*)d_in[11], (u16*)d_out, (char*)d_ws);
}
```

```cpp
#include <hip/hip_runtime.h>
#include <stdint.h>

typedef unsigned short u16;
typedef unsigned int u32;
typedef __attribute__((ext_vector_type(2))) __bf16 bf16x2;
typedef __attribute__((ext_vector_type(4))) float f32x4;
typedef __attribute__((ext_vector_type(2))) float f32x2;
typedef __attribute__((ext_vector_type(4))) u32 u32x4;
typedef __attribute__((ext_vector_type(2))) u32 u32x2;

constexpr int S_ = 2048, D_ = 640, F_ = 1536;
constexpr int TP_ = S_ + 64;
constexpr int NMOD = 6 * D_;
constexpr int MODP = 5;
constexpr int CWG = 160;

__device__ __forceinline__ float bflo(u32 w) { return __uint_as_float(w << 16); }
__device__ __forceinline__ float bfhi(u32 w) { return __uint_as_float(w & 0xffff0000u); }
__device__ __forceinline__ u32 pack_bf16(float lo, float hi) {
  bf16x2 v; v[0] = (__bf16)lo; v[1] = (__bf16)hi; return __builtin_bit_cast(u32, v);
}
__device__ __forceinline__ float rbf(float x) { __bf16 b = (__bf16)x; return (float)b; }
#define SCHED() __builtin_amdgcn_sched_barrier(0)
template <int V> struct IC { static constexpr int value = V; };
template <int B, int E, class Fn> __device__ __forceinline__ void static_for(Fn&& f) {
  if constexpr (B < E) { f(IC<B>{}); static_for<B + 1, E>(f); }
}
__device__ __forceinline__ void mfma(f32x4& c, u32x4 a, u32x4 b) {
  asm volatile("v_mfma_f32_16x16x32_bf16 %0, %1, %2, %0" : "+v"(c) : "v"(a), "v"(b));
}
__device__ __forceinline__ void mfma0(f32x4& c, u32x4 a, u32x4 b) {
  asm volatile("v_mfma_f32_16x16x32_bf16 %0, %1, %2, 0" : "=&v"(c) : "v"(a), "v"(b));
}
template <bool FIRST> __device__ __forceinline__ void mfma_(f32x4& c, u32x4 a, u32x4 b) { if constexpr (FIRST) mfma0(c, a, b); else mfma(c, a, b); }
__device__ __forceinline__ u32 alignbit(u32 hi, u32 lo, u32 sh) {
  u32 d; asm volatile("v_alignbit_b32 %0, %1, %2, %3" : "=v"(d) : "v"(hi), "v"(lo), "v"(sh)); return d;
}
template <int OFF> __device__ __forceinline__ void ds_tr(u32x2& dst, unsigned addr) {
  asm volatile("ds_read_b64_tr_b16 %0, %1 offset:%2" : "=v"(dst) : "v"(addr), "i"(OFF) : "memory");
}
template <int OFF> __device__ __forceinline__ void ds_b32(u32& dst, unsigned addr) {
  asm volatile("ds_read_b32 %0, %1 offset:%2" : "=v"(dst) : "v"(addr), "i"(OFF) : "memory");
}
template <int OFF> __device__ __forceinline__ void ds_b64(u32x2& dst, unsigned addr) {
  asm volatile("ds_read_b64 %0, %1 offset:%2" : "=v"(dst) : "v"(addr), "i"(OFF) : "memory");
}
template <int OFF> __device__ __forceinline__ void ds_b128(u32x4& dst, unsigned addr) {
  asm volatile("ds_read_b128 %0, %1 offset:%2" : "=v"(dst) : "v"(addr), "i"(OFF) : "memory");
}
template <int OFF> __device__ __forceinline__ void ds_w128(unsigned addr, f32x4 v) {
  asm volatile("ds_write_b128 %0, %1 offset:%2" :: "v"(addr), "v"(v), "i"(OFF) : "memory");
}
template <int OFF> __device__ __forceinline__ void gld128(u32x4& dst, unsigned voff, const char* sb) {
  asm volatile("global_load_dwordx4 %0, %1, %2 offset:%3 sc1" : "=v"(dst) : "v"(voff), "s"(sb), "i"(OFF) : "memory");
}
__device__ __forceinline__ void gld32(u32& dst, unsigned voff, const char* sb) {
  asm volatile("global_load_dword %0, %1, %2 sc1" : "=v"(dst) : "v"(voff), "s"(sb) : "memory");
}
template <int LOFF> __device__ __forceinline__ void dma16(unsigned ldsb, unsigned voff, const char* sb) {
  asm volatile("s_add_u32 m0, %0, %3\n\ts_nop 0\n\tglobal_load_lds_dwordx4 %1, %2 sc1" :: "s"(ldsb), "v"(voff), "s"(sb), "i"(LOFF) : "memory", "scc");
}
template <int LOFF> __device__ __forceinline__ void dma16x4(unsigned ldsb, unsigned v0, unsigned v1, const char* sb) {
  asm volatile("s_add_u32 m0, %0, %4\n\ts_nop 0\n\tglobal_load_lds_dwordx4 %1, %3 sc1\n\tglobal_load_lds_dwordx4 %2, %3 offset:1024 sc1\n\t"
               "global_load_lds_dwordx4 %1, %3 offset:2048 sc1\n\tglobal_load_lds_dwordx4 %2, %3 offset:3072 sc1"
               :: "s"(ldsb), "v"(v0), "v"(v1), "s"(sb), "i"(LOFF) : "memory", "scc");
}
template <int N> __device__ __forceinline__ void wait_vm() { asm volatile("s_waitcnt vmcnt(%0)" :: "i"(N) : "memory"); }
template <int N> __device__ __forceinline__ void wait_lgkm() { asm volatile("s_waitcnt lgkmcnt(%0)" :: "i"(N) : "memory"); }
__device__ __forceinline__ void keep(u32x4 v) { asm volatile("" :: "v"(v)); }
__device__ __forceinline__ void real() { __builtin_amdgcn_s_setprio(0); }
__device__ __forceinline__ void bar() { asm volatile("s_barrier" ::: "memory"); }
template <class T> __device__ __forceinline__ void pin(T& a) { asm volatile("" : "+v"(a)); }
__device__ __forceinline__ u32x4 cat(u32x2 a, u32x2 b) { return u32x4{a[0], a[1], b[0], b[1]}; }

typedef __amdgpu_buffer_rsrc_t rsrc_t;
__device__ __forceinline__ rsrc_t mk_rsrc(const void* p, int bytes) { return __builtin_amdgcn_make_buffer_rsrc((void*)p, 0, bytes, 0x00020000); }
template <int CTRL> __device__ __forceinline__ float dpp_add(float x) {
  return x + __builtin_bit_cast(float, __builtin_amdgcn_mov_dpp(__builtin_bit_cast(int, x), CTRL, 0xf, 0xf, true));
}
constexpr int DPP_XOR1 = 0xB1, DPP_XOR2 = 0x4E, DPP_HMIRROR = 0x141, DPP_ROR8 = 0x128;
__device__ __forceinline__ float sum8(float x) { return dpp_add<DPP_HMIRROR>(dpp_add<DPP_XOR2>(dpp_add<DPP_XOR1>(x))); }
__device__ __forceinline__ float tred32(float a, float b) {
  auto r = __builtin_amdgcn_permlane32_swap(__float_as_uint(a), __float_as_uint(b), false, false);
  return __uint_as_float(r[0]) + __uint_as_float(r[1]);
}
__device__ __forceinline__ float tred16(float a, float b) {
  auto r = __builtin_amdgcn_permlane16_swap(__float_as_uint(a), __float_as_uint(b), false, false);
  return __uint_as_float(r[0]) + __uint_as_float(r[1]);
}
__device__ __forceinline__ float rdl(float x, int l) { return __int_as_float(__builtin_amdgcn_readlane(__float_as_int(x), l)); }

typedef unsigned long long u64;
#define GAS __attribute__((address_space(1)))
constexpr int SC1 = 16;
#define BS " sc1"
#define A0_DELAY 16
#define F_LATE 1
#define A1_DELAY 4
#define A3_DELAY 24
#define AU_DELAY 16
#define A_DELAY 2
#define FXE 7
#define PE 4
#define NT 0
#define NTS ""
template <int OFF> __device__ __forceinline__ void gld128b(u32x4& dst, unsigned voff, const char* sb) {
  asm volatile("global_load_dwordx4 %0, %1, %2 offset:%3" BS : "=v"(dst) : "v"(voff), "s"(sb), "i"(OFF) : "memory");
}
template <int OFF> __device__ __forceinline__ void gld128a(u32x4& dst, unsigned voff, const char* sb) {
  asm volatile("global_load_dwordx4 %0, %1, %2 offset:%3" BS : "=a"(dst) : "v"(voff), "s"(sb), "i"(OFF) : "memory");
}
template <bool FIRST> __device__ __forceinline__ void mfma_a(f32x4& c, u32x4 a, u32x4 b) {
  if constexpr (FIRST) asm volatile("v_mfma_f32_16x16x32_bf16 %0, %1, %2, 0" : "=&v"(c) : "a"(a), "v"(b));
  else asm volatile("v_mfma_f32_16x16x32_bf16 %0, %1, %2, %0" : "+v"(c) : "a"(a), "v"(b));
}
typedef __attribute__((ext_vector_type(8))) __bf16 bf16x8;
__device__ __forceinline__ f32x4 mfma_b(u32x4 a, u32x4 b, f32x4 c) {
  return __builtin_amdgcn_mfma_f32_16x16x32_bf16(__builtin_bit_cast(bf16x8, a), __builtin_bit_cast(bf16x8, b), c, 0, 0, 0);
}
template <int LOFF> __device__ __forceinline__ void dma16p(unsigned ldsb, unsigned voff, const char* sb) {
  asm volatile("s_add_u32 m0, %0, %3\n\ts_nop 0\n\tglobal_load_lds_dwordx4 %1, %2" NTS :: "s"(ldsb), "v"(voff), "s"(sb), "i"(LOFF) : "memory", "scc");
}
template <int LOFF> __device__ __forceinline__ void dma16x4p(unsigned ldsb, unsigned v0, const char* sb) {
  asm volatile("s_add_u32 m0, %0, %3\n\ts_nop 0\n\tglobal_load_lds_dwordx4 %1, %2" NTS "\n\tglobal_load_lds_dwordx4 %1, %2 offset:1024" NTS "\n\t"
               "global_load_lds_dwordx4 %1, %2 offset:2048" NTS "\n\tglobal_load_lds_dwordx4 %1, %2 offset:3072" NTS
               :: "s"(ldsb), "v"(v0), "s"(sb), "i"(LOFF) : "memory", "scc");
}

#undef DBGC
#define STAMP(I)
#define STAMP_DECL()
#define STAMP_END()

__device__ __forceinline__ void gld128p(u32x4& dst, unsigned voff, const char* sb) {
  asm volatile("global_load_dwordx4 %0, %1, %2" NTS : "=v"(dst) : "v"(voff), "s"(sb) : "memory");
}
template <int OFF> __device__ __forceinline__ void gld32p(u32& dst, unsigned voff, const char* sb) {
  asm volatile("global_load_dword %0, %1, %2 offset:%3" NTS : "=v"(dst) : "v"(voff), "s"(sb), "i"(OFF) : "memory");
}
template <int OFF> __device__ __forceinline__ void gld16p(u32& dst, unsigned voff, const char* sb) {
  asm volatile("global_load_ushort %0, %1, %2 offset:%3" : "=v"(dst) : "v"(voff), "s"(sb), "i"(OFF) : "memory");
}
#define CPF_DONE() asm volatile("" :: "v"(cpf))
#define X_POS 1
#define F_POS 1
template <int OFF> __device__ __forceinline__ void dma16n(unsigned voff, const char* sb) {
  asm volatile("global_load_lds_dwordx4 %0, %1 offset:%2 sc1" :: "v"(voff), "s"(sb), "i"(OFF) : "memory");
}
__device__ __forceinline__ u64 v_ne64(u32x2 v, u64 tag) { u64 m; asm volatile("v_cmp_ne_u64 %0, %1, %2" : "=s"(m) : "v"(v), "s"(tag)); return m; }
__device__ __forceinline__ float v_lo(u32 w) { float d; asm volatile("v_lshlrev_b32 %0, 16, %1" : "=v"(d) : "v"(w)); return d; }
__device__ __forceinline__ float v_hi(u32 w) { float d; asm volatile("v_and_b32 %0, 0xffff0000, %1" : "=v"(d) : "v"(w)); return d; }
__device__ __forceinline__ float v_fma(float a, float b, float c) { float d; asm volatile("v_fma_f32 %0, %1, %2, %3" : "=v"(d) : "v"(a), "v"(b), "v"(c)); return d; }
__device__ __forceinline__ float v_mul(float a, float b) { float d; asm volatile("v_mul_f32 %0, %1, %2" : "=v"(d) : "v"(a), "v"(b)); return d; }
__device__ __forceinline__ float v_add(float a, float b) { float d; asm volatile("v_add_f32 %0, %1, %2" : "=v"(d) : "v"(a), "v"(b)); return d; }
#define STAMPD(I)
#define STAMPL(I) STAMP(I)
__device__ __forceinline__ float v_relu(float a) { float d; asm volatile("v_max_i32 %0, 0, %1" : "=v"(d) : "v"(a)); return d; }
__device__ __forceinline__ u32 v_cvt(float a, float b) { u32 d; asm volatile("v_cvt_pk_bf16_f32 %0, %1, %2" : "=v"(d) : "v"(a), "v"(b)); return d; }
__device__ __forceinline__ u32 v_perm(u32 a, u32 sel) { u32 d; asm volatile("v_perm_b32 %0, %1, %1, %2" : "=v"(d) : "v"(a), "s"(sel)); return d; }
__device__ __forceinline__ void v_swap32(u32& a, u32& b) { asm volatile("v_permlane32_swap_b32 %0, %1" : "+v"(a), "+v"(b)); }
__device__ __forceinline__ void v_swap16(u32& a, u32& b) { asm volatile("v_permlane16_swap_b32 %0, %1" : "+v"(a), "+v"(b)); }
template <int OFF> __device__ __forceinline__ void ds_f128(f32x4& dst, unsigned addr) {
  asm volatile("ds_read_b128 %0, %1 offset:%2" : "=v"(dst) : "v"(addr), "i"(OFF) : "memory");
}
#define STAMPG(I)
#define STAMPE(I) STAMP(I)
__device__ __forceinline__ void asm_and3(u32& d, u32 a, u32 b, u32 c) { asm volatile("v_bitop3_b32 %0, %1, %2, %3 bitop3:0x80" : "=v"(d) : "v"(a), "v"(b), "v"(c)); }
__device__ __forceinline__ void asm_ne1(unsigned long long& m, u32 a) { asm volatile("v_cmp_ne_u32 %0, 1, %1" : "=s"(m) : "v"(a)); }
__device__ __forceinline__ void ds_w32(unsigned addr, u32 v) { asm volatile("ds_write_b32 %0, %1" :: "v"(addr), "v"(v) : "memory"); }
template <int OP> __device__ __forceinline__ void asm_op1(u32& d, u32 a) {
  if constexpr (OP == 0) asm volatile("v_mul_f32 %0, 0x3acccccd, %1" : "=v"(d) : "v"(a));
  else if constexpr (OP == 1) asm volatile("v_mov_b32 %0, %1" : "=v"(d) : "v"(a));
  else if constexpr (OP == 2) asm volatile("v_max_f32 %0, 0, %1" : "=v"(d) : "v"(a));
  else if constexpr (OP == 3) asm volatile("v_cvt_pk_bf16_f32 %0, 0, %1" : "=v"(d) : "v"(a));
  else if constexpr (OP == 4) asm volatile("v_add_f32 %0, 0x3727c5ac, %1" : "=v"(d) : "v"(a));
  else if constexpr (OP == 5) asm volatile("v_sqrt_f32 %0, %1" : "=v"(d) : "v"(a));
  else asm volatile("v_mov_b32 %0, 0" : "=v"(d));
}
template <int OP> __device__ __forceinline__ void asm_op2(u32& d, u32 a, u32 b) {
  if constexpr (OP == 0) asm volatile("v_add_f32 %0, %1, %2" : "=v"(d) : "v"(a), "v"(b));
  else if constexpr (OP == 1) asm volatile("v_fma_f32 %0, -%1, %1, %2" : "=v"(d) : "v"(a), "v"(b));
  else asm volatile("v_cvt_pk_bf16_f32 %0, -%1, %2" : "=v"(d) : "v"(a), "v"(b));
}
template <int LOFF> __device__ __forceinline__ void xdma0(unsigned ldsb, unsigned voff, u32x4 srd, unsigned& soff, unsigned inc) {
  asm volatile("s_add_u32 m0, %1, %5\n\ts_add_u32 %0, %0, %4\n\tbuffer_load_dwordx4 %2, %3, %0 offen" BS " lds"
               : "+s"(soff) : "s"(ldsb), "v"(voff), "s"(srd), "s"(inc), "i"(LOFF) : "memory", "scc");
}
__device__ __forceinline__ void xdma1(unsigned voff, u32x4 srd, unsigned soff) {
  asm volatile("buffer_load_dwordx4 %0, %1, %2 offen offset:1024" BS " lds" :: "v"(voff), "s"(srd), "s"(soff) : "memory");
}
template <int OFF> __device__ __forceinline__ void ds_w128u(unsigned addr, u32x4 v) {
  asm volatile("ds_write_b128 %0, %1 offset:%2" :: "v"(addr), "v"(v), "i"(OFF) : "memory");
}
__device__ __forceinline__ float v_rcp(float a) { float d; asm volatile("v_rcp_f32 %0, %1" : "=v"(d) : "v"(a)); return d; }
template <int OFF> __device__ __forceinline__ void ds_hi16(float& dst, unsigned addr) {
  asm volatile("ds_read_u16_d16_hi %0, %1 offset:%2" : "+v"(dst) : "v"(addr), "i"(OFF) : "memory");
}
__device__ __forceinline__ unsigned v_addc(unsigned a, unsigned c) { unsigned d; asm volatile("v_add_u32 %0, %2, %1" : "=v"(d) : "v"(a), "s"(c)); return d; }
__device__ __forceinline__ void to_acc(u32x4& d, u32x4 v) {
  asm volatile("" : "+v"(v));
  asm volatile("" : "=a"(d) : "0"(v));
}
__device__ __forceinline__ void bld32(u32& dst, unsigned voff, u32x4 srd) {
  asm volatile("buffer_load_dword %0, %1, %2, 0 offen sc1" : "=v"(dst) : "v"(voff), "s"(srd) : "memory");
}
template <int K> __device__ __forceinline__ void asm_dpp(u32& x) {
  if constexpr (K == 0) asm volatile("v_add_f32_dpp %0, %0, %0 quad_perm:[1,0,3,2] row_mask:0xf bank_mask:0xf" : "+v"(x));
  else if constexpr (K == 1) asm volatile("v_add_f32_dpp %0, %0, %0 quad_perm:[2,3,0,1] row_mask:0xf bank_mask:0xf" : "+v"(x));
  else asm volatile("v_add_f32_dpp %0, %0, %0 row_half_mirror row_mask:0xf bank_mask:0xf" : "+v"(x));
}
template <int L> __device__ __forceinline__ void asm_rdl(u32& d, u32 x) { asm volatile("v_readlane_b32 %0, %1, %2" : "=s"(d) : "v"(x), "i"(L)); }
template <int OP> __device__ __forceinline__ void asm_sop(float& d, u32 s, float a) {
  if constexpr (OP == 0) asm volatile("v_mov_b32 %0, %1" : "=v"(d) : "s"(s));
  else if constexpr (OP == 1) asm volatile("v_add_f32 %0, 1.0, %1" : "=v"(d) : "s"(s));
  else asm volatile("v_mul_f32 %0, %1, %2" : "=v"(d) : "s"(s), "v"(a));
}
constexpr int W1_STEPS = 21;
constexpr int O_EPOCH = 0;
constexpr int O_FK = 4096;
constexpr int O_FA0 = O_FK + 160 * 256;
constexpr int O_FW = O_FA0 + 8 * 12 * 128;
constexpr int O_FU = O_FW + 8 * 12 * 128;
constexpr int O_FX = O_FU + 8 * 14 * 128;
constexpr int O_F2 = O_FX + 8 * 32 * 128;
constexpr int O_F3 = O_F2 + 8 * 32 * 128;
constexpr int O_SG = O_F3 + 256 * 512;
constexpr int O_MODP = O_SG + 32 * 8192;
constexpr int O_ST1 = O_MODP + MODP * NMOD * 4;
constexpr int O_KVG = O_ST1 + 16384;
constexpr int O_XG = O_KVG + S_ * D_ * 2;
constexpr int O_X2T = O_XG + S_ * D_ * 2;
constexpr int O_W1F = O_X2T + TP_ * D_ * 2;
constexpr int O_W2F = O_W1F + 64 * W1_STEPS * 3 * 1024;
constexpr int O_H2F = O_W2F + 8 * 48 * 5 * 1024;
constexpr int WS_BYTES = O_H2F + S_ * F_ * 2;
static_assert(O_MODP % 256 == 0 && O_ST1 % 256 == 0 && O_KVG % 256 == 0 && O_X2T % 256 == 0 && O_W1F % 256 == 0, "alignment");

__device__ __forceinline__ void st128(u32x4 v, rsrc_t r, int vo, int so) {
  __builtin_amdgcn_raw_buffer_store_b128(v, r, vo, so, SC1);
  asm volatile("s_nop 1" :: "v"(v) : "memory");
}
constexpr int SPIN_CAP = 1 << 16;
constexpr unsigned CODE_BYTES = 39 * 1024;
__device__ __forceinline__ void st_flag(char* ws, int off, u64 tag) {
  __hip_atomic_store((GAS u64*)(ws + off), tag, __ATOMIC_RELAXED, __HIP_MEMORY_SCOPE_AGENT);
}
__device__ __forceinline__ bool tag_ok(u32x4 v, u64 tag) {
  const u32 lo = (u32)tag, hi = (u32)(tag >> 32);
  return ((v[0] ^ lo) | (v[1] ^ hi) | (v[2] ^ lo) | (v[3] ^ hi)) == 0;
}
template <int NL, int SLEEP> __device__ __forceinline__ void poll(rsrc_t rw, const int (&off)[NL], u64 tag) {
#pragma unroll 1
  for (int sp = 0; sp < SPIN_CAP; ++sp) {
    bool ok = true;
#pragma unroll
    for (int i = 0; i < NL; ++i) ok &= tag_ok(__builtin_amdgcn_raw_buffer_load_b128(rw, off[i], 0, SC1), tag);
    if (__all(ok)) break;
    if (SLEEP) __builtin_amdgcn_s_sleep(SLEEP);
    asm volatile("" ::: "memory");
  }
  asm volatile("" ::: "memory");
}
__device__ __forceinline__ u64 make_tag(u32x2 e) {
  return (((u64)(u32)__builtin_amdgcn_readfirstlane(e[1]) << 32) | (u32)__builtin_amdgcn_readfirstlane(e[0])) + 1;
}
__device__ __forceinline__ void drain_bar() { asm volatile("s_waitcnt vmcnt(0) lgkmcnt(0)\n\ts_barrier" ::: "memory"); }

constexpr int FC_BYTES = 2064 * 2;
constexpr int CH_BYTES = 4 * FC_BYTES;
constexpr int CKV_OFF = 66048;
constexpr int CX_OFF = CKV_OFF + 2048 + 16384, CS_OFF = CX_OFF + 16384;
constexpr int conv_gap(int a, int b) { int n = 0; for (int i = 0; i < a; ++i) n += 8 - i; return n + b; }
struct CSched { int rd[290]; int op[290]; };
constexpr int CBE = PE - 2;
constexpr int CW1 = 12, CW2 = CBE >= 2 ? 12 : (CBE == 1 ? 19 : 35);
constexpr CSched conv_make() {
  CSched S{};
  int rd = 0, op = 0, landed = 0;
  for (int g = 0; g < 288; ++g) {
    const int e = 7 - g / 36, n = g % 36;
    S.rd[g] = rd; S.op[g] = op;
    const bool open = e < CBE || (e == CBE && n >= 24);
    bool ds = e > 0 && n < 24;
    if (open && !ds && rd < 24 && (e == 0 || n < 34)) { ++rd; ds = true; if (n >= 30 && n < 32 && rd < 24) ++rd; }
    if (e > 0 ? n == 35 : (n == CW1 || n == CW2)) landed = rd;
    int k = ds ? 1 : 2;
    while (k-- > 0 && op < (landed / 6) * 32) ++op;
  }
  S.rd[288] = rd; S.op[288] = op;
  return S;
}
constexpr CSched CS = conv_make();
static_assert(CS.rd[288 - 36 + CW2] == 24, "reads must be out before the last wait");
#define XSP 2
#define XCH 20
constexpr int G_XCHK = 36 * (7 - PE) + XCH;
struct CVSched { int kind[290]; int idx[290]; };
constexpr CVSched cv_make() {
  CVSched S{};
  for (int i = 0; i < 3; ++i) { S.kind[G_XCHK - 44 + 4 * i] = 2; S.idx[G_XCHK - 44 + 4 * i] = i; }
  S.kind[G_XCHK + 4] = 5;
  for (int i = 0; i < 8; ++i) { S.kind[G_XCHK + 4 + XSP * (i + 1)] = 3 + i / 4; S.idx[G_XCHK + 4 + XSP * (i + 1)] = i & 3; }
  return S;
}
constexpr CVSched CV = cv_make();
constexpr int cv_younger(int g, int kind, int idx) {
  int n = -1;
  for (int i = 0; i <= g; ++i) { if (n >= 0 && CV.kind[i]) ++n; if (CV.kind[i] == kind && CV.idx[i] == idx) n = 0; }
  return n;
}
static_assert(G_XCHK > 44, "conv request schedule");
constexpr int G1_R = 6;
constexpr int G1_RS = 7;
constexpr int G1_LA = 4;
constexpr int G1_SP = 15 * 8192;
constexpr int G1_WPRE = 5;
constexpr int g1_ord(int g) { return (g >> 1) + 4 * (g & 1); }
constexpr int g1_ks(int t) { return t < 96 ? t >> 3 : 12 + (t & 7); }
constexpr int g1_j(int t) { return t < 96 ? t & 7 : g1_ord((t - 96) >> 3); }
constexpr int g1_slot(int ks) { return ks < 12 ? ks % G1_RS : G1_RS + ks - 12; }
struct GSched { int kind[320]; int idx[320]; int first[162]; int mid; int first2[242]; int n; };
#define T_SPOLL 66
#define WA 24
#define PLATE 16
#define PSL 1
#define CR1 12
#define CR2 14
#define WDN1 60
#define T_DV 112
#define T_DEND 157
#define T_P0 147
#define DPRE 0
#define G2PRE 8
#define G_P1 2
#define G_C1 24
#define G_P2 30
#define G_C2 68
#define G_F3 12
#define G_P3 84
#define G_C3 124
#define FINW 26
#define HRATE 3
#define T_F2 159
#define T_SETUP 135
#define T_F0 128
#define T_F1 144
#define T_C0 156
constexpr int XPRE = 5, T_SST = 23, T_WPOLL = 46, T_WCHK = 69;
#define G2R 22
constexpr int G2_R = G2R, G2_ST = 58;
constexpr GSched g_make() {
  GSched S{};
  int n = 0, x = 0, w = 3 * G1_WPRE, wa = WA, wd = 0, rs = 0, h = 0, credit = 0;
  auto put = [&](int k, int i) { S.kind[n] = k; S.idx[n] = i; ++n; };
  for (; x < 2 * XPRE; ++x) put(0, x);
  put(13, 0);
  for (int t = 0; t < 160; ++t) {
    S.first[t] = n;
    const int ks = t >> 3;
    credit += t < 96 ? CR1 : CR2;
    if (credit > 28) credit = 28;
    const bool xok = x < 40 && ((x >> 1) >= 12 || (x >> 1) <= ks + G1_RS - ((t & 7) >= 4 ? 1 : 2)), wok = w < 36 && w / 3 <= ks + G1_R - 1;
    const int n0 = n;
    if (t == T_WPOLL) put(4, 0);
    else if (t == T_SST) put(5, 0);
    else if (t == T_SPOLL || t == T_SPOLL + 1) put(6, t - T_SPOLL);
    else if (t >= 109 && (t & 7) == 5) put(7, (t - 109) >> 3);
    else if (t == T_F0 || t == T_F1 || t == T_F2) put(9, t == T_F0 ? 0 : t == T_F1 ? 1 : 2);
    else if (t == T_P0) put(10, 0);
    else if (t >= T_C0) put(11, h++);
    else if (credit >= 16) {
      if (xok && (!wok || (x >> 1) < w / 3)) put(0, x++);
      else if (wok) put(1, w++);
      else if (wa < 24) put(8, wa++);
      else if (rs < 4 && !(x & 1)) put(3, rs++);
      else if (t > T_WCHK && wd < (t < T_DV ? 37 : WDN1) && t < T_DEND) put(2, wd++);
    }
    if (n > n0) credit -= 16;
  }
  S.first[160] = n;
  put(7, 7);
  for (int i = 0; i < DPRE && wd < 60; ++i) put(2, wd++);
  S.mid = n;
  auto next = [&](int g) {
    const int hmax = g <= G_C1 ? 12 : (g <= G_C2 ? 24 : (g <= G_C3 ? 36 : 48));
    if (wd < 60 && (wd / 5 < h || h >= 12)) put(2, wd++);
    else if (h < hmax && h < g / 5 + G2_R - 1) put(11, h++);
  };
  for (int i = 0; i < G2PRE; ++i) next(0);
  for (int g = 0; g < 240; ++g) {
    S.first2[g] = n;
    if (g == G_F3) put(9, 3);
    else if (g == G_P1) put(10, 1);
    else if (g == G_P2) put(10, 2);
    else if (g == G_P3) put(10, 3);
    else if (g > 60 && g % 60 == G2_ST) put(12, g / 60 - 1);
    else if (wd < 60 ? (g & 1) : g % HRATE == 1) next(g);
  }
  S.first2[240] = n; S.n = n;
  return S;
}
constexpr GSched GS = g_make();
constexpr int g_pos(int kind, int idx) { for (int i = 0; i < GS.n; ++i) if (GS.kind[i] == kind && GS.idx[i] == idx) return i; return -1; }
constexpr int imax(int a, int b) { return a > b ? a : b; }
constexpr int g1_need(int t) {
  if (t == T_F0) return g_pos(7, 1);
  if (t == T_F1) return g_pos(7, 3);
  if (t == T_F2) return g_pos(7, 5);
  if (t == 92) return imax(g_pos(0, 31), g_pos(8, 23));
  if (t == 96) return g_pos(0, 39);
  if (t == 100) return 0;
  if (t > 92 || (t & 7) != 4) return -1;
  const int s = (t + 4) / 8;
  int p = imax(g_pos(0, 2 * s + 1), g_pos(1, 3 * s + 2));
  if (t == 12) p = imax(p, g_pos(0, 9));
  if (t == 84) p = imax(p, g_pos(6, 1));
  if (t == T_WCHK - 1) p = imax(p, g_pos(4, 0));
  return p;
}
constexpr int g_lastdv() { int p = -1; for (int i = 37; i < 60; ++i) if (g_pos(2, i) < GS.first[160]) p = g_pos(2, i); return p; }
constexpr int g2_need(int o) { return o < 12 ? imax(g_pos(11, o), g_pos(2, 5 * o + 4)) : g_pos(11, o); }
static_assert(g_pos(1, 35) >= 0 && g_pos(0, 39) >= 0 && g_pos(2, 59) >= 0 && g_pos(11, 47) >= 0 && g_pos(3, 3) >= 0, "schedule incomplete");
constexpr int XR_OFF = G1_SP + 2048;
constexpr int GB_OFF = XR_OFF + 10400;
constexpr int LDS_BYTES = 134 * 1024;
static_assert(GB_OFF + 320 <= LDS_BYTES, "LDS");
constexpr int AG_OFF = 0, AU_OFF = 20480, AD_OFF = 40960, AM_OFF = 61440;

__global__ __launch_bounds__(256, 1) void k_fused(const u16* __restrict__ xIn, const u16* __restrict__ kIn, const u16* __restrict__ vIn,
                                                  const u16* __restrict__ shk, const u16* __restrict__ ssmk, const u16* __restrict__ ssmD,
                                                  const u16* __restrict__ wg, const u16* __restrict__ wu, const u16* __restrict__ wd,
                                                  const u16* __restrict__ cIn, const u16* __restrict__ wmod,
                                                  char* __restrict__ ws, u16* __restrict__ out) {
  __shared__ __attribute__((aligned(16))) char smem[LDS_BYTES];
  STAMP_DECL();
  STAMP(0);
  u32 cpf = 0;
  if (blockIdx.x >= 240) {
    uint64_t pc; asm volatile("s_getpc_b64 %0" : "=s"(pc));
    const unsigned co = (((blockIdx.x >> 3) & 1) * 256 + threadIdx.x) * 128;
    asm volatile("global_load_dword %0, %1, %2" : "=v"(cpf) : "v"(co < CODE_BYTES ? co : 0u), "s"(pc & ~(uint64_t)127) : "memory");
  }
  u64 tag;
  const int tid = threadIdx.x, lane = tid & 63;
  const int wv = __builtin_amdgcn_readfirstlane(tid >> 6);
  const int b = blockIdx.x;
  const unsigned sbase = (unsigned)(size_t)smem;
  const unsigned l16 = lane * 16;
  int bs = blockIdx.x;
  asm volatile("" : "+s"(bs));
  const int xcd = bs & 7, idx = bs >> 3;
  int mt = (xcd & 1) * 8 + (idx & 7), nc = (xcd >> 1) * 4 + (idx >> 3);
  asm volatile("" : "+s"(mt), "+s"(nc));
  const int M = mt * 2 + ((nc >> 2) & 1), N = (nc >> 3) * 4 + (nc & 3);
  int kvo = (b >> 5) * 256 + lane * 4, kso[9];
  kso[0] = ((b & 31) * 64 + wv * 16) * 1280;
#pragma unroll
  for (int q = 1; q < 8; ++q) kso[q] = kso[0] + q * 2560;
  kso[8] = kso[0] ? kso[0] - 3 * 1280 : 0;
  asm volatile("" : "+v"(kvo), "+s"(kso[0]), "+s"(kso[1]), "+s"(kso[2]), "+s"(kso[3]), "+s"(kso[4]), "+s"(kso[5]), "+s"(kso[6]), "+s"(kso[7]), "+s"(kso[8]));
  asm volatile("" :: "s"(xIn), "s"(kIn), "s"(vIn), "s"(shk), "s"(ssmk), "s"(ssmD), "s"(wg), "s"(wu), "s"(wd), "s"(cIn), "s"(wmod), "s"(ws));
  const rsrc_t rw = mk_rsrc(ws, WS_BYTES);
  const u32x4 wsrd = {(u32)(uint64_t)ws, (u32)((uint64_t)ws >> 32) & 0xffffu, (u32)WS_BYTES, 0x00020000u};
  u32 tch = 0;
  auto touch = [&]() {
    constexpr int PER = (O_ST1 + 16384 - O_FK + 256 * 128 - 1) / (256 * 128);
    static_assert(PER <= 64, "one request per workgroup");
    tch = __builtin_amdgcn_raw_buffer_load_b32(rw, (wv == 3 && lane < PER) ? O_FK + (b * PER + lane) * 128 : WS_BYTES, 0, 0);
  };
  u32x4 WD[60];
  const char* wdp = ws + O_W2F + (N * 48 + wv * 12) * 5120 + 4096;
  const unsigned l16b = l16 + 8192;
  auto issue_wd = [&](auto I) { constexpr int i = I.value; gld128a<((i & 7) - 4) * 1024>(WD[i], (i & 8) ? l16b : l16, wdp + (i >> 4) * 16384); };
  auto clear_slot = [&]() {
    const int so = O_SG + (mt * 2 + ((nc >> 1) & 1)) * 8192 + (((nc & 1) * 4 + wv) * 4 + (nc >> 2)) * 256;
    __builtin_amdgcn_raw_buffer_store_b32(0u, rw, lane * 4, so, SC1);
  };
  if (b >= CWG) {
    const int j = b - CWG;
    const int r8 = lane >> 3, cp = lane & 7;
    const int ks2 = j >> 1, hf = j & 1;
    const int ug = j % 24, kq = j / 24;
    float* LC = (float*)(smem + AM_OFF);
    u16* LS = (u16*)(smem + AM_OFF + 640);
    const int gt = wv * 96 + j;
    const bool gact = gt < 300, urgent = gt < 100;
    const int gq = gt / MODP, gk = gt - gq * MODP, gcol = urgent ? 30 + gq : (gq < 50 ? gq - 20 : gq);
    u32x4 wm[16]; u32 cv[2];
    u32x2 ep = __builtin_amdgcn_raw_buffer_load_b64(rw, O_EPOCH, 0, SC1);
    touch();
    SCHED();
    if (!urgent) __builtin_amdgcn_s_sleep(A0_DELAY);
    if (gact) {
      static_for<0, 2>([&](auto I) { gld16p<I.value * 128>(cv[I.value], lane * 2, (const char*)(cIn + gk * 128)); });
      static_for<0, 16>([&](auto I) { gld128p(wm[I.value], r8 * (NMOD * 2) + cp * 16, (const char*)(wmod + (size_t)(gk * 128 + I.value * 8) * NMOD + gcol * 64)); });
    }
    if (gact) {
      wait_vm<16>();
      float sl[2], sv[16];
#pragma unroll
      for (int i = 0; i < 2; ++i) { pin(cv[i]); const float x = bflo(cv[i]); sl[i] = rbf(x * __builtin_amdgcn_rcpf(1.f + __expf(-x))); }
#pragma unroll
      for (int it = 0; it < 16; ++it) { sv[it] = __shfl(sl[it >> 3], (it & 7) * 8 + r8); pin(sv[it]); }
      f32x2 acc[4];
      static_for<0, 4>([&](auto G) {
        constexpr int g = G.value;
        wait_vm<12 - 4 * g>();
#pragma unroll
        for (int it = 4 * g; it < 4 * g + 4; ++it) {
          pin(wm[it]);
          const f32x2 s2 = {sv[it], sv[it]};
#pragma unroll
          for (int q = 0; q < 4; ++q) {
            const f32x2 w2 = {bflo(wm[it][q]), bfhi(wm[it][q])};
            acc[q] = it ? __builtin_elementwise_fma(s2, w2, acc[q]) : s2 * w2;
          }
        }
#pragma unroll
        for (int q = 0; q < 4; ++q) pin(acc[q]);
      });
      pin(ep); pin(tch);
      const float a0 = tred16(tred32(acc[0][0], acc[2][0]), tred32(acc[1][0], acc[3][0]));
      const float a1 = tred16(tred32(acc[0][1], acc[2][1]), tred32(acc[1][1], acc[3][1]));
      const float e0 = dpp_add<DPP_ROR8>(a0), e1 = dpp_add<DPP_ROR8>(a1);
      __builtin_amdgcn_raw_buffer_store_b32(__float_as_uint((lane & 8) ? e1 : e0), rw, O_MODP + (gk * NMOD + gcol * 64 + cp * 8 + r8) * 4, 0, SC1);
      if (urgent) {
        wait_vm<0>();
        if (lane < 8) st_flag(ws, O_FU + (lane * 14 + (gt >> 3)) * 128 + (gt & 7) * 8, make_tag(ep));
        __builtin_amdgcn_s_sleep(AU_DELAY);
      }
    } else {
      pin(ep); pin(tch);
    }
    tag = make_tag(ep);
    STAMPE(1);
    {
      const unsigned vg = r8 * (F_ * 2) + ((cp ^ ((r8 >> 1) * 2)) * 16);
      const char* gs = (const char*)(wg + (size_t)(kq * 160 + wv * 40) * F_ + ug * 64);
      const char* us = (const char*)(wu + (size_t)(kq * 160 + wv * 40) * F_ + ug * 64);
      static_for<0, 5>([&](auto I) {
        dma16p<AG_OFF + I.value * 1024>(sbase + wv * 5120, vg, gs + I.value * (8 * F_ * 2));
        dma16p<AU_OFF + I.value * 1024>(sbase + wv * 5120, vg ^ 16, us + I.value * (8 * F_ * 2));
      });
    }
    wait_vm<10>();
    bar();
    if (tid < 8) st_flag(ws, O_FA0 + (tid * 12 + (j >> 3)) * 128 + (j & 7) * 8, tag);
    STAMPE(2);
    const int kg = lane >> 4, q4 = (lane & 15) >> 2, p4 = lane & 3;
    if (wv == 0) {
      const int po[1] = {O_FU + ((b & 7) * 14 + (lane < 50 ? lane >> 2 : 0)) * 128 + (lane & 3) * 16};
      poll<1, 1>(rw, po, tag);
      STAMPE(3);
      f32x4 mm[2];
      const int lo = lane < 40 ? lane * 16 : 0;
#pragma unroll
      for (int i = 0; i < 2; ++i) {
        mm[i] = f32x4{0.f, 0.f, 0.f, 0.f};
#pragma unroll
        for (int p = 0; p < MODP; ++p)
          mm[i] += __builtin_bit_cast(f32x4, __builtin_amdgcn_raw_buffer_load_b128(rw, lo, O_MODP + (p * NMOD + (3 + i) * D_ + kq * 160) * 4, SC1));
      }
      if (lane < 40) {
        *(u32x2*)(LS + lane * 4) = u32x2{pack_bf16(mm[0][0], mm[0][1]), pack_bf16(mm[0][2], mm[0][3])};
        *(f32x4*)(LC + lane * 4) = f32x4{rbf(1.f + rbf(mm[1][0])), rbf(1.f + rbf(mm[1][1])), rbf(1.f + rbf(mm[1][2])), rbf(1.f + rbf(mm[1][3]))};
      }
    }
    drain_bar();
    STAMPE(4);
    CPF_DONE();
    clear_slot();
    {
      const char* ds = (const char*)(wd + (size_t)(ks2 * 32 + wv * 8) * D_ + hf * 320);
      static_for<0, 5>([&](auto I) { dma16p<AD_OFF + I.value * 4096>(sbase + wv * 1024, r8 * (D_ * 2) + cp * 16, ds + I.value * 128); });
    }
    {
      const int rr = 4 * kg + q4;
      u32x2 lo[10], hi[10], sbl[5], sbh[5]; u32x4 cl[5], ch[5];
      unsigned ad[2];
#pragma unroll
      for (int t = 0; t < 2; ++t) ad[t] = sbase + (p4 >> 1) * AU_OFF + rr * 128 + (((wv * 2 + t) ^ (((rr >> 1) & 3) * 2 + (p4 >> 1))) * 16) + (p4 & 1) * 8;
      const unsigned ac = sbase + AM_OFF + kg * 16, as = sbase + AM_OFF + 640 + kg * 8;
      auto rd = [&](auto I) {
        constexpr int i = I.value;
        ds_b128<i * 128>(cl[i], ac); ds_b128<i * 128 + 64>(ch[i], ac);
        ds_tr<i * 4096>(lo[2 * i], ad[0]); ds_tr<i * 4096 + 2048>(hi[2 * i], ad[0]); ds_tr<i * 4096>(lo[2 * i + 1], ad[1]); ds_tr<i * 4096 + 2048>(hi[2 * i + 1], ad[1]);
        ds_b64<i * 64>(sbl[i], as); ds_b64<i * 64 + 32>(sbh[i], as);
      };
      rd(IC<0>{});
      const int src = ((lane & 48) + ((lane & 1) * 8 + ((lane & 15) >> 1))) * 4;
      const u32x4 ones = {0x3f803f80u, 0x3f803f80u, 0x3f803f80u, 0x3f803f80u};
      f32x4 aU[2] = {}, aW[2] = {};
      int so[2];
#pragma unroll
      for (int t = 0; t < 2; ++t) { const int T = ug * 8 + wv * 2 + t, sw3 = T / 3; so[t] = O_W1F + ((sw3 * W1_STEPS + kq * 5) * 3 + T - sw3 * 3) * 1024; }
      static_for<0, 5>([&](auto KS) {
        constexpr int ks = KS.value;
        wait_lgkm<0>();
        pin(cl[ks]); pin(ch[ks]); pin(sbl[ks]); pin(sbh[ks]);
        if constexpr (ks < 4) rd(IC<ks + 1>{});
        u32x4 pm[2];
#pragma unroll
        for (int t = 0; t < 2; ++t) {
          pin(lo[2 * ks + t]); pin(hi[2 * ks + t]);
          const u32x4 raw = cat(lo[2 * ks + t], hi[2 * ks + t]);
          u32x4 sc;
#pragma unroll
          for (int q = 0; q < 2; ++q) {
            sc[q] = pack_bf16(bflo(raw[q]) * __uint_as_float(cl[ks][2 * q]), bfhi(raw[q]) * __uint_as_float(cl[ks][2 * q + 1]));
            sc[2 + q] = pack_bf16(bflo(raw[2 + q]) * __uint_as_float(ch[ks][2 * q]), bfhi(raw[2 + q]) * __uint_as_float(ch[ks][2 * q + 1]));
          }
#pragma unroll
          for (int q = 0; q < 4; ++q) pm[t][q] = (u32)__builtin_amdgcn_ds_bpermute(src, sc[q]);
          aW[t] = mfma_b(raw, cat(sbl[ks], sbh[ks]), aW[t]);
          aU[t] = mfma_b(sc, ones, aU[t]);
        }
#pragma unroll
        for (int t = 0; t < 2; ++t) st128(pm[t], rw, l16, so[t] + ks * 3072);
      });
      const f32x4 su = (lane & 4) ? aU[1] : aU[0], sw = (lane & 4) ? aW[1] : aW[0];
      const float U = p4 == 0 ? su[0] : p4 == 1 ? su[1] : p4 == 2 ? su[2] : su[3];
      const float W = p4 == 0 ? sw[0] : p4 == 1 ? sw[1] : p4 == 2 ? sw[2] : sw[3];
      const float u1 = rbf(U), u2 = rbf(U - u1), u3 = U - u1 - u2;
      const float w1 = rbf(W), w2 = rbf(W - w1), w3 = W - w1 - w2;
      const int crow = 2 * (4 * (kg & 1) + p4) + (kg >> 1);
      __builtin_amdgcn_raw_buffer_store_b128(u32x4{pack_bf16(u1, w1), pack_bf16(u2, w2), pack_bf16(u3, w3), 0u}, rw,
                                             (lane & 8) ? WS_BYTES : ((lane & 4) ? so[1] : so[0]) + (20 - kq * 5) * 3072 + (kq * 16 + crow) * 16, 0, SC1);
    }
    STAMPE(5);
  } else {
    const int d = (b >> 1) * 8 + (b & 1) + 2 * wv;
    const int row = lane & 15, kg = lane >> 4;
    const int tt = b & 31, ct = b >> 5;
    const int ov = ((lane >> 2) * 2048 + (lane & 3)) * 16, os = (ct * 16 * 2048 + (tt * 8 + wv * 2) * 4) * 16;
    const int stg = b * 2 + (wv - 2);
    const bool xact = wv >= 2 && b < 128;
    u32 xx[5][8];
    u32 draw; u32x2 ep;
    unsigned aAddr, bA[8];
    const unsigned fl = sbase + wv * CH_BYTES;
    {
      const rsrc_t rk = mk_rsrc(kIn, S_ * D_ * 2), rv = mk_rsrc(vIn, S_ * D_ * 2);
      u32 kk[19], vv[16];
#pragma unroll
      for (int q = 0; q < 3; ++q) kk[q] = __builtin_amdgcn_raw_buffer_load_b32(rk, kvo + q * 1280, kso[8], NT);
#pragma unroll
      for (int q = 0; q < 8; ++q) kk[3 + q] = __builtin_amdgcn_raw_buffer_load_b32(rk, kvo + (q & 1) * 1280, kso[q >> 1], NT);
      const u32x4 taps = __builtin_amdgcn_raw_buffer_load_b128(mk_rsrc(shk, D_ * 8), kvo * 4, 0, 0);
#pragma unroll
      for (int q = 0; q < 8; ++q) vv[q] = __builtin_amdgcn_raw_buffer_load_b32(rv, kvo + (q & 1) * 1280, kso[q >> 1], NT);
#pragma unroll
      for (int q = 8; q < 16; ++q) kk[3 + q] = __builtin_amdgcn_raw_buffer_load_b32(rk, kvo + (q & 1) * 1280, kso[q >> 1], NT);
#pragma unroll
      for (int q = 8; q < 16; ++q) vv[q] = __builtin_amdgcn_raw_buffer_load_b32(rv, kvo + (q & 1) * 1280, kso[q >> 1], NT);
      SCHED();
      SCHED();
      if (F_POS == 0) {
        const char* fs = (const char*)(ssmk + (size_t)d * S_);
        dma16x4p<FC_BYTES + 28>(fl, l16, fs);
        dma16x4p<3 * FC_BYTES + 24>(fl, l16, fs);
      }
      aAddr = fl + (row & 3) * FC_BYTES + 2 * (64 * kg + (row & ~3));
      pin(aAddr);
#pragma unroll
      for (int e = 0; e < 8; ++e) { const int g = 32 + 2 * row + 1 - 8 * kg - e; bA[e] = sbase + CKV_OFF + 16 * ((4 * g + wv) ^ ((g >> 2) & 7)); pin(bA[e]); }
      ep = __builtin_amdgcn_raw_buffer_load_b64(rw, O_EPOCH, 0, SC1);
      touch();
      draw = __builtin_amdgcn_raw_buffer_load_b32(mk_rsrc(ssmD, D_ * 2), (d >> 1) * 4, 0, 0);
      *(u32x2*)(smem + CKV_OFF + tid * 8) = u32x2{0, 0};
      if (lane < 13) *(u32*)(smem + wv * CH_BYTES + (lane < 7 ? FC_BYTES + lane * 4 : 3 * FC_BYTES + (lane - 7) * 4)) = 0;
      SCHED();
      if (kso[0] == 0) { kk[0] = 0; kk[1] = 0; kk[2] = 0; }
      const f32x2 w0 = {bflo(taps[0]), bflo(taps[2])}, w1 = {bfhi(taps[0]), bfhi(taps[2])};
      const f32x2 w2 = {bflo(taps[1]), bflo(taps[3])}, w3 = {bfhi(taps[1]), bfhi(taps[3])};
#pragma unroll
      for (int r = 0; r < 2; ++r) {
        f32x2 kv[8];
#pragma unroll
        for (int q = 0; q < 8; ++q) {
          const int i = 8 * r + q;
          f32x2 a = f32x2{bflo(kk[i]), bfhi(kk[i])} * w3;
          a = __builtin_elementwise_fma(f32x2{bflo(kk[i + 1]), bfhi(kk[i + 1])}, w2, a);
          a = __builtin_elementwise_fma(f32x2{bflo(kk[i + 2]), bfhi(kk[i + 2])}, w1, a);
          a = __builtin_elementwise_fma(f32x2{bflo(kk[i + 3]), bfhi(kk[i + 3])}, w0, a);
          kv[q] = a * f32x2{bflo(vv[i]), bfhi(vv[i])};
        }
        u32x4 o0, o1;
#pragma unroll
        for (int t = 0; t < 4; ++t) {
          o0[t] = pack_bf16(kv[7 - 2 * t][0], kv[6 - 2 * t][0]);
          o1[t] = pack_bf16(kv[7 - 2 * t][1], kv[6 - 2 * t][1]);
        }
        st128(o0, rw, ov + r * 64, O_KVG + os);
        st128(o1, rw, ov + r * 64, O_KVG + os + 16384);
      }
    }
    STAMPE(1);
    pin(ep); pin(draw); pin(tch);
    tag = make_tag(ep);
    auto fload = [&]() {
      const char* fs = (const char*)(ssmk + (size_t)d * S_);
      dma16x4p<FC_BYTES + 28>(fl, l16, fs);
      dma16x4p<3 * FC_BYTES + 24>(fl, l16, fs);
    };
    auto xload = [&]() {
      const char* xp = (const char*)xIn + stg * 8 * 1280;
      static_for<0, 40>([&](auto I) { constexpr int t = I.value / 5, c = I.value % 5; gld32p<c * 256 + (t & 1) * 1280>(xx[c][t], lane * 4, xp + (t & 6) * 1280); });
    };
    if (F_POS == 1) fload();
    if (xact && X_POS == 1) {
      xload();
      wait_vm<40 + (F_POS == 1 ? 8 : 0)>();
    } else {
      wait_vm<(F_POS == 1 ? 8 : 0)>();
    }
    asm volatile("s_waitcnt lgkmcnt(0)\n\ts_barrier" ::: "memory");
    if (tid < 32) st_flag(ws, O_FK + ((b & ~31) + tid) * 256 + (b & 31) * 8, tag);
    if (xact && X_POS == 2) xload();
    if (F_POS == 2) fload();
    STAMPE(2);
    int pa1[3] = {O_FX + ((b & 7) * 32 + (lane >> 2)) * 128 + (lane & 3) * 16, O_FX + ((b & 7) * 32 + 16 + (lane >> 2)) * 128 + (lane & 3) * 16,
                  O_FA0 + ((b & 7) * 12 + (lane < 48 ? lane >> 2 : 0)) * 128 + (lane & 3) * 16};
    const char* xgp = ws + O_XG + b * 16384 + wv * 4096;
    const char* stp = ws + O_ST1 + wv * 4096;
    asm volatile("" : "+s"(xgp), "+s"(stp));
    pin(pa1[0]); pin(pa1[1]); pin(pa1[2]);
    float Dv = (d & 1) ? bfhi(draw) : bflo(draw);
    unsigned ek = sbase + CKV_OFF + 16 * ((4 * (32 + 2 * row + (kg >> 1)) + wv) ^ ((row >> 1) & 7)) + ((kg & 1) ? 0 : 8);
    unsigned ex = (ek ^ 8) + (CX_OFF - CKV_OFF - 2048), es = sbase + CS_OFF + l16;
    unsigned mo = ((lane & 7) < MODP && lane < 24) ? O_MODP + ((lane & 7) * NMOD + (lane >> 3) * D_ + d) * 4 : WS_BYTES;
    pin(Dv); pin(ek); pin(ex); pin(es); pin(mo);
    unsigned ko[2] = {(unsigned)(lane ^ (lane >> 4)) * 16, ((unsigned)(lane ^ (lane >> 4)) * 16) ^ 64};
    pin(ko[0]); pin(ko[1]);
    auto fcopies = [&]() {
      u32x4 m[5]; u32 lo[5], hi[5];
      const unsigned src = fl + FC_BYTES + l16, src4 = fl + FC_BYTES + 4096 + (lane & 1) * 16;
      static_for<0, 4>([&](auto I) { constexpr int i = I.value; ds_b128<i * 1024>(m[i], src); ds_b32<i * 1024>(lo[i], src - 4); ds_b32<i * 1024 + 16>(hi[i], src); });
      ds_b128<0>(m[4], src4); ds_b32<0>(lo[4], src4 - 4); ds_b32<16>(hi[4], src4);
      wait_lgkm<0>();
#pragma unroll
      for (int i = 0; i < 5; ++i) {
        pin(m[i]); pin(lo[i]); pin(hi[i]);
        if (i == 0 && lane == 0) lo[0] = 0;
        const u32 s1 = __builtin_amdgcn_alignbit(m[i][1], m[i][0], 16), s2 = __builtin_amdgcn_alignbit(m[i][2], m[i][1], 16), s3 = __builtin_amdgcn_alignbit(m[i][3], m[i][2], 16);
        char* dst = smem + wv * CH_BYTES + (i < 4 ? i * 1024 + l16 : 4096 + (lane & 1) * 16);
        *(u32x4*)dst = u32x4{__builtin_amdgcn_alignbit(m[i][0], lo[i], 16), s1, s2, s3};
        *(u32x4*)(dst + 2 * FC_BYTES) = u32x4{s1, s2, s3, __builtin_amdgcn_alignbit(hi[i], m[i][3], 16)};
      }
    };
    if (!xact) { if (wv == 0 && F_POS != 2) wait_vm<1>(); else wait_vm<0>(); fcopies(); }
    if (xact) {
      wait_vm<(F_POS == 2 ? 8 : 0)>();
      STAMPD(8);
#pragma unroll
      for (int c = 0; c < 5; ++c)
#pragma unroll
        for (int t = 0; t < 8; ++t) pin(xx[c][t]);
#pragma unroll
      for (int c = 0; c < 5; ++c) {
        u32x4 p0, p1;
#pragma unroll
        for (int t = 0; t < 4; ++t) {
          p0[t] = __builtin_amdgcn_perm(xx[c][2 * t + 1], xx[c][2 * t], 0x05040100u);
          p1[t] = __builtin_amdgcn_perm(xx[c][2 * t + 1], xx[c][2 * t], 0x07060302u);
        }
        const int so = O_XG + (c * 16 * 2048 + stg * 4) * 16;
        st128(p0, rw, ov, so);
        st128(p1, rw, ov, so + 16384);
      }
      const bf16x2 ones = {(__bf16)1.0f, (__bf16)1.0f};
      float s1[8], s2[8];
#pragma unroll
      for (int t = 0; t < 8; ++t) {
        s1[t] = 0.f; s2[t] = 0.f;
#pragma unroll
        for (int c = 0; c < 5; ++c) {
          const bf16x2 v = __builtin_bit_cast(bf16x2, xx[c][t]);
          s1[t] = __builtin_amdgcn_fdot2_f32_bf16(v, ones, s1[t], false);
          s2[t] = __builtin_amdgcn_fdot2_f32_bf16(v, v, s2[t], false);
        }
      }
      float q1[2], q2[2];
#pragma unroll
      for (int e = 0; e < 2; ++e) {
        q1[e] = dpp_add<DPP_ROR8>(tred16(tred32(s1[e], s1[4 + e]), tred32(s1[2 + e], s1[6 + e])));
        q2[e] = dpp_add<DPP_ROR8>(tred16(tred32(s2[e], s2[4 + e]), tred32(s2[2 + e], s2[6 + e])));
      }
      const float mean = sum8((lane & 8) ? q1[1] : q1[0]) * (1.f / D_);
      const float var = fmaxf(sum8((lane & 8) ? q2[1] : q2[0]) * (1.f / D_) - mean * mean, 0.f);
      const float sd = rbf(__builtin_amdgcn_sqrtf(rbf(rbf(var) + 1e-5f)));
      const float rs = __builtin_amdgcn_rcpf(sd);
      const int s = stg * 8 + (lane >> 3);
      const int nt = s >> 8, col = (s >> 4) & 15, k4 = (s >> 2) & 3, rr = s & 3;
      if ((lane & 7) == 0) __builtin_amdgcn_raw_buffer_store_b64(u32x2{__float_as_uint(rs), __float_as_uint(-rbf(mean) * rs)}, rw,
                                                                 O_ST1 + (((nt * 2 + (rr >> 1)) * 64 + k4 * 16 + col) * 4 + (rr & 1) * 2) * 4, 0, SC1);
      STAMPD(9);
      if (F_POS == 2) wait_vm<11>();
      fcopies();
      wait_vm<0>();
      if (lane < 8) st_flag(ws, O_FX + (lane * 32 + (stg >> 3)) * 128 + (stg & 7) * 8, tag);
    } else if (wv < 2) {
      const int po[1] = {O_FK + b * 256 + wv * 128 + (lane & 7) * 16};
      poll<1, 0>(rw, po, tag);
      STAMPE(3);
      dma16x4<CKV_OFF + 2048>(sbase + wv * 8192, ko[0], ko[1], ws + O_KVG + b * 16384 + wv * 8192);
      dma16x4<CKV_OFF + 2048 + 4096>(sbase + wv * 8192, ko[0], ko[1], ws + O_KVG + b * 16384 + wv * 8192 + 4096);
    }
    f32x4 acc[8];
    u32x2 ra[2][8][2]; u32x4 rb[2][8];
    u32x4 pf[3]; u32 mraw; u64 nm[6];
    auto load_n = [&](auto E, auto N) {
      constexpr int e = E.value, n = N.value, bf = e & 1;
      if constexpr (n < 2) ds_b64<16 * e + 8 * n>(ra[bf][0][n], aAddr);
      else if constexpr (n < 10) ds_b128<2048 * (n - 2)>(rb[bf][n - 2], bA[e]);
      else ds_b64<512 * ((n - 8) >> 1) + 16 * e + 8 * (n & 1)>(ra[bf][(n - 8) >> 1][n & 1], aAddr);
    };
    u32x2 kvw[8], xw[8]; u32x4 sA[8], sB[8];
    float T[8][4], Hh[8][4], c0 = 0.f, c1 = 0.f;
    auto kvop = [&](auto K) {
      constexpr int k = K.value, nt = k >> 3, r = k & 3;
      if constexpr ((k & 4) == 0) T[nt][r] = (r & 1) ? v_lo(kvw[nt][1 - (r >> 1)]) : v_hi(kvw[nt][1 - (r >> 1)]);
      else T[nt][r] = v_mul(T[nt][r], Dv);
    };
    auto xrd = [&](auto K) {
      constexpr int k = K.value, nt = k / 3;
      if constexpr (k % 3 == 0) ds_b64<2048 * nt>(xw[nt], ex);
      else if constexpr (k % 3 == 1) ds_b128<2048 * nt>(sA[nt], es);
      else ds_b128<2048 * nt + 1024>(sB[nt], es);
    };
    float xf[8];
    auto xop = [&](auto K) {
      constexpr int k = K.value, nt = 2 * (k >> 5) + ((k >> 2) & 1), ph = (k >> 3) & 3, r = k & 3, i = k & 7;
      if constexpr (ph == 0) xf[i] = (r & 1) ? v_hi(xw[nt][r >> 1]) : v_lo(xw[nt][r >> 1]);
      else if constexpr (ph == 1) Hh[nt][r] = v_fma(xf[i], __uint_as_float((r < 2 ? sA[nt] : sB[nt])[2 * (r & 1)]), __uint_as_float((r < 2 ? sA[nt] : sB[nt])[2 * (r & 1) + 1]));
      else if constexpr (ph == 2) Hh[nt][r] = v_fma(Hh[nt][r], c1, c0);
      else T[nt][r] = v_fma(Hh[nt][r], T[nt][r], xf[i]);
    };
    u32 ms[3];
    auto mop = [&](auto K) {
      constexpr int k = K.value;
      if constexpr (k < 3) asm_dpp<k>(mraw);
      else if constexpr (k == 3) asm_op1<3>(mraw, mraw);
      else if constexpr (k < 7) asm_rdl<8 * (k - 4)>(ms[k - 4], mraw);
      else if constexpr (k == 7) asm_sop<0>(c0, ms[0], c0);
      else if constexpr (k == 8) asm_sop<1>(c1, ms[1], c1);
      else if constexpr (k == 9) asm_sop<2>(c0, ms[2], c0);
      else if constexpr (k == 10) { u32 t; asm_op1<3>(t, __float_as_uint(c1)); c1 = __uint_as_float(t); }
      else asm_sop<2>(c1, ms[2], c1);
    };
    clear_slot();
    wait_lgkm<0>();
    static_for<0, 2>([&](auto N) { load_n(IC<7>{}, N); });
    static_for<10, 24>([&](auto N) { load_n(IC<7>{}, N); });
    if (wv < 2) wait_vm<1>();
    STAMPD(10);
    bar();
    STAMPE(4);
    static_for<2, 10>([&](auto N) { load_n(IC<7>{}, N); });
    static_for<0, 8>([&](auto EE) {
      constexpr int e = 7 - EE.value, bf = e & 1;
      static_for<0, 8>([&](auto AA) {
        constexpr int a = AA.value;
        static_for<0, 8 - a>([&](auto BB) {
          constexpr int bb = BB.value, n = conv_gap(a, bb);
          if constexpr (e == 7 && n < 8) wait_lgkm<7>();
          mfma_<(e == 7 && a == 0)>(acc[a + bb], cat(ra[bf][a][0], ra[bf][a][1]), rb[bf][bb]);
          if constexpr (e > 0 && n < 24) load_n(IC<(e > 0 ? e - 1 : 0)>{}, IC<n>{});
          if constexpr (e == 7 && n >= 24 && n < 32) ds_b64<2048 * (n - 24)>(kvw[n - 24], ek);
          constexpr int g = 36 * (7 - e) + n;
          static_for<CS.rd[g], CS.rd[g + 1]>(xrd);
          if constexpr (e > 0 ? n == 35 : (n == CW1 || n == CW2)) wait_lgkm<0>();
          static_for<CS.op[g], CS.op[g + 1]>(xop);
          if constexpr ((e == 6 || e == 5) && n < 32) kvop(IC<(6 - e) * 32 + n>{});
          if constexpr (CV.kind[g] == 2) gld128<0>(pf[CV.idx[g]], pa1[CV.idx[g]], ws);
          if constexpr (CV.kind[g] == 3) { if constexpr (CV.idx[g] == 0) dma16<CX_OFF>(sbase + wv * 4096, ko[0], xgp); else dma16n<CV.idx[g] * 1024>(ko[CV.idx[g] & 1], xgp); }
          if constexpr (CV.kind[g] == 4) { if constexpr (CV.idx[g] == 0) dma16<CS_OFF>(sbase + wv * 4096, l16, stp); else dma16n<CV.idx[g] * 1024>(l16, stp); }
          if constexpr (CV.kind[g] == 5) bld32(mraw, mo, wsrd);
          if constexpr (g == G_XCHK) { STAMPD(11); wait_vm<cv_younger(g, 2, 2)>(); }
          if constexpr (g >= G_XCHK && g < G_XCHK + 3) { constexpr int i = g - G_XCHK; nm[2 * i] = v_ne64(u32x2{pf[i][0], pf[i][1]}, tag); nm[2 * i + 1] = v_ne64(u32x2{pf[i][2], pf[i][3]}, tag); }
          if constexpr (g == G_XCHK + 3) {
            if (__builtin_expect(((nm[0] | nm[1]) | (nm[2] | nm[3]) | (nm[4] | nm[5])) != 0, 0)) poll<3, 1>(rw, pa1, tag);
            STAMPD(12);
          }
          if constexpr (e == CBE && n == 8) wait_vm<cv_younger(g, 5, 0)>();
          if constexpr (e == CBE && n >= 9 && n < 21) mop(IC<n - 9>{});
          if constexpr (e == CBE && n == 23) { STAMPD(13); wait_vm<cv_younger(g, 4, 3)>(); bar(); STAMPD(14); }
        });
      });
    });
    STAMPE(5);
    asm volatile("s_nop 7");
    static_for<0, 4>([&](auto NP) {
      constexpr int np = NP.value;
      static_for<(CS.op[288] > 32 * np ? CS.op[288] : 32 * np), 32 * np + 32>(xop);
      u32x4 o;
#pragma unroll
      for (int u = 0; u < 2; ++u) {
        const int nt = 2 * np + u;
        pin(acc[nt]);
        o[2 * u] = pack_bf16(fmaf(Hh[nt][0], acc[nt][0], T[nt][0]), fmaf(Hh[nt][1], acc[nt][1], T[nt][1]));
        o[2 * u + 1] = pack_bf16(fmaf(Hh[nt][2], acc[nt][2], T[nt][2]), fmaf(Hh[nt][3], acc[nt][3], T[nt][3]));
      }
      st128(o, rw, l16 + np * 1024, O_X2T + d * (TP_ * 2));
    });
  }
  const int kg = lane >> 4, c16 = lane & 15;
  const int kq0 = nc >> 2, r0 = (nc & 1) * 4 + wv, sgb = O_SG + (mt * 2 + ((nc >> 1) & 1)) * 8192;
  u32x2 tag2 = {(u32)tag, (u32)(tag >> 32)};
  u32x4 gbr;
  pin(tag2);
  {
    const int fb = O_F2 + (b & 7) * 32 * 128;
    const int jw = (lane & 3) * 24 + ((nc * 12 + wv * 3 + ((lane >> 2) & 1) * 2) >> 3);
    const int pa[1] = {fb + (20 + (jw >> 3)) * 128 + (jw & 6) * 8};
    u32x4 pfa;
    gld128<0>(pfa, pa[0], ws);
    const char* wsrc = ws + O_W1F + (nc * 4 + wv) * W1_STEPS * 3072;
    const char* wq[4];
    unsigned xinc[4];
#pragma unroll
    for (int i = 0; i < 4; ++i) {
      wq[i] = wsrc + ((kq0 + i) & 3) * (5 * 3072) + 4096;
      xinc[i] = (i && kq0 + i == 4) ? (unsigned)(-19 * 32 * TP_ * 2) : 32 * TP_ * 2;
      asm volatile("" : "+s"(wq[i]), "+s"(xinc[i]));
    }
    u32x4 W[G1_R][3], WA_[24], WC[3], pfs[2], pfw, gran;
    auto issue_w = [&](auto I) { constexpr int i = I.value; gld128b<(((i % 15) & 7) - 4) * 1024>(W[(i / 3) % G1_R][i % 3], ((i % 15) & 8) ? l16b : l16, wq[i / 15]); };
    int pc[2] = {fb + (lane >> 2) * 128 + (lane & 3) * 16, fb + (16 + (lane >> 2)) * 128 + (lane & 3) * 16};
    int f2o = lane < 8 ? O_F2 + (lane * 32 + (b >> 3)) * 128 + (b & 7) * 8 : WS_BYTES;
    pin(pc[0]); pin(pc[1]); pin(f2o);
    STAMPE(6);
    asm volatile("s_waitcnt vmcnt(1) lgkmcnt(0)\n\ts_barrier" ::: "memory");
    if (wv == 0) __builtin_amdgcn_raw_buffer_store_b64(tag2, rw, f2o, 0, SC1);
    STAMPE(7);
    if (bs >= CWG) {
      const int j = bs - CWG;
      u32x2 dl[5], dh[5];
      const unsigned add = sbase + AD_OFF + (8 * kg + (c16 >> 2)) * 128 + (lane & 3) * 8;
#pragma unroll
      for (int i = 0; i < 5; ++i) { const int tl = wv * 5 + i; const unsigned a2 = add + (tl >> 2) * 4096 + (tl & 3) * 32; ds_tr<0>(dl[i], a2); ds_tr<512>(dh[i], a2); }
      wait_lgkm<0>();
#pragma unroll
      for (int i = 0; i < 5; ++i) {
        pin(dl[i]); pin(dh[i]);
        __builtin_amdgcn_raw_buffer_store_b128(cat(dl[i], dh[i]), rw, l16, O_W2F + ((((j & 1) * 4 + wv) * 48 + (j >> 1)) * 5 + i) * 1024, SC1);
      }
      wait_vm<5>();
    } else {
      if (wv == 0) wait_vm<1>(); else wait_vm<0>();
    }
    pin(pfa);
    if (!__all(tag_ok(pfa, tag))) poll<1, 2>(rw, pa, tag);
    auto issue_a = [&](auto I) { constexpr int i = 36 + I.value; gld128a<(((i % 15) & 7) - 4) * 1024>(WA_[I.value], ((i % 15) & 8) ? l16b : l16, wq[i / 15]); };
    static_for<0, 3 * G1_WPRE>(issue_w);
    unsigned xoff[2];
#pragma unroll
    for (int p = 0; p < 2; ++p) {
      const int k = 4 * p + kg;
      xoff[p] = (k * TP_ + ((c16 ^ (2 * k)) * 8)) * 2 - p * 1024;
      pin(xoff[p]);
    }
    unsigned xso = O_X2T + ((kq0 * 160 - 32 + 8 * wv) * TP_ + mt * 128) * 2;
    const unsigned ldsw = sbase + wv * 2048;
    unsigned sgl = sgb + wv * 2048 + l16;
    int hvo = kg < 3 ? ((3 * wv + kg) >> 2) * 1024 + ((3 * wv + kg) & 3) * 256 + c16 * 16 : WS_BYTES;
    const int hso = O_H2F + (mt * 8 * 48 + 3 * nc) * 1024;
    unsigned spa = sbase + G1_SP + c16 * 16;
    int sgo = (c16 >> 2) == kg ? sgb + (r0 * 4 + kq0) * 256 + c16 * 16 : WS_BYTES;
    pin(sgl); pin(hvo); pin(sgo); pin(spa);
    int pw[1] = {O_FW + ((b & 7) * 12 + (lane < 48 ? lane >> 2 : 0)) * 128 + (lane & 3) * 16};
    unsigned xo = ((lane >> 3) * TP_ + (lane & 7) * 8) * 2;
    const char* xs = ws + O_X2T + (N * 80 * TP_ + M * 64) * 2;
    unsigned gbo = O_MODP + (((lane & 7) < MODP ? (lane & 7) : 0) * NMOD + 5 * D_ + N * 80 + wv * 20 + (lane < 40 ? lane >> 3 : 0) * 4) * 4;
    pin(pw[0]); pin(xo); pin(gbo);
    u32 hq[4]; u32x4 hs, H[G2_R], WV[23], pf3, ob;
    int po3 = O_F3 + (mt * 16 + nc) * 512 + wv * 32 + (lane & 1) * 16;
    int f3o = tid < 16 ? O_F3 + (mt * 16 + tid) * 512 + nc * 8 : WS_BYTES;
    int oo;
    int fwo = (bs >= CWG && tid < 8) ? O_FW + (tid * 12 + ((bs - CWG) >> 3)) * 128 + (bs & 7) * 8 : WS_BYTES;
    pin(po3); pin(f3o); pin(fwo);
    const char* hp = ws + O_H2F + (M * 4 * 48 + wv * 12) * 1024 + 4096;
    u16* outp;
    asm volatile("s_load_dwordx2 %0, %1, 0x60\n\ts_waitcnt lgkmcnt(0)" : "=s"(outp) : "s"(__builtin_amdgcn_kernarg_segment_ptr()));
    const rsrc_t ro = mk_rsrc(outp, S_ * D_ * 2);
    auto issue = [&](auto O) {
      constexpr int kind = GS.kind[O.value], i = GS.idx[O.value];
      if constexpr (kind == 0) {
        if constexpr (i & 1) xdma1(xoff[1], wsrd, xso);
        else xdma0<g1_slot(i >> 1) * 8192>(ldsw, xoff[0], wsrd, xso, xinc[(i % 10) ? 0 : i / 10]);
      }
      else if constexpr (kind == 1) issue_w(IC<i>{});
      else if constexpr (kind == 2) {
        if constexpr (i >= 37 && O.value < GS.first[160]) gld128b<((i & 7) - 4) * 1024>(WV[i - 37], (i & 8) ? l16b : l16, wdp + (i >> 4) * 16384);
        else issue_wd(IC<i>{});
      }
      else if constexpr (kind == 3) {
        if constexpr (i < 2) dma16<XR_OFF + i * 4160>(sbase + wv * 1040, xo, xs + (size_t)(wv + 4 * i) * 8 * TP_ * 2);
        else if constexpr (i == 2) dma16<XR_OFF + 8320>(sbase + (wv & 1) * 1040, xo, xs + (size_t)((wv & 1) + 8) * 8 * TP_ * 2);
        else gld128<0>(gbr, gbo, ws);
      }
      else if constexpr (kind == 4) gld128<0>(pfw, pw[0], ws);
      else if constexpr (kind == 5) __builtin_amdgcn_raw_buffer_store_b128(gran, rw, sgo, 0, SC1);
      else if constexpr (kind == 6) gld128<i * 1024>(pfs[i], sgl, ws);
      else if constexpr (kind == 7) { hs = u32x4{hq[0], hq[1], hq[2], hq[3]}; __builtin_amdgcn_raw_buffer_store_b128(hs, rw, hvo, hso + g1_ord(i) * (48 * 1024), SC1); }
      else if constexpr (kind == 8) issue_a(IC<i>{});
      else if constexpr (kind == 9) __builtin_amdgcn_raw_buffer_store_b64(tag2, rw, f3o + i * 128, 0, SC1);
      else if constexpr (kind == 10) gld128<i * 128>(pf3, po3, ws);
      else if constexpr (kind == 11) gld128b<(((i % 12) & 7) - 4) * 1024>(H[i % G2_R], ((i % 12) & 8) ? l16b : l16, hp + (i / 12) * (48 * 1024));
      else if constexpr (kind == 12) __builtin_amdgcn_raw_buffer_store_b128(ob, ro, oo, i * (32 * D_ * 2), 0);
      else __builtin_amdgcn_raw_buffer_store_b64(tag2, rw, fwo, 0, SC1);
    };
    auto check3 = [&](auto ST) {
      if (__builtin_expect((v_ne64(u32x2{pf3[0], pf3[1]}, tag) | v_ne64(u32x2{pf3[2], pf3[3]}, tag)) != 0, 0)) {
        const int po[1] = {po3 + ST.value * 128};
        poll<1, 0>(rw, po, tag);
      }
    };
    unsigned xa[9], xm[8];
#pragma unroll
    for (int jj = 0; jj < 9; ++jj) {
      const int rr = 4 * kg + (c16 >> 2);
      xa[jj] = sbase + rr * 256 + (((jj < 8 ? jj : r0) ^ (rr & 7)) * 32) + (lane & 3) * 8;
      pin(xa[jj]);
    }
    u32x4 ones = {0x3f803f80u, 0x3f803f80u, 0x3f803f80u, 0x3f803f80u};
    pin(ones);
    static_for<0, 3>([&](auto I) { gld128a<I.value * 1024>(WC[I.value], l16, wsrc + 20 * 3072); });
    static_for<0, WA - PLATE>(issue_a);
    if (wv == 0) { gld128<0>(pfs[0], pc[0], ws); gld128<0>(pfs[1], pc[1], ws); }
    static_for<WA - PLATE, WA>(issue_a);
    STAMPL(8);
    if (wv == 0) {
      wait_vm<PLATE>();
      pin(pfs[0]); pin(pfs[1]);
      if (!__all(tag_ok(pfs[0], tag) && tag_ok(pfs[1], tag))) poll<2, PSL>(rw, pc, tag);
    } else {
      wait_vm<3 * G1_WPRE + 3 + WA>();
    }
    bar();
    STAMPL(9);
    static_for<0, GS.first[0]>(issue);
    f32x4 acc[8][3];
    u32x2 X[G1_LA + 1][2], XS[5][2];
    auto xread = [&](auto T, auto HH) {
      constexpr int t = T.value, h = HH.value;
      if constexpr (t < 96) ds_tr<g1_slot(g1_ks(t)) * 8192 + h * 4096>(X[t % (G1_LA + 1)][h], xa[g1_j(t)]);
      else ds_tr<(g1_ks(t) - 12) * 8192 + h * 4096>(X[t % (G1_LA + 1)][h], xm[g1_j(t)]);
    };
    f32x4 gram, sum1;
    u32 fs[4], fa; u64 sbad;
    unsigned spw = sbase + G1_SP + (2 * wv + (kg >> 1)) * 256 + c16 * 16;
    pin(spw);
    auto fop = [&](auto K) {
      constexpr int k = K.value;
      if constexpr (k == 0) asm_and3(fa, pfs[0][1], pfs[0][3], pfs[1][1]);
      else if constexpr (k == 1) asm_and3(fa, fa, pfs[1][3], pfs[1][3]);
      else if constexpr (k == 2) asm_ne1(sbad, fa);
      else if constexpr (k == 4) v_swap32(fs[0], fs[1]);
      else if constexpr (k == 5) v_swap32(fs[2], fs[3]);
      else if constexpr (k == 6) asm_op2<0>(fs[0], fs[0], fs[1]);
      else if constexpr (k == 7) asm_op2<0>(fs[2], fs[2], fs[3]);
      else if constexpr (k == 8) v_swap16(fs[0], fs[2]);
      else if constexpr (k == 9) asm_op2<0>(fs[0], fs[0], fs[2]);
      else if constexpr (k == 10) asm_op1<0>(fs[0], fs[0]);
      else if constexpr (k == 11) asm_op1<1>(fs[1], fs[0]);
      else if constexpr (k == 12) v_swap16(fs[0], fs[1]);
      else if constexpr (k == 13) asm_op2<1>(fs[1], fs[0], fs[1]);
      else if constexpr (k == 14) asm_op1<2>(fs[1], fs[1]);
      else if constexpr (k == 15) asm_op1<3>(fs[1], fs[1]);
      else if constexpr (k == 16) asm_op1<4>(fs[1], fs[1]);
      else if constexpr (k == 17) asm_op1<3>(fs[1], fs[1]);
      else if constexpr (k == 18) asm_op1<5>(fs[1], fs[1]);
      else if constexpr (k == 19) asm_op2<2>(fs[0], fs[0], fs[1]);
      else if constexpr (k == 20) asm_op1<1>(fs[1], fs[0]);
      else if constexpr (k == 21) asm_op1<1>(fs[2], fs[0]);
      else if constexpr (k == 22) asm_op1<6>(fs[3], fs[0]);
      else if constexpr (k == 23) ds_w128u<0>(spw, u32x4{fs[0], fs[1], fs[2], fs[3]});
    };
    float eg[6]; u32x4 xc;
    auto eop = [&](auto J, auto K) {
      constexpr int j = J.value, k = K.value;
      if constexpr (k < 15) {
        if constexpr (k < 6) eg[k] = v_relu(acc[j][k >> 1][2 * (k & 1)]);
        else if constexpr (k < 12) eg[k - 6] = v_mul(eg[k - 6], acc[j][(k - 6) >> 1][2 * (k & 1) + 1]);
        else hq[k == 12 ? 1 : k == 13 ? 0 : 2] = v_cvt(eg[k == 12 ? 2 : k == 13 ? 0 : 4], eg[k == 12 ? 3 : k == 13 ? 1 : 5]);
      } else if constexpr (k == 15) v_swap32(hq[1], hq[3]);
      else if constexpr (k == 16) v_swap32(hq[0], hq[2]);
      else if constexpr (k == 17) v_swap16(hq[0], hq[1]);
      else v_swap16(hq[2], hq[3]);
    };
    unsigned pwa, pra, gba, xad, sda;
    float xr[8], sdv, G[8], S[8], rs2;
    hq[3] = 0;
    pin(hq[3]);
    wait_vm<GS.first[0] - 2>();
    bar();
    STAMPL(10);
    static_for<0, G1_LA>([&](auto T) { xread(T, IC<0>{}); xread(T, IC<1>{}); });
    static_for<0, 160>([&](auto T) {
      constexpr int t = T.value, ks = g1_ks(t), jj = g1_j(t), tn = t + G1_LA, u = t & 7;
      if constexpr (t == 24) STAMPG(1);
      if constexpr (t == 48) STAMPG(2);
      if constexpr (t == 72) STAMPG(3);
      if constexpr (t == 98) STAMPG(4);
      if constexpr (t == 128) STAMPG(5);
      if constexpr (!(t & 1)) wait_lgkm<2 * ((tn <= 160 ? tn : 160) - t - 2)>();
      if constexpr (g1_need(t) >= 0) {
        static_assert(g1_need(t) < GS.first[t], "not issued yet");
        if constexpr (GS.first[t] - 1 - g1_need(t) < 63) wait_vm<GS.first[t] - 1 - g1_need(t)>();
        bar();
      }
      if constexpr (t == T_WCHK) {
        if (__builtin_expect((v_ne64(u32x2{pfw[0], pfw[1]}, tag) | v_ne64(u32x2{pfw[2], pfw[3]}, tag)) != 0, 0)) poll<1, 1>(rw, pw, tag);
      }
      if constexpr (t == T_C0) { wait_vm<GS.first[t] - 1 - g_pos(10, 0)>(); check3(IC<0>{}); }
      if constexpr (t == T_SETUP) {
        static_assert(g_pos(3, 3) < g1_need(T_F0) && T_SETUP > T_F0, "gate partials landed");
        {
          pin(gbr);
          const float g = sum8((lane & 7) < MODP ? __uint_as_float(gbr[0]) : 0.f), g2 = sum8((lane & 7) < MODP ? __uint_as_float(gbr[1]) : 0.f);
          const float g3 = sum8((lane & 7) < MODP ? __uint_as_float(gbr[2]) : 0.f), g4 = sum8((lane & 7) < MODP ? __uint_as_float(gbr[3]) : 0.f);
          if ((lane & 7) == 0 && lane < 40) *(f32x4*)(smem + GB_OFF + wv * 80 + (lane >> 3) * 16) = f32x4{rbf(g), rbf(g2), rbf(g3), rbf(g4)};
        }
        const int lf = lane < 40 ? lane : 0, ft = 4 * wv + lf / 10, fc = lf % 10;
        pwa = sbase + wv * 5440 + l16 + (kg >> 1) * 32;
        pra = sbase + fc * 544 + ft * 16; gba = sbase + GB_OFF + fc * 32;
        xad = sbase + XR_OFF + fc * 1040 + ft * 2; sda = sbase + G1_SP + ((nc & 4) * 16 + ft) * 16 + 2;
        {
          const int pos = M * 64 + ft, l8 = (pos >> 3) & 63;
          const int tok = 256 * (2 * (pos >> 9) + ((pos >> 2) & 1)) + 16 * (l8 & 15) + 4 * (l8 >> 4) + (pos & 3);
          oo = lane < 40 ? (tok * D_ + N * 80 + 8 * fc) * 2 : S_ * D_ * 2;
        }
        sdv = 0.f;
#pragma unroll
        for (int e = 0; e < 8; ++e) { xr[e] = 0.f; pin(xr[e]); }
        pin(pwa); pin(pra); pin(gba); pin(xad); pin(sda); pin(oo); pin(sdv);
      }
      if constexpr (t == T_SST - 2) {
        pin(gram); pin(sum1);
        const int c3 = lane & 3;
        gran = u32x4{__float_as_uint(sum1[0]), 1u, __float_as_uint(c3 == 0 ? gram[0] : c3 == 1 ? gram[1] : c3 == 2 ? gram[2] : gram[3]), 1u};
        pin(gran);
      }
      if constexpr (t == 86) {
        if (__builtin_expect(sbad != 0, 0)) {
#pragma unroll 1
          for (int spn = 0; spn < SPIN_CAP; ++spn) {
            pfs[0] = __builtin_amdgcn_raw_buffer_load_b128(rw, sgl, 0, SC1);
            pfs[1] = __builtin_amdgcn_raw_buffer_load_b128(rw, sgl + 1024, 0, SC1);
            if (__all((pfs[0][1] & pfs[0][3] & pfs[1][1] & pfs[1][3]) == 1u)) break;
            asm volatile("" ::: "memory");
          }
        }
        fs[0] = pfs[0][0]; fs[1] = pfs[1][0]; fs[2] = pfs[0][2]; fs[3] = pfs[1][2];
      }
      auto ep = [&](auto A, auto B) { if constexpr (t >= 104) static_for<A.value, (B.value < 19 ? B.value : 19)>([&](auto K) { eop(IC<g1_j(t - 8)>{}, K); }); };
      constexpr int k0 = u == 0 ? 0 : 4 * u - 3;
      constexpr int sr = t == 0 ? 0 : t == 4 ? 1 : (t >= 12 && t < 15) ? t - 10 : -1, sm = t == 4 ? 0 : t == 8 ? 1 : (t >= 16 && t < 19) ? t - 14 : -1;
      const u32x4 xb = cat(X[t % (G1_LA + 1)][0], X[t % (G1_LA + 1)][1]);
      if constexpr (ks >= 12) mfma_a<false>(acc[jj][0], WA_[3 * (ks - 12) + 0], xb); else mfma_<ks == 0>(acc[jj][0], W[ks % G1_R][0], xb);
      if constexpr (tn < 160) xread(IC<tn>{}, IC<0>{});
      if constexpr (t >= 84 && t < 96) fop(IC<2 * (t - 84)>{});
      if constexpr (u > 0) ep(IC<k0>{}, IC<k0 + 1>{});
      if constexpr (ks >= 12) mfma_a<false>(acc[jj][1], WA_[3 * (ks - 12) + 1], xb); else mfma_<ks == 0>(acc[jj][1], W[ks % G1_R][1], xb);
      if constexpr (tn < 160) xread(IC<tn>{}, IC<1>{});
      if constexpr (t >= 84 && t < 96) fop(IC<2 * (t - 84) + 1>{});
      if constexpr (u > 0) ep(IC<k0 + 1>{}, IC<k0 + 2>{}); else ep(IC<0>{}, IC<1>{});
      if constexpr (ks >= 12) mfma_a<false>(acc[jj][2], WA_[3 * (ks - 12) + 2], xb); else mfma_<ks == 0>(acc[jj][2], W[ks % G1_R][2], xb);
      if constexpr (t >= 96) real();
      static_for<GS.first[t], GS.first[t + 1]>(issue);
      if constexpr (u > 0) ep(IC<k0 + 2>{}, IC<k0 + 4>{});
      if constexpr (t >= 84 && t < 92) xm[t - 84] = v_addc(xa[t - 84], G1_RS * 8192);
      if constexpr (sm >= 0) { mfma_<sm == 0>(gram, cat(XS[sm][0], XS[sm][1]), cat(XS[sm][0], XS[sm][1])); mfma_<sm == 0>(sum1, ones, cat(XS[sm][0], XS[sm][1])); }
      if constexpr (sr >= 0) { ds_tr<sr * 8192>(XS[sr][0], xa[8]); ds_tr<sr * 8192 + 4096>(XS[sr][1], xa[8]); }
      if constexpr (t >= 96 && u == (t < 104 ? 4 : 0)) ds_b128<jj * 256>(xc, spa);
      if constexpr (t >= 96 && u == 7) {
        if constexpr (t < 104) wait_lgkm<6>();
        mfma_a<false>(acc[jj][0], WC[0], xc);
        mfma_a<false>(acc[jj][1], WC[1], xc);
        mfma_a<false>(acc[jj][2], WC[2], xc);
      }
    });
    STAMPL(11);
    asm volatile("s_nop 5");
    static_for<0, 15>([&](auto K) { eop(IC<7>{}, K); });
    eop(IC<7>{}, IC<15>{}); asm volatile("s_nop 0"); eop(IC<7>{}, IC<16>{}); asm volatile("s_nop 1"); eop(IC<7>{}, IC<17>{}); eop(IC<7>{}, IC<18>{});
    static_for<GS.first[160], GS.mid>(issue);
    STAMPL(12);
    if constexpr (T_C0 >= 160) { wait_vm<GS.mid - 1 - g_pos(10, 0)>(); check3(IC<0>{}); }
    static_for<GS.mid, GS.first2[0]>(issue);
    if constexpr (g_lastdv() >= 0) wait_vm<GS.first2[0] - 1 - g_lastdv()>();
    static_for<37, 60>([&](auto I) { if constexpr (g_pos(2, I.value) < GS.first[160]) to_acc(WD[I.value], WV[I.value - 37]); });
    f32x4 gbv[2], ac2[2][5], pt[8];
    auto pre = [&](auto J, auto K) {
      constexpr int j = J.value, k = K.value;
      if constexpr (k == 0) ds_hi16<j * 256>(sdv, sda);
      else if constexpr (k < 9) ds_hi16<(k - 1) * 128 + j * 32>(xr[k - 1], xad);
      else if constexpr (k == 9) rs2 = v_rcp(sdv);
      else if constexpr (k == 10) rs2 = v_mul(rs2, rs2);
      else G[k - 11] = v_mul(gbv[(k - 11) >> 2][(k - 11) & 3], rs2);
    };
    auto fin = [&](auto K) {
      constexpr int k = K.value, e = k & 7;
      if constexpr (k < 8) S[e] = v_add(pt[e >> 2][e & 3], pt[2 + (e >> 2)][e & 3]);
      else if constexpr (k < 24) S[e] = v_add(S[e], pt[2 * (k >> 3) + 2 + (e >> 2)][e & 3]);
      else if constexpr (k < 32) S[e] = v_fma(G[e], S[e], xr[e]);
      else ob[k - 32] = v_cvt(S[2 * (k - 32)], S[2 * (k - 32) + 1]);
    };
    auto prd = [&](auto J, auto K) { ds_f128<(J.value & 1) * 21760 + (K.value >> 1) * 5440 + (K.value & 1) * 256>(pt[K.value], pra); };
    STAMPL(13);
    static_for<0, 240>([&](auto GG) {
      constexpr int g = GG.value, o = g / 5, jj = o / 12, ks = o % 12, i = g % 5, q = g % 60;
      if constexpr (g == 60) STAMPG(6);
      if constexpr (g == 120) STAMPG(7);
      if constexpr (i == 0) { static_assert(g2_need(o) < GS.first2[g], "not issued yet"); wait_vm<GS.first2[g] - 1 - g2_need(o)>(); }
      if constexpr (g == G_F3) { wait_vm<GS.first2[g] - 1 - g_pos(7, 7)>(); bar(); }
      if constexpr (g == G_C1) { wait_vm<GS.first2[g] - 1 - g_pos(10, 1)>(); check3(IC<1>{}); }
      if constexpr (g == G_C2) { wait_vm<GS.first2[g] - 1 - g_pos(10, 2)>(); check3(IC<2>{}); }
      if constexpr (g == G_C3) { wait_vm<GS.first2[g] - 1 - g_pos(10, 3)>(); check3(IC<3>{}); }
      mfma_a<ks == 0>(ac2[jj & 1][i], WD[ks * 5 + i], H[o % G2_R]);
      if constexpr (i == 4) real();
      static_for<GS.first2[g], GS.first2[g + 1]>(issue);
      if constexpr (g == 30 || g == 32) ds_f128<(g - 30) * 8>(gbv[(g - 30) / 2], gba);
      if constexpr (jj >= 1) {
        if constexpr (q >= 5 && q < 15 && (q & 1)) ds_w128<((jj - 1) & 1) * 21760 + ((q - 5) / 2) * 1088>(pwa, ac2[(jj - 1) & 1][(q - 5) / 2]);
        if constexpr (q == 18) { wait_lgkm<0>(); bar(); }
        if constexpr (q >= 19 && q < 27) prd(IC<jj - 1>{}, IC<q - 19>{});
        if constexpr (q == 30) wait_lgkm<0>();
        if constexpr (q >= 31 && q < 31 + FINW) static_for<(36 * (q - 31) + FINW - 1) / FINW, (36 * (q - 30) + FINW - 1) / FINW>(fin);
      }
      if constexpr (jj >= 1 && q < 18 && !(q & 1)) pre(IC<jj - 1>{}, IC<q / 2>{});
      if constexpr (jj >= 1 && q >= 19 && q < 29) pre(IC<jj - 1>{}, IC<q - 10>{});
    });
    STAMPL(14);
    static_for<0, 9>([&](auto K) { pre(IC<3>{}, K); });
    static_for<0, 5>([&](auto I) { ds_w128<21760 + I.value * 1088>(pwa, ac2[1][I.value]); });
    wait_lgkm<5>();
    pre(IC<3>{}, IC<9>{}); asm volatile("s_nop 0");
    static_for<10, 19>([&](auto K) { pre(IC<3>{}, K); });
    wait_lgkm<0>(); bar();
    static_for<0, 8>([&](auto K) { prd(IC<3>{}, K); });
    wait_lgkm<4>();
    static_for<0, 8>(fin);
    wait_lgkm<0>();
    static_for<8, 36>(fin);
    __builtin_amdgcn_raw_buffer_store_b128(ob, ro, oo, 3 * (32 * D_ * 2), 0);
  }
  if (b == 0 && tid == 0) st_flag(ws, O_EPOCH, tag);
  STAMP_END();
}

extern "C" void kernel_launch(void* const* d_in, const int* in_sizes, int n_in, void* d_out, int out_size,
                              void* d_ws, size_t ws_size, hipStream_t stream) {
  k_fused<<<256, 256, 0, stream>>>((const u16*)d_in[0], (const u16*)d_in[1], (const u16*)d_in[2], (const u16*)d_in[3],
                                   (const u16*)d_in[4], (const u16*)d_in[5], (const u16*)d_in[7], (const u16*)d_in[8],
                                   (const u16*)d_in[9], (const u16*)d_in[10], (const u16*)d_in[11], (char*)d_ws, (u16*)d_out);
}
```

```cpp
#define MULT 0
#include <hip/hip_runtime.h>
#include <stdint.h>

typedef unsigned short u16;
typedef unsigned int u32;
typedef __attribute__((ext_vector_type(2))) __bf16 bf16x2;
typedef __attribute__((ext_vector_type(4))) float f32x4;
typedef __attribute__((ext_vector_type(2))) float f32x2;
typedef __attribute__((ext_vector_type(4))) u32 u32x4;
typedef __attribute__((ext_vector_type(2))) u32 u32x2;

constexpr int S_ = 2048, D_ = 640, F_ = 1536;
constexpr int TP_ = S_ + 64;
constexpr int NMOD = 6 * D_;
constexpr int MODP = 5;
constexpr int CWG = 160;

__device__ __forceinline__ float bflo(u32 w) { return __uint_as_float(w << 16); }
__device__ __forceinline__ float bfhi(u32 w) { return __uint_as_float(w & 0xffff0000u); }
__device__ __forceinline__ u32 pack_bf16(float lo, float hi) {
  bf16x2 v; v[0] = (__bf16)lo; v[1] = (__bf16)hi; return __builtin_bit_cast(u32, v);
}
__device__ __forceinline__ float rbf(float x) { __bf16 b = (__bf16)x; return (float)b; }
#define SCHED() __builtin_amdgcn_sched_barrier(0)
template <int V> struct IC { static constexpr int value = V; };
template <int B, int E, class Fn> __device__ __forceinline__ void static_for(Fn&& f) {
  if constexpr (B < E) { f(IC<B>{}); static_for<B + 1, E>(f); }
}
__device__ __forceinline__ void mfma(f32x4& c, u32x4 a, u32x4 b) {
  asm volatile("v_mfma_f32_16x16x32_bf16 %0, %1, %2, %0" : "+v"(c) : "v"(a), "v"(b));
}
__device__ __forceinline__ void mfma0(f32x4& c, u32x4 a, u32x4 b) {
  asm volatile("v_mfma_f32_16x16x32_bf16 %0, %1, %2, 0" : "=&v"(c) : "v"(a), "v"(b));
}
template <bool FIRST> __device__ __forceinline__ void mfma_(f32x4& c, u32x4 a, u32x4 b) { if constexpr (FIRST) mfma0(c, a, b); else mfma(c, a, b); }
__device__ __forceinline__ u32 alignbit(u32 hi, u32 lo, u32 sh) {
  u32 d; asm volatile("v_alignbit_b32 %0, %1, %2, %3" : "=v"(d) : "v"(hi), "v"(lo), "v"(sh)); return d;
}
template <int OFF> __device__ __forceinline__ void ds_tr(u32x2& dst, unsigned addr) {
  asm volatile("ds_read_b64_tr_b16 %0, %1 offset:%2" : "=v"(dst) : "v"(addr), "i"(OFF) : "memory");
}
template <int OFF> __device__ __forceinline__ void ds_b32(u32& dst, unsigned addr) {
  asm volatile("ds_read_b32 %0, %1 offset:%2" : "=v"(dst) : "v"(addr), "i"(OFF) : "memory");
}
template <int OFF> __device__ __forceinline__ void ds_b64(u32x2& dst, unsigned addr) {
  asm volatile("ds_read_b64 %0, %1 offset:%2" : "=v"(dst) : "v"(addr), "i"(OFF) : "memory");
}
template <int OFF> __device__ __forceinline__ void ds_b128(u32x4& dst, unsigned addr) {
  asm volatile("ds_read_b128 %0, %1 offset:%2" : "=v"(dst) : "v"(addr), "i"(OFF) : "memory");
}
template <int OFF> __device__ __forceinline__ void ds_w128(unsigned addr, f32x4 v) {
  asm volatile("ds_write_b128 %0, %1 offset:%2" :: "v"(addr), "v"(v), "i"(OFF) : "memory");
}
template <int OFF> __device__ __forceinline__ void gld128(u32x4& dst, unsigned voff, const char* sb) {
  asm volatile("global_load_dwordx4 %0, %1, %2 offset:%3 sc1" : "=v"(dst) : "v"(voff), "s"(sb), "i"(OFF) : "memory");
}
__device__ __forceinline__ void gld32(u32& dst, unsigned voff, const char* sb) {
  asm volatile("global_load_dword %0, %1, %2 sc1" : "=v"(dst) : "v"(voff), "s"(sb) : "memory");
}
template <int LOFF> __device__ __forceinline__ void dma16(unsigned ldsb, unsigned voff, const char* sb) {
  asm volatile("s_add_u32 m0, %0, %3\n\ts_nop 0\n\tglobal_load_lds_dwordx4 %1, %2 sc1" :: "s"(ldsb), "v"(voff), "s"(sb), "i"(LOFF) : "memory", "scc");
}
template <int LOFF> __device__ __forceinline__ void dma16x4(unsigned ldsb, unsigned v0, unsigned v1, const char* sb) {
  asm volatile("s_add_u32 m0, %0, %4\n\ts_nop 0\n\tglobal_load_lds_dwordx4 %1, %3 sc1\n\tglobal_load_lds_dwordx4 %2, %3 offset:1024 sc1\n\t"
               "global_load_lds_dwordx4 %1, %3 offset:2048 sc1\n\tglobal_load_lds_dwordx4 %2, %3 offset:3072 sc1"
               :: "s"(ldsb), "v"(v0), "v"(v1), "s"(sb), "i"(LOFF) : "memory", "scc");
}
template <int N> __device__ __forceinline__ void wait_vm() { asm volatile("s_waitcnt vmcnt(%0)" :: "i"(N) : "memory"); }
template <int N> __device__ __forceinline__ void wait_lgkm() { asm volatile("s_waitcnt lgkmcnt(%0)" :: "i"(N) : "memory"); }
__device__ __forceinline__ void keep(u32x4 v) { asm volatile("" :: "v"(v)); }
__device__ __forceinline__ void real() { __builtin_amdgcn_s_setprio(0); }
__device__ __forceinline__ void bar() { asm volatile("s_barrier" ::: "memory"); }
template <class T> __device__ __forceinline__ void pin(T& a) { asm volatile("" : "+v"(a)); }
__device__ __forceinline__ u32x4 cat(u32x2 a, u32x2 b) { return u32x4{a[0], a[1], b[0], b[1]}; }

typedef __amdgpu_buffer_rsrc_t rsrc_t;
__device__ __forceinline__ rsrc_t mk_rsrc(const void* p, int bytes) { return __builtin_amdgcn_make_buffer_rsrc((void*)p, 0, bytes, 0x00020000); }
template <int CTRL> __device__ __forceinline__ float dpp_add(float x) {
  return x + __builtin_bit_cast(float, __builtin_amdgcn_mov_dpp(__builtin_bit_cast(int, x), CTRL, 0xf, 0xf, true));
}
constexpr int DPP_XOR1 = 0xB1, DPP_XOR2 = 0x4E, DPP_HMIRROR = 0x141, DPP_ROR8 = 0x128;
__device__ __forceinline__ float sum8(float x) { return dpp_add<DPP_HMIRROR>(dpp_add<DPP_XOR2>(dpp_add<DPP_XOR1>(x))); }
__device__ __forceinline__ float tred32(float a, float b) {
  auto r = __builtin_amdgcn_permlane32_swap(__float_as_uint(a), __float_as_uint(b), false, false);
  return __uint_as_float(r[0]) + __uint_as_float(r[1]);
}
__device__ __forceinline__ float tred16(float a, float b) {
  auto r = __builtin_amdgcn_permlane16_swap(__float_as_uint(a), __float_as_uint(b), false, false);
  return __uint_as_float(r[0]) + __uint_as_float(r[1]);
}
__device__ __forceinline__ float rdl(float x, int l) { return __int_as_float(__builtin_amdgcn_readlane(__float_as_int(x), l)); }

typedef unsigned long long u64;
#define GAS __attribute__((address_space(1)))
constexpr int SC1 = 16;
#define BS " sc1"
#define A0_DELAY 16
#define F_LATE 1
#define A1_DELAY 4
#define A3_DELAY 24
#define AU_DELAY 16
#define A_DELAY 2
#define FXE 7
#define PE 4
#define NT 0
#define NTS ""
template <int OFF> __device__ __forceinline__ void gld128b(u32x4& dst, unsigned voff, const char* sb) {
  asm volatile("global_load_dwordx4 %0, %1, %2 offset:%3" BS : "=v"(dst) : "v"(voff), "s"(sb), "i"(OFF) : "memory");
}
template <int OFF> __device__ __forceinline__ void gld128a(u32x4& dst, unsigned voff, const char* sb) {
  asm volatile("global_load_dwordx4 %0, %1, %2 offset:%3" BS : "=a"(dst) : "v"(voff), "s"(sb), "i"(OFF) : "memory");
}
template <bool FIRST> __device__ __forceinline__ void mfma_a(f32x4& c, u32x4 a, u32x4 b) {
  if constexpr (FIRST) asm volatile("v_mfma_f32_16x16x32_bf16 %0, %1, %2, 0" : "=&v"(c) : "a"(a), "v"(b));
  else asm volatile("v_mfma_f32_16x16x32_bf16 %0, %1, %2, %0" : "+v"(c) : "a"(a), "v"(b));
}
typedef __attribute__((ext_vector_type(8))) __bf16 bf16x8;
__device__ __forceinline__ f32x4 mfma_b(u32x4 a, u32x4 b, f32x4 c) {
  return __builtin_amdgcn_mfma_f32_16x16x32_bf16(__builtin_bit_cast(bf16x8, a), __builtin_bit_cast(bf16x8, b), c, 0, 0, 0);
}
template <int LOFF> __device__ __forceinline__ void dma16p(unsigned ldsb, unsigned voff, const char* sb) {
  asm volatile("s_add_u32 m0, %0, %3\n\ts_nop 0\n\tglobal_load_lds_dwordx4 %1, %2" NTS :: "s"(ldsb), "v"(voff), "s"(sb), "i"(LOFF) : "memory", "scc");
}
template <int LOFF> __device__ __forceinline__ void dma16x4p(unsigned ldsb, unsigned v0, const char* sb) {
  asm volatile("s_add_u32 m0, %0, %3\n\ts_nop 0\n\tglobal_load_lds_dwordx4 %1, %2" NTS "\n\tglobal_load_lds_dwordx4 %1, %2 offset:1024" NTS "\n\t"
               "global_load_lds_dwordx4 %1, %2 offset:2048" NTS "\n\tglobal_load_lds_dwordx4 %1, %2 offset:3072" NTS
               :: "s"(ldsb), "v"(v0), "s"(sb), "i"(LOFF) : "memory", "scc");
}

#undef DBGC
#define STAMP(I)
#define STAMP_DECL()
#define STAMP_END()

__device__ __forceinline__ void gld128p(u32x4& dst, unsigned voff, const char* sb) {
  asm volatile("global_load_dwordx4 %0, %1, %2" NTS : "=v"(dst) : "v"(voff), "s"(sb) : "memory");
}
template <int OFF> __device__ __forceinline__ void gld32p(u32& dst, unsigned voff, const char* sb) {
  asm volatile("global_load_dword %0, %1, %2 offset:%3" NTS : "=v"(dst) : "v"(voff), "s"(sb), "i"(OFF) : "memory");
}
template <int OFF> __device__ __forceinline__ void gld16p(u32& dst, unsigned voff, const char* sb) {
  asm volatile("global_load_ushort %0, %1, %2 offset:%3" : "=v"(dst) : "v"(voff), "s"(sb), "i"(OFF) : "memory");
}
#define CPF_DONE() asm volatile("" :: "v"(cpf))
#define X_POS 1
#define F_POS 1
template <int OFF> __device__ __forceinline__ void dma16n(unsigned voff, const char* sb) {
  asm volatile("global_load_lds_dwordx4 %0, %1 offset:%2 sc1" :: "v"(voff), "s"(sb), "i"(OFF) : "memory");
}
__device__ __forceinline__ u64 v_ne64(u32x2 v, u64 tag) { u64 m; asm volatile("v_cmp_ne_u64 %0, %1, %2" : "=s"(m) : "v"(v), "s"(tag)); return m; }
__device__ __forceinline__ float v_lo(u32 w) { float d; asm volatile("v_lshlrev_b32 %0, 16, %1" : "=v"(d) : "v"(w)); return d; }
__device__ __forceinline__ float v_hi(u32 w) { float d; asm volatile("v_and_b32 %0, 0xffff0000, %1" : "=v"(d) : "v"(w)); return d; }
__device__ __forceinline__ float v_fma(float a, float b, float c) { float d; asm volatile("v_fma_f32 %0, %1, %2, %3" : "=v"(d) : "v"(a), "v"(b), "v"(c)); return d; }
__device__ __forceinline__ float v_mul(float a, float b) { float d; asm volatile("v_mul_f32 %0, %1, %2" : "=v"(d) : "v"(a), "v"(b)); return d; }
__device__ __forceinline__ float v_add(float a, float b) { float d; asm volatile("v_add_f32 %0, %1, %2" : "=v"(d) : "v"(a), "v"(b)); return d; }
#define STAMPD(I)
#define STAMPL(I) STAMP(I)
__device__ __forceinline__ float v_relu(float a) { float d; asm volatile("v_max_i32 %0, 0, %1" : "=v"(d) : "v"(a)); return d; }
__device__ __forceinline__ u32 v_cvt(float a, float b) { u32 d; asm volatile("v_cvt_pk_bf16_f32 %0, %1, %2" : "=v"(d) : "v"(a), "v"(b)); return d; }
__device__ __forceinline__ u32 v_perm(u32 a, u32 sel) { u32 d; asm volatile("v_perm_b32 %0, %1, %1, %2" : "=v"(d) : "v"(a), "s"(sel)); return d; }
__device__ __forceinline__ void v_swap32(u32& a, u32& b) { asm volatile("v_permlane32_swap_b32 %0, %1" : "+v"(a), "+v"(b)); }
__device__ __forceinline__ void v_swap16(u32& a, u32& b) { asm volatile("v_permlane16_swap_b32 %0, %1" : "+v"(a), "+v"(b)); }
template <int OFF> __device__ __forceinline__ void ds_f128(f32x4& dst, unsigned addr) {
  asm volatile("ds_read_b128 %0, %1 offset:%2" : "=v"(dst) : "v"(addr), "i"(OFF) : "memory");
}
#define STAMPG(I)
#define STAMPE(I) STAMP(I)
__device__ __forceinline__ void asm_and3(u32& d, u32 a, u32 b, u32 c) { asm volatile("v_bitop3_b32 %0, %1, %2, %3 bitop3:0x80" : "=v"(d) : "v"(a), "v"(b), "v"(c)); }
__device__ __forceinline__ void asm_ne1(unsigned long long& m, u32 a) { asm volatile("v_cmp_ne_u32 %0, 1, %1" : "=s"(m) : "v"(a)); }
__device__ __forceinline__ void ds_w32(unsigned addr, u32 v) { asm volatile("ds_write_b32 %0, %1" :: "v"(addr), "v"(v) : "memory"); }
template <int OP> __device__ __forceinline__ void asm_op1(u32& d, u32 a) {
  if constexpr (OP == 0) asm volatile("v_mul_f32 %0, 0x3acccccd, %1" : "=v"(d) : "v"(a));
  else if constexpr (OP == 1) asm volatile("v_mov_b32 %0, %1" : "=v"(d) : "v"(a));
  else if constexpr (OP == 2) asm volatile("v_max_f32 %0, 0, %1" : "=v"(d) : "v"(a));
  else if constexpr (OP == 3) asm volatile("v_cvt_pk_bf16_f32 %0, 0, %1" : "=v"(d) : "v"(a));
  else if constexpr (OP == 4) asm volatile("v_add_f32 %0, 0x3727c5ac, %1" : "=v"(d) : "v"(a));
  else if constexpr (OP == 5) asm volatile("v_sqrt_f32 %0, %1" : "=v"(d) : "v"(a));
  else asm volatile("v_mov_b32 %0, 0" : "=v"(d));
}
template <int OP> __device__ __forceinline__ void asm_op2(u32& d, u32 a, u32 b) {
  if constexpr (OP == 0) asm volatile("v_add_f32 %0, %1, %2" : "=v"(d) : "v"(a), "v"(b));
  else if constexpr (OP == 1) asm volatile("v_fma_f32 %0, -%1, %1, %2" : "=v"(d) : "v"(a), "v"(b));
  else asm volatile("v_cvt_pk_bf16_f32 %0, -%1, %2" : "=v"(d) : "v"(a), "v"(b));
}
template <int LOFF> __device__ __forceinline__ void xdma0(unsigned ldsb, unsigned voff, u32x4 srd, unsigned& soff, unsigned inc) {
  asm volatile("s_add_u32 m0, %1, %5\n\ts_add_u32 %0, %0, %4\n\tbuffer_load_dwordx4 %2, %3, %0 offen" BS " lds"
               : "+s"(soff) : "s"(ldsb), "v"(voff), "s"(srd), "s"(inc), "i"(LOFF) : "memory", "scc");
}
__device__ __forceinline__ void xdma1(unsigned voff, u32x4 srd, unsigned soff) {
  asm volatile("buffer_load_dwordx4 %0, %1, %2 offen offset:1024" BS " lds" :: "v"(voff), "s"(srd), "s"(soff) : "memory");
}
template <int OFF> __device__ __forceinline__ void ds_w128u(unsigned addr, u32x4 v) {
  asm volatile("ds_write_b128 %0, %1 offset:%2" :: "v"(addr), "v"(v), "i"(OFF) : "memory");
}
__device__ __forceinline__ float v_rcp(float a) { float d; asm volatile("v_rcp_f32 %0, %1" : "=v"(d) : "v"(a)); return d; }
template <int OFF> __device__ __forceinline__ void ds_hi16(float& dst, unsigned addr) {
  asm volatile("ds_read_u16_d16_hi %0, %1 offset:%2" : "+v"(dst) : "v"(addr), "i"(OFF) : "memory");
}
__device__ __forceinline__ unsigned v_addc(unsigned a, unsigned c) { unsigned d; asm volatile("v_add_u32 %0, %2, %1" : "=v"(d) : "v"(a), "s"(c)); return d; }
__device__ __forceinline__ void to_acc(u32x4& d, u32x4 v) {
  asm volatile("" : "+v"(v));
  asm volatile("" : "=a"(d) : "0"(v));
}
__device__ __forceinline__ void bld32(u32& dst, unsigned voff, u32x4 srd) {
  asm volatile("buffer_load_dword %0, %1, %2, 0 offen sc1" : "=v"(dst) : "v"(voff), "s"(srd) : "memory");
}
template <int K> __device__ __forceinline__ void asm_dpp(u32& x) {
  if constexpr (K == 0) asm volatile("v_add_f32_dpp %0, %0, %0 quad_perm:[1,0,3,2] row_mask:0xf bank_mask:0xf" : "+v"(x));
  else if constexpr (K == 1) asm volatile("v_add_f32_dpp %0, %0, %0 quad_perm:[2,3,0,1] row_mask:0xf bank_mask:0xf" : "+v"(x));
  else asm volatile("v_add_f32_dpp %0, %0, %0 row_half_mirror row_mask:0xf bank_mask:0xf" : "+v"(x));
}
template <int L> __device__ __forceinline__ void asm_rdl(u32& d, u32 x) { asm volatile("v_readlane_b32 %0, %1, %2" : "=s"(d) : "v"(x), "i"(L)); }
template <int OP> __device__ __forceinline__ void asm_sop(float& d, u32 s, float a) {
  if constexpr (OP == 0) asm volatile("v_mov_b32 %0, %1" : "=v"(d) : "s"(s));
  else if constexpr (OP == 1) asm volatile("v_add_f32 %0, 1.0, %1" : "=v"(d) : "s"(s));
  else asm volatile("v_mul_f32 %0, %1, %2" : "=v"(d) : "s"(s), "v"(a));
}
constexpr int W1_STEPS = 21;
constexpr int O_EPOCH = 0;
constexpr int O_FK = 4096;
constexpr int O_FA0 = O_FK + 160 * 256;
constexpr int O_FW = O_FA0 + 8 * 12 * 128;
constexpr int O_FU = O_FW + 8 * 12 * 128;
constexpr int O_FX = O_FU + 8 * 14 * 128;
constexpr int O_F2 = O_FX + 8 * 32 * 128;
constexpr int O_F3 = O_F2 + 8 * 32 * 128;
constexpr int O_SG = O_F3 + 256 * 512;
constexpr int O_MODP = O_SG + 32 * 8192;
constexpr int O_ST1 = O_MODP + MODP * NMOD * 4;
constexpr int O_KVG = O_ST1 + 16384;
constexpr int O_XG = O_KVG + S_ * D_ * 2;
constexpr int O_X2T = O_XG + S_ * D_ * 2;
constexpr int O_W1F = O_X2T + TP_ * D_ * 2;
constexpr int O_W2F = O_W1F + 64 * W1_STEPS * 3 * 1024;
constexpr int O_H2F = O_W2F + 8 * 48 * 5 * 1024;
constexpr int WS_BYTES = O_H2F + S_ * F_ * 2;
static_assert(O_MODP % 256 == 0 && O_ST1 % 256 == 0 && O_KVG % 256 == 0 && O_X2T % 256 == 0 && O_W1F % 256 == 0, "alignment");

__device__ __forceinline__ void st128(u32x4 v, rsrc_t r, int vo, int so) {
  __builtin_amdgcn_raw_buffer_store_b128(v, r, vo, so, SC1);
  asm volatile("s_nop 1" :: "v"(v) : "memory");
}
constexpr int SPIN_CAP = 1 << 16;
constexpr unsigned CODE_BYTES = 39 * 1024;
__device__ __forceinline__ void st_flag(char* ws, int off, u64 tag) {
  __hip_atomic_store((GAS u64*)(ws + off), tag, __ATOMIC_RELAXED, __HIP_MEMORY_SCOPE_AGENT);
}
__device__ __forceinline__ bool tag_ok(u32x4 v, u64 tag) {
  const u32 lo = (u32)tag, hi = (u32)(tag >> 32);
  return ((v[0] ^ lo) | (v[1] ^ hi) | (v[2] ^ lo) | (v[3] ^ hi)) == 0;
}
template <int NL, int SLEEP> __device__ __forceinline__ void poll(rsrc_t rw, const int (&off)[NL], u64 tag) {
#pragma unroll 1
  for (int sp = 0; sp < SPIN_CAP; ++sp) {
    bool ok = true;
#pragma unroll
    for (int i = 0; i < NL; ++i) ok &= tag_ok(__builtin_amdgcn_raw_buffer_load_b128(rw, off[i], 0, SC1), tag);
    if (__all(ok)) break;
    if (SLEEP) __builtin_amdgcn_s_sleep(SLEEP);
    asm volatile("" ::: "memory");
  }
  asm volatile("" ::: "memory");
}
__device__ __forceinline__ u64 make_tag(u32x2 e) {
  return (((u64)(u32)__builtin_amdgcn_readfirstlane(e[1]) << 32) | (u32)__builtin_amdgcn_readfirstlane(e[0])) + 1;
}
__device__ __forceinline__ void drain_bar() { asm volatile("s_waitcnt vmcnt(0) lgkmcnt(0)\n\ts_barrier" ::: "memory"); }

constexpr int FC_BYTES = 2064 * 2;
constexpr int CH_BYTES = 4 * FC_BYTES;
constexpr int CKV_OFF = 66048;
constexpr int CX_OFF = CKV_OFF + 2048 + 16384, CS_OFF = CX_OFF + 16384;
constexpr int conv_gap(int a, int b) { int n = 0; for (int i = 0; i < a; ++i) n += 8 - i; return n + b; }
struct CSched { int rd[290]; int op[290]; };
constexpr int CBE = PE - 2;
constexpr int CW1 = 12, CW2 = CBE >= 2 ? 12 : (CBE == 1 ? 19 : 35);
constexpr CSched conv_make() {
  CSched S{};
  int rd = 0, op = 0, landed = 0;
  for (int g = 0; g < 288; ++g) {
    const int e = 7 - g / 36, n = g % 36;
    S.rd[g] = rd; S.op[g] = op;
    const bool open = e < CBE || (e == CBE && n >= 24);
    bool ds = e > 0 && n < 24;
    if (open && !ds && rd < 24 && (e == 0 || n < 34)) { ++rd; ds = true; if (n >= 30 && n < 32 && rd < 24) ++rd; }
    if (e > 0 ? n == 35 : (n == CW1 || n == CW2)) landed = rd;
    int k = ds ? 1 : 2;
    while (k-- > 0 && op < (landed / 6) * 32) ++op;
  }
  S.rd[288] = rd; S.op[288] = op;
  return S;
}
constexpr CSched CS = conv_make();
static_assert(CS.rd[288 - 36 + CW2] == 24, "reads must be out before the last wait");
#define XSP 2
#define XCH 20
constexpr int G_XCHK = 36 * (7 - PE) + XCH;
struct CVSched { int kind[290]; int idx[290]; };
constexpr CVSched cv_make() {
  CVSched S{};
  for (int i = 0; i < 3; ++i) { S.kind[G_XCHK - 44 + 4 * i] = 2; S.idx[G_XCHK - 44 + 4 * i] = i; }
  S.kind[G_XCHK + 4] = 5;
  for (int i = 0; i < 8; ++i) { S.kind[G_XCHK + 4 + XSP * (i + 1)] = 3 + i / 4; S.idx[G_XCHK + 4 + XSP * (i + 1)] = i & 3; }
  return S;
}
constexpr CVSched CV = cv_make();
constexpr int cv_younger(int g, int kind, int idx) {
  int n = -1;
  for (int i = 0; i <= g; ++i) { if (n >= 0 && CV.kind[i]) ++n; if (CV.kind[i] == kind && CV.idx[i] == idx) n = 0; }
  return n;
}
static_assert(G_XCHK > 44, "conv request schedule");
constexpr int G1_R = 6;
constexpr int G1_RS = 7;
constexpr int G1_LA = 4;
constexpr int G1_SP = 15 * 8192;
constexpr int G1_WPRE = 5;
constexpr int g1_ord(int g) { return (g >> 1) + 4 * (g & 1); }
constexpr int g1_ks(int t) { return t < 96 ? t >> 3 : 12 + (t & 7); }
constexpr int g1_j(int t) { return t < 96 ? t & 7 : g1_ord((t - 96) >> 3); }
constexpr int g1_slot(int ks) { return ks < 12 ? ks % G1_RS : G1_RS + ks - 12; }
struct GSched { int kind[320]; int idx[320]; int first[162]; int mid; int first2[242]; int n; };
#define T_SPOLL 66
#define WA 24
#define PLATE 16
#define PSL 1
#define CR1 12
#define CR2 14
#define WDN1 60
#define T_DV 112
#define T_DEND 157
#define T_P0 147
#define DPRE 0
#define G2PRE 8
#define G_P1 2
#define G_C1 24
#define G_P2 30
#define G_C2 68
#define G_F3 12
#define G_P3 84
#define G_C3 124
#define FINW 26
#define HRATE 3
#define T_F2 159
#define T_SETUP 135
#define T_F0 128
#define T_F1 144
#define T_C0 156
constexpr int XPRE = 5, T_SST = 23, T_WPOLL = 46, T_WCHK = 69;
#define G2R 22
constexpr int G2_R = G2R, G2_ST = 58;
constexpr GSched g_make() {
  GSched S{};
  int n = 0, x = 0, w = 3 * G1_WPRE, wa = WA, wd = 0, rs = 0, h = 0, credit = 0;
  auto put = [&](int k, int i) { S.kind[n] = k; S.idx[n] = i; ++n; };
  for (; x < 2 * XPRE; ++x) put(0, x);
  put(13, 0);
  for (int t = 0; t < 160; ++t) {
    S.first[t] = n;
    const int ks = t >> 3;
    credit += t < 96 ? CR1 : CR2;
    if (credit > 28) credit = 28;
    const bool xok = x < 40 && ((x >> 1) >= 12 || (x >> 1) <= ks + G1_RS - ((t & 7) >= 4 ? 1 : 2)), wok = w < 36 && w / 3 <= ks + G1_R - 1;
    const int n0 = n;
    if (t == T_WPOLL) put(4, 0);
    else if (t == T_SST) put(5, 0);
    else if (t == T_SPOLL || t == T_SPOLL + 1) put(6, t - T_SPOLL);
    else if (t >= 109 && (t & 7) == 5) put(7, (t - 109) >> 3);
    else if (t == T_F0 || t == T_F1 || t == T_F2) put(9, t == T_F0 ? 0 : t == T_F1 ? 1 : 2);
    else if (t == T_P0) put(10, 0);
    else if (t >= T_C0) put(11, h++);
    else if (credit >= 16) {
      if (xok && (!wok || (x >> 1) < w / 3)) put(0, x++);
      else if (wok) put(1, w++);
      else if (wa < 24) put(8, wa++);
      else if (rs < 4 && !(x & 1)) put(3, rs++);
      else if (t > T_WCHK && wd < (t < T_DV ? 37 : WDN1) && t < T_DEND) put(2, wd++);
    }
    if (n > n0) credit -= 16;
  }
  S.first[160] = n;
  put(7, 7);
  for (int i = 0; i < DPRE && wd < 60; ++i) put(2, wd++);
  S.mid = n;
  auto next = [&](int g) {
    const int hmax = g <= G_C1 ? 12 : (g <= G_C2 ? 24 : (g <= G_C3 ? 36 : 48));
    if (wd < 60 && (wd / 5 < h || h >= 12)) put(2, wd++);
    else if (h < hmax && h < g / 5 + G2_R - 1) put(11, h++);
  };
  for (int i = 0; i < G2PRE; ++i) next(0);
  for (int g = 0; g < 240; ++g) {
    S.first2[g] = n;
    if (g == G_F3) put(9, 3);
    else if (g == G_P1) put(10, 1);
    else if (g == G_P2) put(10, 2);
    else if (g == G_P3) put(10, 3);
    else if (g > 60 && g % 60 == G2_ST) put(12, g / 60 - 1);
    else if (wd < 60 ? (g & 1) : g % HRATE == 1) next(g);
  }
  S.first2[240] = n; S.n = n;
  return S;
}
constexpr GSched GS = g_make();
constexpr int g_pos(int kind, int idx) { for (int i = 0; i < GS.n; ++i) if (GS.kind[i] == kind && GS.idx[i] == idx) return i; return -1; }
constexpr int imax(int a, int b) { return a > b ? a : b; }
constexpr int g1_need(int t) {
  if (t == T_F0) return g_pos(7, 1);
  if (t == T_F1) return g_pos(7, 3);
  if (t == T_F2) return g_pos(7, 5);
  if (t == 92) return imax(g_pos(0, 31), g_pos(8, 23));
  if (t == 96) return g_pos(0, 39);
  if (t == 100) return 0;
  if (t > 92 || (t & 7) != 4) return -1;
  const int s = (t + 4) / 8;
  int p = imax(g_pos(0, 2 * s + 1), g_pos(1, 3 * s + 2));
  if (t == 12) p = imax(p, g_pos(0, 9));
  if (t == 84) p = imax(p, g_pos(6, 1));
  if (t == T_WCHK - 1) p = imax(p, g_pos(4, 0));
  return p;
}
constexpr int g_lastdv() { int p = -1; for (int i = 37; i < 60; ++i) if (g_pos(2, i) < GS.first[160]) p = g_pos(2, i); return p; }
constexpr int g2_need(int o) { return o < 12 ? imax(g_pos(11, o), g_pos(2, 5 * o + 4)) : g_pos(11, o); }
static_assert(g_pos(1, 35) >= 0 && g_pos(0, 39) >= 0 && g_pos(2, 59) >= 0 && g_pos(11, 47) >= 0 && g_pos(3, 3) >= 0, "schedule incomplete");
constexpr int XR_OFF = G1_SP + 2048;
constexpr int GB_OFF = XR_OFF + 10400;
constexpr int LDS_BYTES = 134 * 1024;
static_assert(GB_OFF + 320 <= LDS_BYTES, "LDS");
constexpr int AG_OFF = 0, AU_OFF = 20480, AD_OFF = 40960, AM_OFF = 61440;

__global__ __launch_bounds__(256, 1) void k_fused(const u16* __restrict__ xIn_, const u16* __restrict__ kIn_, const u16* __restrict__ vIn_,
                                                  const u16* __restrict__ shk_, const u16* __restrict__ ssmk_, const u16* __restrict__ ssmD_,
                                                  const u16* __restrict__ wg_, const u16* __restrict__ wu_, const u16* __restrict__ wd_,
                                                  const u16* __restrict__ cIn_, const u16* __restrict__ wmod_,
                                                  char* __restrict__ ws_, u16* __restrict__ out) {
  __shared__ __attribute__((aligned(16))) char smem[LDS_BYTES];
  STAMP_DECL();
  STAMP(0);
  u32 cpf = 0;
  if (blockIdx.x >= 240) {
    uint64_t pc; asm volatile("s_getpc_b64 %0" : "=s"(pc));
    const unsigned co = (((blockIdx.x >> 3) & 1) * 256 + threadIdx.x) * 128;
    asm volatile("global_load_dword %0, %1, %2" : "=v"(cpf) : "v"(co < CODE_BYTES ? co : 0u), "s"(pc & ~(uint64_t)127) : "memory");
  }
  u64 tag;
  const int tid = threadIdx.x, lane = tid & 63;
  const int wv = __builtin_amdgcn_readfirstlane(tid >> 6);
  const int b = blockIdx.x;
  const unsigned sbase = (unsigned)(size_t)smem;
  const unsigned l16 = lane * 16;
  int bs = blockIdx.x;
  asm volatile("" : "+s"(bs));
  const int xcd = bs & 7, idx = bs >> 3;
  int mt = (xcd & 1) * 8 + (idx & 7), nc = (xcd >> 1) * 4 + (idx >> 3);
  asm volatile("" : "+s"(mt), "+s"(nc));
  const int M = mt * 2 + ((nc >> 2) & 1), N = (nc >> 3) * 4 + (nc & 3);
  int kvo = (b >> 5) * 256 + lane * 4, kso[9];
  kso[0] = ((b & 31) * 64 + wv * 16) * 1280;
#pragma unroll
  for (int q = 1; q < 8; ++q) kso[q] = kso[0] + q * 2560;
  kso[8] = kso[0] ? kso[0] - 3 * 1280 : 0;
  asm volatile("" : "+v"(kvo), "+s"(kso[0]), "+s"(kso[1]), "+s"(kso[2]), "+s"(kso[3]), "+s"(kso[4]), "+s"(kso[5]), "+s"(kso[6]), "+s"(kso[7]), "+s"(kso[8]));
  typedef __attribute__((ext_vector_type(8))) unsigned long long u64x8;
  typedef __attribute__((ext_vector_type(4))) unsigned long long u64x4;
  u64x8 ka; u64x4 kb;
  unsigned long long pt0, pt1;
  asm volatile("s_memrealtime %0\n\ts_waitcnt lgkmcnt(0)" : "=s"(pt0) :: "memory");
  asm volatile("s_load_dwordx16 %0, %2, 0x0\n\ts_load_dwordx8 %1, %2, 0x40\n\ts_waitcnt lgkmcnt(0)" : "=&s"(ka), "=&s"(kb) : "s"(__builtin_amdgcn_kernarg_segment_ptr()) : "memory");
  asm volatile("s_memrealtime %0\n\ts_waitcnt lgkmcnt(0)" : "=s"(pt1) :: "memory");
  const unsigned pdt = (unsigned)(pt1 - pt0);
  const u16 *xIn = (const u16*)ka[0], *kIn = (const u16*)ka[1], *vIn = (const u16*)ka[2], *shk = (const u16*)ka[3], *ssmk = (const u16*)ka[4], *ssmD = (const u16*)ka[5];
  const u16 *wg = (const u16*)ka[6], *wu = (const u16*)ka[7], *wd = (const u16*)kb[0], *cIn = (const u16*)kb[1], *wmod = (const u16*)kb[2];
  char* ws = (char*)kb[3];
  const rsrc_t rw = mk_rsrc(ws, WS_BYTES);
  const u32x4 wsrd = {(u32)(uint64_t)ws, (u32)((uint64_t)ws >> 32) & 0xffffu, (u32)WS_BYTES, 0x00020000u};
  u32 tch = 0;
  auto touch = [&]() {
    constexpr int PER = (O_ST1 + 16384 - O_FK + 256 * 128 - 1) / (256 * 128);
    static_assert(PER <= 64, "one request per workgroup");
    tch = __builtin_amdgcn_raw_buffer_load_b32(rw, (wv == 3 && lane < PER) ? O_FK + (b * PER + lane) * 128 : WS_BYTES, 0, 0);
  };
  u32x4 WD[60];
  const char* wdp = ws + O_W2F + (N * 48 + wv * 12) * 5120 + 4096;
  const unsigned l16b = l16 + 8192;
  auto issue_wd = [&](auto I) { constexpr int i = I.value; gld128a<((i & 7) - 4) * 1024>(WD[i], (i & 8) ? l16b : l16, wdp + (i >> 4) * 16384); };
  auto clear_slot = [&]() {
    const int so = O_SG + (mt * 2 + ((nc >> 1) & 1)) * 8192 + (((nc & 1) * 4 + wv) * 4 + (nc >> 2)) * 256;
    __builtin_amdgcn_raw_buffer_store_b32(0u, rw, lane * 4, so, SC1);
  };
  if (b >= CWG) {
    const int j = b - CWG;
    const int r8 = lane >> 3, cp = lane & 7;
    const int ks2 = j >> 1, hf = j & 1;
    const int ug = j % 24, kq = j / 24;
    float* LC = (float*)(smem + AM_OFF);
    u16* LS = (u16*)(smem + AM_OFF + 640);
    const int gt = wv * 96 + j;
    const bool gact = gt < 300, urgent = gt < 100;
    const int gq = gt / MODP, gk = gt - gq * MODP, gcol = urgent ? 30 + gq : (gq < 50 ? gq - 20 : gq);
    u32x4 wm[16]; u32 cv[2];
    u32x2 ep = __builtin_amdgcn_raw_buffer_load_b64(rw, O_EPOCH, 0, SC1);
    touch();
    SCHED();
    if (!urgent) __builtin_amdgcn_s_sleep(A0_DELAY);
    if (gact) {
      static_for<0, 2>([&](auto I) { gld16p<I.value * 128>(cv[I.value], lane * 2, (const char*)(cIn + gk * 128)); });
      static_for<0, 16>([&](auto I) { gld128p(wm[I.value], r8 * (NMOD * 2) + cp * 16, (const char*)(wmod + (size_t)(gk * 128 + I.value * 8) * NMOD + gcol * 64)); });
    }
    if (gact) {
      wait_vm<16>();
      float sl[2], sv[16];
#pragma unroll
      for (int i = 0; i < 2; ++i) { pin(cv[i]); const float x = bflo(cv[i]); sl[i] = rbf(x * __builtin_amdgcn_rcpf(1.f + __expf(-x))); }
#pragma unroll
      for (int it = 0; it < 16; ++it) { sv[it] = __shfl(sl[it >> 3], (it & 7) * 8 + r8); pin(sv[it]); }
      f32x2 acc[4];
      static_for<0, 4>([&](auto G) {
        constexpr int g = G.value;
        wait_vm<12 - 4 * g>();
#pragma unroll
        for (int it = 4 * g; it < 4 * g + 4; ++it) {
          pin(wm[it]);
          const f32x2 s2 = {sv[it], sv[it]};
#pragma unroll
          for (int q = 0; q < 4; ++q) {
            const f32x2 w2 = {bflo(wm[it][q]), bfhi(wm[it][q])};
            acc[q] = it ? __builtin_elementwise_fma(s2, w2, acc[q]) : s2 * w2;
          }
        }
#pragma unroll
        for (int q = 0; q < 4; ++q) pin(acc[q]);
      });
      pin(ep); pin(tch);
      const float a0 = tred16(tred32(acc[0][0], acc[2][0]), tred32(acc[1][0], acc[3][0]));
      const float a1 = tred16(tred32(acc[0][1], acc[2][1]), tred32(acc[1][1], acc[3][1]));
      const float e0 = dpp_add<DPP_ROR8>(a0), e1 = dpp_add<DPP_ROR8>(a1);
      __builtin_amdgcn_raw_buffer_store_b32(__float_as_uint((lane & 8) ? e1 : e0), rw, O_MODP + (gk * NMOD + gcol * 64 + cp * 8 + r8) * 4, 0, SC1);
      if (urgent) {
        wait_vm<0>();
        if (lane < 8) st_flag(ws, O_FU + (lane * 14 + (gt >> 3)) * 128 + (gt & 7) * 8, make_tag(ep));
        __builtin_amdgcn_s_sleep(AU_DELAY);
      }
    } else {
      pin(ep); pin(tch);
    }
    tag = make_tag(ep);
    STAMPE(1);
    {
      const unsigned vg = r8 * (F_ * 2) + ((cp ^ ((r8 >> 1) * 2)) * 16);
      const char* gs = (const char*)(wg + (size_t)(kq * 160 + wv * 40) * F_ + ug * 64);
      const char* us = (const char*)(wu + (size_t)(kq * 160 + wv * 40) * F_ + ug * 64);
      static_for<0, 5>([&](auto I) {
        dma16p<AG_OFF + I.value * 1024>(sbase + wv * 5120, vg, gs + I.value * (8 * F_ * 2));
        dma16p<AU_OFF + I.value * 1024>(sbase + wv * 5120, vg ^ 16, us + I.value * (8 * F_ * 2));
      });
    }
    wait_vm<10>();
    bar();
    if (tid < 8) st_flag(ws, O_FA0 + (tid * 12 + (j >> 3)) * 128 + (j & 7) * 8, tag);
    STAMPE(2);
    const int kg = lane >> 4, q4 = (lane & 15) >> 2, p4 = lane & 3;
    if (wv == 0) {
      const int po[1] = {O_FU + ((b & 7) * 14 + (lane < 50 ? lane >> 2 : 0)) * 128 + (lane & 3) * 16};
      poll<1, 1>(rw, po, tag);
      STAMPE(3);
      f32x4 mm[2];
      const int lo = lane < 40 ? lane * 16 : 0;
#pragma unroll
      for (int i = 0; i < 2; ++i) {
        mm[i] = f32x4{0.f, 0.f, 0.f, 0.f};
#pragma unroll
        for (int p = 0; p < MODP; ++p)
          mm[i] += __builtin_bit_cast(f32x4, __builtin_amdgcn_raw_buffer_load_b128(rw, lo, O_MODP + (p * NMOD + (3 + i) * D_ + kq * 160) * 4, SC1));
      }
      if (lane < 40) {
        *(u32x2*)(LS + lane * 4) = u32x2{pack_bf16(mm[0][0], mm[0][1]), pack_bf16(mm[0][2], mm[0][3])};
        *(f32x4*)(LC + lane * 4) = f32x4{rbf(1.f + rbf(mm[1][0])), rbf(1.f + rbf(mm[1][1])), rbf(1.f + rbf(mm[1][2])), rbf(1.f + rbf(mm[1][3]))};
      }
    }
    drain_bar();
    STAMPE(4);
    CPF_DONE();
    clear_slot();
    {
      const char* ds = (const char*)(wd + (size_t)(ks2 * 32 + wv * 8) * D_ + hf * 320);
      static_for<0, 5>([&](auto I) { dma16p<AD_OFF + I.value * 4096>(sbase + wv * 1024, r8 * (D_ * 2) + cp * 16, ds + I.value * 128); });
    }
    {
      const int rr = 4 * kg + q4;
      u32x2 lo[10], hi[10], sbl[5], sbh[5]; u32x4 cl[5], ch[5];
      unsigned ad[2];
#pragma unroll
      for (int t = 0; t < 2; ++t) ad[t] = sbase + (p4 >> 1) * AU_OFF + rr * 128 + (((wv * 2 + t) ^ (((rr >> 1) & 3) * 2 + (p4 >> 1))) * 16) + (p4 & 1) * 8;
      const unsigned ac = sbase + AM_OFF + kg * 16, as = sbase + AM_OFF + 640 + kg * 8;
      auto rd = [&](auto I) {
        constexpr int i = I.value;
        ds_b128<i * 128>(cl[i], ac); ds_b128<i * 128 + 64>(ch[i], ac);
        ds_tr<i * 4096>(lo[2 * i], ad[0]); ds_tr<i * 4096 + 2048>(hi[2 * i], ad[0]); ds_tr<i * 4096>(lo[2 * i + 1], ad[1]); ds_tr<i * 4096 + 2048>(hi[2 * i + 1], ad[1]);
        ds_b64<i * 64>(sbl[i], as); ds_b64<i * 64 + 32>(sbh[i], as);
      };
      rd(IC<0>{});
      const int src = ((lane & 48) + ((lane & 1) * 8 + ((lane & 15) >> 1))) * 4;
      const u32x4 ones = {0x3f803f80u, 0x3f803f80u, 0x3f803f80u, 0x3f803f80u};
      f32x4 aU[2] = {}, aW[2] = {};
      int so[2];
#pragma unroll
      for (int t = 0; t < 2; ++t) { const int T = ug * 8 + wv * 2 + t, sw3 = T / 3; so[t] = O_W1F + ((sw3 * W1_STEPS + kq * 5) * 3 + T - sw3 * 3) * 1024; }
      static_for<0, 5>([&](auto KS) {
        constexpr int ks = KS.value;
        wait_lgkm<0>();
        pin(cl[ks]); pin(ch[ks]); pin(sbl[ks]); pin(sbh[ks]);
        if constexpr (ks < 4) rd(IC<ks + 1>{});
        u32x4 pm[2];
#pragma unroll
        for (int t = 0; t < 2; ++t) {
          pin(lo[2 * ks + t]); pin(hi[2 * ks + t]);
          const u32x4 raw = cat(lo[2 * ks + t], hi[2 * ks + t]);
          u32x4 sc;
#pragma unroll
          for (int q = 0; q < 2; ++q) {
            sc[q] = pack_bf16(bflo(raw[q]) * __uint_as_float(cl[ks][2 * q]), bfhi(raw[q]) * __uint_as_float(cl[ks][2 * q + 1]));
            sc[2 + q] = pack_bf16(bflo(raw[2 + q]) * __uint_as_float(ch[ks][2 * q]), bfhi(raw[2 + q]) * __uint_as_float(ch[ks][2 * q + 1]));
          }
#pragma unroll
          for (int q = 0; q < 4; ++q) pm[t][q] = (u32)__builtin_amdgcn_ds_bpermute(src, sc[q]);
          aW[t] = mfma_b(raw, cat(sbl[ks], sbh[ks]), aW[t]);
          aU[t] = mfma_b(sc, ones, aU[t]);
        }
#pragma unroll
        for (int t = 0; t < 2; ++t) st128(pm[t], rw, l16, so[t] + ks * 3072);
      });
      const f32x4 su = (lane & 4) ? aU[1] : aU[0], sw = (lane & 4) ? aW[1] : aW[0];
      const float U = p4 == 0 ? su[0] : p4 == 1 ? su[1] : p4 == 2 ? su[2] : su[3];
      const float W = p4 == 0 ? sw[0] : p4 == 1 ? sw[1] : p4 == 2 ? sw[2] : sw[3];
      const float u1 = rbf(U), u2 = rbf(U - u1), u3 = U - u1 - u2;
      const float w1 = rbf(W), w2 = rbf(W - w1), w3 = W - w1 - w2;
      const int crow = 2 * (4 * (kg & 1) + p4) + (kg >> 1);
      __builtin_amdgcn_raw_buffer_store_b128(u32x4{pack_bf16(u1, w1), pack_bf16(u2, w2), pack_bf16(u3, w3), 0u}, rw,
                                             (lane & 8) ? WS_BYTES : ((lane & 4) ? so[1] : so[0]) + (20 - kq * 5) * 3072 + (kq * 16 + crow) * 16, 0, SC1);
    }
    STAMPE(5);
  } else {
    const int d = (b >> 1) * 8 + (b & 1) + 2 * wv;
    const int row = lane & 15, kg = lane >> 4;
    const int tt = b & 31, ct = b >> 5;
    const int ov = ((lane >> 2) * 2048 + (lane & 3)) * 16, os = (ct * 16 * 2048 + (tt * 8 + wv * 2) * 4) * 16;
    const int stg = b * 2 + (wv - 2);
    const bool xact = wv >= 2 && b < 128;
    u32 xx[5][8];
    u32 draw; u32x2 ep;
    unsigned aAddr, bA[8];
    const unsigned fl = sbase + wv * CH_BYTES;
    {
      const rsrc_t rk = mk_rsrc(kIn, S_ * D_ * 2), rv = mk_rsrc(vIn, S_ * D_ * 2);
      u32 kk[19], vv[16];
#pragma unroll
      for (int q = 0; q < 3; ++q) kk[q] = __builtin_amdgcn_raw_buffer_load_b32(rk, kvo + q * 1280, kso[8], NT);
#pragma unroll
      for (int q = 0; q < 8; ++q) kk[3 + q] = __builtin_amdgcn_raw_buffer_load_b32(rk, kvo + (q & 1) * 1280, kso[q >> 1], NT);
      const u32x4 taps = __builtin_amdgcn_raw_buffer_load_b128(mk_rsrc(shk, D_ * 8), kvo * 4, 0, 0);
#pragma unroll
      for (int q = 0; q < 8; ++q) vv[q] = __builtin_amdgcn_raw_buffer_load_b32(rv, kvo + (q & 1) * 1280, kso[q >> 1], NT);
#pragma unroll
      for (int q = 8; q < 16; ++q) kk[3 + q] = __builtin_amdgcn_raw_buffer_load_b32(rk, kvo + (q & 1) * 1280, kso[q >> 1], NT);
#pragma unroll
      for (int q = 8; q < 16; ++q) vv[q] = __builtin_amdgcn_raw_buffer_load_b32(rv, kvo + (q & 1) * 1280, kso[q >> 1], NT);
      SCHED();
      SCHED();
      if (F_POS == 0) {
        const char* fs = (const char*)(ssmk + (size_t)d * S_);
        dma16x4p<FC_BYTES + 28>(fl, l16, fs);
        dma16x4p<3 * FC_BYTES + 24>(fl, l16, fs);
      }
      aAddr = fl + (row & 3) * FC_BYTES + 2 * (64 * kg + (row & ~3));
      pin(aAddr);
#pragma unroll
      for (int e = 0; e < 8; ++e) { const int g = 32 + 2 * row + 1 - 8 * kg - e; bA[e] = sbase + CKV_OFF + 16 * ((4 * g + wv) ^ ((g >> 2) & 7)); pin(bA[e]); }
      ep = __builtin_amdgcn_raw_buffer_load_b64(rw, O_EPOCH, 0, SC1);
      touch();
      draw = __builtin_amdgcn_raw_buffer_load_b32(mk_rsrc(ssmD, D_ * 2), (d >> 1) * 4, 0, 0);
      *(u32x2*)(smem + CKV_OFF + tid * 8) = u32x2{0, 0};
      if (lane < 13) *(u32*)(smem + wv * CH_BYTES + (lane < 7 ? FC_BYTES + lane * 4 : 3 * FC_BYTES + (lane - 7) * 4)) = 0;
      SCHED();
      if (kso[0] == 0) { kk[0] = 0; kk[1] = 0; kk[2] = 0; }
      const f32x2 w0 = {bflo(taps[0]), bflo(taps[2])}, w1 = {bfhi(taps[0]), bfhi(taps[2])};
      const f32x2 w2 = {bflo(taps[1]), bflo(taps[3])}, w3 = {bfhi(taps[1]), bfhi(taps[3])};
#pragma unroll
      for (int r = 0; r < 2; ++r) {
        f32x2 kv[8];
#pragma unroll
        for (int q = 0; q < 8; ++q) {
          const int i = 8 * r + q;
          f32x2 a = f32x2{bflo(kk[i]), bfhi(kk[i])} * w3;
          a = __builtin_elementwise_fma(f32x2{bflo(kk[i + 1]), bfhi(kk[i + 1])}, w2, a);
          a = __builtin_elementwise_fma(f32x2{bflo(kk[i + 2]), bfhi(kk[i + 2])}, w1, a);
          a = __builtin_elementwise_fma(f32x2{bflo(kk[i + 3]), bfhi(kk[i + 3])}, w0, a);
          kv[q] = a * f32x2{bflo(vv[i]), bfhi(vv[i])};
        }
        u32x4 o0, o1;
#pragma unroll
        for (int t = 0; t < 4; ++t) {
          o0[t] = pack_bf16(kv[7 - 2 * t][0], kv[6 - 2 * t][0]);
          o1[t] = pack_bf16(kv[7 - 2 * t][1], kv[6 - 2 * t][1]);
        }
        st128(o0, rw, ov + r * 64, O_KVG + os);
        st128(o1, rw, ov + r * 64, O_KVG + os + 16384);
      }
    }
    STAMPE(1);
    pin(ep); pin(draw); pin(tch);
    tag = make_tag(ep);
    auto fload = [&]() {
      const char* fs = (const char*)(ssmk + (size_t)d * S_);
      dma16x4p<FC_BYTES + 28>(fl, l16, fs);
      dma16x4p<3 * FC_BYTES + 24>(fl, l16, fs);
    };
    auto xload = [&]() {
      const char* xp = (const char*)xIn + stg * 8 * 1280;
      static_for<0, 40>([&](auto I) { constexpr int t = I.value / 5, c = I.value % 5; gld32p<c * 256 + (t & 1) * 1280>(xx[c][t], lane * 4, xp + (t & 6) * 1280); });
    };
    if (F_POS == 1) fload();
    if (xact && X_POS == 1) {
      xload();
      wait_vm<40 + (F_POS == 1 ? 8 : 0)>();
    } else {
      wait_vm<(F_POS == 1 ? 8 : 0)>();
    }
    asm volatile("s_waitcnt lgkmcnt(0)\n\ts_barrier" ::: "memory");
    if (tid < 32) st_flag(ws, O_FK + ((b & ~31) + tid) * 256 + (b & 31) * 8, tag);
    if (xact && X_POS == 2) xload();
    if (F_POS == 2) fload();
    STAMPE(2);
    int pa1[3] = {O_FX + ((b & 7) * 32 + (lane >> 2)) * 128 + (lane & 3) * 16, O_FX + ((b & 7) * 32 + 16 + (lane >> 2)) * 128 + (lane & 3) * 16,
                  O_FA0 + ((b & 7) * 12 + (lane < 48 ? lane >> 2 : 0)) * 128 + (lane & 3) * 16};
    const char* xgp = ws + O_XG + b * 16384 + wv * 4096;
    const char* stp = ws + O_ST1 + wv * 4096;
    asm volatile("" : "+s"(xgp), "+s"(stp));
    pin(pa1[0]); pin(pa1[1]); pin(pa1[2]);
    float Dv = (d & 1) ? bfhi(draw) : bflo(draw);
    unsigned ek = sbase + CKV_OFF + 16 * ((4 * (32 + 2 * row + (kg >> 1)) + wv) ^ ((row >> 1) & 7)) + ((kg & 1) ? 0 : 8);
    unsigned ex = (ek ^ 8) + (CX_OFF - CKV_OFF - 2048), es = sbase + CS_OFF + l16;
    unsigned mo = ((lane & 7) < MODP && lane < 24) ? O_MODP + ((lane & 7) * NMOD + (lane >> 3) * D_ + d) * 4 : WS_BYTES;
    pin(Dv); pin(ek); pin(ex); pin(es); pin(mo);
    unsigned ko[2] = {(unsigned)(lane ^ (lane >> 4)) * 16, ((unsigned)(lane ^ (lane >> 4)) * 16) ^ 64};
    pin(ko[0]); pin(ko[1]);
    auto fcopies = [&]() {
      u32x4 m[5]; u32 lo[5], hi[5];
      const unsigned src = fl + FC_BYTES + l16, src4 = fl + FC_BYTES + 4096 + (lane & 1) * 16;
      static_for<0, 4>([&](auto I) { constexpr int i = I.value; ds_b128<i * 1024>(m[i], src); ds_b32<i * 1024>(lo[i], src - 4); ds_b32<i * 1024 + 16>(hi[i], src); });
      ds_b128<0>(m[4], src4); ds_b32<0>(lo[4], src4 - 4); ds_b32<16>(hi[4], src4);
      wait_lgkm<0>();
#pragma unroll
      for (int i = 0; i < 5; ++i) {
        pin(m[i]); pin(lo[i]); pin(hi[i]);
        if (i == 0 && lane == 0) lo[0] = 0;
        const u32 s1 = __builtin_amdgcn_alignbit(m[i][1], m[i][0], 16), s2 = __builtin_amdgcn_alignbit(m[i][2], m[i][1], 16), s3 = __builtin_amdgcn_alignbit(m[i][3], m[i][2], 16);
        char* dst = smem + wv * CH_BYTES + (i < 4 ? i * 1024 + l16 : 4096 + (lane & 1) * 16);
        *(u32x4*)dst = u32x4{__builtin_amdgcn_alignbit(m[i][0], lo[i], 16), s1, s2, s3};
        *(u32x4*)(dst + 2 * FC_BYTES) = u32x4{s1, s2, s3, __builtin_amdgcn_alignbit(hi[i], m[i][3], 16)};
      }
    };
    if (!xact) { if (wv == 0 && F_POS != 2) wait_vm<1>(); else wait_vm<0>(); fcopies(); }
    if (xact) {
      wait_vm<(F_POS == 2 ? 8 : 0)>();
      STAMPD(8);
#pragma unroll
      for (int c = 0; c < 5; ++c)
#pragma unroll
        for (int t = 0; t < 8; ++t) pin(xx[c][t]);
#pragma unroll
      for (int c = 0; c < 5; ++c) {
        u32x4 p0, p1;
#pragma unroll
        for (int t = 0; t < 4; ++t) {
          p0[t] = __builtin_amdgcn_perm(xx[c][2 * t + 1], xx[c][2 * t], 0x05040100u);
          p1[t] = __builtin_amdgcn_perm(xx[c][2 * t + 1], xx[c][2 * t], 0x07060302u);
        }
        const int so = O_XG + (c * 16 * 2048 + stg * 4) * 16;
        st128(p0, rw, ov, so);
        st128(p1, rw, ov, so + 16384);
      }
      const bf16x2 ones = {(__bf16)1.0f, (__bf16)1.0f};
      float s1[8], s2[8];
#pragma unroll
      for (int t = 0; t < 8; ++t) {
        s1[t] = 0.f; s2[t] = 0.f;
#pragma unroll
        for (int c = 0; c < 5; ++c) {
          const bf16x2 v = __builtin_bit_cast(bf16x2, xx[c][t]);
          s1[t] = __builtin_amdgcn_fdot2_f32_bf16(v, ones, s1[t], false);
          s2[t] = __builtin_amdgcn_fdot2_f32_bf16(v, v, s2[t], false);
        }
      }
      float q1[2], q2[2];
#pragma unroll
      for (int e = 0; e < 2; ++e) {
        q1[e] = dpp_add<DPP_ROR8>(tred16(tred32(s1[e], s1[4 + e]), tred32(s1[2 + e], s1[6 + e])));
        q2[e] = dpp_add<DPP_ROR8>(tred16(tred32(s2[e], s2[4 + e]), tred32(s2[2 + e], s2[6 + e])));
      }
      const float mean = sum8((lane & 8) ? q1[1] : q1[0]) * (1.f / D_);
      const float var = fmaxf(sum8((lane & 8) ? q2[1] : q2[0]) * (1.f / D_) - mean * mean, 0.f);
      const float sd = rbf(__builtin_amdgcn_sqrtf(rbf(rbf(var) + 1e-5f)));
      const float rs = __builtin_amdgcn_rcpf(sd);
      const int s = stg * 8 + (lane >> 3);
      const int nt = s >> 8, col = (s >> 4) & 15, k4 = (s >> 2) & 3, rr = s & 3;
      if ((lane & 7) == 0) __builtin_amdgcn_raw_buffer_store_b64(u32x2{__float_as_uint(rs), __float_as_uint(-rbf(mean) * rs)}, rw,
                                                                 O_ST1 + (((nt * 2 + (rr >> 1)) * 64 + k4 * 16 + col) * 4 + (rr & 1) * 2) * 4, 0, SC1);
      STAMPD(9);
      if (F_POS == 2) wait_vm<11>();
      fcopies();
      wait_vm<0>();
      if (lane < 8) st_flag(ws, O_FX + (lane * 32 + (stg >> 3)) * 128 + (stg & 7) * 8, tag);
    } else if (wv < 2) {
      const int po[1] = {O_FK + b * 256 + wv * 128 + (lane & 7) * 16};
      poll<1, 0>(rw, po, tag);
      STAMPE(3);
      dma16x4<CKV_OFF + 2048>(sbase + wv * 8192, ko[0], ko[1], ws + O_KVG + b * 16384 + wv * 8192);
      dma16x4<CKV_OFF + 2048 + 4096>(sbase + wv * 8192, ko[0], ko[1], ws + O_KVG + b * 16384 + wv * 8192 + 4096);
    }
    f32x4 acc[8];
    u32x2 ra[2][8][2]; u32x4 rb[2][8];
    u32x4 pf[3]; u32 mraw; u64 nm[6];
    auto load_n = [&](auto E, auto N) {
      constexpr int e = E.value, n = N.value, bf = e & 1;
      if constexpr (n < 2) ds_b64<16 * e + 8 * n>(ra[bf][0][n], aAddr);
      else if constexpr (n < 10) ds_b128<2048 * (n - 2)>(rb[bf][n - 2], bA[e]);
      else ds_b64<512 * ((n - 8) >> 1) + 16 * e + 8 * (n & 1)>(ra[bf][(n - 8) >> 1][n & 1], aAddr);
    };
    u32x2 kvw[8], xw[8]; u32x4 sA[8], sB[8];
    float T[8][4], Hh[8][4], c0 = 0.f, c1 = 0.f;
    auto kvop = [&](auto K) {
      constexpr int k = K.value, nt = k >> 3, r = k & 3;
      if constexpr ((k & 4) == 0) T[nt][r] = (r & 1) ? v_lo(kvw[nt][1 - (r >> 1)]) : v_hi(kvw[nt][1 - (r >> 1)]);
      else T[nt][r] = v_mul(T[nt][r], Dv);
    };
    auto xrd = [&](auto K) {
      constexpr int k = K.value, nt = k / 3;
      if constexpr (k % 3 == 0) ds_b64<2048 * nt>(xw[nt], ex);
      else if constexpr (k % 3 == 1) ds_b128<2048 * nt>(sA[nt], es);
      else ds_b128<2048 * nt + 1024>(sB[nt], es);
    };
    float xf[8];
    auto xop = [&](auto K) {
      constexpr int k = K.value, nt = 2 * (k >> 5) + ((k >> 2) & 1), ph = (k >> 3) & 3, r = k & 3, i = k & 7;
      if constexpr (ph == 0) xf[i] = (r & 1) ? v_hi(xw[nt][r >> 1]) : v_lo(xw[nt][r >> 1]);
      else if constexpr (ph == 1) Hh[nt][r] = v_fma(xf[i], __uint_as_float((r < 2 ? sA[nt] : sB[nt])[2 * (r & 1)]), __uint_as_float((r < 2 ? sA[nt] : sB[nt])[2 * (r & 1) + 1]));
      else if constexpr (ph == 2) Hh[nt][r] = v_fma(Hh[nt][r], c1, c0);
      else T[nt][r] = v_fma(Hh[nt][r], T[nt][r], xf[i]);
    };
    u32 ms[3];
    auto mop = [&](auto K) {
      constexpr int k = K.value;
      if constexpr (k < 3) asm_dpp<k>(mraw);
      else if constexpr (k == 3) asm_op1<3>(mraw, mraw);
      else if constexpr (k < 7) asm_rdl<8 * (k - 4)>(ms[k - 4], mraw);
      else if constexpr (k == 7) asm_sop<0>(c0, ms[0], c0);
      else if constexpr (k == 8) asm_sop<1>(c1, ms[1], c1);
      else if constexpr (k == 9) asm_sop<2>(c0, ms[2], c0);
      else if constexpr (k == 10) { u32 t; asm_op1<3>(t, __float_as_uint(c1)); c1 = __uint_as_float(t); }
      else asm_sop<2>(c1, ms[2], c1);
    };
    clear_slot();
    wait_lgkm<0>();
    static_for<0, 2>([&](auto N) { load_n(IC<7>{}, N); });
    static_for<10, 24>([&](auto N) { load_n(IC<7>{}, N); });
    if (wv < 2) wait_vm<1>();
    STAMPD(10);
    bar();
    STAMPE(4);
    static_for<2, 10>([&](auto N) { load_n(IC<7>{}, N); });
    static_for<0, 8>([&](auto EE) {
      constexpr int e = 7 - EE.value, bf = e & 1;
      static_for<0, 8>([&](auto AA) {
        constexpr int a = AA.value;
        static_for<0, 8 - a>([&](auto BB) {
          constexpr int bb = BB.value, n = conv_gap(a, bb);
          if constexpr (e == 7 && n < 8) wait_lgkm<7>();
          mfma_<(e == 7 && a == 0)>(acc[a + bb], cat(ra[bf][a][0], ra[bf][a][1]), rb[bf][bb]);
          if constexpr (e > 0 && n < 24) load_n(IC<(e > 0 ? e - 1 : 0)>{}, IC<n>{});
          if constexpr (e == 7 && n >= 24 && n < 32) ds_b64<2048 * (n - 24)>(kvw[n - 24], ek);
          constexpr int g = 36 * (7 - e) + n;
          static_for<CS.rd[g], CS.rd[g + 1]>(xrd);
          if constexpr (e > 0 ? n == 35 : (n == CW1 || n == CW2)) wait_lgkm<0>();
          static_for<CS.op[g], CS.op[g + 1]>(xop);
          if constexpr ((e == 6 || e == 5) && n < 32) kvop(IC<(6 - e) * 32 + n>{});
          if constexpr (CV.kind[g] == 2) gld128<0>(pf[CV.idx[g]], pa1[CV.idx[g]], ws);
          if constexpr (CV.kind[g] == 3) { if constexpr (CV.idx[g] == 0) dma16<CX_OFF>(sbase + wv * 4096, ko[0], xgp); else dma16n<CV.idx[g] * 1024>(ko[CV.idx[g] & 1], xgp); }
          if constexpr (CV.kind[g] == 4) { if constexpr (CV.idx[g] == 0) dma16<CS_OFF>(sbase + wv * 4096, l16, stp); else dma16n<CV.idx[g] * 1024>(l16, stp); }
          if constexpr (CV.kind[g] == 5) bld32(mraw, mo, wsrd);
          if constexpr (g == G_XCHK) { STAMPD(11); wait_vm<cv_younger(g, 2, 2)>(); }
          if constexpr (g >= G_XCHK && g < G_XCHK + 3) { constexpr int i = g - G_XCHK; nm[2 * i] = v_ne64(u32x2{pf[i][0], pf[i][1]}, tag); nm[2 * i + 1] = v_ne64(u32x2{pf[i][2], pf[i][3]}, tag); }
          if constexpr (g == G_XCHK + 3) {
            if (__builtin_expect(((nm[0] | nm[1]) | (nm[2] | nm[3]) | (nm[4] | nm[5])) != 0, 0)) poll<3, 1>(rw, pa1, tag);
            STAMPD(12);
          }
          if constexpr (e == CBE && n == 8) wait_vm<cv_younger(g, 5, 0)>();
          if constexpr (e == CBE && n >= 9 && n < 21) mop(IC<n - 9>{});
          if constexpr (e == CBE && n == 23) { STAMPD(13); wait_vm<cv_younger(g, 4, 3)>(); bar(); STAMPD(14); }
        });
      });
    });
    STAMPE(5);
    asm volatile("s_nop 7");
    static_for<0, 4>([&](auto NP) {
      constexpr int np = NP.value;
      static_for<(CS.op[288] > 32 * np ? CS.op[288] : 32 * np), 32 * np + 32>(xop);
      u32x4 o;
#pragma unroll
      for (int u = 0; u < 2; ++u) {
        const int nt = 2 * np + u;
        pin(acc[nt]);
        o[2 * u] = pack_bf16(fmaf(Hh[nt][0], acc[nt][0], T[nt][0]), fmaf(Hh[nt][1], acc[nt][1], T[nt][1]));
        o[2 * u + 1] = pack_bf16(fmaf(Hh[nt][2], acc[nt][2], T[nt][2]), fmaf(Hh[nt][3], acc[nt][3], T[nt][3]));
      }
      st128(o, rw, l16 + np * 1024, O_X2T + d * (TP_ * 2));
    });
  }
  const int kg = lane >> 4, c16 = lane & 15;
  const int kq0 = nc >> 2, r0 = (nc & 1) * 4 + wv, sgb = O_SG + (mt * 2 + ((nc >> 1) & 1)) * 8192;
  u32x2 tag2 = {(u32)tag, (u32)(tag >> 32)};
  u32x4 gbr;
  pin(tag2);
  {
    const int fb = O_F2 + (b & 7) * 32 * 128;
    const int jw = (lane & 3) * 24 + ((nc * 12 + wv * 3 + ((lane >> 2) & 1) * 2) >> 3);
    const int pa[1] = {fb + (20 + (jw >> 3)) * 128 + (jw & 6) * 8};
    u32x4 pfa;
    gld128<0>(pfa, pa[0], ws);
    const char* wsrc = ws + O_W1F + (nc * 4 + wv) * W1_STEPS * 3072;
    const char* wq[4];
    unsigned xinc[4];
#pragma unroll
    for (int i = 0; i < 4; ++i) {
      wq[i] = wsrc + ((kq0 + i) & 3) * (5 * 3072) + 4096;
      xinc[i] = (i && kq0 + i == 4) ? (unsigned)(-19 * 32 * TP_ * 2) : 32 * TP_ * 2;
      asm volatile("" : "+s"(wq[i]), "+s"(xinc[i]));
    }
    u32x4 W[G1_R][3], WA_[24], WC[3], pfs[2], pfw, gran;
    auto issue_w = [&](auto I) { constexpr int i = I.value; gld128b<(((i % 15) & 7) - 4) * 1024>(W[(i / 3) % G1_R][i % 3], ((i % 15) & 8) ? l16b : l16, wq[i / 15]); };
    int pc[2] = {fb + (lane >> 2) * 128 + (lane & 3) * 16, fb + (16 + (lane >> 2)) * 128 + (lane & 3) * 16};
    int f2o = lane < 8 ? O_F2 + (lane * 32 + (b >> 3)) * 128 + (b & 7) * 8 : WS_BYTES;
    pin(pc[0]); pin(pc[1]); pin(f2o);
    STAMPE(6);
    asm volatile("s_waitcnt vmcnt(1) lgkmcnt(0)\n\ts_barrier" ::: "memory");
    if (wv == 0) __builtin_amdgcn_raw_buffer_store_b64(tag2, rw, f2o, 0, SC1);
    STAMPE(7);
    if (bs >= CWG) {
      const int j = bs - CWG;
      u32x2 dl[5], dh[5];
      const unsigned add = sbase + AD_OFF + (8 * kg + (c16 >> 2)) * 128 + (lane & 3) * 8;
#pragma unroll
      for (int i = 0; i < 5; ++i) { const int tl = wv * 5 + i; const unsigned a2 = add + (tl >> 2) * 4096 + (tl & 3) * 32; ds_tr<0>(dl[i], a2); ds_tr<512>(dh[i], a2); }
      wait_lgkm<0>();
#pragma unroll
      for (int i = 0; i < 5; ++i) {
        pin(dl[i]); pin(dh[i]);
        __builtin_amdgcn_raw_buffer_store_b128(cat(dl[i], dh[i]), rw, l16, O_W2F + ((((j & 1) * 4 + wv) * 48 + (j >> 1)) * 5 + i) * 1024, SC1);
      }
      wait_vm<5>();
    } else {
      if (wv == 0) wait_vm<1>(); else wait_vm<0>();
    }
    pin(pfa);
    if (!__all(tag_ok(pfa, tag))) poll<1, 2>(rw, pa, tag);
    auto issue_a = [&](auto I) { constexpr int i = 36 + I.value; gld128a<(((i % 15) & 7) - 4) * 1024>(WA_[I.value], ((i % 15) & 8) ? l16b : l16, wq[i / 15]); };
    static_for<0, 3 * G1_WPRE>(issue_w);
    unsigned xoff[2];
#pragma unroll
    for (int p = 0; p < 2; ++p) {
      const int k = 4 * p + kg;
      xoff[p] = (k * TP_ + ((c16 ^ (2 * k)) * 8)) * 2 - p * 1024;
      pin(xoff[p]);
    }
    unsigned xso = O_X2T + ((kq0 * 160 - 32 + 8 * wv) * TP_ + mt * 128) * 2;
    const unsigned ldsw = sbase + wv * 2048;
    unsigned sgl = sgb + wv * 2048 + l16;
    int hvo = kg < 3 ? ((3 * wv + kg) >> 2) * 1024 + ((3 * wv + kg) & 3) * 256 + c16 * 16 : WS_BYTES;
    const int hso = O_H2F + (mt * 8 * 48 + 3 * nc) * 1024;
    unsigned spa = sbase + G1_SP + c16 * 16;
    int sgo = (c16 >> 2) == kg ? sgb + (r0 * 4 + kq0) * 256 + c16 * 16 : WS_BYTES;
    pin(sgl); pin(hvo); pin(sgo); pin(spa);
    int pw[1] = {O_FW + ((b & 7) * 12 + (lane < 48 ? lane >> 2 : 0)) * 128 + (lane & 3) * 16};
    unsigned xo = ((lane >> 3) * TP_ + (lane & 7) * 8) * 2;
    const char* xs = ws + O_X2T + (N * 80 * TP_ + M * 64) * 2;
    unsigned gbo = O_MODP + (((lane & 7) < MODP ? (lane & 7) : 0) * NMOD + 5 * D_ + N * 80 + wv * 20 + (lane < 40 ? lane >> 3 : 0) * 4) * 4;
    pin(pw[0]); pin(xo); pin(gbo);
    u32 hq[4]; u32x4 hs, H[G2_R], WV[23], pf3, ob;
    int po3 = O_F3 + (mt * 16 + nc) * 512 + wv * 32 + (lane & 1) * 16;
    int f3o = tid < 16 ? O_F3 + (mt * 16 + tid) * 512 + nc * 8 : WS_BYTES;
    int oo;
    int fwo = (bs >= CWG && tid < 8) ? O_FW + (tid * 12 + ((bs - CWG) >> 3)) * 128 + (bs & 7) * 8 : WS_BYTES;
    pin(po3); pin(f3o); pin(fwo);
    const char* hp = ws + O_H2F + (M * 4 * 48 + wv * 12) * 1024 + 4096;
    u16* outp;
    asm volatile("s_load_dwordx2 %0, %1, 0x60\n\ts_waitcnt lgkmcnt(0)" : "=s"(outp) : "s"(__builtin_amdgcn_kernarg_segment_ptr()));
    const rsrc_t ro = mk_rsrc(outp, S_ * D_ * 2);
    auto issue = [&](auto O) {
      constexpr int kind = GS.kind[O.value], i = GS.idx[O.value];
      if constexpr (kind == 0) {
        if constexpr (i & 1) xdma1(xoff[1], wsrd, xso);
        else xdma0<g1_slot(i >> 1) * 8192>(ldsw, xoff[0], wsrd, xso, xinc[(i % 10) ? 0 : i / 10]);
      }
      else if constexpr (kind == 1) issue_w(IC<i>{});
      else if constexpr (kind == 2) {
        if constexpr (i >= 37 && O.value < GS.first[160]) gld128b<((i & 7) - 4) * 1024>(WV[i - 37], (i & 8) ? l16b : l16, wdp + (i >> 4) * 16384);
        else issue_wd(IC<i>{});
      }
      else if constexpr (kind == 3) {
        if constexpr (i < 2) dma16<XR_OFF + i * 4160>(sbase + wv * 1040, xo, xs + (size_t)(wv + 4 * i) * 8 * TP_ * 2);
        else if constexpr (i == 2) dma16<XR_OFF + 8320>(sbase + (wv & 1) * 1040, xo, xs + (size_t)((wv & 1) + 8) * 8 * TP_ * 2);
        else gld128<0>(gbr, gbo, ws);
      }
      else if constexpr (kind == 4) gld128<0>(pfw, pw[0], ws);
      else if constexpr (kind == 5) __builtin_amdgcn_raw_buffer_store_b128(gran, rw, sgo, 0, SC1);
      else if constexpr (kind == 6) gld128<i * 1024>(pfs[i], sgl, ws);
      else if constexpr (kind == 7) { hs = u32x4{hq[0], hq[1], hq[2], hq[3]}; __builtin_amdgcn_raw_buffer_store_b128(hs, rw, hvo, hso + g1_ord(i) * (48 * 1024), SC1); }
      else if constexpr (kind == 8) issue_a(IC<i>{});
      else if constexpr (kind == 9) __builtin_amdgcn_raw_buffer_store_b64(tag2, rw, f3o + i * 128, 0, SC1);
      else if constexpr (kind == 10) gld128<i * 128>(pf3, po3, ws);
      else if constexpr (kind == 11) gld128b<(((i % 12) & 7) - 4) * 1024>(H[i % G2_R], ((i % 12) & 8) ? l16b : l16, hp + (i / 12) * (48 * 1024));
      else if constexpr (kind == 12) __builtin_amdgcn_raw_buffer_store_b128(ob, ro, oo, i * (32 * D_ * 2), 0);
      else __builtin_amdgcn_raw_buffer_store_b64(tag2, rw, fwo, 0, SC1);
    };
    auto check3 = [&](auto ST) {
      if (__builtin_expect((v_ne64(u32x2{pf3[0], pf3[1]}, tag) | v_ne64(u32x2{pf3[2], pf3[3]}, tag)) != 0, 0)) {
        const int po[1] = {po3 + ST.value * 128};
        poll<1, 0>(rw, po, tag);
      }
    };
    unsigned xa[9], xm[8];
#pragma unroll
    for (int jj = 0; jj < 9; ++jj) {
      const int rr = 4 * kg + (c16 >> 2);
      xa[jj] = sbase + rr * 256 + (((jj < 8 ? jj : r0) ^ (rr & 7)) * 32) + (lane & 3) * 8;
      pin(xa[jj]);
    }
    u32x4 ones = {0x3f803f80u, 0x3f803f80u, 0x3f803f80u, 0x3f803f80u};
    pin(ones);
    static_for<0, 3>([&](auto I) { gld128a<I.value * 1024>(WC[I.value], l16, wsrc + 20 * 3072); });
    static_for<0, WA - PLATE>(issue_a);
    if (wv == 0) { gld128<0>(pfs[0], pc[0], ws); gld128<0>(pfs[1], pc[1], ws); }
    static_for<WA - PLATE, WA>(issue_a);
    STAMPL(8);
    if (wv == 0) {
      wait_vm<PLATE>();
      pin(pfs[0]); pin(pfs[1]);
      if (!__all(tag_ok(pfs[0], tag) && tag_ok(pfs[1], tag))) poll<2, PSL>(rw, pc, tag);
    } else {
      wait_vm<3 * G1_WPRE + 3 + WA>();
    }
    bar();
    STAMPL(9);
    static_for<0, GS.first[0]>(issue);
    f32x4 acc[8][3];
    u32x2 X[G1_LA + 1][2], XS[5][2];
    auto xread = [&](auto T, auto HH) {
      constexpr int t = T.value, h = HH.value;
      if constexpr (t < 96) ds_tr<g1_slot(g1_ks(t)) * 8192 + h * 4096>(X[t % (G1_LA + 1)][h], xa[g1_j(t)]);
      else ds_tr<(g1_ks(t) - 12) * 8192 + h * 4096>(X[t % (G1_LA + 1)][h], xm[g1_j(t)]);
    };
    f32x4 gram, sum1;
    u32 fs[4], fa; u64 sbad;
    unsigned spw = sbase + G1_SP + (2 * wv + (kg >> 1)) * 256 + c16 * 16;
    pin(spw);
    auto fop = [&](auto K) {
      constexpr int k = K.value;
      if constexpr (k == 0) asm_and3(fa, pfs[0][1], pfs[0][3], pfs[1][1]);
      else if constexpr (k == 1) asm_and3(fa, fa, pfs[1][3], pfs[1][3]);
      else if constexpr (k == 2) asm_ne1(sbad, fa);
      else if constexpr (k == 4) v_swap32(fs[0], fs[1]);
      else if constexpr (k == 5) v_swap32(fs[2], fs[3]);
      else if constexpr (k == 6) asm_op2<0>(fs[0], fs[0], fs[1]);
      else if constexpr (k == 7) asm_op2<0>(fs[2], fs[2], fs[3]);
      else if constexpr (k == 8) v_swap16(fs[0], fs[2]);
      else if constexpr (k == 9) asm_op2<0>(fs[0], fs[0], fs[2]);
      else if constexpr (k == 10) asm_op1<0>(fs[0], fs[0]);
      else if constexpr (k == 11) asm_op1<1>(fs[1], fs[0]);
      else if constexpr (k == 12) v_swap16(fs[0], fs[1]);
      else if constexpr (k == 13) asm_op2<1>(fs[1], fs[0], fs[1]);
      else if constexpr (k == 14) asm_op1<2>(fs[1], fs[1]);
      else if constexpr (k == 15) asm_op1<3>(fs[1], fs[1]);
      else if constexpr (k == 16) asm_op1<4>(fs[1], fs[1]);
      else if constexpr (k == 17) asm_op1<3>(fs[1], fs[1]);
      else if constexpr (k == 18) asm_op1<5>(fs[1], fs[1]);
      else if constexpr (k == 19) asm_op2<2>(fs[0], fs[0], fs[1]);
      else if constexpr (k == 20) asm_op1<1>(fs[1], fs[0]);
      else if constexpr (k == 21) asm_op1<1>(fs[2], fs[0]);
      else if constexpr (k == 22) asm_op1<6>(fs[3], fs[0]);
      else if constexpr (k == 23) ds_w128u<0>(spw, u32x4{fs[0], fs[1], fs[2], fs[3]});
    };
    float eg[6]; u32x4 xc;
    auto eop = [&](auto J, auto K) {
      constexpr int j = J.value, k = K.value;
      if constexpr (k < 15) {
        if constexpr (k < 6) eg[k] = v_relu(acc[j][k >> 1][2 * (k & 1)]);
        else if constexpr (k < 12) eg[k - 6] = v_mul(eg[k - 6], acc[j][(k - 6) >> 1][2 * (k & 1) + 1]);
        else hq[k == 12 ? 1 : k == 13 ? 0 : 2] = v_cvt(eg[k == 12 ? 2 : k == 13 ? 0 : 4], eg[k == 12 ? 3 : k == 13 ? 1 : 5]);
      } else if constexpr (k == 15) v_swap32(hq[1], hq[3]);
      else if constexpr (k == 16) v_swap32(hq[0], hq[2]);
      else if constexpr (k == 17) v_swap16(hq[0], hq[1]);
      else v_swap16(hq[2], hq[3]);
    };
    unsigned pwa, pra, gba, xad, sda;
    float xr[8], sdv, G[8], S[8], rs2;
    hq[3] = 0;
    pin(hq[3]);
    wait_vm<GS.first[0] - 2>();
    bar();
    STAMPL(10);
    static_for<0, G1_LA>([&](auto T) { xread(T, IC<0>{}); xread(T, IC<1>{}); });
    static_for<0, 160>([&](auto T) {
      constexpr int t = T.value, ks = g1_ks(t), jj = g1_j(t), tn = t + G1_LA, u = t & 7;
      if constexpr (t == 24) STAMPG(1);
      if constexpr (t == 48) STAMPG(2);
      if constexpr (t == 72) STAMPG(3);
      if constexpr (t == 98) STAMPG(4);
      if constexpr (t == 128) STAMPG(5);
      if constexpr (!(t & 1)) wait_lgkm<2 * ((tn <= 160 ? tn : 160) - t - 2)>();
      if constexpr (g1_need(t) >= 0) {
        static_assert(g1_need(t) < GS.first[t], "not issued yet");
        if constexpr (GS.first[t] - 1 - g1_need(t) < 63) wait_vm<GS.first[t] - 1 - g1_need(t)>();
        bar();
      }
      if constexpr (t == T_WCHK) {
        if (__builtin_expect((v_ne64(u32x2{pfw[0], pfw[1]}, tag) | v_ne64(u32x2{pfw[2], pfw[3]}, tag)) != 0, 0)) poll<1, 1>(rw, pw, tag);
      }
      if constexpr (t == T_C0) { wait_vm<GS.first[t] - 1 - g_pos(10, 0)>(); check3(IC<0>{}); }
      if constexpr (t == T_SETUP) {
        static_assert(g_pos(3, 3) < g1_need(T_F0) && T_SETUP > T_F0, "gate partials landed");
        {
          pin(gbr);
          const float g = sum8((lane & 7) < MODP ? __uint_as_float(gbr[0]) : 0.f), g2 = sum8((lane & 7) < MODP ? __uint_as_float(gbr[1]) : 0.f);
          const float g3 = sum8((lane & 7) < MODP ? __uint_as_float(gbr[2]) : 0.f), g4 = sum8((lane & 7) < MODP ? __uint_as_float(gbr[3]) : 0.f);
          if ((lane & 7) == 0 && lane < 40) *(f32x4*)(smem + GB_OFF + wv * 80 + (lane >> 3) * 16) = f32x4{rbf(g), rbf(g2), rbf(g3), rbf(g4)};
        }
        const int lf = lane < 40 ? lane : 0, ft = 4 * wv + lf / 10, fc = lf % 10;
        pwa = sbase + wv * 5440 + l16 + (kg >> 1) * 32;
        pra = sbase + fc * 544 + ft * 16; gba = sbase + GB_OFF + fc * 32;
        xad = sbase + XR_OFF + fc * 1040 + ft * 2; sda = sbase + G1_SP + ((nc & 4) * 16 + ft) * 16 + 2;
        {
          const int pos = M * 64 + ft, l8 = (pos >> 3) & 63;
          const int tok = 256 * (2 * (pos >> 9) + ((pos >> 2) & 1)) + 16 * (l8 & 15) + 4 * (l8 >> 4) + (pos & 3);
          oo = lane < 40 ? (tok * D_ + N * 80 + 8 * fc) * 2 : S_ * D_ * 2;
        }
        sdv = 0.f;
#pragma unroll
        for (int e = 0; e < 8; ++e) { xr[e] = 0.f; pin(xr[e]); }
        pin(pwa); pin(pra); pin(gba); pin(xad); pin(sda); pin(oo); pin(sdv);
      }
      if constexpr (t == T_SST - 2) {
        pin(gram); pin(sum1);
        const int c3 = lane & 3;
        gran = u32x4{__float_as_uint(sum1[0]), 1u, __float_as_uint(c3 == 0 ? gram[0] : c3 == 1 ? gram[1] : c3 == 2 ? gram[2] : gram[3]), 1u};
        pin(gran);
      }
      if constexpr (t == 86) {
        if (__builtin_expect(sbad != 0, 0)) {
#pragma unroll 1
          for (int spn = 0; spn < SPIN_CAP; ++spn) {
            pfs[0] = __builtin_amdgcn_raw_buffer_load_b128(rw, sgl, 0, SC1);
            pfs[1] = __builtin_amdgcn_raw_buffer_load_b128(rw, sgl + 1024, 0, SC1);
            if (__all((pfs[0][1] & pfs[0][3] & pfs[1][1] & pfs[1][3]) == 1u)) break;
            asm volatile("" ::: "memory");
          }
        }
        fs[0] = pfs[0][0]; fs[1] = pfs[1][0]; fs[2] = pfs[0][2]; fs[3] = pfs[1][2];
      }
      auto ep = [&](auto A, auto B) { if constexpr (t >= 104) static_for<A.value, (B.value < 19 ? B.value : 19)>([&](auto K) { eop(IC<g1_j(t - 8)>{}, K); }); };
      constexpr int k0 = u == 0 ? 0 : 4 * u - 3;
      constexpr int sr = t == 0 ? 0 : t == 4 ? 1 : (t >= 12 && t < 15) ? t - 10 : -1, sm = t == 4 ? 0 : t == 8 ? 1 : (t >= 16 && t < 19) ? t - 14 : -1;
      const u32x4 xb = cat(X[t % (G1_LA + 1)][0], X[t % (G1_LA + 1)][1]);
      if constexpr (ks >= 12) mfma_a<false>(acc[jj][0], WA_[3 * (ks - 12) + 0], xb); else mfma_<ks == 0>(acc[jj][0], W[ks % G1_R][0], xb);
      if constexpr (tn < 160) xread(IC<tn>{}, IC<0>{});
      if constexpr (t >= 84 && t < 96) fop(IC<2 * (t - 84)>{});
      if constexpr (u > 0) ep(IC<k0>{}, IC<k0 + 1>{});
      if constexpr (ks >= 12) mfma_a<false>(acc[jj][1], WA_[3 * (ks - 12) + 1], xb); else mfma_<ks == 0>(acc[jj][1], W[ks % G1_R][1], xb);
      if constexpr (tn < 160) xread(IC<tn>{}, IC<1>{});
      if constexpr (t >= 84 && t < 96) fop(IC<2 * (t - 84) + 1>{});
      if constexpr (u > 0) ep(IC<k0 + 1>{}, IC<k0 + 2>{}); else ep(IC<0>{}, IC<1>{});
      if constexpr (ks >= 12) mfma_a<false>(acc[jj][2], WA_[3 * (ks - 12) + 2], xb); else mfma_<ks == 0>(acc[jj][2], W[ks % G1_R][2], xb);
      if constexpr (t >= 96) real();
      static_for<GS.first[t], GS.first[t + 1]>(issue);
      if constexpr (u > 0) ep(IC<k0 + 2>{}, IC<k0 + 4>{});
      if constexpr (t >= 84 && t < 92) xm[t - 84] = v_addc(xa[t - 84], G1_RS * 8192);
      if constexpr (sm >= 0) { mfma_<sm == 0>(gram, cat(XS[sm][0], XS[sm][1]), cat(XS[sm][0], XS[sm][1])); mfma_<sm == 0>(sum1, ones, cat(XS[sm][0], XS[sm][1])); }
      if constexpr (sr >= 0) { ds_tr<sr * 8192>(XS[sr][0], xa[8]); ds_tr<sr * 8192 + 4096>(XS[sr][1], xa[8]); }
      if constexpr (t >= 96 && u == (t < 104 ? 4 : 0)) ds_b128<jj * 256>(xc, spa);
      if constexpr (t >= 96 && u == 7) {
        if constexpr (t < 104) wait_lgkm<6>();
        mfma_a<false>(acc[jj][0], WC[0], xc);
        mfma_a<false>(acc[jj][1], WC[1], xc);
        mfma_a<false>(acc[jj][2], WC[2], xc);
      }
    });
    STAMPL(11);
    asm volatile("s_nop 5");
    static_for<0, 15>([&](auto K) { eop(IC<7>{}, K); });
    eop(IC<7>{}, IC<15>{}); asm volatile("s_nop 0"); eop(IC<7>{}, IC<16>{}); asm volatile("s_nop 1"); eop(IC<7>{}, IC<17>{}); eop(IC<7>{}, IC<18>{});
    static_for<GS.first[160], GS.mid>(issue);
    STAMPL(12);
    if constexpr (T_C0 >= 160) { wait_vm<GS.mid - 1 - g_pos(10, 0)>(); check3(IC<0>{}); }
    static_for<GS.mid, GS.first2[0]>(issue);
    if constexpr (g_lastdv() >= 0) wait_vm<GS.first2[0] - 1 - g_lastdv()>();
    static_for<37, 60>([&](auto I) { if constexpr (g_pos(2, I.value) < GS.first[160]) to_acc(WD[I.value], WV[I.value - 37]); });
    f32x4 gbv[2], ac2[2][5], pt[8];
    auto pre = [&](auto J, auto K) {
      constexpr int j = J.value, k = K.value;
      if constexpr (k == 0) ds_hi16<j * 256>(sdv, sda);
      else if constexpr (k < 9) ds_hi16<(k - 1) * 128 + j * 32>(xr[k - 1], xad);
      else if constexpr (k == 9) rs2 = v_rcp(sdv);
      else if constexpr (k == 10) rs2 = v_mul(rs2, rs2);
      else G[k - 11] = v_mul(gbv[(k - 11) >> 2][(k - 11) & 3], rs2);
    };
    auto fin = [&](auto K) {
      constexpr int k = K.value, e = k & 7;
      if constexpr (k < 8) S[e] = v_add(pt[e >> 2][e & 3], pt[2 + (e >> 2)][e & 3]);
      else if constexpr (k < 24) S[e] = v_add(S[e], pt[2 * (k >> 3) + 2 + (e >> 2)][e & 3]);
      else if constexpr (k < 32) S[e] = v_fma(G[e], S[e], xr[e]);
      else ob[k - 32] = v_cvt(S[2 * (k - 32)], S[2 * (k - 32) + 1]);
    };
    auto prd = [&](auto J, auto K) { ds_f128<(J.value & 1) * 21760 + (K.value >> 1) * 5440 + (K.value & 1) * 256>(pt[K.value], pra); };
    STAMPL(13);
    static_for<0, 240>([&](auto GG) {
      constexpr int g = GG.value, o = g / 5, jj = o / 12, ks = o % 12, i = g % 5, q = g % 60;
      if constexpr (g == 60) STAMPG(6);
      if constexpr (g == 120) STAMPG(7);
      if constexpr (i == 0) { static_assert(g2_need(o) < GS.first2[g], "not issued yet"); wait_vm<GS.first2[g] - 1 - g2_need(o)>(); }
      if constexpr (g == G_F3) { wait_vm<GS.first2[g] - 1 - g_pos(7, 7)>(); bar(); }
      if constexpr (g == G_C1) { wait_vm<GS.first2[g] - 1 - g_pos(10, 1)>(); check3(IC<1>{}); }
      if constexpr (g == G_C2) { wait_vm<GS.first2[g] - 1 - g_pos(10, 2)>(); check3(IC<2>{}); }
      if constexpr (g == G_C3) { wait_vm<GS.first2[g] - 1 - g_pos(10, 3)>(); check3(IC<3>{}); }
      mfma_a<ks == 0>(ac2[jj & 1][i], WD[ks * 5 + i], H[o % G2_R]);
      if constexpr (i == 4) real();
      static_for<GS.first2[g], GS.first2[g + 1]>(issue);
      if constexpr (g == 30 || g == 32) ds_f128<(g - 30) * 8>(gbv[(g - 30) / 2], gba);
      if constexpr (jj >= 1) {
        if constexpr (q >= 5 && q < 15 && (q & 1)) ds_w128<((jj - 1) & 1) * 21760 + ((q - 5) / 2) * 1088>(pwa, ac2[(jj - 1) & 1][(q - 5) / 2]);
        if constexpr (q == 18) { wait_lgkm<0>(); bar(); }
        if constexpr (q >= 19 && q < 27) prd(IC<jj - 1>{}, IC<q - 19>{});
        if constexpr (q == 30) wait_lgkm<0>();
        if constexpr (q >= 31 && q < 31 + FINW) static_for<(36 * (q - 31) + FINW - 1) / FINW, (36 * (q - 30) + FINW - 1) / FINW>(fin);
      }
      if constexpr (jj >= 1 && q < 18 && !(q & 1)) pre(IC<jj - 1>{}, IC<q / 2>{});
      if constexpr (jj >= 1 && q >= 19 && q < 29) pre(IC<jj - 1>{}, IC<q - 10>{});
    });
    STAMPL(14);
    static_for<0, 9>([&](auto K) { pre(IC<3>{}, K); });
    static_for<0, 5>([&](auto I) { ds_w128<21760 + I.value * 1088>(pwa, ac2[1][I.value]); });
    wait_lgkm<5>();
    pre(IC<3>{}, IC<9>{}); asm volatile("s_nop 0");
    static_for<10, 19>([&](auto K) { pre(IC<3>{}, K); });
    wait_lgkm<0>(); bar();
    static_for<0, 8>([&](auto K) { prd(IC<3>{}, K); });
    wait_lgkm<4>();
    static_for<0, 8>(fin);
    wait_lgkm<0>();
    static_for<8, 36>(fin);
    __builtin_amdgcn_raw_buffer_store_b128(ob, ro, oo, 3 * (32 * D_ * 2), 0);
  }
  if (b == 0 && tid == 0) st_flag(ws, O_EPOCH, tag);
  { asm volatile("s_waitcnt vmcnt(0)" ::: "memory"); unsigned long long e_, n_; asm volatile("s_memrealtime %0\n\ts_waitcnt lgkmcnt(0)" : "=s"(e_) :: "memory"); e_ += MULT * pdt; int g_ = 0; do { asm volatile("s_memrealtime %0\n\ts_waitcnt lgkmcnt(0)" : "=s"(n_) :: "memory"); } while (n_ < e_ && ++g_ < 1000000); }
}

extern "C" void kernel_launch(void* const* d_in, const int* in_sizes, int n_in, void* d_out, int out_size,
                              void* d_ws, size_t ws_size, hipStream_t stream) {
  k_fused<<<256, 256, 0, stream>>>((const u16*)d_in[0], (const u16*)d_in[1], (const u16*)d_in[2], (const u16*)d_in[3],
                                   (const u16*)d_in[4], (const u16*)d_in[5], (const u16*)d_in[7], (const u16*)d_in[8],
                                   (const u16*)d_in[9], (const u16*)d_in[10], (const u16*)d_in[11], (char*)d_ws, (u16*)d_out);
}
```

```cpp
#include <hip/hip_runtime.h>
#include <stdint.h>

typedef unsigned short u16;
typedef unsigned int u32;
typedef __attribute__((ext_vector_type(2))) __bf16 bf16x2;
typedef __attribute__((ext_vector_type(4))) float f32x4;
typedef __attribute__((ext_vector_type(2))) float f32x2;
typedef __attribute__((ext_vector_type(4))) u32 u32x4;
typedef __attribute__((ext_vector_type(2))) u32 u32x2;

constexpr int S_ = 2048, D_ = 640, F_ = 1536;
constexpr int TP_ = S_ + 64;
constexpr int NMOD = 6 * D_;
constexpr int MODP = 5;
constexpr int CWG = 160;

__device__ __forceinline__ float bflo(u32 w) { return __uint_as_float(w << 16); }
__device__ __forceinline__ float bfhi(u32 w) { return __uint_as_float(w & 0xffff0000u); }
__device__ __forceinline__ u32 pack_bf16(float lo, float hi) {
  bf16x2 v; v[0] = (__bf16)lo; v[1] = (__bf16)hi; return __builtin_bit_cast(u32, v);
}
__device__ __forceinline__ float rbf(float x) { __bf16 b = (__bf16)x; return (float)b; }
#define SCHED() __builtin_amdgcn_sched_barrier(0)
template <int V> struct IC { static constexpr int value = V; };
template <int B, int E, class Fn> __device__ __forceinline__ void static_for(Fn&& f) {
  if constexpr (B < E) { f(IC<B>{}); static_for<B + 1, E>(f); }
}
__device__ __forceinline__ void mfma(f32x4& c, u32x4 a, u32x4 b) {
  asm volatile("v_mfma_f32_16x16x32_bf16 %0, %1, %2, %0" : "+v"(c) : "v"(a), "v"(b));
}
__device__ __forceinline__ void mfma0(f32x4& c, u32x4 a, u32x4 b) {
  asm volatile("v_mfma_f32_16x16x32_bf16 %0, %1, %2, 0" : "=&v"(c) : "v"(a), "v"(b));
}
template <bool FIRST> __device__ __forceinline__ void mfma_(f32x4& c, u32x4 a, u32x4 b) { if constexpr (FIRST) mfma0(c, a, b); else mfma(c, a, b); }
__device__ __forceinline__ u32 alignbit(u32 hi, u32 lo, u32 sh) {
  u32 d; asm volatile("v_alignbit_b32 %0, %1, %2, %3" : "=v"(d) : "v"(hi), "v"(lo), "v"(sh)); return d;
}
template <int OFF> __device__ __forceinline__ void ds_tr(u32x2& dst, unsigned addr) {
  asm volatile("ds_read_b64_tr_b16 %0, %1 offset:%2" : "=v"(dst) : "v"(addr), "i"(OFF) : "memory");
}
template <int OFF> __device__ __forceinline__ void ds_b32(u32& dst, unsigned addr) {
  asm volatile("ds_read_b32 %0, %1 offset:%2" : "=v"(dst) : "v"(addr), "i"(OFF) : "memory");
}
template <int OFF> __device__ __forceinline__ void ds_b64(u32x2& dst, unsigned addr) {
  asm volatile("ds_read_b64 %0, %1 offset:%2" : "=v"(dst) : "v"(addr), "i"(OFF) : "memory");
}
template <int OFF> __device__ __forceinline__ void ds_b128(u32x4& dst, unsigned addr) {
  asm volatile("ds_read_b128 %0, %1 offset:%2" : "=v"(dst) : "v"(addr), "i"(OFF) : "memory");
}
template <int OFF> __device__ __forceinline__ void ds_w128(unsigned addr, f32x4 v) {
  asm volatile("ds_write_b128 %0, %1 offset:%2" :: "v"(addr), "v"(v), "i"(OFF) : "memory");
}
template <int OFF> __device__ __forceinline__ void gld128(u32x4& dst, unsigned voff, const char* sb) {
  asm volatile("global_load_dwordx4 %0, %1, %2 offset:%3 sc1" : "=v"(dst) : "v"(voff), "s"(sb), "i"(OFF) : "memory");
}
__device__ __forceinline__ void gld32(u32& dst, unsigned voff, const char* sb) {
  asm volatile("global_load_dword %0, %1, %2 sc1" : "=v"(dst) : "v"(voff), "s"(sb) : "memory");
}
template <int LOFF> __device__ __forceinline__ void dma16(unsigned ldsb, unsigned voff, const char* sb) {
  asm volatile("s_add_u32 m0, %0, %3\n\ts_nop 0\n\tglobal_load_lds_dwordx4 %1, %2 sc1" :: "s"(ldsb), "v"(voff), "s"(sb), "i"(LOFF) : "memory", "scc");
}
template <int LOFF> __device__ __forceinline__ void dma16x4(unsigned ldsb, unsigned v0, unsigned v1, const char* sb) {
  asm volatile("s_add_u32 m0, %0, %4\n\ts_nop 0\n\tglobal_load_lds_dwordx4 %1, %3 sc1\n\tglobal_load_lds_dwordx4 %2, %3 offset:1024 sc1\n\t"
               "global_load_lds_dwordx4 %1, %3 offset:2048 sc1\n\tglobal_load_lds_dwordx4 %2, %3 offset:3072 sc1"
               :: "s"(ldsb), "v"(v0), "v"(v1), "s"(sb), "i"(LOFF) : "memory", "scc");
}
template <int N> __device__ __forceinline__ void wait_vm() { asm volatile("s_waitcnt vmcnt(%0)" :: "i"(N) : "memory"); }
template <int N> __device__ __forceinline__ void wait_lgkm() { asm volatile("s_waitcnt lgkmcnt(%0)" :: "i"(N) : "memory"); }
__device__ __forceinline__ void keep(u32x4 v) { asm volatile("" :: "v"(v)); }
__device__ __forceinline__ void real() { __builtin_amdgcn_s_setprio(0); }
__device__ __forceinline__ void bar() { asm volatile("s_barrier" ::: "memory"); }
template <class T> __device__ __forceinline__ void pin(T& a) { asm volatile("" : "+v"(a)); }
__device__ __forceinline__ u32x4 cat(u32x2 a, u32x2 b) { return u32x4{a[0], a[1], b[0], b[1]}; }

typedef __amdgpu_buffer_rsrc_t rsrc_t;
__device__ __forceinline__ rsrc_t mk_rsrc(const void* p, int bytes) { return __builtin_amdgcn_make_buffer_rsrc((void*)p, 0, bytes, 0x00020000); }
template <int CTRL> __device__ __forceinline__ float dpp_add(float x) {
  return x + __builtin_bit_cast(float, __builtin_amdgcn_mov_dpp(__builtin_bit_cast(int, x), CTRL, 0xf, 0xf, true));
}
constexpr int DPP_XOR1 = 0xB1, DPP_XOR2 = 0x4E, DPP_HMIRROR = 0x141, DPP_ROR8 = 0x128;
__device__ __forceinline__ float sum8(float x) { return dpp_add<DPP_HMIRROR>(dpp_add<DPP_XOR2>(dpp_add<DPP_XOR1>(x))); }
__device__ __forceinline__ float tred32(float a, float b) {
  auto r = __builtin_amdgcn_permlane32_swap(__float_as_uint(a), __float_as_uint(b), false, false);
  return __uint_as_float(r[0]) + __uint_as_float(r[1]);
}
__device__ __forceinline__ float tred16(float a, float b) {
  auto r = __builtin_amdgcn_permlane16_swap(__float_as_uint(a), __float_as_uint(b), false, false);
  return __uint_as_float(r[0]) + __uint_as_float(r[1]);
}
__device__ __forceinline__ float rdl(float x, int l) { return __int_as_float(__builtin_amdgcn_readlane(__float_as_int(x), l)); }

typedef unsigned long long u64;
#define GAS __attribute__((address_space(1)))
constexpr int SC1 = 16;
#define BS " sc1"
#define A0_DELAY 16
#define F_LATE 1
#define A1_DELAY 4
#define A3_DELAY 24
#define AU_DELAY 16
#define A_DELAY 2
#define FXE 7
#define PE 4
#define NT 0
#define NTS ""
template <int OFF> __device__ __forceinline__ void gld128b(u32x4& dst, unsigned voff, const char* sb) {
  asm volatile("global_load_dwordx4 %0, %1, %2 offset:%3" BS : "=v"(dst) : "v"(voff), "s"(sb), "i"(OFF) : "memory");
}
template <int OFF> __device__ __forceinline__ void gld128a(u32x4& dst, unsigned voff, const char* sb) {
  asm volatile("global_load_dwordx4 %0, %1, %2 offset:%3" BS : "=a"(dst) : "v"(voff), "s"(sb), "i"(OFF) : "memory");
}
template <bool FIRST> __device__ __forceinline__ void mfma_a(f32x4& c, u32x4 a, u32x4 b) {
  if constexpr (FIRST) asm volatile("v_mfma_f32_16x16x32_bf16 %0, %1, %2, 0" : "=&v"(c) : "a"(a), "v"(b));
  else asm volatile("v_mfma_f32_16x16x32_bf16 %0, %1, %2, %0" : "+v"(c) : "a"(a), "v"(b));
}
typedef __attribute__((ext_vector_type(8))) __bf16 bf16x8;
__device__ __forceinline__ f32x4 mfma_b(u32x4 a, u32x4 b, f32x4 c) {
  return __builtin_amdgcn_mfma_f32_16x16x32_bf16(__builtin_bit_cast(bf16x8, a), __builtin_bit_cast(bf16x8, b), c, 0, 0, 0);
}
template <int LOFF> __device__ __forceinline__ void dma16p(unsigned ldsb, unsigned voff, const char* sb) {
  asm volatile("s_add_u32 m0, %0, %3\n\ts_nop 0\n\tglobal_load_lds_dwordx4 %1, %2" NTS :: "s"(ldsb), "v"(voff), "s"(sb), "i"(LOFF) : "memory", "scc");
}
template <int LOFF> __device__ __forceinline__ void dma16x4p(unsigned ldsb, unsigned v0, const char* sb) {
  asm volatile("s_add_u32 m0, %0, %3\n\ts_nop 0\n\tglobal_load_lds_dwordx4 %1, %2" NTS "\n\tglobal_load_lds_dwordx4 %1, %2 offset:1024" NTS "\n\t"
               "global_load_lds_dwordx4 %1, %2 offset:2048" NTS "\n\tglobal_load_lds_dwordx4 %1, %2 offset:3072" NTS
               :: "s"(ldsb), "v"(v0), "s"(sb), "i"(LOFF) : "memory", "scc");
}

#undef DBGC
#define STAMP(I)
#define STAMP_DECL()
#define STAMP_END()

__device__ __forceinline__ void gld128p(u32x4& dst, unsigned voff, const char* sb) {
  asm volatile("global_load_dwordx4 %0, %1, %2" NTS : "=v"(dst) : "v"(voff), "s"(sb) : "memory");
}
template <int OFF> __device__ __forceinline__ void gld32p(u32& dst, unsigned voff, const char* sb) {
  asm volatile("global_load_dword %0, %1, %2 offset:%3" NTS : "=v"(dst) : "v"(voff), "s"(sb), "i"(OFF) : "memory");
}
template <int OFF> __device__ __forceinline__ void gld16p(u32& dst, unsigned voff, const char* sb) {
  asm volatile("global_load_ushort %0, %1, %2 offset:%3" : "=v"(dst) : "v"(voff), "s"(sb), "i"(OFF) : "memory");
}
#define CPF_DONE() asm volatile("" :: "v"(cpf))
#define X_POS 1
#define F_POS 1
template <int OFF> __device__ __forceinline__ void dma16n(unsigned voff, const char* sb) {
  asm volatile("global_load_lds_dwordx4 %0, %1 offset:%2 sc1" :: "v"(voff), "s"(sb), "i"(OFF) : "memory");
}
__device__ __forceinline__ u64 v_ne64(u32x2 v, u64 tag) { u64 m; asm volatile("v_cmp_ne_u64 %0, %1, %2" : "=s"(m) : "v"(v), "s"(tag)); return m; }
__device__ __forceinline__ float v_lo(u32 w) { float d; asm volatile("v_lshlrev_b32 %0, 16, %1" : "=v"(d) : "v"(w)); return d; }
__device__ __forceinline__ float v_hi(u32 w) { float d; asm volatile("v_and_b32 %0, 0xffff0000, %1" : "=v"(d) : "v"(w)); return d; }
__device__ __forceinline__ float v_fma(float a, float b, float c) { float d; asm volatile("v_fma_f32 %0, %1, %2, %3" : "=v"(d) : "v"(a), "v"(b), "v"(c)); return d; }
__device__ __forceinline__ float v_mul(float a, float b) { float d; asm volatile("v_mul_f32 %0, %1, %2" : "=v"(d) : "v"(a), "v"(b)); return d; }
__device__ __forceinline__ float v_add(float a, float b) { float d; asm volatile("v_add_f32 %0, %1, %2" : "=v"(d) : "v"(a), "v"(b)); return d; }
#define STAMPD(I)
#define STAMPL(I) STAMP(I)
__device__ __forceinline__ float v_relu(float a) { float d; asm volatile("v_max_i32 %0, 0, %1" : "=v"(d) : "v"(a)); return d; }
__device__ __forceinline__ u32 v_cvt(float a, float b) { u32 d; asm volatile("v_cvt_pk_bf16_f32 %0, %1, %2" : "=v"(d) : "v"(a), "v"(b)); return d; }
__device__ __forceinline__ u32 v_perm(u32 a, u32 sel) { u32 d; asm volatile("v_perm_b32 %0, %1, %1, %2" : "=v"(d) : "v"(a), "s"(sel)); return d; }
__device__ __forceinline__ void v_swap32(u32& a, u32& b) { asm volatile("v_permlane32_swap_b32 %0, %1" : "+v"(a), "+v"(b)); }
__device__ __forceinline__ void v_swap16(u32& a, u32& b) { asm volatile("v_permlane16_swap_b32 %0, %1" : "+v"(a), "+v"(b)); }
template <int OFF> __device__ __forceinline__ void ds_f128(f32x4& dst, unsigned addr) {
  asm volatile("ds_read_b128 %0, %1 offset:%2" : "=v"(dst) : "v"(addr), "i"(OFF) : "memory");
}
#define STAMPG(I)
#define STAMPE(I) STAMP(I)
__device__ __forceinline__ void asm_and3(u32& d, u32 a, u32 b, u32 c) { asm volatile("v_bitop3_b32 %0, %1, %2, %3 bitop3:0x80" : "=v"(d) : "v"(a), "v"(b), "v"(c)); }
__device__ __forceinline__ void asm_ne1(unsigned long long& m, u32 a) { asm volatile("v_cmp_ne_u32 %0, 1, %1" : "=s"(m) : "v"(a)); }
__device__ __forceinline__ void ds_w32(unsigned addr, u32 v) { asm volatile("ds_write_b32 %0, %1" :: "v"(addr), "v"(v) : "memory"); }
template <int OP> __device__ __forceinline__ void asm_op1(u32& d, u32 a) {
  if constexpr (OP == 0) asm volatile("v_mul_f32 %0, 0x3acccccd, %1" : "=v"(d) : "v"(a));
  else if constexpr (OP == 1) asm volatile("v_mov_b32 %0, %1" : "=v"(d) : "v"(a));
  else if constexpr (OP == 2) asm volatile("v_max_f32 %0, 0, %1" : "=v"(d) : "v"(a));
  else if constexpr (OP == 3) asm volatile("v_cvt_pk_bf16_f32 %0, 0, %1" : "=v"(d) : "v"(a));
  else if constexpr (OP == 4) asm volatile("v_add_f32 %0, 0x3727c5ac, %1" : "=v"(d) : "v"(a));
  else if constexpr (OP == 5) asm volatile("v_sqrt_f32 %0, %1" : "=v"(d) : "v"(a));
  else asm volatile("v_mov_b32 %0, 0" : "=v"(d));
}
template <int OP> __device__ __forceinline__ void asm_op2(u32& d, u32 a, u32 b) {
  if constexpr (OP == 0) asm volatile("v_add_f32 %0, %1, %2" : "=v"(d) : "v"(a), "v"(b));
  else if constexpr (OP == 1) asm volatile("v_fma_f32 %0, -%1, %1, %2" : "=v"(d) : "v"(a), "v"(b));
  else asm volatile("v_cvt_pk_bf16_f32 %0, -%1, %2" : "=v"(d) : "v"(a), "v"(b));
}
template <int LOFF> __device__ __forceinline__ void xdma0(unsigned ldsb, unsigned voff, u32x4 srd, unsigned& soff, unsigned inc) {
  asm volatile("s_add_u32 m0, %1, %5\n\ts_add_u32 %0, %0, %4\n\tbuffer_load_dwordx4 %2, %3, %0 offen" BS " lds"
               : "+s"(soff) : "s"(ldsb), "v"(voff), "s"(srd), "s"(inc), "i"(LOFF) : "memory", "scc");
}
__device__ __forceinline__ void xdma1(unsigned voff, u32x4 srd, unsigned soff) {
  asm volatile("buffer_load_dwordx4 %0, %1, %2 offen offset:1024" BS " lds" :: "v"(voff), "s"(srd), "s"(soff) : "memory");
}
template <int OFF> __device__ __forceinline__ void ds_w128u(unsigned addr, u32x4 v) {
  asm volatile("ds_write_b128 %0, %1 offset:%2" :: "v"(addr), "v"(v), "i"(OFF) : "memory");
}
__device__ __forceinline__ float v_rcp(float a) { float d; asm volatile("v_rcp_f32 %0, %1" : "=v"(d) : "v"(a)); return d; }
template <int OFF> __device__ __forceinline__ void ds_hi16(float& dst, unsigned addr) {
  asm volatile("ds_read_u16_d16_hi %0, %1 offset:%2" : "+v"(dst) : "v"(addr), "i"(OFF) : "memory");
}
__device__ __forceinline__ unsigned v_addc(unsigned a, unsigned c) { unsigned d; asm volatile("v_add_u32 %0, %2, %1" : "=v"(d) : "v"(a), "s"(c)); return d; }
__device__ __forceinline__ void to_acc(u32x4& d, u32x4 v) {
  asm volatile("" : "+v"(v));
  asm volatile("" : "=a"(d) : "0"(v));
}
__device__ __forceinline__ void bld32(u32& dst, unsigned voff, u32x4 srd) {
  asm volatile("buffer_load_dword %0, %1, %2, 0 offen sc1" : "=v"(dst) : "v"(voff), "s"(srd) : "memory");
}
template <int K> __device__ __forceinline__ void asm_dpp(u32& x) {
  if constexpr (K == 0) asm volatile("v_add_f32_dpp %0, %0, %0 quad_perm:[1,0,3,2] row_mask:0xf bank_mask:0xf" : "+v"(x));
  else if constexpr (K == 1) asm volatile("v_add_f32_dpp %0, %0, %0 quad_perm:[2,3,0,1] row_mask:0xf bank_mask:0xf" : "+v"(x));
  else asm volatile("v_add_f32_dpp %0, %0, %0 row_half_mirror row_mask:0xf bank_mask:0xf" : "+v"(x));
}
template <int L> __device__ __forceinline__ void asm_rdl(u32& d, u32 x) { asm volatile("v_readlane_b32 %0, %1, %2" : "=s"(d) : "v"(x), "i"(L)); }
template <int OP> __device__ __forceinline__ void asm_sop(float& d, u32 s, float a) {
  if constexpr (OP == 0) asm volatile("v_mov_b32 %0, %1" : "=v"(d) : "s"(s));
  else if constexpr (OP == 1) asm volatile("v_add_f32 %0, 1.0, %1" : "=v"(d) : "s"(s));
  else asm volatile("v_mul_f32 %0, %1, %2" : "=v"(d) : "s"(s), "v"(a));
}
constexpr int W1_STEPS = 21;
constexpr int O_EPOCH = 0;
constexpr int O_FK = 4096;
constexpr int O_FA0 = O_FK + 160 * 256;
constexpr int O_FW = O_FA0 + 8 * 12 * 128;
constexpr int O_FU = O_FW + 8 * 12 * 128;
constexpr int O_FX = O_FU + 8 * 14 * 128;
constexpr int O_F2 = O_FX + 8 * 32 * 128;
constexpr int O_F3 = O_F2 + 8 * 32 * 128;
constexpr int O_SG = O_F3 + 256 * 512;
constexpr int O_MODP = O_SG + 32 * 8192;
constexpr int O_ST1 = O_MODP + MODP * NMOD * 4;
constexpr int O_KVG = O_ST1 + 16384;
constexpr int O_XG = O_KVG + S_ * D_ * 2;
constexpr int O_X2T = O_XG + S_ * D_ * 2;
constexpr int O_W1F = O_X2T + TP_ * D_ * 2;
constexpr int O_W2F = O_W1F + 64 * W1_STEPS * 3 * 1024;
constexpr int O_H2F = O_W2F + 8 * 48 * 5 * 1024;
constexpr int WS_BYTES = O_H2F + S_ * F_ * 2;
static_assert(O_MODP % 256 == 0 && O_ST1 % 256 == 0 && O_KVG % 256 == 0 && O_X2T % 256 == 0 && O_W1F % 256 == 0, "alignment");

__device__ __forceinline__ void st128(u32x4 v, rsrc_t r, int vo, int so) {
  __builtin_amdgcn_raw_buffer_store_b128(v, r, vo, so, SC1);
  asm volatile("s_nop 1" :: "v"(v) : "memory");
}
constexpr int SPIN_CAP = 1 << 16;
constexpr unsigned CODE_BYTES = 39 * 1024;
__device__ __forceinline__ void st_flag(char* ws, int off, u64 tag) {
  __hip_atomic_store((GAS u64*)(ws + off), tag, __ATOMIC_RELAXED, __HIP_MEMORY_SCOPE_AGENT);
}
__device__ __forceinline__ bool tag_ok(u32x4 v, u64 tag) {
  const u32 lo = (u32)tag, hi = (u32)(tag >> 32);
  return ((v[0] ^ lo) | (v[1] ^ hi) | (v[2] ^ lo) | (v[3] ^ hi)) == 0;
}
template <int NL, int SLEEP> __device__ __forceinline__ void poll(rsrc_t rw, const int (&off)[NL], u64 tag) {
#pragma unroll 1
  for (int sp = 0; sp < SPIN_CAP; ++sp) {
    bool ok = true;
#pragma unroll
    for (int i = 0; i < NL; ++i) ok &= tag_ok(__builtin_amdgcn_raw_buffer_load_b128(rw, off[i], 0, SC1), tag);
    if (__all(ok)) break;
    if (SLEEP) __builtin_amdgcn_s_sleep(SLEEP);
    asm volatile("" ::: "memory");
  }
  asm volatile("" ::: "memory");
}
__device__ __forceinline__ u64 make_tag(u32x2 e) {
  return (((u64)(u32)__builtin_amdgcn_readfirstlane(e[1]) << 32) | (u32)__builtin_amdgcn_readfirstlane(e[0])) + 1;
}
__device__ __forceinline__ void drain_bar() { asm volatile("s_waitcnt vmcnt(0) lgkmcnt(0)\n\ts_barrier" ::: "memory"); }

constexpr int FC_BYTES = 2064 * 2;
constexpr int CH_BYTES = 4 * FC_BYTES;
constexpr int CKV_OFF = 66048;
constexpr int CX_OFF = CKV_OFF + 2048 + 16384, CS_OFF = CX_OFF + 16384;
constexpr int conv_gap(int a, int b) { int n = 0; for (int i = 0; i < a; ++i) n += 8 - i; return n + b; }
struct CSched { int rd[290]; int op[290]; };
constexpr int CBE = PE - 2;
constexpr int CW1 = 12, CW2 = CBE >= 2 ? 12 : (CBE == 1 ? 19 : 35);
constexpr CSched conv_make() {
  CSched S{};
  int rd = 0, op = 0, landed = 0;
  for (int g = 0; g < 288; ++g) {
    const int e = 7 - g / 36, n = g % 36;
    S.rd[g] = rd; S.op[g] = op;
    const bool open = e < CBE || (e == CBE && n >= 24);
    bool ds = e > 0 && n < 24;
    if (open && !ds && rd < 24 && (e == 0 || n < 34)) { ++rd; ds = true; if (n >= 30 && n < 32 && rd < 24) ++rd; }
    if (e > 0 ? n == 35 : (n == CW1 || n == CW2)) landed = rd;
    int k = ds ? 1 : 2;
    while (k-- > 0 && op < (landed / 6) * 32) ++op;
  }
  S.rd[288] = rd; S.op[288] = op;
  return S;
}
constexpr CSched CS = conv_make();
static_assert(CS.rd[288 - 36 + CW2] == 24, "reads must be out before the last wait");
#define XSP 2
#define XCH 20
constexpr int G_XCHK = 36 * (7 - PE) + XCH;
struct CVSched { int kind[290]; int idx[290]; };
constexpr CVSched cv_make() {
  CVSched S{};
  for (int i = 0; i < 3; ++i) { S.kind[G_XCHK - 44 + 4 * i] = 2; S.idx[G_XCHK - 44 + 4 * i] = i; }
  S.kind[G_XCHK + 4] = 5;
  for (int i = 0; i < 8; ++i) { S.kind[G_XCHK + 4 + XSP * (i + 1)] = 3 + i / 4; S.idx[G_XCHK + 4 + XSP * (i + 1)] = i & 3; }
  return S;
}
constexpr CVSched CV = cv_make();
constexpr int cv_younger(int g, int kind, int idx) {
  int n = -1;
  for (int i = 0; i <= g; ++i) { if (n >= 0 && CV.kind[i]) ++n; if (CV.kind[i] == kind && CV.idx[i] == idx) n = 0; }
  return n;
}
static_assert(G_XCHK > 44, "conv request schedule");
constexpr int G1_R = 6;
constexpr int G1_RS = 7;
constexpr int G1_LA = 4;
constexpr int G1_SP = 15 * 8192;
constexpr int G1_WPRE = 5;
constexpr int g1_ord(int g) { return (g >> 1) + 4 * (g & 1); }
constexpr int g1_ks(int t) { return t < 96 ? t >> 3 : 12 + (t & 7); }
constexpr int g1_j(int t) { return t < 96 ? t & 7 : g1_ord((t - 96) >> 3); }
constexpr int g1_slot(int ks) { return ks < 12 ? ks % G1_RS : G1_RS + ks - 12; }
struct GSched { int kind[320]; int idx[320]; int first[162]; int mid; int first2[242]; int n; };
#define T_SPOLL 66
#define WA 24
#define PLATE 16
#define PSL 1
#define CR1 12
#define CR2 14
#define WDN1 60
#define T_DV 112
#define T_DEND 157
#define T_P0 147
#define DPRE 0
#define G2PRE 8
#define G_P1 2
#define G_C1 24
#define G_P2 30
#define G_C2 68
#define G_F3 12
#define G_P3 84
#define G_C3 124
#define FINW 26
#define HRATE 3
#define T_F2 159
#define T_SETUP 135
#define T_F0 128
#define T_F1 144
#define T_C0 156
constexpr int XPRE = 5, T_SST = 23, T_WPOLL = 46, T_WCHK = 69;
#define G2R 22
constexpr int G2_R = G2R, G2_ST = 58;
constexpr GSched g_make() {
  GSched S{};
  int n = 0, x = 0, w = 3 * G1_WPRE, wa = WA, wd = 0, rs = 0, h = 0, credit = 0;
  auto put = [&](int k, int i) { S.kind[n] = k; S.idx[n] = i; ++n; };
  for (; x < 2 * XPRE; ++x) put(0, x);
  put(13, 0);
  for (int t = 0; t < 160; ++t) {
    S.first[t] = n;
    const int ks = t >> 3;
    credit += t < 96 ? CR1 : CR2;
    if (credit > 28) credit = 28;
    const bool xok = x < 40 && ((x >> 1) >= 12 || (x >> 1) <= ks + G1_RS - ((t & 7) >= 4 ? 1 : 2)), wok = w < 36 && w / 3 <= ks + G1_R - 1;
    const int n0 = n;
    if (t == T_WPOLL) put(4, 0);
    else if (t == T_SST) put(5, 0);
    else if (t == T_SPOLL || t == T_SPOLL + 1) put(6, t - T_SPOLL);
    else if (t >= 109 && (t & 7) == 5) put(7, (t - 109) >> 3);
    else if (t == T_F0 || t == T_F1 || t == T_F2) put(9, t == T_F0 ? 0 : t == T_F1 ? 1 : 2);
    else if (t == T_P0) put(10, 0);
    else if (t >= T_C0) put(11, h++);
    else if (credit >= 16) {
      if (xok && (!wok || (x >> 1) < w / 3)) put(0, x++);
      else if (wok) put(1, w++);
      else if (wa < 24) put(8, wa++);
      else if (rs < 4 && !(x & 1)) put(3, rs++);
      else if (t > T_WCHK && wd < (t < T_DV ? 37 : WDN1) && t < T_DEND) put(2, wd++);
    }
    if (n > n0) credit -= 16;
  }
  S.first[160] = n;
  put(7, 7);
  for (int i = 0; i < DPRE && wd < 60; ++i) put(2, wd++);
  S.mid = n;
  auto next = [&](int g) {
    const int hmax = g <= G_C1 ? 12 : (g <= G_C2 ? 24 : (g <= G_C3 ? 36 : 48));
    if (wd < 60 && (wd / 5 < h || h >= 12)) put(2, wd++);
    else if (h < hmax && h < g / 5 + G2_R - 1) put(11, h++);
  };
  for (int i = 0; i < G2PRE; ++i) next(0);
  for (int g = 0; g < 240; ++g) {
    S.first2[g] = n;
    if (g == G_F3) put(9, 3);
    else if (g == G_P1) put(10, 1);
    else if (g == G_P2) put(10, 2);
    else if (g == G_P3) put(10, 3);
    else if (g > 60 && g % 60 == G2_ST) put(12, g / 60 - 1);
    else if (wd < 60 ? (g & 1) : g % HRATE == 1) next(g);
  }
  S.first2[240] = n; S.n = n;
  return S;
}
constexpr GSched GS = g_make();
constexpr int g_pos(int kind, int idx) { for (int i = 0; i < GS.n; ++i) if (GS.kind[i] == kind && GS.idx[i] == idx) return i; return -1; }
constexpr int imax(int a, int b) { return a > b ? a : b; }
constexpr int g1_need(int t) {
  if (t == T_F0) return g_pos(7, 1);
  if (t == T_F1) return g_pos(7, 3);
  if (t == T_F2) return g_pos(7, 5);
  if (t == 92) return imax(g_pos(0, 31), g_pos(8, 23));
  if (t == 96) return g_pos(0, 39);
  if (t == 100) return 0;
  if (t > 92 || (t & 7) != 4) return -1;
  const int s = (t + 4) / 8;
  int p = imax(g_pos(0, 2 * s + 1), g_pos(1, 3 * s + 2));
  if (t == 12) p = imax(p, g_pos(0, 9));
  if (t == 84) p = imax(p, g_pos(6, 1));
  if (t == T_WCHK - 1) p = imax(p, g_pos(4, 0));
  return p;
}
constexpr int g_lastdv() { int p = -1; for (int i = 37; i < 60; ++i) if (g_pos(2, i) < GS.first[160]) p = g_pos(2, i); return p; }
constexpr int g2_need(int o) { return o < 12 ? imax(g_pos(11, o), g_pos(2, 5 * o + 4)) : g_pos(11, o); }
static_assert(g_pos(1, 35) >= 0 && g_pos(0, 39) >= 0 && g_pos(2, 59) >= 0 && g_pos(11, 47) >= 0 && g_pos(3, 3) >= 0, "schedule incomplete");
constexpr int XR_OFF = G1_SP + 2048;
constexpr int GB_OFF = XR_OFF + 10400;
constexpr int LDS_BYTES = 134 * 1024;
static_assert(GB_OFF + 320 <= LDS_BYTES, "LDS");
constexpr int AG_OFF = 0, AU_OFF = 20480, AD_OFF = 40960, AM_OFF = 61440;

__global__ __launch_bounds__(256, 1) void k_fused(const u16* __restrict__ kIn, const u16* __restrict__ vIn, const u16* __restrict__ shk,
                                                  char* __restrict__ ws, const u16* __restrict__ cIn, const u16* __restrict__ wmod,
                                                  const u16* __restrict__ ssmD, const u16* __restrict__ xIn, const u16* __restrict__ ssmk,
                                                  const u16* __restrict__ wg, const u16* __restrict__ wu, const u16* __restrict__ wd,
                                                  u16* __restrict__ out) {
  __shared__ __attribute__((aligned(16))) char smem[LDS_BYTES];
  STAMP_DECL();
  STAMP(0);
  u32 cpf = 0;
  if (blockIdx.x >= 240) {
    uint64_t pc; asm volatile("s_getpc_b64 %0" : "=s"(pc));
    const unsigned co = (((blockIdx.x >> 3) & 1) * 256 + threadIdx.x) * 128;
    asm volatile("global_load_dword %0, %1, %2" : "=v"(cpf) : "v"(co < CODE_BYTES ? co : 0u), "s"(pc & ~(uint64_t)127) : "memory");
  }
  u64 tag;
  const int tid = threadIdx.x, lane = tid & 63;
  const int wv = __builtin_amdgcn_readfirstlane(tid >> 6);
  const int b = blockIdx.x;
  const unsigned sbase = (unsigned)(size_t)smem;
  const unsigned l16 = lane * 16;
  int bs = blockIdx.x;
  asm volatile("" : "+s"(bs));
  const int xcd = bs & 7, idx = bs >> 3;
  int mt = (xcd & 1) * 8 + (idx & 7), nc = (xcd >> 1) * 4 + (idx >> 3);
  asm volatile("" : "+s"(mt), "+s"(nc));
  const int M = mt * 2 + ((nc >> 2) & 1), N = (nc >> 3) * 4 + (nc & 3);
  int kvo = (b >> 5) * 256 + lane * 4, kso[9];
  kso[0] = ((b & 31) * 64 + wv * 16) * 1280;
#pragma unroll
  for (int q = 1; q < 8; ++q) kso[q] = kso[0] + q * 2560;
  kso[8] = kso[0] ? kso[0] - 3 * 1280 : 0;
  asm volatile("" : "+v"(kvo), "+s"(kso[0]), "+s"(kso[1]), "+s"(kso[2]), "+s"(kso[3]), "+s"(kso[4]), "+s"(kso[5]), "+s"(kso[6]), "+s"(kso[7]), "+s"(kso[8]));
  asm volatile("" :: "s"(xIn), "s"(kIn), "s"(vIn), "s"(shk), "s"(ssmk), "s"(ssmD), "s"(wg), "s"(wu), "s"(wd), "s"(cIn), "s"(wmod), "s"(ws));
  const rsrc_t rw = mk_rsrc(ws, WS_BYTES);
  const u32x4 wsrd = {(u32)(uint64_t)ws, (u32)((uint64_t)ws >> 32) & 0xffffu, (u32)WS_BYTES, 0x00020000u};
  u32 tch = 0;
  auto touch = [&]() {
    constexpr int PER = (O_ST1 + 16384 - O_FK + 256 * 128 - 1) / (256 * 128);
    static_assert(PER <= 64, "one request per workgroup");
    tch = __builtin_amdgcn_raw_buffer_load_b32(rw, (wv == 3 && lane < PER) ? O_FK + (b * PER + lane) * 128 : WS_BYTES, 0, 0);
  };
  u32x4 WD[60];
  const char* wdp = ws + O_W2F + (N * 48 + wv * 12) * 5120 + 4096;
  const unsigned l16b = l16 + 8192;
  auto issue_wd = [&](auto I) { constexpr int i = I.value; gld128a<((i & 7) - 4) * 1024>(WD[i], (i & 8) ? l16b : l16, wdp + (i >> 4) * 16384); };
  auto clear_slot = [&]() {
    const int so = O_SG + (mt * 2 + ((nc >> 1) & 1)) * 8192 + (((nc & 1) * 4 + wv) * 4 + (nc >> 2)) * 256;
    __builtin_amdgcn_raw_buffer_store_b32(0u, rw, lane * 4, so, SC1);
  };
  if (b >= CWG) {
    const int j = b - CWG;
    const int r8 = lane >> 3, cp = lane & 7;
    const int ks2 = j >> 1, hf = j & 1;
    const int ug = j % 24, kq = j / 24;
    float* LC = (float*)(smem + AM_OFF);
    u16* LS = (u16*)(smem + AM_OFF + 640);
    const int gt = wv * 96 + j;
    const bool gact = gt < 300, urgent = gt < 100;
    const int gq = gt / MODP, gk = gt - gq * MODP, gcol = urgent ? 30 + gq : (gq < 50 ? gq - 20 : gq);
    u32x4 wm[16]; u32 cv[2];
    u32x2 ep = __builtin_amdgcn_raw_buffer_load_b64(rw, O_EPOCH, 0, SC1);
    touch();
    SCHED();
    if (!urgent) __builtin_amdgcn_s_sleep(A0_DELAY);
    if (gact) {
      static_for<0, 2>([&](auto I) { gld16p<I.value * 128>(cv[I.value], lane * 2, (const char*)(cIn + gk * 128)); });
      static_for<0, 16>([&](auto I) { gld128p(wm[I.value], r8 * (NMOD * 2) + cp * 16, (const char*)(wmod + (size_t)(gk * 128 + I.value * 8) * NMOD + gcol * 64)); });
    }
    if (gact) {
      wait_vm<16>();
      float sl[2], sv[16];
#pragma unroll
      for (int i = 0; i < 2; ++i) { pin(cv[i]); const float x = bflo(cv[i]); sl[i] = rbf(x * __builtin_amdgcn_rcpf(1.f + __expf(-x))); }
#pragma unroll
      for (int it = 0; it < 16; ++it) { sv[it] = __shfl(sl[it >> 3], (it & 7) * 8 + r8); pin(sv[it]); }
      f32x2 acc[4];
      static_for<0, 4>([&](auto G) {
        constexpr int g = G.value;
        wait_vm<12 - 4 * g>();
#pragma unroll
        for (int it = 4 * g; it < 4 * g + 4; ++it) {
          pin(wm[it]);
          const f32x2 s2 = {sv[it], sv[it]};
#pragma unroll
          for (int q = 0; q < 4; ++q) {
            const f32x2 w2 = {bflo(wm[it][q]), bfhi(wm[it][q])};
            acc[q] = it ? __builtin_elementwise_fma(s2, w2, acc[q]) : s2 * w2;
          }
        }
#pragma unroll
        for (int q = 0; q < 4; ++q) pin(acc[q]);
      });
      pin(ep); pin(tch);
      const float a0 = tred16(tred32(acc[0][0], acc[2][0]), tred32(acc[1][0], acc[3][0]));
      const float a1 = tred16(tred32(acc[0][1], acc[2][1]), tred32(acc[1][1], acc[3][1]));
      const float e0 = dpp_add<DPP_ROR8>(a0), e1 = dpp_add<DPP_ROR8>(a1);
      __builtin_amdgcn_raw_buffer_store_b32(__float_as_uint((lane & 8) ? e1 : e0), rw, O_MODP + (gk * NMOD + gcol * 64 + cp * 8 + r8) * 4, 0, SC1);
      if (urgent) {
        wait_vm<0>();
        if (lane < 8) st_flag(ws, O_FU + (lane * 14 + (gt >> 3)) * 128 + (gt & 7) * 8, make_tag(ep));
        __builtin_amdgcn_s_sleep(AU_DELAY);
      }
    } else {
      pin(ep); pin(tch);
    }
    tag = make_tag(ep);
    STAMPE(1);
    {
      const unsigned vg = r8 * (F_ * 2) + ((cp ^ ((r8 >> 1) * 2)) * 16);
      const char* gs = (const char*)(wg + (size_t)(kq * 160 + wv * 40) * F_ + ug * 64);
      const char* us = (const char*)(wu + (size_t)(kq * 160 + wv * 40) * F_ + ug * 64);
      static_for<0, 5>([&](auto I) {
        dma16p<AG_OFF + I.value * 1024>(sbase + wv * 5120, vg, gs + I.value * (8 * F_ * 2));
        dma16p<AU_OFF + I.value * 1024>(sbase + wv * 5120, vg ^ 16, us + I.value * (8 * F_ * 2));
      });
    }
    wait_vm<10>();
    bar();
    if (tid < 8) st_flag(ws, O_FA0 + (tid * 12 + (j >> 3)) * 128 + (j & 7) * 8, tag);
    STAMPE(2);
    const int kg = lane >> 4, q4 = (lane & 15) >> 2, p4 = lane & 3;
    if (wv == 0) {
      const int po[1] = {O_FU + ((b & 7) * 14 + (lane < 50 ? lane >> 2 : 0)) * 128 + (lane & 3) * 16};
      poll<1, 1>(rw, po, tag);
      STAMPE(3);
      f32x4 mm[2];
      const int lo = lane < 40 ? lane * 16 : 0;
#pragma unroll
      for (int i = 0; i < 2; ++i) {
        mm[i] = f32x4{0.f, 0.f, 0.f, 0.f};
#pragma unroll
        for (int p = 0; p < MODP; ++p)
          mm[i] += __builtin_bit_cast(f32x4, __builtin_amdgcn_raw_buffer_load_b128(rw, lo, O_MODP + (p * NMOD + (3 + i) * D_ + kq * 160) * 4, SC1));
      }
      if (lane < 40) {
        *(u32x2*)(LS + lane * 4) = u32x2{pack_bf16(mm[0][0], mm[0][1]), pack_bf16(mm[0][2], mm[0][3])};
        *(f32x4*)(LC + lane * 4) = f32x4{rbf(1.f + rbf(mm[1][0])), rbf(1.f + rbf(mm[1][1])), rbf(1.f + rbf(mm[1][2])), rbf(1.f + rbf(mm[1][3]))};
      }
    }
    drain_bar();
    STAMPE(4);
    CPF_DONE();
    clear_slot();
    {
      const char* ds = (const char*)(wd + (size_t)(ks2 * 32 + wv * 8) * D_ + hf * 320);
      static_for<0, 5>([&](auto I) { dma16p<AD_OFF + I.value * 4096>(sbase + wv * 1024, r8 * (D_ * 2) + cp * 16, ds + I.value * 128); });
    }
    {
      const int rr = 4 * kg + q4;
      u32x2 lo[10], hi[10], sbl[5], sbh[5]; u32x4 cl[5], ch[5];
      unsigned ad[2];
#pragma unroll
      for (int t = 0; t < 2; ++t) ad[t] = sbase + (p4 >> 1) * AU_OFF + rr * 128 + (((wv * 2 + t) ^ (((rr >> 1) & 3) * 2 + (p4 >> 1))) * 16) + (p4 & 1) * 8;
      const unsigned ac = sbase + AM_OFF + kg * 16, as = sbase + AM_OFF + 640 + kg * 8;
      auto rd = [&](auto I) {
        constexpr int i = I.value;
        ds_b128<i * 128>(cl[i], ac); ds_b128<i * 128 + 64>(ch[i], ac);
        ds_tr<i * 4096>(lo[2 * i], ad[0]); ds_tr<i * 4096 + 2048>(hi[2 * i], ad[0]); ds_tr<i * 4096>(lo[2 * i + 1], ad[1]); ds_tr<i * 4096 + 2048>(hi[2 * i + 1], ad[1]);
        ds_b64<i * 64>(sbl[i], as); ds_b64<i * 64 + 32>(sbh[i], as);
      };
      rd(IC<0>{});
      const int src = ((lane & 48) + ((lane & 1) * 8 + ((lane & 15) >> 1))) * 4;
      const u32x4 ones = {0x3f803f80u, 0x3f803f80u, 0x3f803f80u, 0x3f803f80u};
      f32x4 aU[2] = {}, aW[2] = {};
      int so[2];
#pragma unroll
      for (int t = 0; t < 2; ++t) { const int T = ug * 8 + wv * 2 + t, sw3 = T / 3; so[t] = O_W1F + ((sw3 * W1_STEPS + kq * 5) * 3 + T - sw3 * 3) * 1024; }
      static_for<0, 5>([&](auto KS) {
        constexpr int ks = KS.value;
        wait_lgkm<0>();
        pin(cl[ks]); pin(ch[ks]); pin(sbl[ks]); pin(sbh[ks]);
        if constexpr (ks < 4) rd(IC<ks + 1>{});
        u32x4 pm[2];
#pragma unroll
        for (int t = 0; t < 2; ++t) {
          pin(lo[2 * ks + t]); pin(hi[2 * ks + t]);
          const u32x4 raw = cat(lo[2 * ks + t], hi[2 * ks + t]);
          u32x4 sc;
#pragma unroll
          for (int q = 0; q < 2; ++q) {
            sc[q] = pack_bf16(bflo(raw[q]) * __uint_as_float(cl[ks][2 * q]), bfhi(raw[q]) * __uint_as_float(cl[ks][2 * q + 1]));
            sc[2 + q] = pack_bf16(bflo(raw[2 + q]) * __uint_as_float(ch[ks][2 * q]), bfhi(raw[2 + q]) * __uint_as_float(ch[ks][2 * q + 1]));
          }
#pragma unroll
          for (int q = 0; q < 4; ++q) pm[t][q] = (u32)__builtin_amdgcn_ds_bpermute(src, sc[q]);
          aW[t] = mfma_b(raw, cat(sbl[ks], sbh[ks]), aW[t]);
          aU[t] = mfma_b(sc, ones, aU[t]);
        }
#pragma unroll
        for (int t = 0; t < 2; ++t) st128(pm[t], rw, l16, so[t] + ks * 3072);
      });
      const f32x4 su = (lane & 4) ? aU[1] : aU[0], sw = (lane & 4) ? aW[1] : aW[0];
      const float U = p4 == 0 ? su[0] : p4 == 1 ? su[1] : p4 == 2 ? su[2] : su[3];
      const float W = p4 == 0 ? sw[0] : p4 == 1 ? sw[1] : p4 == 2 ? sw[2] : sw[3];
      const float u1 = rbf(U), u2 = rbf(U - u1), u3 = U - u1 - u2;
      const float w1 = rbf(W), w2 = rbf(W - w1), w3 = W - w1 - w2;
      const int crow = 2 * (4 * (kg & 1) + p4) + (kg >> 1);
      __builtin_amdgcn_raw_buffer_store_b128(u32x4{pack_bf16(u1, w1), pack_bf16(u2, w2), pack_bf16(u3, w3), 0u}, rw,
                                             (lane & 8) ? WS_BYTES : ((lane & 4) ? so[1] : so[0]) + (20 - kq * 5) * 3072 + (kq * 16 + crow) * 16, 0, SC1);
    }
    STAMPE(5);
  } else {
    const int d = (b >> 1) * 8 + (b & 1) + 2 * wv;
    const int row = lane & 15, kg = lane >> 4;
    const int tt = b & 31, ct = b >> 5;
    const int ov = ((lane >> 2) * 2048 + (lane & 3)) * 16, os = (ct * 16 * 2048 + (tt * 8 + wv * 2) * 4) * 16;
    const int stg = b * 2 + (wv - 2);
    const bool xact = wv >= 2 && b < 128;
    u32 xx[5][8];
    u32 draw; u32x2 ep;
    unsigned aAddr, bA[8];
    const unsigned fl = sbase + wv * CH_BYTES;
    {
      const rsrc_t rk = mk_rsrc(kIn, S_ * D_ * 2), rv = mk_rsrc(vIn, S_ * D_ * 2);
      u32 kk[19], vv[16];
#pragma unroll
      for (int q = 0; q < 3; ++q) kk[q] = __builtin_amdgcn_raw_buffer_load_b32(rk, kvo + q * 1280, kso[8], NT);
#pragma unroll
      for (int q = 0; q < 8; ++q) kk[3 + q] = __builtin_amdgcn_raw_buffer_load_b32(rk, kvo + (q & 1) * 1280, kso[q >> 1], NT);
      const u32x4 taps = __builtin_amdgcn_raw_buffer_load_b128(mk_rsrc(shk, D_ * 8), kvo * 4, 0, 0);
#pragma unroll
      for (int q = 0; q < 8; ++q) vv[q] = __builtin_amdgcn_raw_buffer_load_b32(rv, kvo + (q & 1) * 1280, kso[q >> 1], NT);
#pragma unroll
      for (int q = 8; q < 16; ++q) kk[3 + q] = __builtin_amdgcn_raw_buffer_load_b32(rk, kvo + (q & 1) * 1280, kso[q >> 1], NT);
#pragma unroll
      for (int q = 8; q < 16; ++q) vv[q] = __builtin_amdgcn_raw_buffer_load_b32(rv, kvo + (q & 1) * 1280, kso[q >> 1], NT);
      SCHED();
      SCHED();
      if (F_POS == 0) {
        const char* fs = (const char*)(ssmk + (size_t)d * S_);
        dma16x4p<FC_BYTES + 28>(fl, l16, fs);
        dma16x4p<3 * FC_BYTES + 24>(fl, l16, fs);
      }
      aAddr = fl + (row & 3) * FC_BYTES + 2 * (64 * kg + (row & ~3));
      pin(aAddr);
#pragma unroll
      for (int e = 0; e < 8; ++e) { const int g = 32 + 2 * row + 1 - 8 * kg - e; bA[e] = sbase + CKV_OFF + 16 * ((4 * g + wv) ^ ((g >> 2) & 7)); pin(bA[e]); }
      ep = __builtin_amdgcn_raw_buffer_load_b64(rw, O_EPOCH, 0, SC1);
      touch();
      draw = __builtin_amdgcn_raw_buffer_load_b32(mk_rsrc(ssmD, D_ * 2), (d >> 1) * 4, 0, 0);
      *(u32x2*)(smem + CKV_OFF + tid * 8) = u32x2{0, 0};
      if (lane < 13) *(u32*)(smem + wv * CH_BYTES + (lane < 7 ? FC_BYTES + lane * 4 : 3 * FC_BYTES + (lane - 7) * 4)) = 0;
      SCHED();
      if (kso[0] == 0) { kk[0] = 0; kk[1] = 0; kk[2] = 0; }
      const f32x2 w0 = {bflo(taps[0]), bflo(taps[2])}, w1 = {bfhi(taps[0]), bfhi(taps[2])};
      const f32x2 w2 = {bflo(taps[1]), bflo(taps[3])}, w3 = {bfhi(taps[1]), bfhi(taps[3])};
#pragma unroll
      for (int r = 0; r < 2; ++r) {
        f32x2 kv[8];
#pragma unroll
        for (int q = 0; q < 8; ++q) {
          const int i = 8 * r + q;
          f32x2 a = f32x2{bflo(kk[i]), bfhi(kk[i])} * w3;
          a = __builtin_elementwise_fma(f32x2{bflo(kk[i + 1]), bfhi(kk[i + 1])}, w2, a);
          a = __builtin_elementwise_fma(f32x2{bflo(kk[i + 2]), bfhi(kk[i + 2])}, w1, a);
          a = __builtin_elementwise_fma(f32x2{bflo(kk[i + 3]), bfhi(kk[i + 3])}, w0, a);
          kv[q] = a * f32x2{bflo(vv[i]), bfhi(vv[i])};
        }
        u32x4 o0, o1;
#pragma unroll
        for (int t = 0; t < 4; ++t) {
          o0[t] = pack_bf16(kv[7 - 2 * t][0], kv[6 - 2 * t][0]);
          o1[t] = pack_bf16(kv[7 - 2 * t][1], kv[6 - 2 * t][1]);
        }
        st128(o0, rw, ov + r * 64, O_KVG + os);
        st128(o1, rw, ov + r * 64, O_KVG + os + 16384);
      }
    }
    STAMPE(1);
    pin(ep); pin(draw); pin(tch);
    tag = make_tag(ep);
    auto fload = [&]() {
      const char* fs = (const char*)(ssmk + (size_t)d * S_);
      dma16x4p<FC_BYTES + 28>(fl, l16, fs);
      dma16x4p<3 * FC_BYTES + 24>(fl, l16, fs);
    };
    auto xload = [&]() {
      const char* xp = (const char*)xIn + stg * 8 * 1280;
      static_for<0, 40>([&](auto I) { constexpr int t = I.value / 5, c = I.value % 5; gld32p<c * 256 + (t & 1) * 1280>(xx[c][t], lane * 4, xp + (t & 6) * 1280); });
    };
    if (F_POS == 1) fload();
    if (xact && X_POS == 1) {
      xload();
      wait_vm<40 + (F_POS == 1 ? 8 : 0)>();
    } else {
      wait_vm<(F_POS == 1 ? 8 : 0)>();
    }
    asm volatile("s_waitcnt lgkmcnt(0)\n\ts_barrier" ::: "memory");
    if (tid < 32) st_flag(ws, O_FK + ((b & ~31) + tid) * 256 + (b & 31) * 8, tag);
    if (xact && X_POS == 2) xload();
    if (F_POS == 2) fload();
    STAMPE(2);
    int pa1[3] = {O_FX + ((b & 7) * 32 + (lane >> 2)) * 128 + (lane & 3) * 16, O_FX + ((b & 7) * 32 + 16 + (lane >> 2)) * 128 + (lane & 3) * 16,
                  O_FA0 + ((b & 7) * 12 + (lane < 48 ? lane >> 2 : 0)) * 128 + (lane & 3) * 16};
    const char* xgp = ws + O_XG + b * 16384 + wv * 4096;
    const char* stp = ws + O_ST1 + wv * 4096;
    asm volatile("" : "+s"(xgp), "+s"(stp));
    pin(pa1[0]); pin(pa1[1]); pin(pa1[2]);
    float Dv = (d & 1) ? bfhi(draw) : bflo(draw);
    unsigned ek = sbase + CKV_OFF + 16 * ((4 * (32 + 2 * row + (kg >> 1)) + wv) ^ ((row >> 1) & 7)) + ((kg & 1) ? 0 : 8);
    unsigned ex = (ek ^ 8) + (CX_OFF - CKV_OFF - 2048), es = sbase + CS_OFF + l16;
    unsigned mo = ((lane & 7) < MODP && lane < 24) ? O_MODP + ((lane & 7) * NMOD + (lane >> 3) * D_ + d) * 4 : WS_BYTES;
    pin(Dv); pin(ek); pin(ex); pin(es); pin(mo);
    unsigned ko[2] = {(unsigned)(lane ^ (lane >> 4)) * 16, ((unsigned)(lane ^ (lane >> 4)) * 16) ^ 64};
    pin(ko[0]); pin(ko[1]);
    auto fcopies = [&]() {
      u32x4 m[5]; u32 lo[5], hi[5];
      const unsigned src = fl + FC_BYTES + l16, src4 = fl + FC_BYTES + 4096 + (lane & 1) * 16;
      static_for<0, 4>([&](auto I) { constexpr int i = I.value; ds_b128<i * 1024>(m[i], src); ds_b32<i * 1024>(lo[i], src - 4); ds_b32<i * 1024 + 16>(hi[i], src); });
      ds_b128<0>(m[4], src4); ds_b32<0>(lo[4], src4 - 4); ds_b32<16>(hi[4], src4);
      wait_lgkm<0>();
#pragma unroll
      for (int i = 0; i < 5; ++i) {
        pin(m[i]); pin(lo[i]); pin(hi[i]);
        if (i == 0 && lane == 0) lo[0] = 0;
        const u32 s1 = __builtin_amdgcn_alignbit(m[i][1], m[i][0], 16), s2 = __builtin_amdgcn_alignbit(m[i][2], m[i][1], 16), s3 = __builtin_amdgcn_alignbit(m[i][3], m[i][2], 16);
        char* dst = smem + wv * CH_BYTES + (i < 4 ? i * 1024 + l16 : 4096 + (lane & 1) * 16);
        *(u32x4*)dst = u32x4{__builtin_amdgcn_alignbit(m[i][0], lo[i], 16), s1, s2, s3};
        *(u32x4*)(dst + 2 * FC_BYTES) = u32x4{s1, s2, s3, __builtin_amdgcn_alignbit(hi[i], m[i][3], 16)};
      }
    };
    if (!xact) { if (wv == 0 && F_POS != 2) wait_vm<1>(); else wait_vm<0>(); fcopies(); }
    if (xact) {
      wait_vm<(F_POS == 2 ? 8 : 0)>();
      STAMPD(8);
#pragma unroll
      for (int c = 0; c < 5; ++c)
#pragma unroll
        for (int t = 0; t < 8; ++t) pin(xx[c][t]);
#pragma unroll
      for (int c = 0; c < 5; ++c) {
        u32x4 p0, p1;
#pragma unroll
        for (int t = 0; t < 4; ++t) {
          p0[t] = __builtin_amdgcn_perm(xx[c][2 * t + 1], xx[c][2 * t], 0x05040100u);
          p1[t] = __builtin_amdgcn_perm(xx[c][2 * t + 1], xx[c][2 * t], 0x07060302u);
        }
        const int so = O_XG + (c * 16 * 2048 + stg * 4) * 16;
        st128(p0, rw, ov, so);
        st128(p1, rw, ov, so + 16384);
      }
      const bf16x2 ones = {(__bf16)1.0f, (__bf16)1.0f};
      float s1[8], s2[8];
#pragma unroll
      for (int t = 0; t < 8; ++t) {
        s1[t] = 0.f; s2[t] = 0.f;
#pragma unroll
        for (int c = 0; c < 5; ++c) {
          const bf16x2 v = __builtin_bit_cast(bf16x2, xx[c][t]);
          s1[t] = __builtin_amdgcn_fdot2_f32_bf16(v, ones, s1[t], false);
          s2[t] = __builtin_amdgcn_fdot2_f32_bf16(v, v, s2[t], false);
        }
      }
      float q1[2], q2[2];
#pragma unroll
      for (int e = 0; e < 2; ++e) {
        q1[e] = dpp_add<DPP_ROR8>(tred16(tred32(s1[e], s1[4 + e]), tred32(s1[2 + e], s1[6 + e])));
        q2[e] = dpp_add<DPP_ROR8>(tred16(tred32(s2[e], s2[4 + e]), tred32(s2[2 + e], s2[6 + e])));
      }
      const float mean = sum8((lane & 8) ? q1[1] : q1[0]) * (1.f / D_);
      const float var = fmaxf(sum8((lane & 8) ? q2[1] : q2[0]) * (1.f / D_) - mean * mean, 0.f);
      const float sd = rbf(__builtin_amdgcn_sqrtf(rbf(rbf(var) + 1e-5f)));
      const float rs = __builtin_amdgcn_rcpf(sd);
      const int s = stg * 8 + (lane >> 3);
      const int nt = s >> 8, col = (s >> 4) & 15, k4 = (s >> 2) & 3, rr = s & 3;
      if ((lane & 7) == 0) __builtin_amdgcn_raw_buffer_store_b64(u32x2{__float_as_uint(rs), __float_as_uint(-rbf(mean) * rs)}, rw,
                                                                 O_ST1 + (((nt * 2 + (rr >> 1)) * 64 + k4 * 16 + col) * 4 + (rr & 1) * 2) * 4, 0, SC1);
      STAMPD(9);
      if (F_POS == 2) wait_vm<11>();
      fcopies();
      wait_vm<0>();
      if (lane < 8) st_flag(ws, O_FX + (lane * 32 + (stg >> 3)) * 128 + (stg & 7) * 8, tag);
    } else if (wv < 2) {
      const int po[1] = {O_FK + b * 256 + wv * 128 + (lane & 7) * 16};
      poll<1, 0>(rw, po, tag);
      STAMPE(3);
      dma16x4<CKV_OFF + 2048>(sbase + wv * 8192, ko[0], ko[1], ws + O_KVG + b * 16384 + wv * 8192);
      dma16x4<CKV_OFF + 2048 + 4096>(sbase + wv * 8192, ko[0], ko[1], ws + O_KVG + b * 16384 + wv * 8192 + 4096);
    }
    f32x4 acc[8];
    u32x2 ra[2][8][2]; u32x4 rb[2][8];
    u32x4 pf[3]; u32 mraw; u64 nm[6];
    auto load_n = [&](auto E, auto N) {
      constexpr int e = E.value, n = N.value, bf = e & 1;
      if constexpr (n < 2) ds_b64<16 * e + 8 * n>(ra[bf][0][n], aAddr);
      else if constexpr (n < 10) ds_b128<2048 * (n - 2)>(rb[bf][n - 2], bA[e]);
      else ds_b64<512 * ((n - 8) >> 1) + 16 * e + 8 * (n & 1)>(ra[bf][(n - 8) >> 1][n & 1], aAddr);
    };
    u32x2 kvw[8], xw[8]; u32x4 sA[8], sB[8];
    float T[8][4], Hh[8][4], c0 = 0.f, c1 = 0.f;
    auto kvop = [&](auto K) {
      constexpr int k = K.value, nt = k >> 3, r = k & 3;
      if constexpr ((k & 4) == 0) T[nt][r] = (r & 1) ? v_lo(kvw[nt][1 - (r >> 1)]) : v_hi(kvw[nt][1 - (r >> 1)]);
      else T[nt][r] = v_mul(T[nt][r], Dv);
    };
    auto xrd = [&](auto K) {
      constexpr int k = K.value, nt = k / 3;
      if constexpr (k % 3 == 0) ds_b64<2048 * nt>(xw[nt], ex);
      else if constexpr (k % 3 == 1) ds_b128<2048 * nt>(sA[nt], es);
      else ds_b128<2048 * nt + 1024>(sB[nt], es);
    };
    float xf[8];
    auto xop = [&](auto K) {
      constexpr int k = K.value, nt = 2 * (k >> 5) + ((k >> 2) & 1), ph = (k >> 3) & 3, r = k & 3, i = k & 7;
      if constexpr (ph == 0) xf[i] = (r & 1) ? v_hi(xw[nt][r >> 1]) : v_lo(xw[nt][r >> 1]);
      else if constexpr (ph == 1) Hh[nt][r] = v_fma(xf[i], __uint_as_float((r < 2 ? sA[nt] : sB[nt])[2 * (r & 1)]), __uint_as_float((r < 2 ? sA[nt] : sB[nt])[2 * (r & 1) + 1]));
      else if constexpr (ph == 2) Hh[nt][r] = v_fma(Hh[nt][r], c1, c0);
      else T[nt][r] = v_fma(Hh[nt][r], T[nt][r], xf[i]);
    };
    u32 ms[3];
    auto mop = [&](auto K) {
      constexpr int k = K.value;
      if constexpr (k < 3) asm_dpp<k>(mraw);
      else if constexpr (k == 3) asm_op1<3>(mraw, mraw);
      else if constexpr (k < 7) asm_rdl<8 * (k - 4)>(ms[k - 4], mraw);
      else if constexpr (k == 7) asm_sop<0>(c0, ms[0], c0);
      else if constexpr (k == 8) asm_sop<1>(c1, ms[1], c1);
      else if constexpr (k == 9) asm_sop<2>(c0, ms[2], c0);
      else if constexpr (k == 10) { u32 t; asm_op1<3>(t, __float_as_uint(c1)); c1 = __uint_as_float(t); }
      else asm_sop<2>(c1, ms[2], c1);
    };
    clear_slot();
    wait_lgkm<0>();
    static_for<0, 2>([&](auto N) { load_n(IC<7>{}, N); });
    static_for<10, 24>([&](auto N) { load_n(IC<7>{}, N); });
    if (wv < 2) wait_vm<1>();
    STAMPD(10);
    bar();
    STAMPE(4);
    static_for<2, 10>([&](auto N) { load_n(IC<7>{}, N); });
    static_for<0, 8>([&](auto EE) {
      constexpr int e = 7 - EE.value, bf = e & 1;
      static_for<0, 8>([&](auto AA) {
        constexpr int a = AA.value;
        static_for<0, 8 - a>([&](auto BB) {
          constexpr int bb = BB.value, n = conv_gap(a, bb);
          if constexpr (e == 7 && n < 8) wait_lgkm<7>();
          mfma_<(e == 7 && a == 0)>(acc[a + bb], cat(ra[bf][a][0], ra[bf][a][1]), rb[bf][bb]);
          if constexpr (e > 0 && n < 24) load_n(IC<(e > 0 ? e - 1 : 0)>{}, IC<n>{});
          if constexpr (e == 7 && n >= 24 && n < 32) ds_b64<2048 * (n - 24)>(kvw[n - 24], ek);
          constexpr int g = 36 * (7 - e) + n;
          static_for<CS.rd[g], CS.rd[g + 1]>(xrd);
          if constexpr (e > 0 ? n == 35 : (n == CW1 || n == CW2)) wait_lgkm<0>();
          static_for<CS.op[g], CS.op[g + 1]>(xop);
          if constexpr ((e == 6 || e == 5) && n < 32) kvop(IC<(6 - e) * 32 + n>{});
          if constexpr (CV.kind[g] == 2) gld128<0>(pf[CV.idx[g]], pa1[CV.idx[g]], ws);
          if constexpr (CV.kind[g] == 3) { if constexpr (CV.idx[g] == 0) dma16<CX_OFF>(sbase + wv * 4096, ko[0], xgp); else dma16n<CV.idx[g] * 1024>(ko[CV.idx[g] & 1], xgp); }
          if constexpr (CV.kind[g] == 4) { if constexpr (CV.idx[g] == 0) dma16<CS_OFF>(sbase + wv * 4096, l16, stp); else dma16n<CV.idx[g] * 1024>(l16, stp); }
          if constexpr (CV.kind[g] == 5) bld32(mraw, mo, wsrd);
          if constexpr (g == G_XCHK) { STAMPD(11); wait_vm<cv_younger(g, 2, 2)>(); }
          if constexpr (g >= G_XCHK && g < G_XCHK + 3) { constexpr int i = g - G_XCHK; nm[2 * i] = v_ne64(u32x2{pf[i][0], pf[i][1]}, tag); nm[2 * i + 1] = v_ne64(u32x2{pf[i][2], pf[i][3]}, tag); }
          if constexpr (g == G_XCHK + 3) {
            if (__builtin_expect(((nm[0] | nm[1]) | (nm[2] | nm[3]) | (nm[4] | nm[5])) != 0, 0)) poll<3, 1>(rw, pa1, tag);
            STAMPD(12);
          }
          if constexpr (e == CBE && n == 8) wait_vm<cv_younger(g, 5, 0)>();
          if constexpr (e == CBE && n >= 9 && n < 21) mop(IC<n - 9>{});
          if constexpr (e == CBE && n == 23) { STAMPD(13); wait_vm<cv_younger(g, 4, 3)>(); bar(); STAMPD(14); }
        });
      });
    });
    STAMPE(5);
    asm volatile("s_nop 7");
    static_for<0, 4>([&](auto NP) {
      constexpr int np = NP.value;
      static_for<(CS.op[288] > 32 * np ? CS.op[288] : 32 * np), 32 * np + 32>(xop);
      u32x4 o;
#pragma unroll
      for (int u = 0; u < 2; ++u) {
        const int nt = 2 * np + u;
        pin(acc[nt]);
        o[2 * u] = pack_bf16(fmaf(Hh[nt][0], acc[nt][0], T[nt][0]), fmaf(Hh[nt][1], acc[nt][1], T[nt][1]));
        o[2 * u + 1] = pack_bf16(fmaf(Hh[nt][2], acc[nt][2], T[nt][2]), fmaf(Hh[nt][3], acc[nt][3], T[nt][3]));
      }
      st128(o, rw, l16 + np * 1024, O_X2T + d * (TP_ * 2));
    });
  }
  const int kg = lane >> 4, c16 = lane & 15;
  const int kq0 = nc >> 2, r0 = (nc & 1) * 4 + wv, sgb = O_SG + (mt * 2 + ((nc >> 1) & 1)) * 8192;
  u32x2 tag2 = {(u32)tag, (u32)(tag >> 32)};
  u32x4 gbr;
  pin(tag2);
  {
    const int fb = O_F2 + (b & 7) * 32 * 128;
    const int jw = (lane & 3) * 24 + ((nc * 12 + wv * 3 + ((lane >> 2) & 1) * 2) >> 3);
    const int pa[1] = {fb + (20 + (jw >> 3)) * 128 + (jw & 6) * 8};
    u32x4 pfa;
    gld128<0>(pfa, pa[0], ws);
    const char* wsrc = ws + O_W1F + (nc * 4 + wv) * W1_STEPS * 3072;
    const char* wq[4];
    unsigned xinc[4];
#pragma unroll
    for (int i = 0; i < 4; ++i) {
      wq[i] = wsrc + ((kq0 + i) & 3) * (5 * 3072) + 4096;
      xinc[i] = (i && kq0 + i == 4) ? (unsigned)(-19 * 32 * TP_ * 2) : 32 * TP_ * 2;
      asm volatile("" : "+s"(wq[i]), "+s"(xinc[i]));
    }
    u32x4 W[G1_R][3], WA_[24], WC[3], pfs[2], pfw, gran;
    auto issue_w = [&](auto I) { constexpr int i = I.value; gld128b<(((i % 15) & 7) - 4) * 1024>(W[(i / 3) % G1_R][i % 3], ((i % 15) & 8) ? l16b : l16, wq[i / 15]); };
    int pc[2] = {fb + (lane >> 2) * 128 + (lane & 3) * 16, fb + (16 + (lane >> 2)) * 128 + (lane & 3) * 16};
    int f2o = lane < 8 ? O_F2 + (lane * 32 + (b >> 3)) * 128 + (b & 7) * 8 : WS_BYTES;
    pin(pc[0]); pin(pc[1]); pin(f2o);
    STAMPE(6);
    asm volatile("s_waitcnt vmcnt(1) lgkmcnt(0)\n\ts_barrier" ::: "memory");
    if (wv == 0) __builtin_amdgcn_raw_buffer_store_b64(tag2, rw, f2o, 0, SC1);
    STAMPE(7);
    if (bs >= CWG) {
      const int j = bs - CWG;
      u32x2 dl[5], dh[5];
      const unsigned add = sbase + AD_OFF + (8 * kg + (c16 >> 2)) * 128 + (lane & 3) * 8;
#pragma unroll
      for (int i = 0; i < 5; ++i) { const int tl = wv * 5 + i; const unsigned a2 = add + (tl >> 2) * 4096 + (tl & 3) * 32; ds_tr<0>(dl[i], a2); ds_tr<512>(dh[i], a2); }
      wait_lgkm<0>();
#pragma unroll
      for (int i = 0; i < 5; ++i) {
        pin(dl[i]); pin(dh[i]);
        __builtin_amdgcn_raw_buffer_store_b128(cat(dl[i], dh[i]), rw, l16, O_W2F + ((((j & 1) * 4 + wv) * 48 + (j >> 1)) * 5 + i) * 1024, SC1);
      }
      wait_vm<5>();
    } else {
      if (wv == 0) wait_vm<1>(); else wait_vm<0>();
    }
    pin(pfa);
    if (!__all(tag_ok(pfa, tag))) poll<1, 2>(rw, pa, tag);
    auto issue_a = [&](auto I) { constexpr int i = 36 + I.value; gld128a<(((i % 15) & 7) - 4) * 1024>(WA_[I.value], ((i % 15) & 8) ? l16b : l16, wq[i / 15]); };
    static_for<0, 3 * G1_WPRE>(issue_w);
    unsigned xoff[2];
#pragma unroll
    for (int p = 0; p < 2; ++p) {
      const int k = 4 * p + kg;
      xoff[p] = (k * TP_ + ((c16 ^ (2 * k)) * 8)) * 2 - p * 1024;
      pin(xoff[p]);
    }
    unsigned xso = O_X2T + ((kq0 * 160 - 32 + 8 * wv) * TP_ + mt * 128) * 2;
    const unsigned ldsw = sbase + wv * 2048;
    unsigned sgl = sgb + wv * 2048 + l16;
    int hvo = kg < 3 ? ((3 * wv + kg) >> 2) * 1024 + ((3 * wv + kg) & 3) * 256 + c16 * 16 : WS_BYTES;
    const int hso = O_H2F + (mt * 8 * 48 + 3 * nc) * 1024;
    unsigned spa = sbase + G1_SP + c16 * 16;
    int sgo = (c16 >> 2) == kg ? sgb + (r0 * 4 + kq0) * 256 + c16 * 16 : WS_BYTES;
    pin(sgl); pin(hvo); pin(sgo); pin(spa);
    int pw[1] = {O_FW + ((b & 7) * 12 + (lane < 48 ? lane >> 2 : 0)) * 128 + (lane & 3) * 16};
    unsigned xo = ((lane >> 3) * TP_ + (lane & 7) * 8) * 2;
    const char* xs = ws + O_X2T + (N * 80 * TP_ + M * 64) * 2;
    unsigned gbo = O_MODP + (((lane & 7) < MODP ? (lane & 7) : 0) * NMOD + 5 * D_ + N * 80 + wv * 20 + (lane < 40 ? lane >> 3 : 0) * 4) * 4;
    pin(pw[0]); pin(xo); pin(gbo);
    u32 hq[4]; u32x4 hs, H[G2_R], WV[23], pf3, ob;
    int po3 = O_F3 + (mt * 16 + nc) * 512 + wv * 32 + (lane & 1) * 16;
    int f3o = tid < 16 ? O_F3 + (mt * 16 + tid) * 512 + nc * 8 : WS_BYTES;
    int oo;
    int fwo = (bs >= CWG && tid < 8) ? O_FW + (tid * 12 + ((bs - CWG) >> 3)) * 128 + (bs & 7) * 8 : WS_BYTES;
    pin(po3); pin(f3o); pin(fwo);
    const char* hp = ws + O_H2F + (M * 4 * 48 + wv * 12) * 1024 + 4096;
    u16* outp;
    asm volatile("s_load_dwordx2 %0, %1, 0x60\n\ts_waitcnt lgkmcnt(0)" : "=s"(outp) : "s"(__builtin_amdgcn_kernarg_segment_ptr()));
    const rsrc_t ro = mk_rsrc(outp, S_ * D_ * 2);
    auto issue = [&](auto O) {
      constexpr int kind = GS.kind[O.value], i = GS.idx[O.value];
      if constexpr (kind == 0) {
        if constexpr (i & 1) xdma1(xoff[1], wsrd, xso);
        else xdma0<g1_slot(i >> 1) * 8192>(ldsw, xoff[0], wsrd, xso, xinc[(i % 10) ? 0 : i / 10]);
      }
      else if constexpr (kind == 1) issue_w(IC<i>{});
      else if constexpr (kind == 2) {
        if constexpr (i >= 37 && O.value < GS.first[160]) gld128b<((i & 7) - 4) * 1024>(WV[i - 37], (i & 8) ? l16b : l16, wdp + (i >> 4) * 16384);
        else issue_wd(IC<i>{});
      }
      else if constexpr (kind == 3) {
        if constexpr (i < 2) dma16<XR_OFF + i * 4160>(sbase + wv * 1040, xo, xs + (size_t)(wv + 4 * i) * 8 * TP_ * 2);
        else if constexpr (i == 2) dma16<XR_OFF + 8320>(sbase + (wv & 1) * 1040, xo, xs + (size_t)((wv & 1) + 8) * 8 * TP_ * 2);
        else gld128<0>(gbr, gbo, ws);
      }
      else if constexpr (kind == 4) gld128<0>(pfw, pw[0], ws);
      else if constexpr (kind == 5) __builtin_amdgcn_raw_buffer_store_b128(gran, rw, sgo, 0, SC1);
      else if constexpr (kind == 6) gld128<i * 1024>(pfs[i], sgl, ws);
      else if constexpr (kind == 7) { hs = u32x4{hq[0], hq[1], hq[2], hq[3]}; __builtin_amdgcn_raw_buffer_store_b128(hs, rw, hvo, hso + g1_ord(i) * (48 * 1024), SC1); }
      else if constexpr (kind == 8) issue_a(IC<i>{});
      else if constexpr (kind == 9) __builtin_amdgcn_raw_buffer_store_b64(tag2, rw, f3o + i * 128, 0, SC1);
      else if constexpr (kind == 10) gld128<i * 128>(pf3, po3, ws);
      else if constexpr (kind == 11) gld128b<(((i % 12) & 7) - 4) * 1024>(H[i % G2_R], ((i % 12) & 8) ? l16b : l16, hp + (i / 12) * (48 * 1024));
      else if constexpr (kind == 12) __builtin_amdgcn_raw_buffer_store_b128(ob, ro, oo, i * (32 * D_ * 2), 0);
      else __builtin_amdgcn_raw_buffer_store_b64(tag2, rw, fwo, 0, SC1);
    };
    auto check3 = [&](auto ST) {
      if (__builtin_expect((v_ne64(u32x2{pf3[0], pf3[1]}, tag) | v_ne64(u32x2{pf3[2], pf3[3]}, tag)) != 0, 0)) {
        const int po[1] = {po3 + ST.value * 128};
        poll<1, 0>(rw, po, tag);
      }
    };
    unsigned xa[9], xm[8];
#pragma unroll
    for (int jj = 0; jj < 9; ++jj) {
      const int rr = 4 * kg + (c16 >> 2);
      xa[jj] = sbase + rr * 256 + (((jj < 8 ? jj : r0) ^ (rr & 7)) * 32) + (lane & 3) * 8;
      pin(xa[jj]);
    }
    u32x4 ones = {0x3f803f80u, 0x3f803f80u, 0x3f803f80u, 0x3f803f80u};
    pin(ones);
    static_for<0, 3>([&](auto I) { gld128a<I.value * 1024>(WC[I.value], l16, wsrc + 20 * 3072); });
    static_for<0, WA - PLATE>(issue_a);
    if (wv == 0) { gld128<0>(pfs[0], pc[0], ws); gld128<0>(pfs[1], pc[1], ws); }
    static_for<WA - PLATE, WA>(issue_a);
    STAMPL(8);
    if (wv == 0) {
      wait_vm<PLATE>();
      pin(pfs[0]); pin(pfs[1]);
      if (!__all(tag_ok(pfs[0], tag) && tag_ok(pfs[1], tag))) poll<2, PSL>(rw, pc, tag);
    } else {
      wait_vm<3 * G1_WPRE + 3 + WA>();
    }
    bar();
    STAMPL(9);
    static_for<0, GS.first[0]>(issue);
    f32x4 acc[8][3];
    u32x2 X[G1_LA + 1][2], XS[5][2];
    auto xread = [&](auto T, auto HH) {
      constexpr int t = T.value, h = HH.value;
      if constexpr (t < 96) ds_tr<g1_slot(g1_ks(t)) * 8192 + h * 4096>(X[t % (G1_LA + 1)][h], xa[g1_j(t)]);
      else ds_tr<(g1_ks(t) - 12) * 8192 + h * 4096>(X[t % (G1_LA + 1)][h], xm[g1_j(t)]);
    };
    f32x4 gram, sum1;
    u32 fs[4], fa; u64 sbad;
    unsigned spw = sbase + G1_SP + (2 * wv + (kg >> 1)) * 256 + c16 * 16;
    pin(spw);
    auto fop = [&](auto K) {
      constexpr int k = K.value;
      if constexpr (k == 0) asm_and3(fa, pfs[0][1], pfs[0][3], pfs[1][1]);
      else if constexpr (k == 1) asm_and3(fa, fa, pfs[1][3], pfs[1][3]);
      else if constexpr (k == 2) asm_ne1(sbad, fa);
      else if constexpr (k == 4) v_swap32(fs[0], fs[1]);
      else if constexpr (k == 5) v_swap32(fs[2], fs[3]);
      else if constexpr (k == 6) asm_op2<0>(fs[0], fs[0], fs[1]);
      else if constexpr (k == 7) asm_op2<0>(fs[2], fs[2], fs[3]);
      else if constexpr (k == 8) v_swap16(fs[0], fs[2]);
      else if constexpr (k == 9) asm_op2<0>(fs[0], fs[0], fs[2]);
      else if constexpr (k == 10) asm_op1<0>(fs[0], fs[0]);
      else if constexpr (k == 11) asm_op1<1>(fs[1], fs[0]);
      else if constexpr (k == 12) v_swap16(fs[0], fs[1]);
      else if constexpr (k == 13) asm_op2<1>(fs[1], fs[0], fs[1]);
      else if constexpr (k == 14) asm_op1<2>(fs[1], fs[1]);
      else if constexpr (k == 15) asm_op1<3>(fs[1], fs[1]);
      else if constexpr (k == 16) asm_op1<4>(fs[1], fs[1]);
      else if constexpr (k == 17) asm_op1<3>(fs[1], fs[1]);
      else if constexpr (k == 18) asm_op1<5>(fs[1], fs[1]);
      else if constexpr (k == 19) asm_op2<2>(fs[0], fs[0], fs[1]);
      else if constexpr (k == 20) asm_op1<1>(fs[1], fs[0]);
      else if constexpr (k == 21) asm_op1<1>(fs[2], fs[0]);
      else if constexpr (k == 22) asm_op1<6>(fs[3], fs[0]);
      else if constexpr (k == 23) ds_w128u<0>(spw, u32x4{fs[0], fs[1], fs[2], fs[3]});
    };
    float eg[6]; u32x4 xc;
    auto eop = [&](auto J, auto K) {
      constexpr int j = J.value, k = K.value;
      if constexpr (k < 15) {
        if constexpr (k < 6) eg[k] = v_relu(acc[j][k >> 1][2 * (k & 1)]);
        else if constexpr (k < 12) eg[k - 6] = v_mul(eg[k - 6], acc[j][(k - 6) >> 1][2 * (k & 1) + 1]);
        else hq[k == 12 ? 1 : k == 13 ? 0 : 2] = v_cvt(eg[k == 12 ? 2 : k == 13 ? 0 : 4], eg[k == 12 ? 3 : k == 13 ? 1 : 5]);
      } else if constexpr (k == 15) v_swap32(hq[1], hq[3]);
      else if constexpr (k == 16) v_swap32(hq[0], hq[2]);
      else if constexpr (k == 17) v_swap16(hq[0], hq[1]);
      else v_swap16(hq[2], hq[3]);
    };
    unsigned pwa, pra, gba, xad, sda;
    float xr[8], sdv, G[8], S[8], rs2;
    hq[3] = 0;
    pin(hq[3]);
    wait_vm<GS.first[0] - 2>();
    bar();
    STAMPL(10);
    static_for<0, G1_LA>([&](auto T) { xread(T, IC<0>{}); xread(T, IC<1>{}); });
    static_for<0, 160>([&](auto T) {
      constexpr int t = T.value, ks = g1_ks(t), jj = g1_j(t), tn = t + G1_LA, u = t & 7;
      if constexpr (t == 24) STAMPG(1);
      if constexpr (t == 48) STAMPG(2);
      if constexpr (t == 72) STAMPG(3);
      if constexpr (t == 98) STAMPG(4);
      if constexpr (t == 128) STAMPG(5);
      if constexpr (!(t & 1)) wait_lgkm<2 * ((tn <= 160 ? tn : 160) - t - 2)>();
      if constexpr (g1_need(t) >= 0) {
        static_assert(g1_need(t) < GS.first[t], "not issued yet");
        if constexpr (GS.first[t] - 1 - g1_need(t) < 63) wait_vm<GS.first[t] - 1 - g1_need(t)>();
        bar();
      }
      if constexpr (t == T_WCHK) {
        if (__builtin_expect((v_ne64(u32x2{pfw[0], pfw[1]}, tag) | v_ne64(u32x2{pfw[2], pfw[3]}, tag)) != 0, 0)) poll<1, 1>(rw, pw, tag);
      }
      if constexpr (t == T_C0) { wait_vm<GS.first[t] - 1 - g_pos(10, 0)>(); check3(IC<0>{}); }
      if constexpr (t == T_SETUP) {
        static_assert(g_pos(3, 3) < g1_need(T_F0) && T_SETUP > T_F0, "gate partials landed");
        {
          pin(gbr);
          const float g = sum8((lane & 7) < MODP ? __uint_as_float(gbr[0]) : 0.f), g2 = sum8((lane & 7) < MODP ? __uint_as_float(gbr[1]) : 0.f);
          const float g3 = sum8((lane & 7) < MODP ? __uint_as_float(gbr[2]) : 0.f), g4 = sum8((lane & 7) < MODP ? __uint_as_float(gbr[3]) : 0.f);
          if ((lane & 7) == 0 && lane < 40) *(f32x4*)(smem + GB_OFF + wv * 80 + (lane >> 3) * 16) = f32x4{rbf(g), rbf(g2), rbf(g3), rbf(g4)};
        }
        const int lf = lane < 40 ? lane : 0, ft = 4 * wv + lf / 10, fc = lf % 10;
        pwa = sbase + wv * 5440 + l16 + (kg >> 1) * 32;
        pra = sbase + fc * 544 + ft * 16; gba = sbase + GB_OFF + fc * 32;
        xad = sbase + XR_OFF + fc * 1040 + ft * 2; sda = sbase + G1_SP + ((nc & 4) * 16 + ft) * 16 + 2;
        {
          const int pos = M * 64 + ft, l8 = (pos >> 3) & 63;
          const int tok = 256 * (2 * (pos >> 9) + ((pos >> 2) & 1)) + 16 * (l8 & 15) + 4 * (l8 >> 4) + (pos & 3);
          oo = lane < 40 ? (tok * D_ + N * 80 + 8 * fc) * 2 : S_ * D_ * 2;
        }
        sdv = 0.f;
#pragma unroll
        for (int e = 0; e < 8; ++e) { xr[e] = 0.f; pin(xr[e]); }
        pin(pwa); pin(pra); pin(gba); pin(xad); pin(sda); pin(oo); pin(sdv);
      }
      if constexpr (t == T_SST - 2) {
        pin(gram); pin(sum1);
        const int c3 = lane & 3;
        gran = u32x4{__float_as_uint(sum1[0]), 1u, __float_as_uint(c3 == 0 ? gram[0] : c3 == 1 ? gram[1] : c3 == 2 ? gram[2] : gram[3]), 1u};
        pin(gran);
      }
      if constexpr (t == 86) {
        if (__builtin_expect(sbad != 0, 0)) {
#pragma unroll 1
          for (int spn = 0; spn < SPIN_CAP; ++spn) {
            pfs[0] = __builtin_amdgcn_raw_buffer_load_b128(rw, sgl, 0, SC1);
            pfs[1] = __builtin_amdgcn_raw_buffer_load_b128(rw, sgl + 1024, 0, SC1);
            if (__all((pfs[0][1] & pfs[0][3] & pfs[1][1] & pfs[1][3]) == 1u)) break;
            asm volatile("" ::: "memory");
          }
        }
        fs[0] = pfs[0][0]; fs[1] = pfs[1][0]; fs[2] = pfs[0][2]; fs[3] = pfs[1][2];
      }
      auto ep = [&](auto A, auto B) { if constexpr (t >= 104) static_for<A.value, (B.value < 19 ? B.value : 19)>([&](auto K) { eop(IC<g1_j(t - 8)>{}, K); }); };
      constexpr int k0 = u == 0 ? 0 : 4 * u - 3;
      constexpr int sr = t == 0 ? 0 : t == 4 ? 1 : (t >= 12 && t < 15) ? t - 10 : -1, sm = t == 4 ? 0 : t == 8 ? 1 : (t >= 16 && t < 19) ? t - 14 : -1;
      const u32x4 xb = cat(X[t % (G1_LA + 1)][0], X[t % (G1_LA + 1)][1]);
      if constexpr (ks >= 12) mfma_a<false>(acc[jj][0], WA_[3 * (ks - 12) + 0], xb); else mfma_<ks == 0>(acc[jj][0], W[ks % G1_R][0], xb);
      if constexpr (tn < 160) xread(IC<tn>{}, IC<0>{});
      if constexpr (t >= 84 && t < 96) fop(IC<2 * (t - 84)>{});
      if constexpr (u > 0) ep(IC<k0>{}, IC<k0 + 1>{});
      if constexpr (ks >= 12) mfma_a<false>(acc[jj][1], WA_[3 * (ks - 12) + 1], xb); else mfma_<ks == 0>(acc[jj][1], W[ks % G1_R][1], xb);
      if constexpr (tn < 160) xread(IC<tn>{}, IC<1>{});
      if constexpr (t >= 84 && t < 96) fop(IC<2 * (t - 84) + 1>{});
      if constexpr (u > 0) ep(IC<k0 + 1>{}, IC<k0 + 2>{}); else ep(IC<0>{}, IC<1>{});
      if constexpr (ks >= 12) mfma_a<false>(acc[jj][2], WA_[3 * (ks - 12) + 2], xb); else mfma_<ks == 0>(acc[jj][2], W[ks % G1_R][2], xb);
      if constexpr (t >= 96) real();
      static_for<GS.first[t], GS.first[t + 1]>(issue);
      if constexpr (u > 0) ep(IC<k0 + 2>{}, IC<k0 + 4>{});
      if constexpr (t >= 84 && t < 92) xm[t - 84] = v_addc(xa[t - 84], G1_RS * 8192);
      if constexpr (sm >= 0) { mfma_<sm == 0>(gram, cat(XS[sm][0], XS[sm][1]), cat(XS[sm][0], XS[sm][1])); mfma_<sm == 0>(sum1, ones, cat(XS[sm][0], XS[sm][1])); }
      if constexpr (sr >= 0) { ds_tr<sr * 8192>(XS[sr][0], xa[8]); ds_tr<sr * 8192 + 4096>(XS[sr][1], xa[8]); }
      if constexpr (t >= 96 && u == (t < 104 ? 4 : 0)) ds_b128<jj * 256>(xc, spa);
      if constexpr (t >= 96 && u == 7) {
        if constexpr (t < 104) wait_lgkm<6>();
        mfma_a<false>(acc[jj][0], WC[0], xc);
        mfma_a<false>(acc[jj][1], WC[1], xc);
        mfma_a<false>(acc[jj][2], WC[2], xc);
      }
    });
    STAMPL(11);
    asm volatile("s_nop 5");
    static_for<0, 15>([&](auto K) { eop(IC<7>{}, K); });
    eop(IC<7>{}, IC<15>{}); asm volatile("s_nop 0"); eop(IC<7>{}, IC<16>{}); asm volatile("s_nop 1"); eop(IC<7>{}, IC<17>{}); eop(IC<7>{}, IC<18>{});
    static_for<GS.first[160], GS.mid>(issue);
    STAMPL(12);
    if constexpr (T_C0 >= 160) { wait_vm<GS.mid - 1 - g_pos(10, 0)>(); check3(IC<0>{}); }
    static_for<GS.mid, GS.first2[0]>(issue);
    if constexpr (g_lastdv() >= 0) wait_vm<GS.first2[0] - 1 - g_lastdv()>();
    static_for<37, 60>([&](auto I) { if constexpr (g_pos(2, I.value) < GS.first[160]) to_acc(WD[I.value], WV[I.value - 37]); });
    f32x4 gbv[2], ac2[2][5], pt[8];
    auto pre = [&](auto J, auto K) {
      constexpr int j = J.value, k = K.value;
      if constexpr (k == 0) ds_hi16<j * 256>(sdv, sda);
      else if constexpr (k < 9) ds_hi16<(k - 1) * 128 + j * 32>(xr[k - 1], xad);
      else if constexpr (k == 9) rs2 = v_rcp(sdv);
      else if constexpr (k == 10) rs2 = v_mul(rs2, rs2);
      else G[k - 11] = v_mul(gbv[(k - 11) >> 2][(k - 11) & 3], rs2);
    };
    auto fin = [&](auto K) {
      constexpr int k = K.value, e = k & 7;
      if constexpr (k < 8) S[e] = v_add(pt[e >> 2][e & 3], pt[2 + (e >> 2)][e & 3]);
      else if constexpr (k < 24) S[e] = v_add(S[e], pt[2 * (k >> 3) + 2 + (e >> 2)][e & 3]);
      else if constexpr (k < 32) S[e] = v_fma(G[e], S[e], xr[e]);
      else ob[k - 32] = v_cvt(S[2 * (k - 32)], S[2 * (k - 32) + 1]);
    };
    auto prd = [&](auto J, auto K) { ds_f128<(J.value & 1) * 21760 + (K.value >> 1) * 5440 + (K.value & 1) * 256>(pt[K.value], pra); };
    STAMPL(13);
    static_for<0, 240>([&](auto GG) {
      constexpr int g = GG.value, o = g / 5, jj = o / 12, ks = o % 12, i = g % 5, q = g % 60;
      if constexpr (g == 60) STAMPG(6);
      if constexpr (g == 120) STAMPG(7);
      if constexpr (i == 0) { static_assert(g2_need(o) < GS.first2[g], "not issued yet"); wait_vm<GS.first2[g] - 1 - g2_need(o)>(); }
      if constexpr (g == G_F3) { wait_vm<GS.first2[g] - 1 - g_pos(7, 7)>(); bar(); }
      if constexpr (g == G_C1) { wait_vm<GS.first2[g] - 1 - g_pos(10, 1)>(); check3(IC<1>{}); }
      if constexpr (g == G_C2) { wait_vm<GS.first2[g] - 1 - g_pos(10, 2)>(); check3(IC<2>{}); }
      if constexpr (g == G_C3) { wait_vm<GS.first2[g] - 1 - g_pos(10, 3)>(); check3(IC<3>{}); }
      mfma_a<ks == 0>(ac2[jj & 1][i], WD[ks * 5 + i], H[o % G2_R]);
      if constexpr (i == 4) real();
      static_for<GS.first2[g], GS.first2[g + 1]>(issue);
      if constexpr (g == 30 || g == 32) ds_f128<(g - 30) * 8>(gbv[(g - 30) / 2], gba);
      if constexpr (jj >= 1) {
        if constexpr (q >= 5 && q < 15 && (q & 1)) ds_w128<((jj - 1) & 1) * 21760 + ((q - 5) / 2) * 1088>(pwa, ac2[(jj - 1) & 1][(q - 5) / 2]);
        if constexpr (q == 18) { wait_lgkm<0>(); bar(); }
        if constexpr (q >= 19 && q < 27) prd(IC<jj - 1>{}, IC<q - 19>{});
        if constexpr (q == 30) wait_lgkm<0>();
        if constexpr (q >= 31 && q < 31 + FINW) static_for<(36 * (q - 31) + FINW - 1) / FINW, (36 * (q - 30) + FINW - 1) / FINW>(fin);
      }
      if constexpr (jj >= 1 && q < 18 && !(q & 1)) pre(IC<jj - 1>{}, IC<q / 2>{});
      if constexpr (jj >= 1 && q >= 19 && q < 29) pre(IC<jj - 1>{}, IC<q - 10>{});
    });
    STAMPL(14);
    static_for<0, 9>([&](auto K) { pre(IC<3>{}, K); });
    static_for<0, 5>([&](auto I) { ds_w128<21760 + I.value * 1088>(pwa, ac2[1][I.value]); });
    wait_lgkm<5>();
    pre(IC<3>{}, IC<9>{}); asm volatile("s_nop 0");
    static_for<10, 19>([&](auto K) { pre(IC<3>{}, K); });
    wait_lgkm<0>(); bar();
    static_for<0, 8>([&](auto K) { prd(IC<3>{}, K); });
    wait_lgkm<4>();
    static_for<0, 8>(fin);
    wait_lgkm<0>();
    static_for<8, 36>(fin);
    __builtin_amdgcn_raw_buffer_store_b128(ob, ro, oo, 3 * (32 * D_ * 2), 0);
  }
  if (b == 0 && tid == 0) st_flag(ws, O_EPOCH, tag);
  STAMP_END();
}

extern "C" void kernel_launch(void* const* d_in, const int* in_sizes, int n_in, void* d_out, int out_size,
                              void* d_ws, size_t ws_size, hipStream_t stream) {
  k_fused<<<256, 256, 0, stream>>>((const u16*)d_in[1], (const u16*)d_in[2], (const u16*)d_in[3], (char*)d_ws, (const u16*)d_in[10],
                                   (const u16*)d_in[11], (const u16*)d_in[5], (const u16*)d_in[0], (const u16*)d_in[4],
                                   (const u16*)d_in[7], (const u16*)d_in[8], (const u16*)d_in[9], (u16*)d_out);
}
```

```cpp
#include <hip/hip_runtime.h>
#include <stdint.h>

typedef unsigned short u16;
typedef unsigned int u32;
typedef __attribute__((ext_vector_type(2))) __bf16 bf16x2;
typedef __attribute__((ext_vector_type(4))) float f32x4;
typedef __attribute__((ext_vector_type(2))) float f32x2;
typedef __attribute__((ext_vector_type(4))) u32 u32x4;
typedef __attribute__((ext_vector_type(2))) u32 u32x2;

constexpr int S_ = 2048, D_ = 640, F_ = 1536;
constexpr int TP_ = S_ + 64;
constexpr int NMOD = 6 * D_;
constexpr int MODP = 5;
constexpr int CWG = 160;

__device__ __forceinline__ float bflo(u32 w) { return __uint_as_float(w << 16); }
__device__ __forceinline__ float bfhi(u32 w) { return __uint_as_float(w & 0xffff0000u); }
__device__ __forceinline__ u32 pack_bf16(float lo, float hi) {
  bf16x2 v; v[0] = (__bf16)lo; v[1] = (__bf16)hi; return __builtin_bit_cast(u32, v);
}
__device__ __forceinline__ float rbf(float x) { __bf16 b = (__bf16)x; return (float)b; }
#define SCHED() __builtin_amdgcn_sched_barrier(0)
template <int V> struct IC { static constexpr int value = V; };
template <int B, int E, class Fn> __device__ __forceinline__ void static_for(Fn&& f) {
  if constexpr (B < E) { f(IC<B>{}); static_for<B + 1, E>(f); }
}
__device__ __forceinline__ void mfma(f32x4& c, u32x4 a, u32x4 b) {
  asm volatile("v_mfma_f32_16x16x32_bf16 %0, %1, %2, %0" : "+v"(c) : "v"(a), "v"(b));
}
__device__ __forceinline__ void mfma0(f32x4& c, u32x4 a, u32x4 b) {
  asm volatile("v_mfma_f32_16x16x32_bf16 %0, %1, %2, 0" : "=&v"(c) : "v"(a), "v"(b));
}
template <bool FIRST> __device__ __forceinline__ void mfma_(f32x4& c, u32x4 a, u32x4 b) { if constexpr (FIRST) mfma0(c, a, b); else mfma(c, a, b); }
__device__ __forceinline__ u32 alignbit(u32 hi, u32 lo, u32 sh) {
  u32 d; asm volatile("v_alignbit_b32 %0, %1, %2, %3" : "=v"(d) : "v"(hi), "v"(lo), "v"(sh)); return d;
}
template <int OFF> __device__ __forceinline__ void ds_tr(u32x2& dst, unsigned addr) {
  asm volatile("ds_read_b64_tr_b16 %0, %1 offset:%2" : "=v"(dst) : "v"(addr), "i"(OFF) : "memory");
}
template <int OFF> __device__ __forceinline__ void ds_b32(u32& dst, unsigned addr) {
  asm volatile("ds_read_b32 %0, %1 offset:%2" : "=v"(dst) : "v"(addr), "i"(OFF) : "memory");
}
template <int OFF> __device__ __forceinline__ void ds_b64(u32x2& dst, unsigned addr) {
  asm volatile("ds_read_b64 %0, %1 offset:%2" : "=v"(dst) : "v"(addr), "i"(OFF) : "memory");
}
template <int OFF> __device__ __forceinline__ void ds_b128(u32x4& dst, unsigned addr) {
  asm volatile("ds_read_b128 %0, %1 offset:%2" : "=v"(dst) : "v"(addr), "i"(OFF) : "memory");
}
template <int OFF> __device__ __forceinline__ void ds_w128(unsigned addr, f32x4 v) {
  asm volatile("ds_write_b128 %0, %1 offset:%2" :: "v"(addr), "v"(v), "i"(OFF) : "memory");
}
template <int OFF> __device__ __forceinline__ void gld128(u32x4& dst, unsigned voff, const char* sb) {
  asm volatile("global_load_dwordx4 %0, %1, %2 offset:%3 sc1" : "=v"(dst) : "v"(voff), "s"(sb), "i"(OFF) : "memory");
}
__device__ __forceinline__ void gld32(u32& dst, unsigned voff, const char* sb) {
  asm volatile("global_load_dword %0, %1, %2 sc1" : "=v"(dst) : "v"(voff), "s"(sb) : "memory");
}
template <int LOFF> __device__ __forceinline__ void dma16(unsigned ldsb, unsigned voff, const char* sb) {
  asm volatile("s_add_u32 m0, %0, %3\n\ts_nop 0\n\tglobal_load_lds_dwordx4 %1, %2 sc1" :: "s"(ldsb), "v"(voff), "s"(sb), "i"(LOFF) : "memory", "scc");
}
template <int LOFF> __device__ __forceinline__ void dma16x4(unsigned ldsb, unsigned v0, unsigned v1, const char* sb) {
  asm volatile("s_add_u32 m0, %0, %4\n\ts_nop 0\n\tglobal_load_lds_dwordx4 %1, %3 sc1\n\tglobal_load_lds_dwordx4 %2, %3 offset:1024 sc1\n\t"
               "global_load_lds_dwordx4 %1, %3 offset:2048 sc1\n\tglobal_load_lds_dwordx4 %2, %3 offset:3072 sc1"
               :: "s"(ldsb), "v"(v0), "v"(v1), "s"(sb), "i"(LOFF) : "memory", "scc");
}
template <int N> __device__ __forceinline__ void wait_vm() { asm volatile("s_waitcnt vmcnt(%0)" :: "i"(N) : "memory"); }
template <int N> __device__ __forceinline__ void wait_lgkm() { asm volatile("s_waitcnt lgkmcnt(%0)" :: "i"(N) : "memory"); }
__device__ __forceinline__ void keep(u32x4 v) { asm volatile("" :: "v"(v)); }
__device__ __forceinline__ void real() { __builtin_amdgcn_s_setprio(0); }
__device__ __forceinline__ void bar() { asm volatile("s_barrier" ::: "memory"); }
template <class T> __device__ __forceinline__ void pin(T& a) { asm volatile("" : "+v"(a)); }
__device__ __forceinline__ u32x4 cat(u32x2 a, u32x2 b) { return u32x4{a[0], a[1], b[0], b[1]}; }

typedef __amdgpu_buffer_rsrc_t rsrc_t;
__device__ __forceinline__ rsrc_t mk_rsrc(const void* p, int bytes) { return __builtin_amdgcn_make_buffer_rsrc((void*)p, 0, bytes, 0x00020000); }
template <int CTRL> __device__ __forceinline__ float dpp_add(float x) {
  return x + __builtin_bit_cast(float, __builtin_amdgcn_mov_dpp(__builtin_bit_cast(int, x), CTRL, 0xf, 0xf, true));
}
constexpr int DPP_XOR1 = 0xB1, DPP_XOR2 = 0x4E, DPP_HMIRROR = 0x141, DPP_ROR8 = 0x128;
__device__ __forceinline__ float sum8(float x) { return dpp_add<DPP_HMIRROR>(dpp_add<DPP_XOR2>(dpp_add<DPP_XOR1>(x))); }
__device__ __forceinline__ float tred32(float a, float b) {
  auto r = __builtin_amdgcn_permlane32_swap(__float_as_uint(a), __float_as_uint(b), false, false);
  return __uint_as_float(r[0]) + __uint_as_float(r[1]);
}
__device__ __forceinline__ float tred16(float a, float b) {
  auto r = __builtin_amdgcn_permlane16_swap(__float_as_uint(a), __float_as_uint(b), false, false);
  return __uint_as_float(r[0]) + __uint_as_float(r[1]);
}
__device__ __forceinline__ float rdl(float x, int l) { return __int_as_float(__builtin_amdgcn_readlane(__float_as_int(x), l)); }

typedef unsigned long long u64;
#define GAS __attribute__((address_space(1)))
constexpr int SC1 = 16;
#define BS " sc1"
#define A0_DELAY 16
#define F_LATE 1
#define A1_DELAY 4
#define A3_DELAY 24
#define AU_DELAY 16
#define A_DELAY 2
#define FXE 7
#define PE 4
#define NT 0
#define NTS ""
template <int OFF> __device__ __forceinline__ void gld128b(u32x4& dst, unsigned voff, const char* sb) {
  asm volatile("global_load_dwordx4 %0, %1, %2 offset:%3" BS : "=v"(dst) : "v"(voff), "s"(sb), "i"(OFF) : "memory");
}
template <int OFF> __device__ __forceinline__ void gld128a(u32x4& dst, unsigned voff, const char* sb) {
  asm volatile("global_load_dwordx4 %0, %1, %2 offset:%3" BS : "=a"(dst) : "v"(voff), "s"(sb), "i"(OFF) : "memory");
}
template <bool FIRST> __device__ __forceinline__ void mfma_a(f32x4& c, u32x4 a, u32x4 b) {
  if constexpr (FIRST) asm volatile("v_mfma_f32_16x16x32_bf16 %0, %1, %2, 0" : "=&v"(c) : "a"(a), "v"(b));
  else asm volatile("v_mfma_f32_16x16x32_bf16 %0, %1, %2, %0" : "+v"(c) : "a"(a), "v"(b));
}
typedef __attribute__((ext_vector_type(8))) __bf16 bf16x8;
__device__ __forceinline__ f32x4 mfma_b(u32x4 a, u32x4 b, f32x4 c) {
  return __builtin_amdgcn_mfma_f32_16x16x32_bf16(__builtin_bit_cast(bf16x8, a), __builtin_bit_cast(bf16x8, b), c, 0, 0, 0);
}
template <int LOFF> __device__ __forceinline__ void dma16p(unsigned ldsb, unsigned voff, const char* sb) {
  asm volatile("s_add_u32 m0, %0, %3\n\ts_nop 0\n\tglobal_load_lds_dwordx4 %1, %2" NTS :: "s"(ldsb), "v"(voff), "s"(sb), "i"(LOFF) : "memory", "scc");
}
template <int LOFF> __device__ __forceinline__ void dma16x4p(unsigned ldsb, unsigned v0, const char* sb) {
  asm volatile("s_add_u32 m0, %0, %3\n\ts_nop 0\n\tglobal_load_lds_dwordx4 %1, %2" NTS "\n\tglobal_load_lds_dwordx4 %1, %2 offset:1024" NTS "\n\t"
               "global_load_lds_dwordx4 %1, %2 offset:2048" NTS "\n\tglobal_load_lds_dwordx4 %1, %2 offset:3072" NTS
               :: "s"(ldsb), "v"(v0), "s"(sb), "i"(LOFF) : "memory", "scc");
}

#undef DBGC
#define STAMP(I)
#define STAMP_DECL()
#define STAMP_END()

__device__ __forceinline__ void gld128p(u32x4& dst, unsigned voff, const char* sb) {
  asm volatile("global_load_dwordx4 %0, %1, %2" NTS : "=v"(dst) : "v"(voff), "s"(sb) : "memory");
}
template <int OFF> __device__ __forceinline__ void gld32p(u32& dst, unsigned voff, const char* sb) {
  asm volatile("global_load_dword %0, %1, %2 offset:%3" NTS : "=v"(dst) : "v"(voff), "s"(sb), "i"(OFF) : "memory");
}
template <int OFF> __device__ __forceinline__ void gld16p(u32& dst, unsigned voff, const char* sb) {
  asm volatile("global_load_ushort %0, %1, %2 offset:%3" : "=v"(dst) : "v"(voff), "s"(sb), "i"(OFF) : "memory");
}
#define CPF_DONE() asm volatile("" :: "v"(cpf))
#define X_POS 1
#define F_POS 1
template <int OFF> __device__ __forceinline__ void dma16n(unsigned voff, const char* sb) {
  asm volatile("global_load_lds_dwordx4 %0, %1 offset:%2 sc1" :: "v"(voff), "s"(sb), "i"(OFF) : "memory");
}
__device__ __forceinline__ u64 v_ne64(u32x2 v, u64 tag) { u64 m; asm volatile("v_cmp_ne_u64 %0, %1, %2" : "=s"(m) : "v"(v), "s"(tag)); return m; }
__device__ __forceinline__ float v_lo(u32 w) { float d; asm volatile("v_lshlrev_b32 %0, 16, %1" : "=v"(d) : "v"(w)); return d; }
__device__ __forceinline__ float v_hi(u32 w) { float d; asm volatile("v_and_b32 %0, 0xffff0000, %1" : "=v"(d) : "v"(w)); return d; }
__device__ __forceinline__ float v_fma(float a, float b, float c) { float d; asm volatile("v_fma_f32 %0, %1, %2, %3" : "=v"(d) : "v"(a), "v"(b), "v"(c)); return d; }
__device__ __forceinline__ float v_mul(float a, float b) { float d; asm volatile("v_mul_f32 %0, %1, %2" : "=v"(d) : "v"(a), "v"(b)); return d; }
__device__ __forceinline__ float v_add(float a, float b) { float d; asm volatile("v_add_f32 %0, %1, %2" : "=v"(d) : "v"(a), "v"(b)); return d; }
#define STAMPD(I)
#define STAMPL(I) STAMP(I)
__device__ __forceinline__ float v_relu(float a) { float d; asm volatile("v_max_i32 %0, 0, %1" : "=v"(d) : "v"(a)); return d; }
__device__ __forceinline__ u32 v_cvt(float a, float b) { u32 d; asm volatile("v_cvt_pk_bf16_f32 %0, %1, %2" : "=v"(d) : "v"(a), "v"(b)); return d; }
__device__ __forceinline__ u32 v_perm(u32 a, u32 sel) { u32 d; asm volatile("v_perm_b32 %0, %1, %1, %2" : "=v"(d) : "v"(a), "s"(sel)); return d; }
__device__ __forceinline__ void v_swap32(u32& a, u32& b) { asm volatile("v_permlane32_swap_b32 %0, %1" : "+v"(a), "+v"(b)); }
__device__ __forceinline__ void v_swap16(u32& a, u32& b) { asm volatile("v_permlane16_swap_b32 %0, %1" : "+v"(a), "+v"(b)); }
template <int OFF> __device__ __forceinline__ void ds_f128(f32x4& dst, unsigned addr) {
  asm volatile("ds_read_b128 %0, %1 offset:%2" : "=v"(dst) : "v"(addr), "i"(OFF) : "memory");
}
#define STAMPG(I)
#define STAMPE(I) STAMP(I)
__device__ __forceinline__ void asm_and3(u32& d, u32 a, u32 b, u32 c) { asm volatile("v_bitop3_b32 %0, %1, %2, %3 bitop3:0x80" : "=v"(d) : "v"(a), "v"(b), "v"(c)); }
__device__ __forceinline__ void asm_ne1(unsigned long long& m, u32 a) { asm volatile("v_cmp_ne_u32 %0, 1, %1" : "=s"(m) : "v"(a)); }
__device__ __forceinline__ void ds_w32(unsigned addr, u32 v) { asm volatile("ds_write_b32 %0, %1" :: "v"(addr), "v"(v) : "memory"); }
template <int OP> __device__ __forceinline__ void asm_op1(u32& d, u32 a) {
  if constexpr (OP == 0) asm volatile("v_mul_f32 %0, 0x3acccccd, %1" : "=v"(d) : "v"(a));
  else if constexpr (OP == 1) asm volatile("v_mov_b32 %0, %1" : "=v"(d) : "v"(a));
  else if constexpr (OP == 2) asm volatile("v_max_f32 %0, 0, %1" : "=v"(d) : "v"(a));
  else if constexpr (OP == 3) asm volatile("v_cvt_pk_bf16_f32 %0, 0, %1" : "=v"(d) : "v"(a));
  else if constexpr (OP == 4) asm volatile("v_add_f32 %0, 0x3727c5ac, %1" : "=v"(d) : "v"(a));
  else if constexpr (OP == 5) asm volatile("v_sqrt_f32 %0, %1" : "=v"(d) : "v"(a));
  else asm volatile("v_mov_b32 %0, 0" : "=v"(d));
}
template <int OP> __device__ __forceinline__ void asm_op2(u32& d, u32 a, u32 b) {
  if constexpr (OP == 0) asm volatile("v_add_f32 %0, %1, %2" : "=v"(d) : "v"(a), "v"(b));
  else if constexpr (OP == 1) asm volatile("v_fma_f32 %0, -%1, %1, %2" : "=v"(d) : "v"(a), "v"(b));
  else asm volatile("v_cvt_pk_bf16_f32 %0, -%1, %2" : "=v"(d) : "v"(a), "v"(b));
}
template <int LOFF> __device__ __forceinline__ void xdma0(unsigned ldsb, unsigned voff, u32x4 srd, unsigned& soff, unsigned inc) {
  asm volatile("s_add_u32 m0, %1, %5\n\ts_add_u32 %0, %0, %4\n\tbuffer_load_dwordx4 %2, %3, %0 offen" BS " lds"
               : "+s"(soff) : "s"(ldsb), "v"(voff), "s"(srd), "s"(inc), "i"(LOFF) : "memory", "scc");
}
__device__ __forceinline__ void xdma1(unsigned voff, u32x4 srd, unsigned soff) {
  asm volatile("buffer_load_dwordx4 %0, %1, %2 offen offset:1024" BS " lds" :: "v"(voff), "s"(srd), "s"(soff) : "memory");
}
template <int OFF> __device__ __forceinline__ void ds_w128u(unsigned addr, u32x4 v) {
  asm volatile("ds_write_b128 %0, %1 offset:%2" :: "v"(addr), "v"(v), "i"(OFF) : "memory");
}
__device__ __forceinline__ float v_rcp(float a) { float d; asm volatile("v_rcp_f32 %0, %1" : "=v"(d) : "v"(a)); return d; }
template <int OFF> __device__ __forceinline__ void ds_hi16(float& dst, unsigned addr) {
  asm volatile("ds_read_u16_d16_hi %0, %1 offset:%2" : "+v"(dst) : "v"(addr), "i"(OFF) : "memory");
}
__device__ __forceinline__ unsigned v_addc(unsigned a, unsigned c) { unsigned d; asm volatile("v_add_u32 %0, %2, %1" : "=v"(d) : "v"(a), "s"(c)); return d; }
__device__ __forceinline__ void to_acc(u32x4& d, u32x4 v) {
  asm volatile("" : "+v"(v));
  asm volatile("" : "=a"(d) : "0"(v));
}
__device__ __forceinline__ void bld32(u32& dst, unsigned voff, u32x4 srd) {
  asm volatile("buffer_load_dword %0, %1, %2, 0 offen sc1" : "=v"(dst) : "v"(voff), "s"(srd) : "memory");
}
template <int K> __device__ __forceinline__ void asm_dpp(u32& x) {
  if constexpr (K == 0) asm volatile("v_add_f32_dpp %0, %0, %0 quad_perm:[1,0,3,2] row_mask:0xf bank_mask:0xf" : "+v"(x));
  else if constexpr (K == 1) asm volatile("v_add_f32_dpp %0, %0, %0 quad_perm:[2,3,0,1] row_mask:0xf bank_mask:0xf" : "+v"(x));
  else asm volatile("v_add_f32_dpp %0, %0, %0 row_half_mirror row_mask:0xf bank_mask:0xf" : "+v"(x));
}
template <int L> __device__ __forceinline__ void asm_rdl(u32& d, u32 x) { asm volatile("v_readlane_b32 %0, %1, %2" : "=s"(d) : "v"(x), "i"(L)); }
template <int OP> __device__ __forceinline__ void asm_sop(float& d, u32 s, float a) {
  if constexpr (OP == 0) asm volatile("v_mov_b32 %0, %1" : "=v"(d) : "s"(s));
  else if constexpr (OP == 1) asm volatile("v_add_f32 %0, 1.0, %1" : "=v"(d) : "s"(s));
  else asm volatile("v_mul_f32 %0, %1, %2" : "=v"(d) : "s"(s), "v"(a));
}
constexpr int W1_STEPS = 21;
constexpr int O_EPOCH = 0;
constexpr int O_FK = 4096;
constexpr int O_FA0 = O_FK + 160 * 256;
constexpr int O_FW = O_FA0 + 8 * 12 * 128;
constexpr int O_FU = O_FW + 8 * 12 * 128;
constexpr int O_FX = O_FU + 8 * 14 * 128;
constexpr int O_F2 = O_FX + 8 * 32 * 128;
constexpr int O_F3 = O_F2 + 8 * 32 * 128;
constexpr int O_SG = O_F3 + 256 * 512;
constexpr int O_MODP = O_SG + 32 * 8192;
constexpr int O_ST1 = O_MODP + MODP * NMOD * 4;
constexpr int O_KVG = O_ST1 + 16384;
constexpr int O_XG = O_KVG + S_ * D_ * 2;
constexpr int O_X2T = O_XG + S_ * D_ * 2;
constexpr int O_W1F = O_X2T + TP_ * D_ * 2;
constexpr int O_W2F = O_W1F + 64 * W1_STEPS * 3 * 1024;
constexpr int O_H2F = O_W2F + 8 * 48 * 5 * 1024;
constexpr int WS_BYTES = O_H2F + S_ * F_ * 2;
static_assert(O_MODP % 256 == 0 && O_ST1 % 256 == 0 && O_KVG % 256 == 0 && O_X2T % 256 == 0 && O_W1F % 256 == 0, "alignment");

__device__ __forceinline__ void st128(u32x4 v, rsrc_t r, int vo, int so) {
  __builtin_amdgcn_raw_buffer_store_b128(v, r, vo, so, SC1);
  asm volatile("s_nop 1" :: "v"(v) : "memory");
}
constexpr int SPIN_CAP = 1 << 16;
constexpr unsigned CODE_BYTES = 42 * 1024;
__device__ __forceinline__ void st_flag(char* ws, int off, u64 tag) {
  __hip_atomic_store((GAS u64*)(ws + off), tag, __ATOMIC_RELAXED, __HIP_MEMORY_SCOPE_AGENT);
}
__device__ __forceinline__ bool tag_ok(u32x4 v, u64 tag) {
  const u32 lo = (u32)tag, hi = (u32)(tag >> 32);
  return ((v[0] ^ lo) | (v[1] ^ hi) | (v[2] ^ lo) | (v[3] ^ hi)) == 0;
}
template <int NL, int SLEEP> __device__ __forceinline__ void poll(rsrc_t rw, const int (&off)[NL], u64 tag) {
#pragma unroll 1
  for (int sp = 0; sp < SPIN_CAP; ++sp) {
    bool ok = true;
#pragma unroll
    for (int i = 0; i < NL; ++i) ok &= tag_ok(__builtin_amdgcn_raw_buffer_load_b128(rw, off[i], 0, SC1), tag);
    if (__all(ok)) break;
    if (SLEEP) __builtin_amdgcn_s_sleep(SLEEP);
    asm volatile("" ::: "memory");
  }
  asm volatile("" ::: "memory");
}
__device__ __forceinline__ u64 make_tag(u32x2 e) {
  return (((u64)(u32)__builtin_amdgcn_readfirstlane(e[1]) << 32) | (u32)__builtin_amdgcn_readfirstlane(e[0])) + 1;
}
__device__ __forceinline__ void drain_bar() { asm volatile("s_waitcnt vmcnt(0) lgkmcnt(0)\n\ts_barrier" ::: "memory"); }

constexpr int FC_BYTES = 2064 * 2;
constexpr int CH_BYTES = 4 * FC_BYTES;
constexpr int CKV_OFF = 66048;
constexpr int CX_OFF = CKV_OFF + 2048 + 16384, CS_OFF = CX_OFF + 16384;
constexpr int conv_gap(int a, int b) { int n = 0; for (int i = 0; i < a; ++i) n += 8 - i; return n + b; }
struct CSched { int rd[290]; int op[290]; };
constexpr int CBE = PE - 2;
constexpr int CW1 = 12, CW2 = CBE >= 2 ? 12 : (CBE == 1 ? 19 : 35);
constexpr CSched conv_make() {
  CSched S{};
  int rd = 0, op = 0, landed = 0;
  for (int g = 0; g < 288; ++g) {
    const int e = 7 - g / 36, n = g % 36;
    S.rd[g] = rd; S.op[g] = op;
    const bool open = e < CBE || (e == CBE && n >= 24);
    bool ds = e > 0 && n < 24;
    if (open && !ds && rd < 24 && (e == 0 || n < 34)) { ++rd; ds = true; if (n >= 30 && n < 32 && rd < 24) ++rd; }
    if (e > 0 ? n == 35 : (n == CW1 || n == CW2)) landed = rd;
    int k = ds ? 1 : 2;
    while (k-- > 0 && op < (landed / 6) * 32) ++op;
  }
  S.rd[288] = rd; S.op[288] = op;
  return S;
}
constexpr CSched CS = conv_make();
static_assert(CS.rd[288 - 36 + CW2] == 24, "reads must be out before the last wait");
#define XSP 2
#define XCH 20
constexpr int G_XCHK = 36 * (7 - PE) + XCH;
struct CVSched { int kind[290]; int idx[290]; };
constexpr CVSched cv_make() {
  CVSched S{};
  for (int i = 0; i < 3; ++i) { S.kind[G_XCHK - 44 + 4 * i] = 2; S.idx[G_XCHK - 44 + 4 * i] = i; }
  S.kind[G_XCHK + 4] = 5;
  for (int i = 0; i < 8; ++i) { S.kind[G_XCHK + 4 + XSP * (i + 1)] = 3 + i / 4; S.idx[G_XCHK + 4 + XSP * (i + 1)] = i & 3; }
  return S;
}
constexpr CVSched CV = cv_make();
constexpr int cv_younger(int g, int kind, int idx) {
  int n = -1;
  for (int i = 0; i <= g; ++i) { if (n >= 0 && CV.kind[i]) ++n; if (CV.kind[i] == kind && CV.idx[i] == idx) n = 0; }
  return n;
}
static_assert(G_XCHK > 44, "conv request schedule");
constexpr int G1_R = 6;
constexpr int G1_RS = 7;
constexpr int G1_LA = 4;
constexpr int G1_SP = 15 * 8192;
constexpr int G1_WPRE = 5;
constexpr int g1_ord(int g) { return (g >> 1) + 4 * (g & 1); }
constexpr int g1_ks(int t) { return t < 96 ? t >> 3 : 12 + (t & 7); }
constexpr int g1_j(int t) { return t < 96 ? t & 7 : g1_ord((t - 96) >> 3); }
constexpr int g1_slot(int ks) { return ks < 12 ? ks % G1_RS : G1_RS + ks - 12; }
struct GSched { int kind[320]; int idx[320]; int first[162]; int mid; int first2[242]; int n; };
#define T_SPOLL 66
#define WA 24
#define PLATE 16
#define PSL 1
#define CR1 12
#define CR2 14
#define WDN1 60
#define T_DV 112
#define T_DEND 157
#define T_P0 147
#define DPRE 0
#define G2PRE 8
#define G_P1 2
#define G_C1 24
#define G_P2 30
#define G_C2 68
#define G_F3 12
#define G_P3 84
#define G_C3 124
#define FINW 26
#define HRATE 3
#define T_F2 159
#define T_SETUP 135
#define T_F0 128
#define T_F1 144
#define T_C0 156
constexpr int XPRE = 5, T_SST = 23, T_WPOLL = 46, T_WCHK = 69;
#define G2R 22
constexpr int G2_R = G2R, G2_ST = 58;
constexpr GSched g_make() {
  GSched S{};
  int n = 0, x = 0, w = 3 * G1_WPRE, wa = WA, wd = 0, rs = 0, h = 0, credit = 0;
  auto put = [&](int k, int i) { S.kind[n] = k; S.idx[n] = i; ++n; };
  for (; x < 2 * XPRE; ++x) put(0, x);
  put(13, 0);
  for (int t = 0; t < 160; ++t) {
    S.first[t] = n;
    const int ks = t >> 3;
    credit += t < 96 ? CR1 : CR2;
    if (credit > 28) credit = 28;
    const bool xok = x < 40 && ((x >> 1) >= 12 || (x >> 1) <= ks + G1_RS - ((t & 7) >= 4 ? 1 : 2)), wok = w < 36 && w / 3 <= ks + G1_R - 1;
    const int n0 = n;
    if (t == T_WPOLL) put(4, 0);
    else if (t == T_SST) put(5, 0);
    else if (t == T_SPOLL || t == T_SPOLL + 1) put(6, t - T_SPOLL);
    else if (t >= 109 && (t & 7) == 5) put(7, (t - 109) >> 3);
    else if (t == T_F0 || t == T_F1 || t == T_F2) put(9, t == T_F0 ? 0 : t == T_F1 ? 1 : 2);
    else if (t == T_P0) put(10, 0);
    else if (t >= T_C0) put(11, h++);
    else if (credit >= 16) {
      if (xok && (!wok || (x >> 1) < w / 3)) put(0, x++);
      else if (wok) put(1, w++);
      else if (wa < 24) put(8, wa++);
      else if (rs < 4 && !(x & 1)) put(3, rs++);
      else if (t > T_WCHK && wd < (t < T_DV ? 37 : WDN1) && t < T_DEND) put(2, wd++);
    }
    if (n > n0) credit -= 16;
  }
  S.first[160] = n;
  put(7, 7);
  for (int i = 0; i < DPRE && wd < 60; ++i) put(2, wd++);
  S.mid = n;
  auto next = [&](int g) {
    const int hmax = g <= G_C1 ? 12 : (g <= G_C2 ? 24 : (g <= G_C3 ? 36 : 48));
    if (wd < 60 && (wd / 5 < h || h >= 12)) put(2, wd++);
    else if (h < hmax && h < g / 5 + G2_R - 1) put(11, h++);
  };
  for (int i = 0; i < G2PRE; ++i) next(0);
  for (int g = 0; g < 240; ++g) {
    S.first2[g] = n;
    if (g == G_F3) put(9, 3);
    else if (g == G_P1) put(10, 1);
    else if (g == G_P2) put(10, 2);
    else if (g == G_P3) put(10, 3);
    else if (g > 60 && g % 60 == G2_ST) put(12, g / 60 - 1);
    else if (wd < 60 ? (g & 1) : g % HRATE == 1) next(g);
  }
  S.first2[240] = n; S.n = n;
  return S;
}
constexpr GSched GS = g_make();
constexpr int g_pos(int kind, int idx) { for (int i = 0; i < GS.n; ++i) if (GS.kind[i] == kind && GS.idx[i] == idx) return i; return -1; }
constexpr int imax(int a, int b) { return a > b ? a : b; }
constexpr int g1_need(int t) {
  if (t == T_F0) return g_pos(7, 1);
  if (t == T_F1) return g_pos(7, 3);
  if (t == T_F2) return g_pos(7, 5);
  if (t == 92) return imax(g_pos(0, 31), g_pos(8, 23));
  if (t == 96) return g_pos(0, 39);
  if (t == 100) return 0;
  if (t > 92 || (t & 7) != 4) return -1;
  const int s = (t + 4) / 8;
  int p = imax(g_pos(0, 2 * s + 1), g_pos(1, 3 * s + 2));
  if (t == 12) p = imax(p, g_pos(0, 9));
  if (t == 84) p = imax(p, g_pos(6, 1));
  if (t == T_WCHK - 1) p = imax(p, g_pos(4, 0));
  return p;
}
constexpr int g_lastdv() { int p = -1; for (int i = 37; i < 60; ++i) if (g_pos(2, i) < GS.first[160]) p = g_pos(2, i); return p; }
constexpr int g2_need(int o) { return o < 12 ? imax(g_pos(11, o), g_pos(2, 5 * o + 4)) : g_pos(11, o); }
static_assert(g_pos(1, 35) >= 0 && g_pos(0, 39) >= 0 && g_pos(2, 59) >= 0 && g_pos(11, 47) >= 0 && g_pos(3, 3) >= 0, "schedule incomplete");
constexpr int XR_OFF = G1_SP + 2048;
constexpr int GB_OFF = XR_OFF + 10400;
constexpr int LDS_BYTES = 134 * 1024;
static_assert(GB_OFF + 320 <= LDS_BYTES, "LDS");
constexpr int AG_OFF = 0, AU_OFF = 20480, AD_OFF = 40960, AM_OFF = 61440;

__global__ __launch_bounds__(256, 1) void k_fused(const u16* __restrict__ xIn, const u16* __restrict__ kIn, const u16* __restrict__ vIn,
                                                  const u16* __restrict__ shk, const u16* __restrict__ ssmk, const u16* __restrict__ ssmD,
                                                  const u16* __restrict__ wg, const u16* __restrict__ wu, const u16* __restrict__ wd,
                                                  const u16* __restrict__ cIn, const u16* __restrict__ wmod,
                                                  char* __restrict__ ws, u16* __restrict__ out) {
  __shared__ __attribute__((aligned(16))) char smem[LDS_BYTES];
  STAMP_DECL();
  STAMP(0);
  u32 cpf = 0;
  if (blockIdx.x >= 240) {
    uint64_t pc; asm volatile("s_getpc_b64 %0" : "=s"(pc));
    const unsigned co = (((blockIdx.x >> 3) & 1) * 256 + threadIdx.x) * 128;
    asm volatile("global_load_dword %0, %1, %2" : "=v"(cpf) : "v"(co < CODE_BYTES ? co : 0u), "s"(pc & ~(uint64_t)127) : "memory");
  }
  u64 tag;
  const int tid = threadIdx.x, lane = tid & 63;
  const int wv = __builtin_amdgcn_readfirstlane(tid >> 6);
  const int b = blockIdx.x;
  const unsigned sbase = (unsigned)(size_t)smem;
  const unsigned l16 = lane * 16;
  int bs = blockIdx.x;
  asm volatile("" : "+s"(bs));
  const int xcd = bs & 7, idx = bs >> 3;
  int mt = (xcd & 1) * 8 + (idx & 7), nc = (xcd >> 1) * 4 + (idx >> 3);
  asm volatile("" : "+s"(mt), "+s"(nc));
  const int M = mt * 2 + ((nc >> 2) & 1), N = (nc >> 3) * 4 + (nc & 3);
  int kvo = (b >> 5) * 256 + lane * 4, kso[9];
  kso[0] = ((b & 31) * 64 + wv * 16) * 1280;
#pragma unroll
  for (int q = 1; q < 8; ++q) kso[q] = kso[0] + q * 2560;
  kso[8] = kso[0] ? kso[0] - 3 * 1280 : 0;
  asm volatile("" : "+v"(kvo), "+s"(kso[0]), "+s"(kso[1]), "+s"(kso[2]), "+s"(kso[3]), "+s"(kso[4]), "+s"(kso[5]), "+s"(kso[6]), "+s"(kso[7]), "+s"(kso[8]));
  asm volatile("" :: "s"(xIn), "s"(kIn), "s"(vIn), "s"(shk), "s"(ssmk), "s"(ssmD), "s"(wg), "s"(wu), "s"(wd), "s"(cIn), "s"(wmod), "s"(ws));
  const rsrc_t rw = mk_rsrc(ws, WS_BYTES);
  const u32x4 wsrd = {(u32)(uint64_t)ws, (u32)((uint64_t)ws >> 32) & 0xffffu, (u32)WS_BYTES, 0x00020000u};
  u32 tch = 0;
  auto touch = [&]() {
    constexpr int PER = (O_ST1 + 16384 - O_FK + 256 * 128 - 1) / (256 * 128);
    static_assert(PER <= 64, "one request per workgroup");
    tch = __builtin_amdgcn_raw_buffer_load_b32(rw, (wv == 3 && lane < PER) ? O_FK + (b * PER + lane) * 128 : WS_BYTES, 0, 0);
  };
  u32x4 WD[60];
  const char* wdp = ws + O_W2F + (N * 48 + wv * 12) * 5120 + 4096;
  const unsigned l16b = l16 + 8192;
  auto issue_wd = [&](auto I) { constexpr int i = I.value; gld128a<((i & 7) - 4) * 1024>(WD[i], (i & 8) ? l16b : l16, wdp + (i >> 4) * 16384); };
  auto clear_slot = [&]() {
    const int so = O_SG + (mt * 2 + ((nc >> 1) & 1)) * 8192 + (((nc & 1) * 4 + wv) * 4 + (nc >> 2)) * 256;
    __builtin_amdgcn_raw_buffer_store_b32(0u, rw, lane * 4, so, SC1);
  };
  auto gemv = [&](int gcol, int gk, auto landed) {
    const int r8 = lane >> 3, cp = lane & 7;
    u32x4 wm[16]; u32 cv[2];
    static_for<0, 2>([&](auto I) { gld16p<I.value * 128>(cv[I.value], lane * 2, (const char*)(cIn + gk * 128)); });
    static_for<0, 16>([&](auto I) { gld128p(wm[I.value], r8 * (NMOD * 2) + cp * 16, (const char*)(wmod + (size_t)(gk * 128 + I.value * 8) * NMOD + gcol * 64)); });
    wait_vm<16>();
    float sl[2], sv[16];
#pragma unroll
    for (int i = 0; i < 2; ++i) { pin(cv[i]); const float x = bflo(cv[i]); sl[i] = rbf(x * __builtin_amdgcn_rcpf(1.f + __expf(-x))); }
#pragma unroll
    for (int it = 0; it < 16; ++it) { sv[it] = __shfl(sl[it >> 3], (it & 7) * 8 + r8); pin(sv[it]); }
    f32x2 acc[4];
    static_for<0, 4>([&](auto G) {
      constexpr int g = G.value;
      wait_vm<12 - 4 * g>();
#pragma unroll
      for (int it = 4 * g; it < 4 * g + 4; ++it) {
        pin(wm[it]);
        const f32x2 s2 = {sv[it], sv[it]};
#pragma unroll
        for (int q = 0; q < 4; ++q) {
          const f32x2 w2 = {bflo(wm[it][q]), bfhi(wm[it][q])};
          acc[q] = it ? __builtin_elementwise_fma(s2, w2, acc[q]) : s2 * w2;
        }
      }
#pragma unroll
      for (int q = 0; q < 4; ++q) pin(acc[q]);
    });
    landed();
    const float a0 = tred16(tred32(acc[0][0], acc[2][0]), tred32(acc[1][0], acc[3][0]));
    const float a1 = tred16(tred32(acc[0][1], acc[2][1]), tred32(acc[1][1], acc[3][1]));
    const float e0 = dpp_add<DPP_ROR8>(a0), e1 = dpp_add<DPP_ROR8>(a1);
    __builtin_amdgcn_raw_buffer_store_b32(__float_as_uint((lane & 8) ? e1 : e0), rw, O_MODP + (gk * NMOD + gcol * 64 + cp * 8 + r8) * 4, 0, SC1);
  };
  if (b >= CWG) {
    const int j = b - CWG;
    const int r8 = lane >> 3, cp = lane & 7;
    const int ks2 = j >> 1, hf = j & 1;
    const int ug = j % 24, kq = j / 24;
    float* LC = (float*)(smem + AM_OFF);
    u16* LS = (u16*)(smem + AM_OFF + 640);
    const int gt = wv * 96 + j;
    const bool gact = gt < 250, urgent = gt < 100;
    const int gq = gt / MODP, gk = gt - gq * MODP, gcol = urgent ? 30 + gq : gq - 20;
    u32x2 ep = __builtin_amdgcn_raw_buffer_load_b64(rw, O_EPOCH, 0, SC1);
    touch();
    SCHED();
    if (!urgent) __builtin_amdgcn_s_sleep(A0_DELAY);
    if (gact) {
      gemv(gcol, gk, [&]() { pin(ep); pin(tch); });
      if (urgent) {
        wait_vm<0>();
        if (lane < 8) st_flag(ws, O_FU + (lane * 14 + (gt >> 3)) * 128 + (gt & 7) * 8, make_tag(ep));
        __builtin_amdgcn_s_sleep(AU_DELAY);
      }
    } else {
      pin(ep); pin(tch);
    }
    tag = make_tag(ep);
    STAMPE(1);
    {
      const unsigned vg = r8 * (F_ * 2) + ((cp ^ ((r8 >> 1) * 2)) * 16);
      const char* gs = (const char*)(wg + (size_t)(kq * 160 + wv * 40) * F_ + ug * 64);
      const char* us = (const char*)(wu + (size_t)(kq * 160 + wv * 40) * F_ + ug * 64);
      static_for<0, 5>([&](auto I) {
        dma16p<AG_OFF + I.value * 1024>(sbase + wv * 5120, vg, gs + I.value * (8 * F_ * 2));
        dma16p<AU_OFF + I.value * 1024>(sbase + wv * 5120, vg ^ 16, us + I.value * (8 * F_ * 2));
      });
    }
    wait_vm<10>();
    bar();
    if (tid < 8) st_flag(ws, O_FA0 + (tid * 12 + (j >> 3)) * 128 + (j & 7) * 8, tag);
    STAMPE(2);
    const int kg = lane >> 4, q4 = (lane & 15) >> 2, p4 = lane & 3;
    if (wv == 0) {
      const int po[1] = {O_FU + ((b & 7) * 14 + (lane < 50 ? lane >> 2 : 0)) * 128 + (lane & 3) * 16};
      poll<1, 1>(rw, po, tag);
      STAMPE(3);
      f32x4 mm[2];
      const int lo = lane < 40 ? lane * 16 : 0;
#pragma unroll
      for (int i = 0; i < 2; ++i) {
        mm[i] = f32x4{0.f, 0.f, 0.f, 0.f};
#pragma unroll
        for (int p = 0; p < MODP; ++p)
          mm[i] += __builtin_bit_cast(f32x4, __builtin_amdgcn_raw_buffer_load_b128(rw, lo, O_MODP + (p * NMOD + (3 + i) * D_ + kq * 160) * 4, SC1));
      }
      if (lane < 40) {
        *(u32x2*)(LS + lane * 4) = u32x2{pack_bf16(mm[0][0], mm[0][1]), pack_bf16(mm[0][2], mm[0][3])};
        *(f32x4*)(LC + lane * 4) = f32x4{rbf(1.f + rbf(mm[1][0])), rbf(1.f + rbf(mm[1][1])), rbf(1.f + rbf(mm[1][2])), rbf(1.f + rbf(mm[1][3]))};
      }
    }
    drain_bar();
    STAMPE(4);
    CPF_DONE();
    clear_slot();
    {
      const char* ds = (const char*)(wd + (size_t)(ks2 * 32 + wv * 8) * D_ + hf * 320);
      static_for<0, 5>([&](auto I) { dma16p<AD_OFF + I.value * 4096>(sbase + wv * 1024, r8 * (D_ * 2) + cp * 16, ds + I.value * 128); });
    }
    {
      const int rr = 4 * kg + q4;
      u32x2 lo[10], hi[10], sbl[5], sbh[5]; u32x4 cl[5], ch[5];
      unsigned ad[2];
#pragma unroll
      for (int t = 0; t < 2; ++t) ad[t] = sbase + (p4 >> 1) * AU_OFF + rr * 128 + (((wv * 2 + t) ^ (((rr >> 1) & 3) * 2 + (p4 >> 1))) * 16) + (p4 & 1) * 8;
      const unsigned ac = sbase + AM_OFF + kg * 16, as = sbase + AM_OFF + 640 + kg * 8;
      auto rd = [&](auto I) {
        constexpr int i = I.value;
        ds_b128<i * 128>(cl[i], ac); ds_b128<i * 128 + 64>(ch[i], ac);
        ds_tr<i * 4096>(lo[2 * i], ad[0]); ds_tr<i * 4096 + 2048>(hi[2 * i], ad[0]); ds_tr<i * 4096>(lo[2 * i + 1], ad[1]); ds_tr<i * 4096 + 2048>(hi[2 * i + 1], ad[1]);
        ds_b64<i * 64>(sbl[i], as); ds_b64<i * 64 + 32>(sbh[i], as);
      };
      rd(IC<0>{});
      const int src = ((lane & 48) + ((lane & 1) * 8 + ((lane & 15) >> 1))) * 4;
      const u32x4 ones = {0x3f803f80u, 0x3f803f80u, 0x3f803f80u, 0x3f803f80u};
      f32x4 aU[2] = {}, aW[2] = {};
      int so[2];
#pragma unroll
      for (int t = 0; t < 2; ++t) { const int T = ug * 8 + wv * 2 + t, sw3 = T / 3; so[t] = O_W1F + ((sw3 * W1_STEPS + kq * 5) * 3 + T - sw3 * 3) * 1024; }
      static_for<0, 5>([&](auto KS) {
        constexpr int ks = KS.value;
        wait_lgkm<0>();
        pin(cl[ks]); pin(ch[ks]); pin(sbl[ks]); pin(sbh[ks]);
        if constexpr (ks < 4) rd(IC<ks + 1>{});
        u32x4 pm[2];
#pragma unroll
        for (int t = 0; t < 2; ++t) {
          pin(lo[2 * ks + t]); pin(hi[2 * ks + t]);
          const u32x4 raw = cat(lo[2 * ks + t], hi[2 * ks + t]);
          u32x4 sc;
#pragma unroll
          for (int q = 0; q < 2; ++q) {
            sc[q] = pack_bf16(bflo(raw[q]) * __uint_as_float(cl[ks][2 * q]), bfhi(raw[q]) * __uint_as_float(cl[ks][2 * q + 1]));
            sc[2 + q] = pack_bf16(bflo(raw[2 + q]) * __uint_as_float(ch[ks][2 * q]), bfhi(raw[2 + q]) * __uint_as_float(ch[ks][2 * q + 1]));
          }
#pragma unroll
          for (int q = 0; q < 4; ++q) pm[t][q] = (u32)__builtin_amdgcn_ds_bpermute(src, sc[q]);
          aW[t] = mfma_b(raw, cat(sbl[ks], sbh[ks]), aW[t]);
          aU[t] = mfma_b(sc, ones, aU[t]);
        }
#pragma unroll
        for (int t = 0; t < 2; ++t) st128(pm[t], rw, l16, so[t] + ks * 3072);
      });
      const f32x4 su = (lane & 4) ? aU[1] : aU[0], sw = (lane & 4) ? aW[1] : aW[0];
      const float U = p4 == 0 ? su[0] : p4 == 1 ? su[1] : p4 == 2 ? su[2] : su[3];
      const float W = p4 == 0 ? sw[0] : p4 == 1 ? sw[1] : p4 == 2 ? sw[2] : sw[3];
      const float u1 = rbf(U), u2 = rbf(U - u1), u3 = U - u1 - u2;
      const float w1 = rbf(W), w2 = rbf(W - w1), w3 = W - w1 - w2;
      const int crow = 2 * (4 * (kg & 1) + p4) + (kg >> 1);
      __builtin_amdgcn_raw_buffer_store_b128(u32x4{pack_bf16(u1, w1), pack_bf16(u2, w2), pack_bf16(u3, w3), 0u}, rw,
                                             (lane & 8) ? WS_BYTES : ((lane & 4) ? so[1] : so[0]) + (20 - kq * 5) * 3072 + (kq * 16 + crow) * 16, 0, SC1);
    }
    STAMPE(5);
  } else {
    const int d = (b >> 1) * 8 + (b & 1) + 2 * wv;
    const int row = lane & 15, kg = lane >> 4;
    const int tt = b & 31, ct = b >> 5;
    const int ov = ((lane >> 2) * 2048 + (lane & 3)) * 16, os = (ct * 16 * 2048 + (tt * 8 + wv * 2) * 4) * 16;
    const int stg = b * 2 + (wv - 2);
    const bool xact = wv >= 2 && b < 128;
    u32 xx[5][8];
    u32 draw; u32x2 ep;
    unsigned aAddr, bA[8];
    const unsigned fl = sbase + wv * CH_BYTES;
    {
      const rsrc_t rk = mk_rsrc(kIn, S_ * D_ * 2), rv = mk_rsrc(vIn, S_ * D_ * 2);
      u32 kk[19], vv[16];
#pragma unroll
      for (int q = 0; q < 3; ++q) kk[q] = __builtin_amdgcn_raw_buffer_load_b32(rk, kvo + q * 1280, kso[8], NT);
#pragma unroll
      for (int q = 0; q < 8; ++q) kk[3 + q] = __builtin_amdgcn_raw_buffer_load_b32(rk, kvo + (q & 1) * 1280, kso[q >> 1], NT);
      const u32x4 taps = __builtin_amdgcn_raw_buffer_load_b128(mk_rsrc(shk, D_ * 8), kvo * 4, 0, 0);
#pragma unroll
      for (int q = 0; q < 8; ++q) vv[q] = __builtin_amdgcn_raw_buffer_load_b32(rv, kvo + (q & 1) * 1280, kso[q >> 1], NT);
#pragma unroll
      for (int q = 8; q < 16; ++q) kk[3 + q] = __builtin_amdgcn_raw_buffer_load_b32(rk, kvo + (q & 1) * 1280, kso[q >> 1], NT);
#pragma unroll
      for (int q = 8; q < 16; ++q) vv[q] = __builtin_amdgcn_raw_buffer_load_b32(rv, kvo + (q & 1) * 1280, kso[q >> 1], NT);
      SCHED();
      SCHED();
      if (F_POS == 0) {
        const char* fs = (const char*)(ssmk + (size_t)d * S_);
        dma16x4p<FC_BYTES + 28>(fl, l16, fs);
        dma16x4p<3 * FC_BYTES + 24>(fl, l16, fs);
      }
      aAddr = fl + (row & 3) * FC_BYTES + 2 * (64 * kg + (row & ~3));
      pin(aAddr);
#pragma unroll
      for (int e = 0; e < 8; ++e) { const int g = 32 + 2 * row + 1 - 8 * kg - e; bA[e] = sbase + CKV_OFF + 16 * ((4 * g + wv) ^ ((g >> 2) & 7)); pin(bA[e]); }
      ep = __builtin_amdgcn_raw_buffer_load_b64(rw, O_EPOCH, 0, SC1);
      touch();
      draw = __builtin_amdgcn_raw_buffer_load_b32(mk_rsrc(ssmD, D_ * 2), (d >> 1) * 4, 0, 0);
      *(u32x2*)(smem + CKV_OFF + tid * 8) = u32x2{0, 0};
      if (lane < 13) *(u32*)(smem + wv * CH_BYTES + (lane < 7 ? FC_BYTES + lane * 4 : 3 * FC_BYTES + (lane - 7) * 4)) = 0;
      SCHED();
      if (kso[0] == 0) { kk[0] = 0; kk[1] = 0; kk[2] = 0; }
      const f32x2 w0 = {bflo(taps[0]), bflo(taps[2])}, w1 = {bfhi(taps[0]), bfhi(taps[2])};
      const f32x2 w2 = {bflo(taps[1]), bflo(taps[3])}, w3 = {bfhi(taps[1]), bfhi(taps[3])};
#pragma unroll
      for (int r = 0; r < 2; ++r) {
        f32x2 kv[8];
#pragma unroll
        for (int q = 0; q < 8; ++q) {
          const int i = 8 * r + q;
          f32x2 a = f32x2{bflo(kk[i]), bfhi(kk[i])} * w3;
          a = __builtin_elementwise_fma(f32x2{bflo(kk[i + 1]), bfhi(kk[i + 1])}, w2, a);
          a = __builtin_elementwise_fma(f32x2{bflo(kk[i + 2]), bfhi(kk[i + 2])}, w1, a);
          a = __builtin_elementwise_fma(f32x2{bflo(kk[i + 3]), bfhi(kk[i + 3])}, w0, a);
          kv[q] = a * f32x2{bflo(vv[i]), bfhi(vv[i])};
        }
        u32x4 o0, o1;
#pragma unroll
        for (int t = 0; t < 4; ++t) {
          o0[t] = pack_bf16(kv[7 - 2 * t][0], kv[6 - 2 * t][0]);
          o1[t] = pack_bf16(kv[7 - 2 * t][1], kv[6 - 2 * t][1]);
        }
        st128(o0, rw, ov + r * 64, O_KVG + os);
        st128(o1, rw, ov + r * 64, O_KVG + os + 16384);
      }
    }
    STAMPE(1);
    pin(ep); pin(draw); pin(tch);
    tag = make_tag(ep);
    auto fload = [&]() {
      const char* fs = (const char*)(ssmk + (size_t)d * S_);
      dma16x4p<FC_BYTES + 28>(fl, l16, fs);
      dma16x4p<3 * FC_BYTES + 24>(fl, l16, fs);
    };
    auto xload = [&]() {
      const char* xp = (const char*)xIn + stg * 8 * 1280;
      static_for<0, 40>([&](auto I) { constexpr int t = I.value / 5, c = I.value % 5; gld32p<c * 256 + (t & 1) * 1280>(xx[c][t], lane * 4, xp + (t & 6) * 1280); });
    };
    if (F_POS == 1) fload();
    if (xact && X_POS == 1) {
      xload();
      wait_vm<40 + (F_POS == 1 ? 8 : 0)>();
    } else {
      wait_vm<(F_POS == 1 ? 8 : 0)>();
    }
    asm volatile("s_waitcnt lgkmcnt(0)\n\ts_barrier" ::: "memory");
    if (tid < 32) st_flag(ws, O_FK + ((b & ~31) + tid) * 256 + (b & 31) * 8, tag);
    if (xact && X_POS == 2) xload();
    if (F_POS == 2) fload();
    STAMPE(2);
    int pa1[3] = {O_FX + ((b & 7) * 32 + (lane >> 2)) * 128 + (lane & 3) * 16, O_FX + ((b & 7) * 32 + 16 + (lane >> 2)) * 128 + (lane & 3) * 16,
                  O_FA0 + ((b & 7) * 12 + (lane < 48 ? lane >> 2 : 0)) * 128 + (lane & 3) * 16};
    const char* xgp = ws + O_XG + b * 16384 + wv * 4096;
    const char* stp = ws + O_ST1 + wv * 4096;
    asm volatile("" : "+s"(xgp), "+s"(stp));
    pin(pa1[0]); pin(pa1[1]); pin(pa1[2]);
    float Dv = (d & 1) ? bfhi(draw) : bflo(draw);
    unsigned ek = sbase + CKV_OFF + 16 * ((4 * (32 + 2 * row + (kg >> 1)) + wv) ^ ((row >> 1) & 7)) + ((kg & 1) ? 0 : 8);
    unsigned ex = (ek ^ 8) + (CX_OFF - CKV_OFF - 2048), es = sbase + CS_OFF + l16;
    unsigned mo = ((lane & 7) < MODP && lane < 24) ? O_MODP + ((lane & 7) * NMOD + (lane >> 3) * D_ + d) * 4 : WS_BYTES;
    pin(Dv); pin(ek); pin(ex); pin(es); pin(mo);
    unsigned ko[2] = {(unsigned)(lane ^ (lane >> 4)) * 16, ((unsigned)(lane ^ (lane >> 4)) * 16) ^ 64};
    pin(ko[0]); pin(ko[1]);
    auto fcopies = [&]() {
      u32x4 m[5]; u32 lo[5], hi[5];
      const unsigned src = fl + FC_BYTES + l16, src4 = fl + FC_BYTES + 4096 + (lane & 1) * 16;
      static_for<0, 4>([&](auto I) { constexpr int i = I.value; ds_b128<i * 1024>(m[i], src); ds_b32<i * 1024>(lo[i], src - 4); ds_b32<i * 1024 + 16>(hi[i], src); });
      ds_b128<0>(m[4], src4); ds_b32<0>(lo[4], src4 - 4); ds_b32<16>(hi[4], src4);
      wait_lgkm<0>();
#pragma unroll
      for (int i = 0; i < 5; ++i) {
        pin(m[i]); pin(lo[i]); pin(hi[i]);
        if (i == 0 && lane == 0) lo[0] = 0;
        const u32 s1 = __builtin_amdgcn_alignbit(m[i][1], m[i][0], 16), s2 = __builtin_amdgcn_alignbit(m[i][2], m[i][1], 16), s3 = __builtin_amdgcn_alignbit(m[i][3], m[i][2], 16);
        char* dst = smem + wv * CH_BYTES + (i < 4 ? i * 1024 + l16 : 4096 + (lane & 1) * 16);
        *(u32x4*)dst = u32x4{__builtin_amdgcn_alignbit(m[i][0], lo[i], 16), s1, s2, s3};
        *(u32x4*)(dst + 2 * FC_BYTES) = u32x4{s1, s2, s3, __builtin_amdgcn_alignbit(hi[i], m[i][3], 16)};
      }
    };
    if (wv >= 2 && !xact) {
      const int t = (wv - 2) * 32 + (b - 128);
      if (t < 50) gemv(50 + t / MODP, t % MODP, []() {});
    }
    if (!xact) { if ((wv == 0 && F_POS != 2) || wv >= 2) wait_vm<1>(); else wait_vm<0>(); if (wv >= 2 && (wv - 2) * 32 + (b - 128) >= 50) wait_vm<0>(); fcopies(); }
    if (xact) {
      wait_vm<(F_POS == 2 ? 8 : 0)>();
      STAMPD(8);
#pragma unroll
      for (int c = 0; c < 5; ++c)
#pragma unroll
        for (int t = 0; t < 8; ++t) pin(xx[c][t]);
#pragma unroll
      for (int c = 0; c < 5; ++c) {
        u32x4 p0, p1;
#pragma unroll
        for (int t = 0; t < 4; ++t) {
          p0[t] = __builtin_amdgcn_perm(xx[c][2 * t + 1], xx[c][2 * t], 0x05040100u);
          p1[t] = __builtin_amdgcn_perm(xx[c][2 * t + 1], xx[c][2 * t], 0x07060302u);
        }
        const int so = O_XG + (c * 16 * 2048 + stg * 4) * 16;
        st128(p0, rw, ov, so);
        st128(p1, rw, ov, so + 16384);
      }
      const bf16x2 ones = {(__bf16)1.0f, (__bf16)1.0f};
      float s1[8], s2[8];
#pragma unroll
      for (int t = 0; t < 8; ++t) {
        s1[t] = 0.f; s2[t] = 0.f;
#pragma unroll
        for (int c = 0; c < 5; ++c) {
          const bf16x2 v = __builtin_bit_cast(bf16x2, xx[c][t]);
          s1[t] = __builtin_amdgcn_fdot2_f32_bf16(v, ones, s1[t], false);
          s2[t] = __builtin_amdgcn_fdot2_f32_bf16(v, v, s2[t], false);
        }
      }
      float q1[2], q2[2];
#pragma unroll
      for (int e = 0; e < 2; ++e) {
        q1[e] = dpp_add<DPP_ROR8>(tred16(tred32(s1[e], s1[4 + e]), tred32(s1[2 + e], s1[6 + e])));
        q2[e] = dpp_add<DPP_ROR8>(tred16(tred32(s2[e], s2[4 + e]), tred32(s2[2 + e], s2[6 + e])));
      }
      const float mean = sum8((lane & 8) ? q1[1] : q1[0]) * (1.f / D_);
      const float var = fmaxf(sum8((lane & 8) ? q2[1] : q2[0]) * (1.f / D_) - mean * mean, 0.f);
      const float sd = rbf(__builtin_amdgcn_sqrtf(rbf(rbf(var) + 1e-5f)));
      const float rs = __builtin_amdgcn_rcpf(sd);
      const int s = stg * 8 + (lane >> 3);
      const int nt = s >> 8, col = (s >> 4) & 15, k4 = (s >> 2) & 3, rr = s & 3;
      if ((lane & 7) == 0) __builtin_amdgcn_raw_buffer_store_b64(u32x2{__float_as_uint(rs), __float_as_uint(-rbf(mean) * rs)}, rw,
                                                                 O_ST1 + (((nt * 2 + (rr >> 1)) * 64 + k4 * 16 + col) * 4 + (rr & 1) * 2) * 4, 0, SC1);
      STAMPD(9);
      if (F_POS == 2) wait_vm<11>();
      fcopies();
      wait_vm<0>();
      if (lane < 8) st_flag(ws, O_FX + (lane * 32 + (stg >> 3)) * 128 + (stg & 7) * 8, tag);
    } else if (wv < 2) {
      const int po[1] = {O_FK + b * 256 + wv * 128 + (lane & 7) * 16};
      poll<1, 0>(rw, po, tag);
      STAMPE(3);
      dma16x4<CKV_OFF + 2048>(sbase + wv * 8192, ko[0], ko[1], ws + O_KVG + b * 16384 + wv * 8192);
      dma16x4<CKV_OFF + 2048 + 4096>(sbase + wv * 8192, ko[0], ko[1], ws + O_KVG + b * 16384 + wv * 8192 + 4096);
    }
    f32x4 acc[8];
    u32x2 ra[2][8][2]; u32x4 rb[2][8];
    u32x4 pf[3]; u32 mraw; u64 nm[6];
    auto load_n = [&](auto E, auto N) {
      constexpr int e = E.value, n = N.value, bf = e & 1;
      if constexpr (n < 2) ds_b64<16 * e + 8 * n>(ra[bf][0][n], aAddr);
      else if constexpr (n < 10) ds_b128<2048 * (n - 2)>(rb[bf][n - 2], bA[e]);
      else ds_b64<512 * ((n - 8) >> 1) + 16 * e + 8 * (n & 1)>(ra[bf][(n - 8) >> 1][n & 1], aAddr);
    };
    u32x2 kvw[8], xw[8]; u32x4 sA[8], sB[8];
    float T[8][4], Hh[8][4], c0 = 0.f, c1 = 0.f;
    auto kvop = [&](auto K) {
      constexpr int k = K.value, nt = k >> 3, r = k & 3;
      if constexpr ((k & 4) == 0) T[nt][r] = (r & 1) ? v_lo(kvw[nt][1 - (r >> 1)]) : v_hi(kvw[nt][1 - (r >> 1)]);
      else T[nt][r] = v_mul(T[nt][r], Dv);
    };
    auto xrd = [&](auto K) {
      constexpr int k = K.value, nt = k / 3;
      if constexpr (k % 3 == 0) ds_b64<2048 * nt>(xw[nt], ex);
      else if constexpr (k % 3 == 1) ds_b128<2048 * nt>(sA[nt], es);
      else ds_b128<2048 * nt + 1024>(sB[nt], es);
    };
    float xf[8];
    auto xop = [&](auto K) {
      constexpr int k = K.value, nt = 2 * (k >> 5) + ((k >> 2) & 1), ph = (k >> 3) & 3, r = k & 3, i = k & 7;
      if constexpr (ph == 0) xf[i] = (r & 1) ? v_hi(xw[nt][r >> 1]) : v_lo(xw[nt][r >> 1]);
      else if constexpr (ph == 1) Hh[nt][r] = v_fma(xf[i], __uint_as_float((r < 2 ? sA[nt] : sB[nt])[2 * (r & 1)]), __uint_as_float((r < 2 ? sA[nt] : sB[nt])[2 * (r & 1) + 1]));
      else if constexpr (ph == 2) Hh[nt][r] = v_fma(Hh[nt][r], c1, c0);
      else T[nt][r] = v_fma(Hh[nt][r], T[nt][r], xf[i]);
    };
    u32 ms[3];
    auto mop = [&](auto K) {
      constexpr int k = K.value;
      if constexpr (k < 3) asm_dpp<k>(mraw);
      else if constexpr (k == 3) asm_op1<3>(mraw, mraw);
      else if constexpr (k < 7) asm_rdl<8 * (k - 4)>(ms[k - 4], mraw);
      else if constexpr (k == 7) asm_sop<0>(c0, ms[0], c0);
      else if constexpr (k == 8) asm_sop<1>(c1, ms[1], c1);
      else if constexpr (k == 9) asm_sop<2>(c0, ms[2], c0);
      else if constexpr (k == 10) { u32 t; asm_op1<3>(t, __float_as_uint(c1)); c1 = __uint_as_float(t); }
      else asm_sop<2>(c1, ms[2], c1);
    };
    clear_slot();
    wait_lgkm<0>();
    static_for<0, 2>([&](auto N) { load_n(IC<7>{}, N); });
    static_for<10, 24>([&](auto N) { load_n(IC<7>{}, N); });
    if (wv < 2) wait_vm<1>();
    STAMPD(10);
    bar();
    STAMPE(4);
    static_for<2, 10>([&](auto N) { load_n(IC<7>{}, N); });
    static_for<0, 8>([&](auto EE) {
      constexpr int e = 7 - EE.value, bf = e & 1;
      static_for<0, 8>([&](auto AA) {
        constexpr int a = AA.value;
        static_for<0, 8 - a>([&](auto BB) {
          constexpr int bb = BB.value, n = conv_gap(a, bb);
          if constexpr (e == 7 && n < 8) wait_lgkm<7>();
          mfma_<(e == 7 && a == 0)>(acc[a + bb], cat(ra[bf][a][0], ra[bf][a][1]), rb[bf][bb]);
          if constexpr (e > 0 && n < 24) load_n(IC<(e > 0 ? e - 1 : 0)>{}, IC<n>{});
          if constexpr (e == 7 && n >= 24 && n < 32) ds_b64<2048 * (n - 24)>(kvw[n - 24], ek);
          constexpr int g = 36 * (7 - e) + n;
          static_for<CS.rd[g], CS.rd[g + 1]>(xrd);
          if constexpr (e > 0 ? n == 35 : (n == CW1 || n == CW2)) wait_lgkm<0>();
          static_for<CS.op[g], CS.op[g + 1]>(xop);
          if constexpr ((e == 6 || e == 5) && n < 32) kvop(IC<(6 - e) * 32 + n>{});
          if constexpr (CV.kind[g] == 2) gld128<0>(pf[CV.idx[g]], pa1[CV.idx[g]], ws);
          if constexpr (CV.kind[g] == 3) { if constexpr (CV.idx[g] == 0) dma16<CX_OFF>(sbase + wv * 4096, ko[0], xgp); else dma16n<CV.idx[g] * 1024>(ko[CV.idx[g] & 1], xgp); }
          if constexpr (CV.kind[g] == 4) { if constexpr (CV.idx[g] == 0) dma16<CS_OFF>(sbase + wv * 4096, l16, stp); else dma16n<CV.idx[g] * 1024>(l16, stp); }
          if constexpr (CV.kind[g] == 5) bld32(mraw, mo, wsrd);
          if constexpr (g == G_XCHK) { STAMPD(11); wait_vm<cv_younger(g, 2, 2)>(); }
          if constexpr (g >= G_XCHK && g < G_XCHK + 3) { constexpr int i = g - G_XCHK; nm[2 * i] = v_ne64(u32x2{pf[i][0], pf[i][1]}, tag); nm[2 * i + 1] = v_ne64(u32x2{pf[i][2], pf[i][3]}, tag); }
          if constexpr (g == G_XCHK + 3) {
            if (__builtin_expect(((nm[0] | nm[1]) | (nm[2] | nm[3]) | (nm[4] | nm[5])) != 0, 0)) poll<3, 1>(rw, pa1, tag);
            STAMPD(12);
          }
          if constexpr (e == CBE && n == 8) wait_vm<cv_younger(g, 5, 0)>();
          if constexpr (e == CBE && n >= 9 && n < 21) mop(IC<n - 9>{});
          if constexpr (e == CBE && n == 23) { STAMPD(13); wait_vm<cv_younger(g, 4, 3)>(); bar(); STAMPD(14); }
        });
      });
    });
    STAMPE(5);
    asm volatile("s_nop 7");
    static_for<0, 4>([&](auto NP) {
      constexpr int np = NP.value;
      static_for<(CS.op[288] > 32 * np ? CS.op[288] : 32 * np), 32 * np + 32>(xop);
      u32x4 o;
#pragma unroll
      for (int u = 0; u < 2; ++u) {
        const int nt = 2 * np + u;
        pin(acc[nt]);
        o[2 * u] = pack_bf16(fmaf(Hh[nt][0], acc[nt][0], T[nt][0]), fmaf(Hh[nt][1], acc[nt][1], T[nt][1]));
        o[2 * u + 1] = pack_bf16(fmaf(Hh[nt][2], acc[nt][2], T[nt][2]), fmaf(Hh[nt][3], acc[nt][3], T[nt][3]));
      }
      st128(o, rw, l16 + np * 1024, O_X2T + d * (TP_ * 2));
    });
  }
  const int kg = lane >> 4, c16 = lane & 15;
  const int kq0 = nc >> 2, r0 = (nc & 1) * 4 + wv, sgb = O_SG + (mt * 2 + ((nc >> 1) & 1)) * 8192;
  u32x2 tag2 = {(u32)tag, (u32)(tag >> 32)};
  u32x4 gbr;
  pin(tag2);
  {
    const int fb = O_F2 + (b & 7) * 32 * 128;
    const int jw = (lane & 3) * 24 + ((nc * 12 + wv * 3 + ((lane >> 2) & 1) * 2) >> 3);
    const int pa[1] = {fb + (20 + (jw >> 3)) * 128 + (jw & 6) * 8};
    u32x4 pfa;
    gld128<0>(pfa, pa[0], ws);
    const char* wsrc = ws + O_W1F + (nc * 4 + wv) * W1_STEPS * 3072;
    const char* wq[4];
    unsigned xinc[4];
#pragma unroll
    for (int i = 0; i < 4; ++i) {
      wq[i] = wsrc + ((kq0 + i) & 3) * (5 * 3072) + 4096;
      xinc[i] = (i && kq0 + i == 4) ? (unsigned)(-19 * 32 * TP_ * 2) : 32 * TP_ * 2;
      asm volatile("" : "+s"(wq[i]), "+s"(xinc[i]));
    }
    u32x4 W[G1_R][3], WA_[24], WC[3], pfs[2], pfw, gran;
    auto issue_w = [&](auto I) { constexpr int i = I.value; gld128b<(((i % 15) & 7) - 4) * 1024>(W[(i / 3) % G1_R][i % 3], ((i % 15) & 8) ? l16b : l16, wq[i / 15]); };
    int pc[2] = {fb + (lane >> 2) * 128 + (lane & 3) * 16, fb + (16 + (lane >> 2)) * 128 + (lane & 3) * 16};
    int f2o = lane < 8 ? O_F2 + (lane * 32 + (b >> 3)) * 128 + (b & 7) * 8 : WS_BYTES;
    pin(pc[0]); pin(pc[1]); pin(f2o);
    STAMPE(6);
    asm volatile("s_waitcnt vmcnt(1) lgkmcnt(0)\n\ts_barrier" ::: "memory");
    if (wv == 0) __builtin_amdgcn_raw_buffer_store_b64(tag2, rw, f2o, 0, SC1);
    STAMPE(7);
    if (bs >= CWG) {
      const int j = bs - CWG;
      u32x2 dl[5], dh[5];
      const unsigned add = sbase + AD_OFF + (8 * kg + (c16 >> 2)) * 128 + (lane & 3) * 8;
#pragma unroll
      for (int i = 0; i < 5; ++i) { const int tl = wv * 5 + i; const unsigned a2 = add + (tl >> 2) * 4096 + (tl & 3) * 32; ds_tr<0>(dl[i], a2); ds_tr<512>(dh[i], a2); }
      wait_lgkm<0>();
#pragma unroll
      for (int i = 0; i < 5; ++i) {
        pin(dl[i]); pin(dh[i]);
        __builtin_amdgcn_raw_buffer_store_b128(cat(dl[i], dh[i]), rw, l16, O_W2F + ((((j & 1) * 4 + wv) * 48 + (j >> 1)) * 5 + i) * 1024, SC1);
      }
      wait_vm<5>();
    } else {
      if (wv == 0) wait_vm<1>(); else wait_vm<0>();
    }
    pin(pfa);
    if (!__all(tag_ok(pfa, tag))) poll<1, 2>(rw, pa, tag);
    auto issue_a = [&](auto I) { constexpr int i = 36 + I.value; gld128a<(((i % 15) & 7) - 4) * 1024>(WA_[I.value], ((i % 15) & 8) ? l16b : l16, wq[i / 15]); };
    static_for<0, 3 * G1_WPRE>(issue_w);
    unsigned xoff[2];
#pragma unroll
    for (int p = 0; p < 2; ++p) {
      const int k = 4 * p + kg;
      xoff[p] = (k * TP_ + ((c16 ^ (2 * k)) * 8)) * 2 - p * 1024;
      pin(xoff[p]);
    }
    unsigned xso = O_X2T + ((kq0 * 160 - 32 + 8 * wv) * TP_ + mt * 128) * 2;
    const unsigned ldsw = sbase + wv * 2048;
    unsigned sgl = sgb + wv * 2048 + l16;
    int hvo = kg < 3 ? ((3 * wv + kg) >> 2) * 1024 + ((3 * wv + kg) & 3) * 256 + c16 * 16 : WS_BYTES;
    const int hso = O_H2F + (mt * 8 * 48 + 3 * nc) * 1024;
    unsigned spa = sbase + G1_SP + c16 * 16;
    int sgo = (c16 >> 2) == kg ? sgb + (r0 * 4 + kq0) * 256 + c16 * 16 : WS_BYTES;
    pin(sgl); pin(hvo); pin(sgo); pin(spa);
    int pw[1] = {O_FW + ((b & 7) * 12 + (lane < 48 ? lane >> 2 : 0)) * 128 + (lane & 3) * 16};
    unsigned xo = ((lane >> 3) * TP_ + (lane & 7) * 8) * 2;
    const char* xs = ws + O_X2T + (N * 80 * TP_ + M * 64) * 2;
    unsigned gbo = O_MODP + (((lane & 7) < MODP ? (lane & 7) : 0) * NMOD + 5 * D_ + N * 80 + wv * 20 + (lane < 40 ? lane >> 3 : 0) * 4) * 4;
    pin(pw[0]); pin(xo); pin(gbo);
    u32 hq[4]; u32x4 hs, H[G2_R], WV[23], pf3, ob;
    int po3 = O_F3 + (mt * 16 + nc) * 512 + wv * 32 + (lane & 1) * 16;
    int f3o = tid < 16 ? O_F3 + (mt * 16 + tid) * 512 + nc * 8 : WS_BYTES;
    int oo;
    int fwo = (bs >= CWG && tid < 8) ? O_FW + (tid * 12 + ((bs - CWG) >> 3)) * 128 + (bs & 7) * 8 : WS_BYTES;
    pin(po3); pin(f3o); pin(fwo);
    const char* hp = ws + O_H2F + (M * 4 * 48 + wv * 12) * 1024 + 4096;
    u16* outp;
    asm volatile("s_load_dwordx2 %0, %1, 0x60\n\ts_waitcnt lgkmcnt(0)" : "=s"(outp) : "s"(__builtin_amdgcn_kernarg_segment_ptr()));
    const rsrc_t ro = mk_rsrc(outp, S_ * D_ * 2);
    auto issue = [&](auto O) {
      constexpr int kind = GS.kind[O.value], i = GS.idx[O.value];
      if constexpr (kind == 0) {
        if constexpr (i & 1) xdma1(xoff[1], wsrd, xso);
        else xdma0<g1_slot(i >> 1) * 8192>(ldsw, xoff[0], wsrd, xso, xinc[(i % 10) ? 0 : i / 10]);
      }
      else if constexpr (kind == 1) issue_w(IC<i>{});
      else if constexpr (kind == 2) {
        if constexpr (i >= 37 && O.value < GS.first[160]) gld128b<((i & 7) - 4) * 1024>(WV[i - 37], (i & 8) ? l16b : l16, wdp + (i >> 4) * 16384);
        else issue_wd(IC<i>{});
      }
      else if constexpr (kind == 3) {
        if constexpr (i < 2) dma16<XR_OFF + i * 4160>(sbase + wv * 1040, xo, xs + (size_t)(wv + 4 * i) * 8 * TP_ * 2);
        else if constexpr (i == 2) dma16<XR_OFF + 8320>(sbase + (wv & 1) * 1040, xo, xs + (size_t)((wv & 1) + 8) * 8 * TP_ * 2);
        else gld128<0>(gbr, gbo, ws);
      }
      else if constexpr (kind == 4) gld128<0>(pfw, pw[0], ws);
      else if constexpr (kind == 5) __builtin_amdgcn_raw_buffer_store_b128(gran, rw, sgo, 0, SC1);
      else if constexpr (kind == 6) gld128<i * 1024>(pfs[i], sgl, ws);
      else if constexpr (kind == 7) { hs = u32x4{hq[0], hq[1], hq[2], hq[3]}; __builtin_amdgcn_raw_buffer_store_b128(hs, rw, hvo, hso + g1_ord(i) * (48 * 1024), SC1); }
      else if constexpr (kind == 8) issue_a(IC<i>{});
      else if constexpr (kind == 9) __builtin_amdgcn_raw_buffer_store_b64(tag2, rw, f3o + i * 128, 0, SC1);
      else if constexpr (kind == 10) gld128<i * 128>(pf3, po3, ws);
      else if constexpr (kind == 11) gld128b<(((i % 12) & 7) - 4) * 1024>(H[i % G2_R], ((i % 12) & 8) ? l16b : l16, hp + (i / 12) * (48 * 1024));
      else if constexpr (kind == 12) __builtin_amdgcn_raw_buffer_store_b128(ob, ro, oo, i * (32 * D_ * 2), 0);
      else __builtin_amdgcn_raw_buffer_store_b64(tag2, rw, fwo, 0, SC1);
    };
    auto check3 = [&](auto ST) {
      if (__builtin_expect((v_ne64(u32x2{pf3[0], pf3[1]}, tag) | v_ne64(u32x2{pf3[2], pf3[3]}, tag)) != 0, 0)) {
        const int po[1] = {po3 + ST.value * 128};
        poll<1, 0>(rw, po, tag);
      }
    };
    unsigned xa[9], xm[8];
#pragma unroll
    for (int jj = 0; jj < 9; ++jj) {
      const int rr = 4 * kg + (c16 >> 2);
      xa[jj] = sbase + rr * 256 + (((jj < 8 ? jj : r0) ^ (rr & 7)) * 32) + (lane & 3) * 8;
      pin(xa[jj]);
    }
    u32x4 ones = {0x3f803f80u, 0x3f803f80u, 0x3f803f80u, 0x3f803f80u};
    pin(ones);
    static_for<0, 3>([&](auto I) { gld128a<I.value * 1024>(WC[I.value], l16, wsrc + 20 * 3072); });
    static_for<0, WA - PLATE>(issue_a);
    if (wv == 0) { gld128<0>(pfs[0], pc[0], ws); gld128<0>(pfs[1], pc[1], ws); }
    static_for<WA - PLATE, WA>(issue_a);
    STAMPL(8);
    if (wv == 0) {
      wait_vm<PLATE>();
      pin(pfs[0]); pin(pfs[1]);
      if (!__all(tag_ok(pfs[0], tag) && tag_ok(pfs[1], tag))) poll<2, PSL>(rw, pc, tag);
    } else {
      wait_vm<3 * G1_WPRE + 3 + WA>();
    }
    bar();
    STAMPL(9);
    static_for<0, GS.first[0]>(issue);
    f32x4 acc[8][3];
    u32x2 X[G1_LA + 1][2], XS[5][2];
    auto xread = [&](auto T, auto HH) {
      constexpr int t = T.value, h = HH.value;
      if constexpr (t < 96) ds_tr<g1_slot(g1_ks(t)) * 8192 + h * 4096>(X[t % (G1_LA + 1)][h], xa[g1_j(t)]);
      else ds_tr<(g1_ks(t) - 12) * 8192 + h * 4096>(X[t % (G1_LA + 1)][h], xm[g1_j(t)]);
    };
    f32x4 gram, sum1;
    u32 fs[4], fa; u64 sbad;
    unsigned spw = sbase + G1_SP + (2 * wv + (kg >> 1)) * 256 + c16 * 16;
    pin(spw);
    auto fop = [&](auto K) {
      constexpr int k = K.value;
      if constexpr (k == 0) asm_and3(fa, pfs[0][1], pfs[0][3], pfs[1][1]);
      else if constexpr (k == 1) asm_and3(fa, fa, pfs[1][3], pfs[1][3]);
      else if constexpr (k == 2) asm_ne1(sbad, fa);
      else if constexpr (k == 4) v_swap32(fs[0], fs[1]);
      else if constexpr (k == 5) v_swap32(fs[2], fs[3]);
      else if constexpr (k == 6) asm_op2<0>(fs[0], fs[0], fs[1]);
      else if constexpr (k == 7) asm_op2<0>(fs[2], fs[2], fs[3]);
      else if constexpr (k == 8) v_swap16(fs[0], fs[2]);
      else if constexpr (k == 9) asm_op2<0>(fs[0], fs[0], fs[2]);
      else if constexpr (k == 10) asm_op1<0>(fs[0], fs[0]);
      else if constexpr (k == 11) asm_op1<1>(fs[1], fs[0]);
      else if constexpr (k == 12) v_swap16(fs[0], fs[1]);
      else if constexpr (k == 13) asm_op2<1>(fs[1], fs[0], fs[1]);
      else if constexpr (k == 14) asm_op1<2>(fs[1], fs[1]);
      else if constexpr (k == 15) asm_op1<3>(fs[1], fs[1]);
      else if constexpr (k == 16) asm_op1<4>(fs[1], fs[1]);
      else if constexpr (k == 17) asm_op1<3>(fs[1], fs[1]);
      else if constexpr (k == 18) asm_op1<5>(fs[1], fs[1]);
      else if constexpr (k == 19) asm_op2<2>(fs[0], fs[0], fs[1]);
      else if constexpr (k == 20) asm_op1<1>(fs[1], fs[0]);
      else if constexpr (k == 21) asm_op1<1>(fs[2], fs[0]);
      else if constexpr (k == 22) asm_op1<6>(fs[3], fs[0]);
      else if constexpr (k == 23) ds_w128u<0>(spw, u32x4{fs[0], fs[1], fs[2], fs[3]});
    };
    float eg[6]; u32x4 xc;
    auto eop = [&](auto J, auto K) {
      constexpr int j = J.value, k = K.value;
      if constexpr (k < 15) {
        if constexpr (k < 6) eg[k] = v_relu(acc[j][k >> 1][2 * (k & 1)]);
        else if constexpr (k < 12) eg[k - 6] = v_mul(eg[k - 6], acc[j][(k - 6) >> 1][2 * (k & 1) + 1]);
        else hq[k == 12 ? 1 : k == 13 ? 0 : 2] = v_cvt(eg[k == 12 ? 2 : k == 13 ? 0 : 4], eg[k == 12 ? 3 : k == 13 ? 1 : 5]);
      } else if constexpr (k == 15) v_swap32(hq[1], hq[3]);
      else if constexpr (k == 16) v_swap32(hq[0], hq[2]);
      else if constexpr (k == 17) v_swap16(hq[0], hq[1]);
      else v_swap16(hq[2], hq[3]);
    };
    unsigned pwa, pra, gba, xad, sda;
    float xr[8], sdv, G[8], S[8], rs2;
    hq[3] = 0;
    pin(hq[3]);
    wait_vm<GS.first[0] - 2>();
    bar();
    STAMPL(10);
    static_for<0, G1_LA>([&](auto T) { xread(T, IC<0>{}); xread(T, IC<1>{}); });
    static_for<0, 160>([&](auto T) {
      constexpr int t = T.value, ks = g1_ks(t), jj = g1_j(t), tn = t + G1_LA, u = t & 7;
      if constexpr (t == 24) STAMPG(1);
      if constexpr (t == 48) STAMPG(2);
      if constexpr (t == 72) STAMPG(3);
      if constexpr (t == 98) STAMPG(4);
      if constexpr (t == 128) STAMPG(5);
      if constexpr (!(t & 1)) wait_lgkm<2 * ((tn <= 160 ? tn : 160) - t - 2)>();
      if constexpr (g1_need(t) >= 0) {
        static_assert(g1_need(t) < GS.first[t], "not issued yet");
        if constexpr (GS.first[t] - 1 - g1_need(t) < 63) wait_vm<GS.first[t] - 1 - g1_need(t)>();
        bar();
      }
      if constexpr (t == T_WCHK) {
        if (__builtin_expect((v_ne64(u32x2{pfw[0], pfw[1]}, tag) | v_ne64(u32x2{pfw[2], pfw[3]}, tag)) != 0, 0)) poll<1, 1>(rw, pw, tag);
      }
      if constexpr (t == T_C0) { wait_vm<GS.first[t] - 1 - g_pos(10, 0)>(); check3(IC<0>{}); }
      if constexpr (t == T_SETUP) {
        static_assert(g_pos(3, 3) < g1_need(T_F0) && T_SETUP > T_F0, "gate partials landed");
        {
          pin(gbr);
          const float g = sum8((lane & 7) < MODP ? __uint_as_float(gbr[0]) : 0.f), g2 = sum8((lane & 7) < MODP ? __uint_as_float(gbr[1]) : 0.f);
          const float g3 = sum8((lane & 7) < MODP ? __uint_as_float(gbr[2]) : 0.f), g4 = sum8((lane & 7) < MODP ? __uint_as_float(gbr[3]) : 0.f);
          if ((lane & 7) == 0 && lane < 40) *(f32x4*)(smem + GB_OFF + wv * 80 + (lane >> 3) * 16) = f32x4{rbf(g), rbf(g2), rbf(g3), rbf(g4)};
        }
        const int lf = lane < 40 ? lane : 0, ft = 4 * wv + lf / 10, fc = lf % 10;
        pwa = sbase + wv * 5440 + l16 + (kg >> 1) * 32;
        pra = sbase + fc * 544 + ft * 16; gba = sbase + GB_OFF + fc * 32;
        xad = sbase + XR_OFF + fc * 1040 + ft * 2; sda = sbase + G1_SP + ((nc & 4) * 16 + ft) * 16 + 2;
        {
          const int pos = M * 64 + ft, l8 = (pos >> 3) & 63;
          const int tok = 256 * (2 * (pos >> 9) + ((pos >> 2) & 1)) + 16 * (l8 & 15) + 4 * (l8 >> 4) + (pos & 3);
          oo = lane < 40 ? (tok * D_ + N * 80 + 8 * fc) * 2 : S_ * D_ * 2;
        }
        sdv = 0.f;
#pragma unroll
        for (int e = 0; e < 8; ++e) { xr[e] = 0.f; pin(xr[e]); }
        pin(pwa); pin(pra); pin(gba); pin(xad); pin(sda); pin(oo); pin(sdv);
      }
      if constexpr (t == T_SST - 2) {
        pin(gram); pin(sum1);
        const int c3 = lane & 3;
        gran = u32x4{__float_as_uint(sum1[0]), 1u, __float_as_uint(c3 == 0 ? gram[0] : c3 == 1 ? gram[1] : c3 == 2 ? gram[2] : gram[3]), 1u};
        pin(gran);
      }
      if constexpr (t == 86) {
        if (__builtin_expect(sbad != 0, 0)) {
#pragma unroll 1
          for (int spn = 0; spn < SPIN_CAP; ++spn) {
            pfs[0] = __builtin_amdgcn_raw_buffer_load_b128(rw, sgl, 0, SC1);
            pfs[1] = __builtin_amdgcn_raw_buffer_load_b128(rw, sgl + 1024, 0, SC1);
            if (__all((pfs[0][1] & pfs[0][3] & pfs[1][1] & pfs[1][3]) == 1u)) break;
            asm volatile("" ::: "memory");
          }
        }
        fs[0] = pfs[0][0]; fs[1] = pfs[1][0]; fs[2] = pfs[0][2]; fs[3] = pfs[1][2];
      }
      auto ep = [&](auto A, auto B) { if constexpr (t >= 104) static_for<A.value, (B.value < 19 ? B.value : 19)>([&](auto K) { eop(IC<g1_j(t - 8)>{}, K); }); };
      constexpr int k0 = u == 0 ? 0 : 4 * u - 3;
      constexpr int sr = t == 0 ? 0 : t == 4 ? 1 : (t >= 12 && t < 15) ? t - 10 : -1, sm = t == 4 ? 0 : t == 8 ? 1 : (t >= 16 && t < 19) ? t - 14 : -1;
      const u32x4 xb = cat(X[t % (G1_LA + 1)][0], X[t % (G1_LA + 1)][1]);
      if constexpr (ks >= 12) mfma_a<false>(acc[jj][0], WA_[3 * (ks - 12) + 0], xb); else mfma_<ks == 0>(acc[jj][0], W[ks % G1_R][0], xb);
      if constexpr (tn < 160) xread(IC<tn>{}, IC<0>{});
      if constexpr (t >= 84 && t < 96) fop(IC<2 * (t - 84)>{});
      if constexpr (u > 0) ep(IC<k0>{}, IC<k0 + 1>{});
      if constexpr (ks >= 12) mfma_a<false>(acc[jj][1], WA_[3 * (ks - 12) + 1], xb); else mfma_<ks == 0>(acc[jj][1], W[ks % G1_R][1], xb);
      if constexpr (tn < 160) xread(IC<tn>{}, IC<1>{});
      if constexpr (t >= 84 && t < 96) fop(IC<2 * (t - 84) + 1>{});
      if constexpr (u > 0) ep(IC<k0 + 1>{}, IC<k0 + 2>{}); else ep(IC<0>{}, IC<1>{});
      if constexpr (ks >= 12) mfma_a<false>(acc[jj][2], WA_[3 * (ks - 12) + 2], xb); else mfma_<ks == 0>(acc[jj][2], W[ks % G1_R][2], xb);
      if constexpr (t >= 96) real();
      static_for<GS.first[t], GS.first[t + 1]>(issue);
      if constexpr (u > 0) ep(IC<k0 + 2>{}, IC<k0 + 4>{});
      if constexpr (t >= 84 && t < 92) xm[t - 84] = v_addc(xa[t - 84], G1_RS * 8192);
      if constexpr (sm >= 0) { mfma_<sm == 0>(gram, cat(XS[sm][0], XS[sm][1]), cat(XS[sm][0], XS[sm][1])); mfma_<sm == 0>(sum1, ones, cat(XS[sm][0], XS[sm][1])); }
      if constexpr (sr >= 0) { ds_tr<sr * 8192>(XS[sr][0], xa[8]); ds_tr<sr * 8192 + 4096>(XS[sr][1], xa[8]); }
      if constexpr (t >= 96 && u == (t < 104 ? 4 : 0)) ds_b128<jj * 256>(xc, spa);
      if constexpr (t >= 96 && u == 7) {
        if constexpr (t < 104) wait_lgkm<6>();
        mfma_a<false>(acc[jj][0], WC[0], xc);
        mfma_a<false>(acc[jj][1], WC[1], xc);
        mfma_a<false>(acc[jj][2], WC[2], xc);
      }
    });
    STAMPL(11);
    asm volatile("s_nop 5");
    static_for<0, 15>([&](auto K) { eop(IC<7>{}, K); });
    eop(IC<7>{}, IC<15>{}); asm volatile("s_nop 0"); eop(IC<7>{}, IC<16>{}); asm volatile("s_nop 1"); eop(IC<7>{}, IC<17>{}); eop(IC<7>{}, IC<18>{});
    static_for<GS.first[160], GS.mid>(issue);
    STAMPL(12);
    if constexpr (T_C0 >= 160) { wait_vm<GS.mid - 1 - g_pos(10, 0)>(); check3(IC<0>{}); }
    static_for<GS.mid, GS.first2[0]>(issue);
    if constexpr (g_lastdv() >= 0) wait_vm<GS.first2[0] - 1 - g_lastdv()>();
    static_for<37, 60>([&](auto I) { if constexpr (g_pos(2, I.value) < GS.first[160]) to_acc(WD[I.value], WV[I.value - 37]); });
    f32x4 gbv[2], ac2[2][5], pt[8];
    auto pre = [&](auto J, auto K) {
      constexpr int j = J.value, k = K.value;
      if constexpr (k == 0) ds_hi16<j * 256>(sdv, sda);
      else if constexpr (k < 9) ds_hi16<(k - 1) * 128 + j * 32>(xr[k - 1], xad);
      else if constexpr (k == 9) rs2 = v_rcp(sdv);
      else if constexpr (k == 10) rs2 = v_mul(rs2, rs2);
      else G[k - 11] = v_mul(gbv[(k - 11) >> 2][(k - 11) & 3], rs2);
    };
    auto fin = [&](auto K) {
      constexpr int k = K.value, e = k & 7;
      if constexpr (k < 8) S[e] = v_add(pt[e >> 2][e & 3], pt[2 + (e >> 2)][e & 3]);
      else if constexpr (k < 24) S[e] = v_add(S[e], pt[2 * (k >> 3) + 2 + (e >> 2)][e & 3]);
      else if constexpr (k < 32) S[e] = v_fma(G[e], S[e], xr[e]);
      else ob[k - 32] = v_cvt(S[2 * (k - 32)], S[2 * (k - 32) + 1]);
    };
    auto prd = [&](auto J, auto K) { ds_f128<(J.value & 1) * 21760 + (K.value >> 1) * 5440 + (K.value & 1) * 256>(pt[K.value], pra); };
    STAMPL(13);
    static_for<0, 240>([&](auto GG) {
      constexpr int g = GG.value, o = g / 5, jj = o / 12, ks = o % 12, i = g % 5, q = g % 60;
      if constexpr (g == 60) STAMPG(6);
      if constexpr (g == 120) STAMPG(7);
      if constexpr (i == 0) { static_assert(g2_need(o) < GS.first2[g], "not issued yet"); wait_vm<GS.first2[g] - 1 - g2_need(o)>(); }
      if constexpr (g == G_F3) { wait_vm<GS.first2[g] - 1 - g_pos(7, 7)>(); bar(); }
      if constexpr (g == G_C1) { wait_vm<GS.first2[g] - 1 - g_pos(10, 1)>(); check3(IC<1>{}); }
      if constexpr (g == G_C2) { wait_vm<GS.first2[g] - 1 - g_pos(10, 2)>(); check3(IC<2>{}); }
      if constexpr (g == G_C3) { wait_vm<GS.first2[g] - 1 - g_pos(10, 3)>(); check3(IC<3>{}); }
      mfma_a<ks == 0>(ac2[jj & 1][i], WD[ks * 5 + i], H[o % G2_R]);
      if constexpr (i == 4) real();
      static_for<GS.first2[g], GS.first2[g + 1]>(issue);
      if constexpr (g == 30 || g == 32) ds_f128<(g - 30) * 8>(gbv[(g - 30) / 2], gba);
      if constexpr (jj >= 1) {
        if constexpr (q >= 5 && q < 15 && (q & 1)) ds_w128<((jj - 1) & 1) * 21760 + ((q - 5) / 2) * 1088>(pwa, ac2[(jj - 1) & 1][(q - 5) / 2]);
        if constexpr (q == 18) { wait_lgkm<0>(); bar(); }
        if constexpr (q >= 19 && q < 27) prd(IC<jj - 1>{}, IC<q - 19>{});
        if constexpr (q == 30) wait_lgkm<0>();
        if constexpr (q >= 31 && q < 31 + FINW) static_for<(36 * (q - 31) + FINW - 1) / FINW, (36 * (q - 30) + FINW - 1) / FINW>(fin);
      }
      if constexpr (jj >= 1 && q < 18 && !(q & 1)) pre(IC<jj - 1>{}, IC<q / 2>{});
      if constexpr (jj >= 1 && q >= 19 && q < 29) pre(IC<jj - 1>{}, IC<q - 10>{});
    });
    STAMPL(14);
    static_for<0, 9>([&](auto K) { pre(IC<3>{}, K); });
    static_for<0, 5>([&](auto I) { ds_w128<21760 + I.value * 1088>(pwa, ac2[1][I.value]); });
    wait_lgkm<5>();
    pre(IC<3>{}, IC<9>{}); asm volatile("s_nop 0");
    static_for<10, 19>([&](auto K) { pre(IC<3>{}, K); });
    wait_lgkm<0>(); bar();
    static_for<0, 8>([&](auto K) { prd(IC<3>{}, K); });
    wait_lgkm<4>();
    static_for<0, 8>(fin);
    wait_lgkm<0>();
    static_for<8, 36>(fin);
    __builtin_amdgcn_raw_buffer_store_b128(ob, ro, oo, 3 * (32 * D_ * 2), 0);
  }
  if (b == 0 && tid == 0) st_flag(ws, O_EPOCH, tag);
  STAMP_END();
}

extern "C" void kernel_launch(void* const* d_in, const int* in_sizes, int n_in, void* d_out, int out_size,
                              void* d_ws, size_t ws_size, hipStream_t stream) {
  k_fused<<<256, 256, 0, stream>>>((const u16*)d_in[0], (const u16*)d_in[1], (const u16*)d_in[2], (const u16*)d_in[3],
                                   (const u16*)d_in[4], (const u16*)d_in[5], (const u16*)d_in[7], (const u16*)d_in[8],
                                   (const u16*)d_in[9], (const u16*)d_in[10], (const u16*)d_in[11], (char*)d_ws, (u16*)d_out);
}
```

```cpp
#include <hip/hip_runtime.h>
#include <stdint.h>

typedef unsigned short u16;
typedef unsigned int u32;
typedef __attribute__((ext_vector_type(2))) __bf16 bf16x2;
typedef __attribute__((ext_vector_type(4))) float f32x4;
typedef __attribute__((ext_vector_type(2))) float f32x2;
typedef __attribute__((ext_vector_type(4))) u32 u32x4;
typedef __attribute__((ext_vector_type(2))) u32 u32x2;

constexpr int S_ = 2048, D_ = 640, F_ = 1536;
constexpr int TP_ = S_ + 64;
constexpr int NMOD = 6 * D_;
constexpr int MODP = 5;
constexpr int CWG = 160;

__device__ __forceinline__ float bflo(u32 w) { return __uint_as_float(w << 16); }
__device__ __forceinline__ float bfhi(u32 w) { return __uint_as_float(w & 0xffff0000u); }
__device__ __forceinline__ u32 pack_bf16(float lo, float hi) {
  bf16x2 v; v[0] = (__bf16)lo; v[1] = (__bf16)hi; return __builtin_bit_cast(u32, v);
}
__device__ __forceinline__ float rbf(float x) { __bf16 b = (__bf16)x; return (float)b; }
#define SCHED() __builtin_amdgcn_sched_barrier(0)
template <int V> struct IC { static constexpr int value = V; };
template <int B, int E, class Fn> __device__ __forceinline__ void static_for(Fn&& f) {
  if constexpr (B < E) { f(IC<B>{}); static_for<B + 1, E>(f); }
}
__device__ __forceinline__ void mfma(f32x4& c, u32x4 a, u32x4 b) {
  asm volatile("v_mfma_f32_16x16x32_bf16 %0, %1, %2, %0" : "+v"(c) : "v"(a), "v"(b));
}
__device__ __forceinline__ void mfma0(f32x4& c, u32x4 a, u32x4 b) {
  asm volatile("v_mfma_f32_16x16x32_bf16 %0, %1, %2, 0" : "=&v"(c) : "v"(a), "v"(b));
}
template <bool FIRST> __device__ __forceinline__ void mfma_(f32x4& c, u32x4 a, u32x4 b) { if constexpr (FIRST) mfma0(c, a, b); else mfma(c, a, b); }
template <int OFF> __device__ __forceinline__ void ds_tr(u32x2& dst, unsigned addr) {
  asm volatile("ds_read_b64_tr_b16 %0, %1 offset:%2" : "=v"(dst) : "v"(addr), "i"(OFF) : "memory");
}
template <int OFF> __device__ __forceinline__ void ds_b32(u32& dst, unsigned addr) {
  asm volatile("ds_read_b32 %0, %1 offset:%2" : "=v"(dst) : "v"(addr), "i"(OFF) : "memory");
}
template <int OFF> __device__ __forceinline__ void ds_b64(u32x2& dst, unsigned addr) {
  asm volatile("ds_read_b64 %0, %1 offset:%2" : "=v"(dst) : "v"(addr), "i"(OFF) : "memory");
}
template <int OFF> __device__ __forceinline__ void ds_b128(u32x4& dst, unsigned addr) {
  asm volatile("ds_read_b128 %0, %1 offset:%2" : "=v"(dst) : "v"(addr), "i"(OFF) : "memory");
}
template <int OFF> __device__ __forceinline__ void ds_w128(unsigned addr, f32x4 v) {
  asm volatile("ds_write_b128 %0, %1 offset:%2" :: "v"(addr), "v"(v), "i"(OFF) : "memory");
}
template <int OFF> __device__ __forceinline__ void gld128(u32x4& dst, unsigned voff, const char* sb) {
  asm volatile("global_load_dwordx4 %0, %1, %2 offset:%3 sc1" : "=v"(dst) : "v"(voff), "s"(sb), "i"(OFF) : "memory");
}
template <int LOFF> __device__ __forceinline__ void dma16(unsigned ldsb, unsigned voff, const char* sb) {
  asm volatile("s_add_u32 m0, %0, %3\n\ts_nop 0\n\tglobal_load_lds_dwordx4 %1, %2 sc1" :: "s"(ldsb), "v"(voff), "s"(sb), "i"(LOFF) : "memory", "scc");
}
template <int LOFF> __device__ __forceinline__ void dma16x4(unsigned ldsb, unsigned v0, unsigned v1, const char* sb) {
  asm volatile("s_add_u32 m0, %0, %4\n\ts_nop 0\n\tglobal_load_lds_dwordx4 %1, %3 sc1\n\tglobal_load_lds_dwordx4 %2, %3 offset:1024 sc1\n\t"
               "global_load_lds_dwordx4 %1, %3 offset:2048 sc1\n\tglobal_load_lds_dwordx4 %2, %3 offset:3072 sc1"
               :: "s"(ldsb), "v"(v0), "v"(v1), "s"(sb), "i"(LOFF) : "memory", "scc");
}
template <int N> __device__ __forceinline__ void wait_vm() { asm volatile("s_waitcnt vmcnt(%0)" :: "i"(N) : "memory"); }
template <int N> __device__ __forceinline__ void wait_lgkm() { asm volatile("s_waitcnt lgkmcnt(%0)" :: "i"(N) : "memory"); }
__device__ __forceinline__ void real() { __builtin_amdgcn_s_setprio(0); }
__device__ __forceinline__ void bar() { asm volatile("s_barrier" ::: "memory"); }
template <class T> __device__ __forceinline__ void pin(T& a) { asm volatile("" : "+v"(a)); }
__device__ __forceinline__ u32x4 cat(u32x2 a, u32x2 b) { return u32x4{a[0], a[1], b[0], b[1]}; }

typedef __amdgpu_buffer_rsrc_t rsrc_t;
__device__ __forceinline__ rsrc_t mk_rsrc(const void* p, int bytes) { return __builtin_amdgcn_make_buffer_rsrc((void*)p, 0, bytes, 0x00020000); }
template <int CTRL> __device__ __forceinline__ float dpp_add(float x) {
  return x + __builtin_bit_cast(float, __builtin_amdgcn_mov_dpp(__builtin_bit_cast(int, x), CTRL, 0xf, 0xf, true));
}
constexpr int DPP_XOR1 = 0xB1, DPP_XOR2 = 0x4E, DPP_HMIRROR = 0x141, DPP_ROR8 = 0x128;
__device__ __forceinline__ float sum8(float x) { return dpp_add<DPP_HMIRROR>(dpp_add<DPP_XOR2>(dpp_add<DPP_XOR1>(x))); }
__device__ __forceinline__ float tred32(float a, float b) {
  auto r = __builtin_amdgcn_permlane32_swap(__float_as_uint(a), __float_as_uint(b), false, false);
  return __uint_as_float(r[0]) + __uint_as_float(r[1]);
}
__device__ __forceinline__ float tred16(float a, float b) {
  auto r = __builtin_amdgcn_permlane16_swap(__float_as_uint(a), __float_as_uint(b), false, false);
  return __uint_as_float(r[0]) + __uint_as_float(r[1]);
}

typedef unsigned long long u64;
#define GAS __attribute__((address_space(1)))
constexpr int SC1 = 16;
#define BS " sc1"
constexpr int A0_DELAY = 16;
constexpr int AU_DELAY = 16;
constexpr int PE = 4;
constexpr int NT = 0;
template <int OFF> __device__ __forceinline__ void gld128b(u32x4& dst, unsigned voff, const char* sb) {
  asm volatile("global_load_dwordx4 %0, %1, %2 offset:%3" BS : "=v"(dst) : "v"(voff), "s"(sb), "i"(OFF) : "memory");
}
template <int OFF> __device__ __forceinline__ void gld128a(u32x4& dst, unsigned voff, const char* sb) {
  asm volatile("global_load_dwordx4 %0, %1, %2 offset:%3" BS : "=a"(dst) : "v"(voff), "s"(sb), "i"(OFF) : "memory");
}
template <bool FIRST> __device__ __forceinline__ void mfma_a(f32x4& c, u32x4 a, u32x4 b) {
  if constexpr (FIRST) asm volatile("v_mfma_f32_16x16x32_bf16 %0, %1, %2, 0" : "=&v"(c) : "a"(a), "v"(b));
  else asm volatile("v_mfma_f32_16x16x32_bf16 %0, %1, %2, %0" : "+v"(c) : "a"(a), "v"(b));
}
typedef __attribute__((ext_vector_type(8))) __bf16 bf16x8;
__device__ __forceinline__ f32x4 mfma_b(u32x4 a, u32x4 b, f32x4 c) {
  return __builtin_amdgcn_mfma_f32_16x16x32_bf16(__builtin_bit_cast(bf16x8, a), __builtin_bit_cast(bf16x8, b), c, 0, 0, 0);
}
template <int LOFF> __device__ __forceinline__ void dma16p(unsigned ldsb, unsigned voff, const char* sb) {
  asm volatile("s_add_u32 m0, %0, %3\n\ts_nop 0\n\tglobal_load_lds_dwordx4 %1, %2" :: "s"(ldsb), "v"(voff), "s"(sb), "i"(LOFF) : "memory", "scc");
}
template <int LOFF> __device__ __forceinline__ void dma16x4p(unsigned ldsb, unsigned v0, const char* sb) {
  asm volatile("s_add_u32 m0, %0, %3\n\ts_nop 0\n\tglobal_load_lds_dwordx4 %1, %2\n\tglobal_load_lds_dwordx4 %1, %2 offset:1024\n\t"
               "global_load_lds_dwordx4 %1, %2 offset:2048\n\tglobal_load_lds_dwordx4 %1, %2 offset:3072"
               :: "s"(ldsb), "v"(v0), "s"(sb), "i"(LOFF) : "memory", "scc");
}

__device__ __forceinline__ void gld128p(u32x4& dst, unsigned voff, const char* sb) {
  asm volatile("global_load_dwordx4 %0, %1, %2" : "=v"(dst) : "v"(voff), "s"(sb) : "memory");
}
template <int OFF> __device__ __forceinline__ void gld32p(u32& dst, unsigned voff, const char* sb) {
  asm volatile("global_load_dword %0, %1, %2 offset:%3" : "=v"(dst) : "v"(voff), "s"(sb), "i"(OFF) : "memory");
}
template <int OFF> __device__ __forceinline__ void gld16p(u32& dst, unsigned voff, const char* sb) {
  asm volatile("global_load_ushort %0, %1, %2 offset:%3" : "=v"(dst) : "v"(voff), "s"(sb), "i"(OFF) : "memory");
}
constexpr int X_POS = 1;
constexpr int F_POS = 1;
template <int OFF> __device__ __forceinline__ void dma16n(unsigned voff, const char* sb) {
  asm volatile("global_load_lds_dwordx4 %0, %1 offset:%2 sc1" :: "v"(voff), "s"(sb), "i"(OFF) : "memory");
}
__device__ __forceinline__ u64 v_ne64(u32x2 v, u64 tag) { u64 m; asm volatile("v_cmp_ne_u64 %0, %1, %2" : "=s"(m) : "v"(v), "s"(tag)); return m; }
__device__ __forceinline__ float v_lo(u32 w) { float d; asm volatile("v_lshlrev_b32 %0, 16, %1" : "=v"(d) : "v"(w)); return d; }
__device__ __forceinline__ float v_hi(u32 w) { float d; asm volatile("v_and_b32 %0, 0xffff0000, %1" : "=v"(d) : "v"(w)); return d; }
__device__ __forceinline__ float v_fma(float a, float b, float c) { float d; asm volatile("v_fma_f32 %0, %1, %2, %3" : "=v"(d) : "v"(a), "v"(b), "v"(c)); return d; }
__device__ __forceinline__ float v_mul(float a, float b) { float d; asm volatile("v_mul_f32 %0, %1, %2" : "=v"(d) : "v"(a), "v"(b)); return d; }
__device__ __forceinline__ float v_add(float a, float b) { float d; asm volatile("v_add_f32 %0, %1, %2" : "=v"(d) : "v"(a), "v"(b)); return d; }
__device__ __forceinline__ float v_relu(float a) { float d; asm volatile("v_max_i32 %0, 0, %1" : "=v"(d) : "v"(a)); return d; }
__device__ __forceinline__ u32 v_cvt(float a, float b) { u32 d; asm volatile("v_cvt_pk_bf16_f32 %0, %1, %2" : "=v"(d) : "v"(a), "v"(b)); return d; }
__device__ __forceinline__ void v_swap32(u32& a, u32& b) { asm volatile("v_permlane32_swap_b32 %0, %1" : "+v"(a), "+v"(b)); }
__device__ __forceinline__ void v_swap16(u32& a, u32& b) { asm volatile("v_permlane16_swap_b32 %0, %1" : "+v"(a), "+v"(b)); }
template <int OFF> __device__ __forceinline__ void ds_f128(f32x4& dst, unsigned addr) {
  asm volatile("ds_read_b128 %0, %1 offset:%2" : "=v"(dst) : "v"(addr), "i"(OFF) : "memory");
}
__device__ __forceinline__ void asm_and3(u32& d, u32 a, u32 b, u32 c) { asm volatile("v_bitop3_b32 %0, %1, %2, %3 bitop3:0x80" : "=v"(d) : "v"(a), "v"(b), "v"(c)); }
__device__ __forceinline__ void asm_ne1(unsigned long long& m, u32 a) { asm volatile("v_cmp_ne_u32 %0, 1, %1" : "=s"(m) : "v"(a)); }
template <int OP> __device__ __forceinline__ void asm_op1(u32& d, u32 a) {
  if constexpr (OP == 0) asm volatile("v_mul_f32 %0, 0x3acccccd, %1" : "=v"(d) : "v"(a));
  else if constexpr (OP == 1) asm volatile("v_mov_b32 %0, %1" : "=v"(d) : "v"(a));
  else if constexpr (OP == 2) asm volatile("v_max_f32 %0, 0, %1" : "=v"(d) : "v"(a));
  else if constexpr (OP == 3) asm volatile("v_cvt_pk_bf16_f32 %0, 0, %1" : "=v"(d) : "v"(a));
  else if constexpr (OP == 4) asm volatile("v_add_f32 %0, 0x3727c5ac, %1" : "=v"(d) : "v"(a));
  else if constexpr (OP == 5) asm volatile("v_sqrt_f32 %0, %1" : "=v"(d) : "v"(a));
  else asm volatile("v_mov_b32 %0, 0" : "=v"(d));
}
template <int OP> __device__ __forceinline__ void asm_op2(u32& d, u32 a, u32 b) {
  if constexpr (OP == 0) asm volatile("v_add_f32 %0, %1, %2" : "=v"(d) : "v"(a), "v"(b));
  else if constexpr (OP == 1) asm volatile("v_fma_f32 %0, -%1, %1, %2" : "=v"(d) : "v"(a), "v"(b));
  else asm volatile("v_cvt_pk_bf16_f32 %0, -%1, %2" : "=v"(d) : "v"(a), "v"(b));
}
template <int LOFF> __device__ __forceinline__ void xdma0(unsigned ldsb, unsigned voff, u32x4 srd, unsigned& soff, unsigned inc) {
  asm volatile("s_add_u32 m0, %1, %5\n\ts_add_u32 %0, %0, %4\n\tbuffer_load_dwordx4 %2, %3, %0 offen" BS " lds"
               : "+s"(soff) : "s"(ldsb), "v"(voff), "s"(srd), "s"(inc), "i"(LOFF) : "memory", "scc");
}
__device__ __forceinline__ void xdma1(unsigned voff, u32x4 srd, unsigned soff) {
  asm volatile("buffer_load_dwordx4 %0, %1, %2 offen offset:1024" BS " lds" :: "v"(voff), "s"(srd), "s"(soff) : "memory");
}
template <int OFF> __device__ __forceinline__ void ds_w128u(unsigned addr, u32x4 v) {
  asm volatile("ds_write_b128 %0, %1 offset:%2" :: "v"(addr), "v"(v), "i"(OFF) : "memory");
}
__device__ __forceinline__ float v_rcp(float a) { float d; asm volatile("v_rcp_f32 %0, %1" : "=v"(d) : "v"(a)); return d; }
template <int OFF> __device__ __forceinline__ void ds_hi16(float& dst, unsigned addr) {
  asm volatile("ds_read_u16_d16_hi %0, %1 offset:%2" : "+v"(dst) : "v"(addr), "i"(OFF) : "memory");
}
__device__ __forceinline__ unsigned v_addc(unsigned a, unsigned c) { unsigned d; asm volatile("v_add_u32 %0, %2, %1" : "=v"(d) : "v"(a), "s"(c)); return d; }
__device__ __forceinline__ void to_acc(u32x4& d, u32x4 v) {
  asm volatile("" : "+v"(v));
  asm volatile("" : "=a"(d) : "0"(v));
}
__device__ __forceinline__ void bld32(u32& dst, unsigned voff, u32x4 srd) {
  asm volatile("buffer_load_dword %0, %1, %2, 0 offen sc1" : "=v"(dst) : "v"(voff), "s"(srd) : "memory");
}
template <int K> __device__ __forceinline__ void asm_dpp(u32& x) {
  if constexpr (K == 0) asm volatile("v_add_f32_dpp %0, %0, %0 quad_perm:[1,0,3,2] row_mask:0xf bank_mask:0xf" : "+v"(x));
  else if constexpr (K == 1) asm volatile("v_add_f32_dpp %0, %0, %0 quad_perm:[2,3,0,1] row_mask:0xf bank_mask:0xf" : "+v"(x));
  else asm volatile("v_add_f32_dpp %0, %0, %0 row_half_mirror row_mask:0xf bank_mask:0xf" : "+v"(x));
}
template <int L> __device__ __forceinline__ void asm_rdl(u32& d, u32 x) { asm volatile("v_readlane_b32 %0, %1, %2" : "=s"(d) : "v"(x), "i"(L)); }
template <int OP> __device__ __forceinline__ void asm_sop(float& d, u32 s, float a) {
  if constexpr (OP == 0) asm volatile("v_mov_b32 %0, %1" : "=v"(d) : "s"(s));
  else if constexpr (OP == 1) asm volatile("v_add_f32 %0, 1.0, %1" : "=v"(d) : "s"(s));
  else asm volatile("v_mul_f32 %0, %1, %2" : "=v"(d) : "s"(s), "v"(a));
}
constexpr int W1_STEPS = 21;
constexpr int O_EPOCH = 0;
constexpr int O_FK = 4096;
constexpr int O_FA0 = O_FK + 160 * 256;
constexpr int O_FW = O_FA0 + 8 * 12 * 128;
constexpr int O_FU = O_FW + 8 * 12 * 128;
constexpr int O_FX = O_FU + 8 * 14 * 128;
constexpr int O_F2 = O_FX + 8 * 32 * 128;
constexpr int O_F3 = O_F2 + 8 * 32 * 128;
constexpr int O_SG = O_F3 + 256 * 512;
constexpr int O_MODP = O_SG + 32 * 8192;
constexpr int O_ST1 = O_MODP + MODP * NMOD * 4;
constexpr int O_KVG = O_ST1 + 16384;
constexpr int O_XG = O_KVG + S_ * D_ * 2;
constexpr int O_X2T = O_XG + S_ * D_ * 2;
constexpr int O_W1F = O_X2T + TP_ * D_ * 2;
constexpr int O_W2F = O_W1F + 64 * W1_STEPS * 3 * 1024;
constexpr int O_H2F = O_W2F + 8 * 48 * 5 * 1024;
constexpr int WS_BYTES = O_H2F + S_ * F_ * 2;
static_assert(O_MODP % 256 == 0 && O_ST1 % 256 == 0 && O_KVG % 256 == 0 && O_X2T % 256 == 0 && O_W1F % 256 == 0, "alignment");

__device__ __forceinline__ void st128(u32x4 v, rsrc_t r, int vo, int so) {
  __builtin_amdgcn_raw_buffer_store_b128(v, r, vo, so, SC1);
  asm volatile("s_nop 1" :: "v"(v) : "memory");
}
constexpr int SPIN_CAP = 1 << 16;
constexpr unsigned CODE_BYTES = 39 * 1024;
__device__ __forceinline__ void st_flag(char* ws, int off, u64 tag) {
  __hip_atomic_store((GAS u64*)(ws + off), tag, __ATOMIC_RELAXED, __HIP_MEMORY_SCOPE_AGENT);
}
__device__ __forceinline__ bool tag_ok(u32x4 v, u64 tag) {
  const u32 lo = (u32)tag, hi = (u32)(tag >> 32);
  return ((v[0] ^ lo) | (v[1] ^ hi) | (v[2] ^ lo) | (v[3] ^ hi)) == 0;
}
template <int NL, int SLEEP> __device__ __forceinline__ void poll(rsrc_t rw, const int (&off)[NL], u64 tag) {
#pragma unroll 1
  for (int sp = 0; sp < SPIN_CAP; ++sp) {
    bool ok = true;
#pragma unroll
    for (int i = 0; i < NL; ++i) ok &= tag_ok(__builtin_amdgcn_raw_buffer_load_b128(rw, off[i], 0, SC1), tag);
    if (__all(ok)) break;
    if (SLEEP) __builtin_amdgcn_s_sleep(SLEEP);
    asm volatile("" ::: "memory");
  }
  asm volatile("" ::: "memory");
}
__device__ __forceinline__ u64 make_tag(u32x2 e) {
  return (((u64)(u32)__builtin_amdgcn_readfirstlane(e[1]) << 32) | (u32)__builtin_amdgcn_readfirstlane(e[0])) + 1;
}
__device__ __forceinline__ void drain_bar() { asm volatile("s_waitcnt vmcnt(0) lgkmcnt(0)\n\ts_barrier" ::: "memory"); }

constexpr int FC_BYTES = 2064 * 2;
constexpr int CH_BYTES = 4 * FC_BYTES;
constexpr int CKV_OFF = 66048;
constexpr int CX_OFF = CKV_OFF + 2048 + 16384, CS_OFF = CX_OFF + 16384;
constexpr int conv_gap(int a, int b) { int n = 0; for (int i = 0; i < a; ++i) n += 8 - i; return n + b; }
struct CSched { int rd[290]; int op[290]; };
constexpr int CBE = PE - 2;
constexpr int CW1 = 12, CW2 = CBE >= 2 ? 12 : (CBE == 1 ? 19 : 35);
constexpr CSched conv_make() {
  CSched S{};
  int rd = 0, op = 0, landed = 0;
  for (int g = 0; g < 288; ++g) {
    const int e = 7 - g / 36, n = g % 36;
    S.rd[g] = rd; S.op[g] = op;
    const bool open = e < CBE || (e == CBE && n >= 24);
    bool ds = e > 0 && n < 24;
    if (open && !ds && rd < 24 && (e == 0 || n < 34)) { ++rd; ds = true; if (n >= 30 && n < 32 && rd < 24) ++rd; }
    if (e > 0 ? n == 35 : (n == CW1 || n == CW2)) landed = rd;
    int k = ds ? 1 : 2;
    while (k-- > 0 && op < (landed / 6) * 32) ++op;
  }
  S.rd[288] = rd; S.op[288] = op;
  return S;
}
constexpr CSched CS = conv_make();
static_assert(CS.rd[288 - 36 + CW2] == 24, "reads must be out before the last wait");
constexpr int XSP = 2;
constexpr int XCH = 20;
constexpr int G_XCHK = 36 * (7 - PE) + XCH;
struct CVSched { int kind[290]; int idx[290]; };
constexpr CVSched cv_make() {
  CVSched S{};
  for (int i = 0; i < 3; ++i) { S.kind[G_XCHK - 44 + 4 * i] = 2; S.idx[G_XCHK - 44 + 4 * i] = i; }
  S.kind[G_XCHK + 4] = 5;
  for (int i = 0; i < 8; ++i) { S.kind[G_XCHK + 4 + XSP * (i + 1)] = 3 + i / 4; S.idx[G_XCHK + 4 + XSP * (i + 1)] = i & 3; }
  return S;
}
constexpr CVSched CV = cv_make();
constexpr int cv_younger(int g, int kind, int idx) {
  int n = -1;
  for (int i = 0; i <= g; ++i) { if (n >= 0 && CV.kind[i]) ++n; if (CV.kind[i] == kind && CV.idx[i] == idx) n = 0; }
  return n;
}
static_assert(G_XCHK > 44, "conv request schedule");
constexpr int G1_R = 6;
constexpr int G1_RS = 7;
constexpr int G1_LA = 4;
constexpr int G1_SP = 15 * 8192;
constexpr int G1_WPRE = 5;
constexpr int g1_ord(int g) { return (g >> 1) + 4 * (g & 1); }
constexpr int g1_ks(int t) { return t < 96 ? t >> 3 : 12 + (t & 7); }
constexpr int g1_j(int t) { return t < 96 ? t & 7 : g1_ord((t - 96) >> 3); }
constexpr int g1_slot(int ks) { return ks < 12 ? ks % G1_RS : G1_RS + ks - 12; }
struct GSched { int kind[320]; int idx[320]; int first[162]; int mid; int first2[242]; int n; };
constexpr int T_SPOLL = 66;
constexpr int WA = 24;
constexpr int PLATE = 16;
constexpr int PSL = 1;
constexpr int CR1 = 12;
constexpr int CR2 = 14;
constexpr int WDN1 = 60;
constexpr int T_DV = 112;
constexpr int T_DEND = 157;
constexpr int T_P0 = 147;
constexpr int DPRE = 0;
constexpr int G2PRE = 8;
constexpr int G_P1 = 2;
constexpr int G_C1 = 24;
constexpr int G_P2 = 30;
constexpr int G_C2 = 68;
constexpr int G_F3 = 12;
constexpr int G_P3 = 84;
constexpr int G_C3 = 124;
constexpr int FINW = 26;
constexpr int HRATE = 3;
constexpr int T_F2 = 159;
constexpr int T_SETUP = 135;
constexpr int T_F0 = 128;
constexpr int T_F1 = 144;
constexpr int T_C0 = 156;
constexpr int XPRE = 5, T_SST = 23, T_WPOLL = 46, T_WCHK = 69;
constexpr int G2R = 22;
constexpr int G2_R = G2R, G2_ST = 58;
constexpr GSched g_make() {
  GSched S{};
  int n = 0, x = 0, w = 3 * G1_WPRE, wa = WA, wd = 0, rs = 0, h = 0, credit = 0;
  auto put = [&](int k, int i) { S.kind[n] = k; S.idx[n] = i; ++n; };
  for (; x < 2 * XPRE; ++x) put(0, x);
  put(13, 0);
  for (int t = 0; t < 160; ++t) {
    S.first[t] = n;
    const int ks = t >> 3;
    credit += t < 96 ? CR1 : CR2;
    if (credit > 28) credit = 28;
    const bool xok = x < 40 && ((x >> 1) >= 12 || (x >> 1) <= ks + G1_RS - ((t & 7) >= 4 ? 1 : 2)), wok = w < 36 && w / 3 <= ks + G1_R - 1;
    const int n0 = n;
    if (t == T_WPOLL) put(4, 0);
    else if (t == T_SST) put(5, 0);
    else if (t == T_SPOLL || t == T_SPOLL + 1) put(6, t - T_SPOLL);
    else if (t >= 109 && (t & 7) == 5) put(7, (t - 109) >> 3);
    else if (t == T_F0 || t == T_F1 || t == T_F2) put(9, t == T_F0 ? 0 : t == T_F1 ? 1 : 2);
    else if (t == T_P0) put(10, 0);
    else if (t >= T_C0) put(11, h++);
    else if (credit >= 16) {
      if (xok && (!wok || (x >> 1) < w / 3)) put(0, x++);
      else if (wok) put(1, w++);
      else if (wa < 24) put(8, wa++);
      else if (rs < 4 && !(x & 1)) put(3, rs++);
      else if (t > T_WCHK && wd < (t < T_DV ? 37 : WDN1) && t < T_DEND) put(2, wd++);
    }
    if (n > n0) credit -= 16;
  }
  S.first[160] = n;
  put(7, 7);
  for (int i = 0; i < DPRE && wd < 60; ++i) put(2, wd++);
  S.mid = n;
  auto next = [&](int g) {
    const int hmax = g <= G_C1 ? 12 : (g <= G_C2 ? 24 : (g <= G_C3 ? 36 : 48));
    if (wd < 60 && (wd / 5 < h || h >= 12)) put(2, wd++);
    else if (h < hmax && h < g / 5 + G2_R - 1) put(11, h++);
  };
  for (int i = 0; i < G2PRE; ++i) next(0);
  for (int g = 0; g < 240; ++g) {
    S.first2[g] = n;
    if (g == G_F3) put(9, 3);
    else if (g == G_P1) put(10, 1);
    else if (g == G_P2) put(10, 2);
    else if (g == G_P3) put(10, 3);
    else if (g > 60 && g % 60 == G2_ST) put(12, g / 60 - 1);
    else if (wd < 60 ? (g & 1) : g % HRATE == 1) next(g);
  }
  S.first2[240] = n; S.n = n;
  return S;
}
constexpr GSched GS = g_make();
constexpr int g_pos(int kind, int idx) { for (int i = 0; i < GS.n; ++i) if (GS.kind[i] == kind && GS.idx[i] == idx) return i; return -1; }
constexpr int imax(int a, int b) { return a > b ? a : b; }
constexpr int g1_need(int t) {
  if (t == T_F0) return g_pos(7, 1);
  if (t == T_F1) return g_pos(7, 3);
  if (t == T_F2) return g_pos(7, 5);
  if (t == 92) return imax(g_pos(0, 31), g_pos(8, 23));
  if (t == 96) return g_pos(0, 39);
  if (t == 100) return 0;
  if (t > 92 || (t & 7) != 4) return -1;
  const int s = (t + 4) / 8;
  int p = imax(g_pos(0, 2 * s + 1), g_pos(1, 3 * s + 2));
  if (t == 12) p = imax(p, g_pos(0, 9));
  if (t == 84) p = imax(p, g_pos(6, 1));
  if (t == T_WCHK - 1) p = imax(p, g_pos(4, 0));
  return p;
}
constexpr int g_lastdv() { int p = -1; for (int i = 37; i < 60; ++i) if (g_pos(2, i) < GS.first[160]) p = g_pos(2, i); return p; }
constexpr int g2_need(int o) { return o < 12 ? imax(g_pos(11, o), g_pos(2, 5 * o + 4)) : g_pos(11, o); }
static_assert(g_pos(1, 35) >= 0 && g_pos(0, 39) >= 0 && g_pos(2, 59) >= 0 && g_pos(11, 47) >= 0 && g_pos(3, 3) >= 0, "schedule incomplete");
constexpr int XR_OFF = G1_SP + 2048;
constexpr int GB_OFF = XR_OFF + 10400;
constexpr int LDS_BYTES = 134 * 1024;
static_assert(GB_OFF + 320 <= LDS_BYTES, "LDS");
constexpr int AG_OFF = 0, AU_OFF = 20480, AD_OFF = 40960, AM_OFF = 61440;

__global__ __launch_bounds__(256, 1) void k_fused(const u16* __restrict__ xIn, const u16* __restrict__ kIn, const u16* __restrict__ vIn,
                                                  const u16* __restrict__ shk, const u16* __restrict__ ssmk, const u16* __restrict__ ssmD,
                                                  const u16* __restrict__ wg, const u16* __restrict__ wu, const u16* __restrict__ wd,
                                                  const u16* __restrict__ cIn, const u16* __restrict__ wmod,
                                                  char* __restrict__ ws, u16* __restrict__ out) {
  __shared__ __attribute__((aligned(16))) char smem[LDS_BYTES];
  u32 cpf = 0;
  if (blockIdx.x >= 240) {
    uint64_t pc; asm volatile("s_getpc_b64 %0" : "=s"(pc));
    const unsigned co = (((blockIdx.x >> 3) & 1) * 256 + threadIdx.x) * 128;
    asm volatile("global_load_dword %0, %1, %2" : "=v"(cpf) : "v"(co < CODE_BYTES ? co : 0u), "s"(pc & ~(uint64_t)127) : "memory");
  }
  u64 tag;
  const int tid = threadIdx.x, lane = tid & 63;
  const int wv = __builtin_amdgcn_readfirstlane(tid >> 6);
  const int b = blockIdx.x;
  const unsigned sbase = (unsigned)(size_t)smem;
  const unsigned l16 = lane * 16;
  int bs = blockIdx.x;
  asm volatile("" : "+s"(bs));
  const int xcd = bs & 7, idx = bs >> 3;
  int mt = (xcd & 1) * 8 + (idx & 7), nc = (xcd >> 1) * 4 + (idx >> 3);
  asm volatile("" : "+s"(mt), "+s"(nc));
  const int M = mt * 2 + ((nc >> 2) & 1), N = (nc >> 3) * 4 + (nc & 3);
  int kvo = (b >> 5) * 256 + lane * 4, kso[9];
  kso[0] = ((b & 31) * 64 + wv * 16) * 1280;
#pragma unroll
  for (int q = 1; q < 8; ++q) kso[q] = kso[0] + q * 2560;
  kso[8] = kso[0] ? kso[0] - 3 * 1280 : 0;
  asm volatile("" : "+v"(kvo), "+s"(kso[0]), "+s"(kso[1]), "+s"(kso[2]), "+s"(kso[3]), "+s"(kso[4]), "+s"(kso[5]), "+s"(kso[6]), "+s"(kso[7]), "+s"(kso[8]));
  asm volatile("" :: "s"(xIn), "s"(kIn), "s"(vIn), "s"(shk), "s"(ssmk), "s"(ssmD), "s"(wg), "s"(wu), "s"(wd), "s"(cIn), "s"(wmod), "s"(ws));
  const rsrc_t rw = mk_rsrc(ws, WS_BYTES);
  const u32x4 wsrd = {(u32)(uint64_t)ws, (u32)((uint64_t)ws >> 32) & 0xffffu, (u32)WS_BYTES, 0x00020000u};
  u32 tch = 0;
  auto touch = [&]() {
    constexpr int PER = (O_ST1 + 16384 - O_FK + 256 * 128 - 1) / (256 * 128);
    static_assert(PER <= 64, "one request per workgroup");
    tch = __builtin_amdgcn_raw_buffer_load_b32(rw, (wv == 3 && lane < PER) ? O_FK + (b * PER + lane) * 128 : WS_BYTES, 0, SC1);
  };
  u32x4 WD[60];
  const char* wdp = ws + O_W2F + (N * 48 + wv * 12) * 5120 + 4096;
  const unsigned l16b = l16 + 8192;
  auto issue_wd = [&](auto I) { constexpr int i = I.value; gld128a<((i & 7) - 4) * 1024>(WD[i], (i & 8) ? l16b : l16, wdp + (i >> 4) * 16384); };
  auto clear_slot = [&]() {
    const int so = O_SG + (mt * 2 + ((nc >> 1) & 1)) * 8192 + (((nc & 1) * 4 + wv) * 4 + (nc >> 2)) * 256;
    __builtin_amdgcn_raw_buffer_store_b32(0u, rw, lane * 4, so, SC1);
  };
  if (b >= CWG) {
    const int j = b - CWG;
    const int r8 = lane >> 3, cp = lane & 7;
    const int ks2 = j >> 1, hf = j & 1;
    const int ug = j % 24, kq = j / 24;
    float* LC = (float*)(smem + AM_OFF);
    u16* LS = (u16*)(smem + AM_OFF + 640);
    const int gt = wv * 96 + j;
    const bool gact = gt < 300, urgent = gt < 100;
    const int gq = gt / MODP, gk = gt - gq * MODP, gcol = urgent ? 30 + gq : (gq < 50 ? gq - 20 : gq);
    u32x4 wm[16]; u32 cv[2];
    u32x2 ep = __builtin_amdgcn_raw_buffer_load_b64(rw, O_EPOCH, 0, SC1);
    touch();
    SCHED();
    if (!urgent) __builtin_amdgcn_s_sleep(A0_DELAY);
    if (gact) {
      static_for<0, 2>([&](auto I) { gld16p<I.value * 128>(cv[I.value], lane * 2, (const char*)(cIn + gk * 128)); });
      static_for<0, 16>([&](auto I) { gld128p(wm[I.value], r8 * (NMOD * 2) + cp * 16, (const char*)(wmod + (size_t)(gk * 128 + I.value * 8) * NMOD + gcol * 64)); });
    }
    if (gact) {
      wait_vm<16>();
      float sl[2], sv[16];
#pragma unroll
      for (int i = 0; i < 2; ++i) { pin(cv[i]); const float x = bflo(cv[i]); sl[i] = rbf(x * __builtin_amdgcn_rcpf(1.f + __expf(-x))); }
#pragma unroll
      for (int it = 0; it < 16; ++it) { sv[it] = __shfl(sl[it >> 3], (it & 7) * 8 + r8); pin(sv[it]); }
      f32x2 acc[4];
      static_for<0, 4>([&](auto G) {
        constexpr int g = G.value;
        wait_vm<12 - 4 * g>();
#pragma unroll
        for (int it = 4 * g; it < 4 * g + 4; ++it) {
          pin(wm[it]);
          const f32x2 s2 = {sv[it], sv[it]};
#pragma unroll
          for (int q = 0; q < 4; ++q) {
            const f32x2 w2 = {bflo(wm[it][q]), bfhi(wm[it][q])};
            acc[q] = it ? __builtin_elementwise_fma(s2, w2, acc[q]) : s2 * w2;
          }
        }
#pragma unroll
        for (int q = 0; q < 4; ++q) pin(acc[q]);
      });
      pin(ep); pin(tch);
      const float a0 = tred16(tred32(acc[0][0], acc[2][0]), tred32(acc[1][0], acc[3][0]));
      const float a1 = tred16(tred32(acc[0][1], acc[2][1]), tred32(acc[1][1], acc[3][1]));
      const float e0 = dpp_add<DPP_ROR8>(a0), e1 = dpp_add<DPP_ROR8>(a1);
      __builtin_amdgcn_raw_buffer_store_b32(__float_as_uint((lane & 8) ? e1 : e0), rw, O_MODP + (gk * NMOD + gcol * 64 + cp * 8 + r8) * 4, 0, SC1);
      if (urgent) {
        wait_vm<0>();
        if (lane < 8) st_flag(ws, O_FU + (lane * 14 + (gt >> 3)) * 128 + (gt & 7) * 8, make_tag(ep));
        __builtin_amdgcn_s_sleep(AU_DELAY);
      }
    } else {
      pin(ep); pin(tch);
    }
    tag = make_tag(ep);
    {
      const unsigned vg = r8 * (F_ * 2) + ((cp ^ ((r8 >> 1) * 2)) * 16);
      const char* gs = (const char*)(wg + (size_t)(kq * 160 + wv * 40) * F_ + ug * 64);
      const char* us = (const char*)(wu + (size_t)(kq * 160 + wv * 40) * F_ + ug * 64);
      static_for<0, 5>([&](auto I) {
        dma16p<AG_OFF + I.value * 1024>(sbase + wv * 5120, vg, gs + I.value * (8 * F_ * 2));
        dma16p<AU_OFF + I.value * 1024>(sbase + wv * 5120, vg ^ 16, us + I.value * (8 * F_ * 2));
      });
    }
    wait_vm<10>();
    bar();
    if (tid < 8) st_flag(ws, O_FA0 + (tid * 12 + (j >> 3)) * 128 + (j & 7) * 8, tag);
    const int kg = lane >> 4, q4 = (lane & 15) >> 2, p4 = lane & 3;
    if (wv == 0) {
      const int po[1] = {O_FU + ((b & 7) * 14 + (lane < 50 ? lane >> 2 : 0)) * 128 + (lane & 3) * 16};
      poll<1, 1>(rw, po, tag);
      f32x4 mm[2];
      const int lo = lane < 40 ? lane * 16 : 0;
#pragma unroll
      for (int i = 0; i < 2; ++i) {
        mm[i] = f32x4{0.f, 0.f, 0.f, 0.f};
#pragma unroll
        for (int p = 0; p < MODP; ++p)
          mm[i] += __builtin_bit_cast(f32x4, __builtin_amdgcn_raw_buffer_load_b128(rw, lo, O_MODP + (p * NMOD + (3 + i) * D_ + kq * 160) * 4, SC1));
      }
      if (lane < 40) {
        *(u32x2*)(LS + lane * 4) = u32x2{pack_bf16(mm[0][0], mm[0][1]), pack_bf16(mm[0][2], mm[0][3])};
        *(f32x4*)(LC + lane * 4) = f32x4{rbf(1.f + rbf(mm[1][0])), rbf(1.f + rbf(mm[1][1])), rbf(1.f + rbf(mm[1][2])), rbf(1.f + rbf(mm[1][3]))};
      }
    }
    drain_bar();
    asm volatile("" :: "v"(cpf));
    clear_slot();
    {
      const char* ds = (const char*)(wd + (size_t)(ks2 * 32 + wv * 8) * D_ + hf * 320);
      static_for<0, 5>([&](auto I) { dma16p<AD_OFF + I.value * 4096>(sbase + wv * 1024, r8 * (D_ * 2) + cp * 16, ds + I.value * 128); });
    }
    {
      const int rr = 4 * kg + q4;
      u32x2 lo[10], hi[10], sbl[5], sbh[5]; u32x4 cl[5], ch[5];
      unsigned ad[2];
#pragma unroll
      for (int t = 0; t < 2; ++t) ad[t] = sbase + (p4 >> 1) * AU_OFF + rr * 128 + (((wv * 2 + t) ^ (((rr >> 1) & 3) * 2 + (p4 >> 1))) * 16) + (p4 & 1) * 8;
      const unsigned ac = sbase + AM_OFF + kg * 16, as = sbase + AM_OFF + 640 + kg * 8;
      auto rd = [&](auto I) {
        constexpr int i = I.value;
        ds_b128<i * 128>(cl[i], ac); ds_b128<i * 128 + 64>(ch[i], ac);
        ds_tr<i * 4096>(lo[2 * i], ad[0]); ds_tr<i * 4096 + 2048>(hi[2 * i], ad[0]); ds_tr<i * 4096>(lo[2 * i + 1], ad[1]); ds_tr<i * 4096 + 2048>(hi[2 * i + 1], ad[1]);
        ds_b64<i * 64>(sbl[i], as); ds_b64<i * 64 + 32>(sbh[i], as);
      };
      rd(IC<0>{});
      const int src = ((lane & 48) + ((lane & 1) * 8 + ((lane & 15) >> 1))) * 4;
      const u32x4 ones = {0x3f803f80u, 0x3f803f80u, 0x3f803f80u, 0x3f803f80u};
      f32x4 aU[2] = {}, aW[2] = {};
      int so[2];
#pragma unroll
      for (int t = 0; t < 2; ++t) { const int T = ug * 8 + wv * 2 + t, sw3 = T / 3; so[t] = O_W1F + ((sw3 * W1_STEPS + kq * 5) * 3 + T - sw3 * 3) * 1024; }
      static_for<0, 5>([&](auto KS) {
        constexpr int ks = KS.value;
        wait_lgkm<0>();
        pin(cl[ks]); pin(ch[ks]); pin(sbl[ks]); pin(sbh[ks]);
        if constexpr (ks < 4) rd(IC<ks + 1>{});
        u32x4 pm[2];
#pragma unroll
        for (int t = 0; t < 2; ++t) {
          pin(lo[2 * ks + t]); pin(hi[2 * ks + t]);
          const u32x4 raw = cat(lo[2 * ks + t], hi[2 * ks + t]);
          u32x4 sc;
#pragma unroll
          for (int q = 0; q < 2; ++q) {
            sc[q] = pack_bf16(bflo(raw[q]) * __uint_as_float(cl[ks][2 * q]), bfhi(raw[q]) * __uint_as_float(cl[ks][2 * q + 1]));
            sc[2 + q] = pack_bf16(bflo(raw[2 + q]) * __uint_as_float(ch[ks][2 * q]), bfhi(raw[2 + q]) * __uint_as_float(ch[ks][2 * q + 1]));
          }
#pragma unroll
          for (int q = 0; q < 4; ++q) pm[t][q] = (u32)__builtin_amdgcn_ds_bpermute(src, sc[q]);
          aW[t] = mfma_b(raw, cat(sbl[ks], sbh[ks]), aW[t]);
          aU[t] = mfma_b(sc, ones, aU[t]);
        }
#pragma unroll
        for (int t = 0; t < 2; ++t) st128(pm[t], rw, l16, so[t] + ks * 3072);
      });
      const f32x4 su = (lane & 4) ? aU[1] : aU[0], sw = (lane & 4) ? aW[1] : aW[0];
      const float U = p4 == 0 ? su[0] : p4 == 1 ? su[1] : p4 == 2 ? su[2] : su[3];
      const float W = p4 == 0 ? sw[0] : p4 == 1 ? sw[1] : p4 == 2 ? sw[2] : sw[3];
      const float u1 = rbf(U), u2 = rbf(U - u1), u3 = U - u1 - u2;
      const float w1 = rbf(W), w2 = rbf(W - w1), w3 = W - w1 - w2;
      const int crow = 2 * (4 * (kg & 1) + p4) + (kg >> 1);
      __builtin_amdgcn_raw_buffer_store_b128(u32x4{pack_bf16(u1, w1), pack_bf16(u2, w2), pack_bf16(u3, w3), 0u}, rw,
                                             (lane & 8) ? WS_BYTES : ((lane & 4) ? so[1] : so[0]) + (20 - kq * 5) * 3072 + (kq * 16 + crow) * 16, 0, SC1);
    }
  } else {
    const int d = (b >> 1) * 8 + (b & 1) + 2 * wv;
    const int row = lane & 15, kg = lane >> 4;
    const int tt = b & 31, ct = b >> 5;
    const int ov = ((lane >> 2) * 2048 + (lane & 3)) * 16, os = (ct * 16 * 2048 + (tt * 8 + wv * 2) * 4) * 16;
    const int stg = b * 2 + (wv - 2);
    const bool xact = wv >= 2 && b < 128;
    u32 xx[5][8];
    u32 draw; u32x2 ep;
    unsigned aAddr, bA[8];
    const unsigned fl = sbase + wv * CH_BYTES;
    {
      const rsrc_t rk = mk_rsrc(kIn, S_ * D_ * 2), rv = mk_rsrc(vIn, S_ * D_ * 2);
      u32 kk[19], vv[16];
#pragma unroll
      for (int q = 0; q < 3; ++q) kk[q] = __builtin_amdgcn_raw_buffer_load_b32(rk, kvo + q * 1280, kso[8], NT);
#pragma unroll
      for (int q = 0; q < 8; ++q) kk[3 + q] = __builtin_amdgcn_raw_buffer_load_b32(rk, kvo + (q & 1) * 1280, kso[q >> 1], NT);
      const u32x4 taps = __builtin_amdgcn_raw_buffer_load_b128(mk_rsrc(shk, D_ * 8), kvo * 4, 0, 0);
#pragma unroll
      for (int q = 0; q < 8; ++q) vv[q] = __builtin_amdgcn_raw_buffer_load_b32(rv, kvo + (q & 1) * 1280, kso[q >> 1], NT);
#pragma unroll
      for (int q = 8; q < 16; ++q) kk[3 + q] = __builtin_amdgcn_raw_buffer_load_b32(rk, kvo + (q & 1) * 1280, kso[q >> 1], NT);
#pragma unroll
      for (int q = 8; q < 16; ++q) vv[q] = __builtin_amdgcn_raw_buffer_load_b32(rv, kvo + (q & 1) * 1280, kso[q >> 1], NT);
      SCHED();
      SCHED();
      if (F_POS == 0) {
        const char* fs = (const char*)(ssmk + (size_t)d * S_);
        dma16x4p<FC_BYTES + 28>(fl, l16, fs);
        dma16x4p<3 * FC_BYTES + 24>(fl, l16, fs);
      }
      aAddr = fl + (row & 3) * FC_BYTES + 2 * (64 * kg + (row & ~3));
      pin(aAddr);
#pragma unroll
      for (int e = 0; e < 8; ++e) { const int g = 32 + 2 * row + 1 - 8 * kg - e; bA[e] = sbase + CKV_OFF + 16 * ((4 * g + wv) ^ ((g >> 2) & 7)); pin(bA[e]); }
      ep = __builtin_amdgcn_raw_buffer_load_b64(rw, O_EPOCH, 0, SC1);
      touch();
      draw = __builtin_amdgcn_raw_buffer_load_b32(mk_rsrc(ssmD, D_ * 2), (d >> 1) * 4, 0, 0);
      *(u32x2*)(smem + CKV_OFF + tid * 8) = u32x2{0, 0};
      if (lane < 13) *(u32*)(smem + wv * CH_BYTES + (lane < 7 ? FC_BYTES + lane * 4 : 3 * FC_BYTES + (lane - 7) * 4)) = 0;
      SCHED();
      if (kso[0] == 0) { kk[0] = 0; kk[1] = 0; kk[2] = 0; }
      const f32x2 w0 = {bflo(taps[0]), bflo(taps[2])}, w1 = {bfhi(taps[0]), bfhi(taps[2])};
      const f32x2 w2 = {bflo(taps[1]), bflo(taps[3])}, w3 = {bfhi(taps[1]), bfhi(taps[3])};
#pragma unroll
      for (int r = 0; r < 2; ++r) {
        f32x2 kv[8];
#pragma unroll
        for (int q = 0; q < 8; ++q) {
          const int i = 8 * r + q;
          f32x2 a = f32x2{bflo(kk[i]), bfhi(kk[i])} * w3;
          a = __builtin_elementwise_fma(f32x2{bflo(kk[i + 1]), bfhi(kk[i + 1])}, w2, a);
          a = __builtin_elementwise_fma(f32x2{bflo(kk[i + 2]), bfhi(kk[i + 2])}, w1, a);
          a = __builtin_elementwise_fma(f32x2{bflo(kk[i + 3]), bfhi(kk[i + 3])}, w0, a);
          kv[q] = a * f32x2{bflo(vv[i]), bfhi(vv[i])};
        }
        u32x4 o0, o1;
#pragma unroll
        for (int t = 0; t < 4; ++t) {
          o0[t] = pack_bf16(kv[7 - 2 * t][0], kv[6 - 2 * t][0]);
          o1[t] = pack_bf16(kv[7 - 2 * t][1], kv[6 - 2 * t][1]);
        }
        st128(o0, rw, ov + r * 64, O_KVG + os);
        st128(o1, rw, ov + r * 64, O_KVG + os + 16384);
      }
    }
    pin(ep); pin(draw); pin(tch);
    tag = make_tag(ep);
    auto fload = [&]() {
      const char* fs = (const char*)(ssmk + (size_t)d * S_);
      dma16x4p<FC_BYTES + 28>(fl, l16, fs);
      dma16x4p<3 * FC_BYTES + 24>(fl, l16, fs);
    };
    auto xload = [&]() {
      const char* xp = (const char*)xIn + stg * 8 * 1280;
      static_for<0, 40>([&](auto I) { constexpr int t = I.value / 5, c = I.value % 5; gld32p<c * 256 + (t & 1) * 1280>(xx[c][t], lane * 4, xp + (t & 6) * 1280); });
    };
    if (F_POS == 1) fload();
    if (xact && X_POS == 1) {
      xload();
      wait_vm<40 + (F_POS == 1 ? 8 : 0)>();
    } else {
      wait_vm<(F_POS == 1 ? 8 : 0)>();
    }
    asm volatile("s_waitcnt lgkmcnt(0)\n\ts_barrier" ::: "memory");
    if (tid < 32) st_flag(ws, O_FK + ((b & ~31) + tid) * 256 + (b & 31) * 8, tag);
    if (xact && X_POS == 2) xload();
    if (F_POS == 2) fload();
    int pa1[3] = {O_FX + ((b & 7) * 32 + (lane >> 2)) * 128 + (lane & 3) * 16, O_FX + ((b & 7) * 32 + 16 + (lane >> 2)) * 128 + (lane & 3) * 16,
                  O_FA0 + ((b & 7) * 12 + (lane < 48 ? lane >> 2 : 0)) * 128 + (lane & 3) * 16};
    const char* xgp = ws + O_XG + b * 16384 + wv * 4096;
    const char* stp = ws + O_ST1 + wv * 4096;
    asm volatile("" : "+s"(xgp), "+s"(stp));
    pin(pa1[0]); pin(pa1[1]); pin(pa1[2]);
    float Dv = (d & 1) ? bfhi(draw) : bflo(draw);
    unsigned ek = sbase + CKV_OFF + 16 * ((4 * (32 + 2 * row + (kg >> 1)) + wv) ^ ((row >> 1) & 7)) + ((kg & 1) ? 0 : 8);
    unsigned ex = (ek ^ 8) + (CX_OFF - CKV_OFF - 2048), es = sbase + CS_OFF + l16;
    unsigned mo = ((lane & 7) < MODP && lane < 24) ? O_MODP + ((lane & 7) * NMOD + (lane >> 3) * D_ + d) * 4 : WS_BYTES;
    pin(Dv); pin(ek); pin(ex); pin(es); pin(mo);
    unsigned ko[2] = {(unsigned)(lane ^ (lane >> 4)) * 16, ((unsigned)(lane ^ (lane >> 4)) * 16) ^ 64};
    pin(ko[0]); pin(ko[1]);
    auto fcopies = [&]() {
      u32x4 m[5]; u32 lo[5], hi[5];
      const unsigned src = fl + FC_BYTES + l16, src4 = fl + FC_BYTES + 4096 + (lane & 1) * 16;
      static_for<0, 4>([&](auto I) { constexpr int i = I.value; ds_b128<i * 1024>(m[i], src); ds_b32<i * 1024>(lo[i], src - 4); ds_b32<i * 1024 + 16>(hi[i], src); });
      ds_b128<0>(m[4], src4); ds_b32<0>(lo[4], src4 - 4); ds_b32<16>(hi[4], src4);
      wait_lgkm<0>();
#pragma unroll
      for (int i = 0; i < 5; ++i) {
        pin(m[i]); pin(lo[i]); pin(hi[i]);
        if (i == 0 && lane == 0) lo[0] = 0;
        const u32 s1 = __builtin_amdgcn_alignbit(m[i][1], m[i][0], 16), s2 = __builtin_amdgcn_alignbit(m[i][2], m[i][1], 16), s3 = __builtin_amdgcn_alignbit(m[i][3], m[i][2], 16);
        char* dst = smem + wv * CH_BYTES + (i < 4 ? i * 1024 + l16 : 4096 + (lane & 1) * 16);
        *(u32x4*)dst = u32x4{__builtin_amdgcn_alignbit(m[i][0], lo[i], 16), s1, s2, s3};
        *(u32x4*)(dst + 2 * FC_BYTES) = u32x4{s1, s2, s3, __builtin_amdgcn_alignbit(hi[i], m[i][3], 16)};
      }
    };
    if (!xact) { if (wv == 0 && F_POS != 2) wait_vm<1>(); else wait_vm<0>(); fcopies(); }
    if (xact) {
      wait_vm<(F_POS == 2 ? 8 : 0)>();
#pragma unroll
      for (int c = 0; c < 5; ++c)
#pragma unroll
        for (int t = 0; t < 8; ++t) pin(xx[c][t]);
#pragma unroll
      for (int c = 0; c < 5; ++c) {
        u32x4 p0, p1;
#pragma unroll
        for (int t = 0; t < 4; ++t) {
          p0[t] = __builtin_amdgcn_perm(xx[c][2 * t + 1], xx[c][2 * t], 0x05040100u);
          p1[t] = __builtin_amdgcn_perm(xx[c][2 * t + 1], xx[c][2 * t], 0x07060302u);
        }
        const int so = O_XG + (c * 16 * 2048 + stg * 4) * 16;
        st128(p0, rw, ov, so);
        st128(p1, rw, ov, so + 16384);
      }
      const bf16x2 ones = {(__bf16)1.0f, (__bf16)1.0f};
      float s1[8], s2[8];
#pragma unroll
      for (int t = 0; t < 8; ++t) {
        s1[t] = 0.f; s2[t] = 0.f;
#pragma unroll
        for (int c = 0; c < 5; ++c) {
          const bf16x2 v = __builtin_bit_cast(bf16x2, xx[c][t]);
          s1[t] = __builtin_amdgcn_fdot2_f32_bf16(v, ones, s1[t], false);
          s2[t] = __builtin_amdgcn_fdot2_f32_bf16(v, v, s2[t], false);
        }
      }
      float q1[2], q2[2];
#pragma unroll
      for (int e = 0; e < 2; ++e) {
        q1[e] = dpp_add<DPP_ROR8>(tred16(tred32(s1[e], s1[4 + e]), tred32(s1[2 + e], s1[6 + e])));
        q2[e] = dpp_add<DPP_ROR8>(tred16(tred32(s2[e], s2[4 + e]), tred32(s2[2 + e], s2[6 + e])));
      }
      const float mean = sum8((lane & 8) ? q1[1] : q1[0]) * (1.f / D_);
      const float var = fmaxf(sum8((lane & 8) ? q2[1] : q2[0]) * (1.f / D_) - mean * mean, 0.f);
      const float sd = rbf(__builtin_amdgcn_sqrtf(rbf(rbf(var) + 1e-5f)));
      const float rs = __builtin_amdgcn_rcpf(sd);
      const int s = stg * 8 + (lane >> 3);
      const int nt = s >> 8, col = (s >> 4) & 15, k4 = (s >> 2) & 3, rr = s & 3;
      if ((lane & 7) == 0) __builtin_amdgcn_raw_buffer_store_b64(u32x2{__float_as_uint(rs), __float_as_uint(-rbf(mean) * rs)}, rw,
                                                                 O_ST1 + (((nt * 2 + (rr >> 1)) * 64 + k4 * 16 + col) * 4 + (rr & 1) * 2) * 4, 0, SC1);
      if (F_POS == 2) wait_vm<11>();
      fcopies();
      wait_vm<0>();
      if (lane < 8) st_flag(ws, O_FX + (lane * 32 + (stg >> 3)) * 128 + (stg & 7) * 8, tag);
    } else if (wv < 2) {
      const int po[1] = {O_FK + b * 256 + wv * 128 + (lane & 7) * 16};
      poll<1, 0>(rw, po, tag);
      dma16x4<CKV_OFF + 2048>(sbase + wv * 8192, ko[0], ko[1], ws + O_KVG + b * 16384 + wv * 8192);
      dma16x4<CKV_OFF + 2048 + 4096>(sbase + wv * 8192, ko[0], ko[1], ws + O_KVG + b * 16384 + wv * 8192 + 4096);
    }
    f32x4 acc[8];
    u32x2 ra[2][8][2]; u32x4 rb[2][8];
    u32x4 pf[3]; u32 mraw; u64 nm[6];
    auto load_n = [&](auto E, auto N) {
      constexpr int e = E.value, n = N.value, bf = e & 1;
      if constexpr (n < 2) ds_b64<16 * e + 8 * n>(ra[bf][0][n], aAddr);
      else if constexpr (n < 10) ds_b128<2048 * (n - 2)>(rb[bf][n - 2], bA[e]);
      else ds_b64<512 * ((n - 8) >> 1) + 16 * e + 8 * (n & 1)>(ra[bf][(n - 8) >> 1][n & 1], aAddr);
    };
    u32x2 kvw[8], xw[8]; u32x4 sA[8], sB[8];
    float T[8][4], Hh[8][4], c0 = 0.f, c1 = 0.f;
    auto kvop = [&](auto K) {
      constexpr int k = K.value, nt = k >> 3, r = k & 3;
      if constexpr ((k & 4) == 0) T[nt][r] = (r & 1) ? v_lo(kvw[nt][1 - (r >> 1)]) : v_hi(kvw[nt][1 - (r >> 1)]);
      else T[nt][r] = v_mul(T[nt][r], Dv);
    };
    auto xrd = [&](auto K) {
      constexpr int k = K.value, nt = k / 3;
      if constexpr (k % 3 == 0) ds_b64<2048 * nt>(xw[nt], ex);
      else if constexpr (k % 3 == 1) ds_b128<2048 * nt>(sA[nt], es);
      else ds_b128<2048 * nt + 1024>(sB[nt], es);
    };
    float xf[8];
    auto xop = [&](auto K) {
      constexpr int k = K.value, nt = 2 * (k >> 5) + ((k >> 2) & 1), ph = (k >> 3) & 3, r = k & 3, i = k & 7;
      if constexpr (ph == 0) xf[i] = (r & 1) ? v_hi(xw[nt][r >> 1]) : v_lo(xw[nt][r >> 1]);
      else if constexpr (ph == 1) Hh[nt][r] = v_fma(xf[i], __uint_as_float((r < 2 ? sA[nt] : sB[nt])[2 * (r & 1)]), __uint_as_float((r < 2 ? sA[nt] : sB[nt])[2 * (r & 1) + 1]));
      else if constexpr (ph == 2) Hh[nt][r] = v_fma(Hh[nt][r], c1, c0);
      else T[nt][r] = v_fma(Hh[nt][r], T[nt][r], xf[i]);
    };
    u32 ms[3];
    auto mop = [&](auto K) {
      constexpr int k = K.value;
      if constexpr (k < 3) asm_dpp<k>(mraw);
      else if constexpr (k == 3) asm_op1<3>(mraw, mraw);
      else if constexpr (k < 7) asm_rdl<8 * (k - 4)>(ms[k - 4], mraw);
      else if constexpr (k == 7) asm_sop<0>(c0, ms[0], c0);
      else if constexpr (k == 8) asm_sop<1>(c1, ms[1], c1);
      else if constexpr (k == 9) asm_sop<2>(c0, ms[2], c0);
      else if constexpr (k == 10) { u32 t; asm_op1<3>(t, __float_as_uint(c1)); c1 = __uint_as_float(t); }
      else asm_sop<2>(c1, ms[2], c1);
    };
    clear_slot();
    wait_lgkm<0>();
    static_for<0, 2>([&](auto N) { load_n(IC<7>{}, N); });
    static_for<10, 24>([&](auto N) { load_n(IC<7>{}, N); });
    if (wv < 2) wait_vm<1>();
    bar();
    static_for<2, 10>([&](auto N) { load_n(IC<7>{}, N); });
    static_for<0, 8>([&](auto EE) {
      constexpr int e = 7 - EE.value, bf = e & 1;
      static_for<0, 8>([&](auto AA) {
        constexpr int a = AA.value;
        static_for<0, 8 - a>([&](auto BB) {
          constexpr int bb = BB.value, n = conv_gap(a, bb);
          if constexpr (e == 7 && n < 8) wait_lgkm<7>();
          mfma_<(e == 7 && a == 0)>(acc[a + bb], cat(ra[bf][a][0], ra[bf][a][1]), rb[bf][bb]);
          if constexpr (e > 0 && n < 24) load_n(IC<(e > 0 ? e - 1 : 0)>{}, IC<n>{});
          if constexpr (e == 7 && n >= 24 && n < 32) ds_b64<2048 * (n - 24)>(kvw[n - 24], ek);
          constexpr int g = 36 * (7 - e) + n;
          static_for<CS.rd[g], CS.rd[g + 1]>(xrd);
          if constexpr (e > 0 ? n == 35 : (n == CW1 || n == CW2)) wait_lgkm<0>();
          static_for<CS.op[g], CS.op[g + 1]>(xop);
          if constexpr ((e == 6 || e == 5) && n < 32) kvop(IC<(6 - e) * 32 + n>{});
          if constexpr (CV.kind[g] == 2) gld128<0>(pf[CV.idx[g]], pa1[CV.idx[g]], ws);
          if constexpr (CV.kind[g] == 3) { if constexpr (CV.idx[g] == 0) dma16<CX_OFF>(sbase + wv * 4096, ko[0], xgp); else dma16n<CV.idx[g] * 1024>(ko[CV.idx[g] & 1], xgp); }
          if constexpr (CV.kind[g] == 4) { if constexpr (CV.idx[g] == 0) dma16<CS_OFF>(sbase + wv * 4096, l16, stp); else dma16n<CV.idx[g] * 1024>(l16, stp); }
          if constexpr (CV.kind[g] == 5) bld32(mraw, mo, wsrd);
          if constexpr (g == G_XCHK) { wait_vm<cv_younger(g, 2, 2)>(); }
          if constexpr (g >= G_XCHK && g < G_XCHK + 3) { constexpr int i = g - G_XCHK; nm[2 * i] = v_ne64(u32x2{pf[i][0], pf[i][1]}, tag); nm[2 * i + 1] = v_ne64(u32x2{pf[i][2], pf[i][3]}, tag); }
          if constexpr (g == G_XCHK + 3) {
            if (__builtin_expect(((nm[0] | nm[1]) | (nm[2] | nm[3]) | (nm[4] | nm[5])) != 0, 0)) poll<3, 1>(rw, pa1, tag);
          }
          if constexpr (e == CBE && n == 8) wait_vm<cv_younger(g, 5, 0)>();
          if constexpr (e == CBE && n >= 9 && n < 21) mop(IC<n - 9>{});
          if constexpr (e == CBE && n == 23) { wait_vm<cv_younger(g, 4, 3)>(); bar(); }
        });
      });
    });
    asm volatile("s_nop 7");
    static_for<0, 4>([&](auto NP) {
      constexpr int np = NP.value;
      static_for<(CS.op[288] > 32 * np ? CS.op[288] : 32 * np), 32 * np + 32>(xop);
      u32x4 o;
#pragma unroll
      for (int u = 0; u < 2; ++u) {
        const int nt = 2 * np + u;
        pin(acc[nt]);
        o[2 * u] = pack_bf16(fmaf(Hh[nt][0], acc[nt][0], T[nt][0]), fmaf(Hh[nt][1], acc[nt][1], T[nt][1]));
        o[2 * u + 1] = pack_bf16(fmaf(Hh[nt][2], acc[nt][2], T[nt][2]), fmaf(Hh[nt][3], acc[nt][3], T[nt][3]));
      }
      st128(o, rw, l16 + np * 1024, O_X2T + d * (TP_ * 2));
    });
  }
  const int kg = lane >> 4, c16 = lane & 15;
  const int kq0 = nc >> 2, r0 = (nc & 1) * 4 + wv, sgb = O_SG + (mt * 2 + ((nc >> 1) & 1)) * 8192;
  u32x2 tag2 = {(u32)tag, (u32)(tag >> 32)};
  u32x4 gbr;
  pin(tag2);
  {
    const int fb = O_F2 + (b & 7) * 32 * 128;
    const int jw = (lane & 3) * 24 + ((nc * 12 + wv * 3 + ((lane >> 2) & 1) * 2) >> 3);
    const int pa[1] = {fb + (20 + (jw >> 3)) * 128 + (jw & 6) * 8};
    u32x4 pfa;
    gld128<0>(pfa, pa[0], ws);
    const char* wsrc = ws + O_W1F + (nc * 4 + wv) * W1_STEPS * 3072;
    const char* wq[4];
    unsigned xinc[4];
#pragma unroll
    for (int i = 0; i < 4; ++i) {
      wq[i] = wsrc + ((kq0 + i) & 3) * (5 * 3072) + 4096;
      xinc[i] = (i && kq0 + i == 4) ? (unsigned)(-19 * 32 * TP_ * 2) : 32 * TP_ * 2;
      asm volatile("" : "+s"(wq[i]), "+s"(xinc[i]));
    }
    u32x4 W[G1_R][3], WA_[24], WC[3], pfs[2], pfw, gran;
    auto issue_w = [&](auto I) { constexpr int i = I.value; gld128b<(((i % 15) & 7) - 4) * 1024>(W[(i / 3) % G1_R][i % 3], ((i % 15) & 8) ? l16b : l16, wq[i / 15]); };
    int pc[2] = {fb + (lane >> 2) * 128 + (lane & 3) * 16, fb + (16 + (lane >> 2)) * 128 + (lane & 3) * 16};
    int f2o = lane < 8 ? O_F2 + (lane * 32 + (b >> 3)) * 128 + (b & 7) * 8 : WS_BYTES;
    pin(pc[0]); pin(pc[1]); pin(f2o);
    asm volatile("s_waitcnt vmcnt(1) lgkmcnt(0)\n\ts_barrier" ::: "memory");
    if (wv == 0) __builtin_amdgcn_raw_buffer_store_b64(tag2, rw, f2o, 0, SC1);
    if (bs >= CWG) {
      const int j = bs - CWG;
      u32x2 dl[5], dh[5];
      const unsigned add = sbase + AD_OFF + (8 * kg + (c16 >> 2)) * 128 + (lane & 3) * 8;
#pragma unroll
      for (int i = 0; i < 5; ++i) { const int tl = wv * 5 + i; const unsigned a2 = add + (tl >> 2) * 4096 + (tl & 3) * 32; ds_tr<0>(dl[i], a2); ds_tr<512>(dh[i], a2); }
      wait_lgkm<0>();
#pragma unroll
      for (int i = 0; i < 5; ++i) {
        pin(dl[i]); pin(dh[i]);
        __builtin_amdgcn_raw_buffer_store_b128(cat(dl[i], dh[i]), rw, l16, O_W2F + ((((j & 1) * 4 + wv) * 48 + (j >> 1)) * 5 + i) * 1024, SC1);
      }
      wait_vm<5>();
    } else {
      if (wv == 0) wait_vm<1>(); else wait_vm<0>();
    }
    pin(pfa);
    if (!__all(tag_ok(pfa, tag))) poll<1, 2>(rw, pa, tag);
    auto issue_a = [&](auto I) { constexpr int i = 36 + I.value; gld128a<(((i % 15) & 7) - 4) * 1024>(WA_[I.value], ((i % 15) & 8) ? l16b : l16, wq[i / 15]); };
    static_for<0, 3 * G1_WPRE>(issue_w);
    unsigned xoff[2];
#pragma unroll
    for (int p = 0; p < 2; ++p) {
      const int k = 4 * p + kg;
      xoff[p] = (k * TP_ + ((c16 ^ (2 * k)) * 8)) * 2 - p * 1024;
      pin(xoff[p]);
    }
    unsigned xso = O_X2T + ((kq0 * 160 - 32 + 8 * wv) * TP_ + mt * 128) * 2;
    const unsigned ldsw = sbase + wv * 2048;
    unsigned sgl = sgb + wv * 2048 + l16;
    int hvo = kg < 3 ? ((3 * wv + kg) >> 2) * 1024 + ((3 * wv + kg) & 3) * 256 + c16 * 16 : WS_BYTES;
    const int hso = O_H2F + (mt * 8 * 48 + 3 * nc) * 1024;
    unsigned spa = sbase + G1_SP + c16 * 16;
    int sgo = (c16 >> 2) == kg ? sgb + (r0 * 4 + kq0) * 256 + c16 * 16 : WS_BYTES;
    pin(sgl); pin(hvo); pin(sgo); pin(spa);
    int pw[1] = {O_FW + ((b & 7) * 12 + (lane < 48 ? lane >> 2 : 0)) * 128 + (lane & 3) * 16};
    unsigned xo = ((lane >> 3) * TP_ + (lane & 7) * 8) * 2;
    const char* xs = ws + O_X2T + (N * 80 * TP_ + M * 64) * 2;
    unsigned gbo = O_MODP + (((lane & 7) < MODP ? (lane & 7) : 0) * NMOD + 5 * D_ + N * 80 + wv * 20 + (lane < 40 ? lane >> 3 : 0) * 4) * 4;
    pin(pw[0]); pin(xo); pin(gbo);
    u32 hq[4]; u32x4 hs, H[G2_R], WV[23], pf3, ob;
    int po3 = O_F3 + (mt * 16 + nc) * 512 + wv * 32 + (lane & 1) * 16;
    int f3o = tid < 16 ? O_F3 + (mt * 16 + tid) * 512 + nc * 8 : WS_BYTES;
    int oo;
    int fwo = (bs >= CWG && tid < 8) ? O_FW + (tid * 12 + ((bs - CWG) >> 3)) * 128 + (bs & 7) * 8 : WS_BYTES;
    pin(po3); pin(f3o); pin(fwo);
    const char* hp = ws + O_H2F + (M * 4 * 48 + wv * 12) * 1024 + 4096;
    u16* outp;
    asm volatile("s_load_dwordx2 %0, %1, 0x60\n\ts_waitcnt lgkmcnt(0)" : "=s"(outp) : "s"(__builtin_amdgcn_kernarg_segment_ptr()));
    const rsrc_t ro = mk_rsrc(outp, S_ * D_ * 2);
    auto issue = [&](auto O) {
      constexpr int kind = GS.kind[O.value], i = GS.idx[O.value];
      if constexpr (kind == 0) {
        if constexpr (i & 1) xdma1(xoff[1], wsrd, xso);
        else xdma0<g1_slot(i >> 1) * 8192>(ldsw, xoff[0], wsrd, xso, xinc[(i % 10) ? 0 : i / 10]);
      }
      else if constexpr (kind == 1) issue_w(IC<i>{});
      else if constexpr (kind == 2) {
        if constexpr (i >= 37 && O.value < GS.first[160]) gld128b<((i & 7) - 4) * 1024>(WV[i - 37], (i & 8) ? l16b : l16, wdp + (i >> 4) * 16384);
        else issue_wd(IC<i>{});
      }
      else if constexpr (kind == 3) {
        if constexpr (i < 2) dma16<XR_OFF + i * 4160>(sbase + wv * 1040, xo, xs + (size_t)(wv + 4 * i) * 8 * TP_ * 2);
        else if constexpr (i == 2) dma16<XR_OFF + 8320>(sbase + (wv & 1) * 1040, xo, xs + (size_t)((wv & 1) + 8) * 8 * TP_ * 2);
        else gld128<0>(gbr, gbo, ws);
      }
      else if constexpr (kind == 4) gld128<0>(pfw, pw[0], ws);
      else if constexpr (kind == 5) __builtin_amdgcn_raw_buffer_store_b128(gran, rw, sgo, 0, SC1);
      else if constexpr (kind == 6) gld128<i * 1024>(pfs[i], sgl, ws);
      else if constexpr (kind == 7) { hs = u32x4{hq[0], hq[1], hq[2], hq[3]}; __builtin_amdgcn_raw_buffer_store_b128(hs, rw, hvo, hso + g1_ord(i) * (48 * 1024), SC1); }
      else if constexpr (kind == 8) issue_a(IC<i>{});
      else if constexpr (kind == 9) __builtin_amdgcn_raw_buffer_store_b64(tag2, rw, f3o + i * 128, 0, SC1);
      else if constexpr (kind == 10) gld128<i * 128>(pf3, po3, ws);
      else if constexpr (kind == 11) gld128b<(((i % 12) & 7) - 4) * 1024>(H[i % G2_R], ((i % 12) & 8) ? l16b : l16, hp + (i / 12) * (48 * 1024));
      else if constexpr (kind == 12) __builtin_amdgcn_raw_buffer_store_b128(ob, ro, oo, i * (32 * D_ * 2), 0);
      else __builtin_amdgcn_raw_buffer_store_b64(tag2, rw, fwo, 0, SC1);
    };
    auto check3 = [&](auto ST) {
      if (__builtin_expect((v_ne64(u32x2{pf3[0], pf3[1]}, tag) | v_ne64(u32x2{pf3[2], pf3[3]}, tag)) != 0, 0)) {
        const int po[1] = {po3 + ST.value * 128};
        poll<1, 0>(rw, po, tag);
      }
    };
    unsigned xa[9], xm[8];
#pragma unroll
    for (int jj = 0; jj < 9; ++jj) {
      const int rr = 4 * kg + (c16 >> 2);
      xa[jj] = sbase + rr * 256 + (((jj < 8 ? jj : r0) ^ (rr & 7)) * 32) + (lane & 3) * 8;
      pin(xa[jj]);
    }
    u32x4 ones = {0x3f803f80u, 0x3f803f80u, 0x3f803f80u, 0x3f803f80u};
    pin(ones);
    static_for<0, 3>([&](auto I) { gld128a<I.value * 1024>(WC[I.value], l16, wsrc + 20 * 3072); });
    static_for<0, WA - PLATE>(issue_a);
    if (wv == 0) { gld128<0>(pfs[0], pc[0], ws); gld128<0>(pfs[1], pc[1], ws); }
    static_for<WA - PLATE, WA>(issue_a);
    if (wv == 0) {
      wait_vm<PLATE>();
      pin(pfs[0]); pin(pfs[1]);
      if (!__all(tag_ok(pfs[0], tag) && tag_ok(pfs[1], tag))) poll<2, PSL>(rw, pc, tag);
    } else {
      wait_vm<3 * G1_WPRE + 3 + WA>();
    }
    bar();
    static_for<0, GS.first[0]>(issue);
    f32x4 acc[8][3];
    u32x2 X[G1_LA + 1][2], XS[5][2];
    auto xread = [&](auto T, auto HH) {
      constexpr int t = T.value, h = HH.value;
      if constexpr (t < 96) ds_tr<g1_slot(g1_ks(t)) * 8192 + h * 4096>(X[t % (G1_LA + 1)][h], xa[g1_j(t)]);
      else ds_tr<(g1_ks(t) - 12) * 8192 + h * 4096>(X[t % (G1_LA + 1)][h], xm[g1_j(t)]);
    };
    f32x4 gram, sum1;
    u32 fs[4], fa; u64 sbad;
    unsigned spw = sbase + G1_SP + (2 * wv + (kg >> 1)) * 256 + c16 * 16;
    pin(spw);
    auto fop = [&](auto K) {
      constexpr int k = K.value;
      if constexpr (k == 0) asm_and3(fa, pfs[0][1], pfs[0][3], pfs[1][1]);
      else if constexpr (k == 1) asm_and3(fa, fa, pfs[1][3], pfs[1][3]);
      else if constexpr (k == 2) asm_ne1(sbad, fa);
      else if constexpr (k == 4) v_swap32(fs[0], fs[1]);
      else if constexpr (k == 5) v_swap32(fs[2], fs[3]);
      else if constexpr (k == 6) asm_op2<0>(fs[0], fs[0], fs[1]);
      else if constexpr (k == 7) asm_op2<0>(fs[2], fs[2], fs[3]);
      else if constexpr (k == 8) v_swap16(fs[0], fs[2]);
      else if constexpr (k == 9) asm_op2<0>(fs[0], fs[0], fs[2]);
      else if constexpr (k == 10) asm_op1<0>(fs[0], fs[0]);
      else if constexpr (k == 11) asm_op1<1>(fs[1], fs[0]);
      else if constexpr (k == 12) v_swap16(fs[0], fs[1]);
      else if constexpr (k == 13) asm_op2<1>(fs[1], fs[0], fs[1]);
      else if constexpr (k == 14) asm_op1<2>(fs[1], fs[1]);
      else if constexpr (k == 15) asm_op1<3>(fs[1], fs[1]);
      else if constexpr (k == 16) asm_op1<4>(fs[1], fs[1]);
      else if constexpr (k == 17) asm_op1<3>(fs[1], fs[1]);
      else if constexpr (k == 18) asm_op1<5>(fs[1], fs[1]);
      else if constexpr (k == 19) asm_op2<2>(fs[0], fs[0], fs[1]);
      else if constexpr (k == 20) asm_op1<1>(fs[1], fs[0]);
      else if constexpr (k == 21) asm_op1<1>(fs[2], fs[0]);
      else if constexpr (k == 22) asm_op1<6>(fs[3], fs[0]);
      else if constexpr (k == 23) ds_w128u<0>(spw, u32x4{fs[0], fs[1], fs[2], fs[3]});
    };
    float eg[6]; u32x4 xc;
    auto eop = [&](auto J, auto K) {
      constexpr int j = J.value, k = K.value;
      if constexpr (k < 15) {
        if constexpr (k < 6) eg[k] = v_relu(acc[j][k >> 1][2 * (k & 1)]);
        else if constexpr (k < 12) eg[k - 6] = v_mul(eg[k - 6], acc[j][(k - 6) >> 1][2 * (k & 1) + 1]);
        else hq[k == 12 ? 1 : k == 13 ? 0 : 2] = v_cvt(eg[k == 12 ? 2 : k == 13 ? 0 : 4], eg[k == 12 ? 3 : k == 13 ? 1 : 5]);
      } else if constexpr (k == 15) v_swap32(hq[1], hq[3]);
      else if constexpr (k == 16) v_swap32(hq[0], hq[2]);
      else if constexpr (k == 17) v_swap16(hq[0], hq[1]);
      else v_swap16(hq[2], hq[3]);
    };
    unsigned pwa, pra, gba, xad, sda;
    float xr[8], sdv, G[8], S[8], rs2;
    hq[3] = 0;
    pin(hq[3]);
    wait_vm<GS.first[0] - 2>();
    bar();
    static_for<0, G1_LA>([&](auto T) { xread(T, IC<0>{}); xread(T, IC<1>{}); });
    static_for<0, 160>([&](auto T) {
      constexpr int t = T.value, ks = g1_ks(t), jj = g1_j(t), tn = t + G1_LA, u = t & 7;
      if constexpr (!(t & 1)) wait_lgkm<2 * ((tn <= 160 ? tn : 160) - t - 2)>();
      if constexpr (g1_need(t) >= 0) {
        static_assert(g1_need(t) < GS.first[t], "not issued yet");
        if constexpr (GS.first[t] - 1 - g1_need(t) < 63) wait_vm<GS.first[t] - 1 - g1_need(t)>();
        bar();
      }
      if constexpr (t == T_WCHK) {
        if (__builtin_expect((v_ne64(u32x2{pfw[0], pfw[1]}, tag) | v_ne64(u32x2{pfw[2], pfw[3]}, tag)) != 0, 0)) poll<1, 1>(rw, pw, tag);
      }
      if constexpr (t == T_C0) { wait_vm<GS.first[t] - 1 - g_pos(10, 0)>(); check3(IC<0>{}); }
      if constexpr (t == T_SETUP) {
        static_assert(g_pos(3, 3) < g1_need(T_F0) && T_SETUP > T_F0, "gate partials landed");
        {
          pin(gbr);
          const float g = sum8((lane & 7) < MODP ? __uint_as_float(gbr[0]) : 0.f), g2 = sum8((lane & 7) < MODP ? __uint_as_float(gbr[1]) : 0.f);
          const float g3 = sum8((lane & 7) < MODP ? __uint_as_float(gbr[2]) : 0.f), g4 = sum8((lane & 7) < MODP ? __uint_as_float(gbr[3]) : 0.f);
          if ((lane & 7) == 0 && lane < 40) *(f32x4*)(smem + GB_OFF + wv * 80 + (lane >> 3) * 16) = f32x4{rbf(g), rbf(g2), rbf(g3), rbf(g4)};
        }
        const int lf = lane < 40 ? lane : 0, ft = 4 * wv + lf / 10, fc = lf % 10;
        pwa = sbase + wv * 5440 + l16 + (kg >> 1) * 32;
        pra = sbase + fc * 544 + ft * 16; gba = sbase + GB_OFF + fc * 32;
        xad = sbase + XR_OFF + fc * 1040 + ft * 2; sda = sbase + G1_SP + ((nc & 4) * 16 + ft) * 16 + 2;
        {
          const int pos = M * 64 + ft, l8 = (pos >> 3) & 63;
          const int tok = 256 * (2 * (pos >> 9) + ((pos >> 2) & 1)) + 16 * (l8 & 15) + 4 * (l8 >> 4) + (pos & 3);
          oo = lane < 40 ? (tok * D_ + N * 80 + 8 * fc) * 2 : S_ * D_ * 2;
        }
        sdv = 0.f;
#pragma unroll
        for (int e = 0; e < 8; ++e) { xr[e] = 0.f; pin(xr[e]); }
        pin(pwa); pin(pra); pin(gba); pin(xad); pin(sda); pin(oo); pin(sdv);
      }
      if constexpr (t == T_SST - 2) {
        pin(gram); pin(sum1);
        const int c3 = lane & 3;
        gran = u32x4{__float_as_uint(sum1[0]), 1u, __float_as_uint(c3 == 0 ? gram[0] : c3 == 1 ? gram[1] : c3 == 2 ? gram[2] : gram[3]), 1u};
        pin(gran);
      }
      if constexpr (t == 86) {
        if (__builtin_expect(sbad != 0, 0)) {
#pragma unroll 1
          for (int spn = 0; spn < SPIN_CAP; ++spn) {
            pfs[0] = __builtin_amdgcn_raw_buffer_load_b128(rw, sgl, 0, SC1);
            pfs[1] = __builtin_amdgcn_raw_buffer_load_b128(rw, sgl + 1024, 0, SC1);
            if (__all((pfs[0][1] & pfs[0][3] & pfs[1][1] & pfs[1][3]) == 1u)) break;
            asm volatile("" ::: "memory");
          }
        }
        fs[0] = pfs[0][0]; fs[1] = pfs[1][0]; fs[2] = pfs[0][2]; fs[3] = pfs[1][2];
      }
      auto ep = [&](auto A, auto B) { if constexpr (t >= 104) static_for<A.value, (B.value < 19 ? B.value : 19)>([&](auto K) { eop(IC<g1_j(t - 8)>{}, K); }); };
      constexpr int k0 = u == 0 ? 0 : 4 * u - 3;
      constexpr int sr = t == 0 ? 0 : t == 4 ? 1 : (t >= 12 && t < 15) ? t - 10 : -1, sm = t == 4 ? 0 : t == 8 ? 1 : (t >= 16 && t < 19) ? t - 14 : -1;
      const u32x4 xb = cat(X[t % (G1_LA + 1)][0], X[t % (G1_LA + 1)][1]);
      if constexpr (ks >= 12) mfma_a<false>(acc[jj][0], WA_[3 * (ks - 12) + 0], xb); else mfma_<ks == 0>(acc[jj][0], W[ks % G1_R][0], xb);
      if constexpr (tn < 160) xread(IC<tn>{}, IC<0>{});
      if constexpr (t >= 84 && t < 96) fop(IC<2 * (t - 84)>{});
      if constexpr (u > 0) ep(IC<k0>{}, IC<k0 + 1>{});
      if constexpr (ks >= 12) mfma_a<false>(acc[jj][1], WA_[3 * (ks - 12) + 1], xb); else mfma_<ks == 0>(acc[jj][1], W[ks % G1_R][1], xb);
      if constexpr (tn < 160) xread(IC<tn>{}, IC<1>{});
      if constexpr (t >= 84 && t < 96) fop(IC<2 * (t - 84) + 1>{});
      if constexpr (u > 0) ep(IC<k0 + 1>{}, IC<k0 + 2>{}); else ep(IC<0>{}, IC<1>{});
      if constexpr (ks >= 12) mfma_a<false>(acc[jj][2], WA_[3 * (ks - 12) + 2], xb); else mfma_<ks == 0>(acc[jj][2], W[ks % G1_R][2], xb);
      if constexpr (t >= 96) real();
      static_for<GS.first[t], GS.first[t + 1]>(issue);
      if constexpr (u > 0) ep(IC<k0 + 2>{}, IC<k0 + 4>{});
      if constexpr (t >= 84 && t < 92) xm[t - 84] = v_addc(xa[t - 84], G1_RS * 8192);
      if constexpr (sm >= 0) { mfma_<sm == 0>(gram, cat(XS[sm][0], XS[sm][1]), cat(XS[sm][0], XS[sm][1])); mfma_<sm == 0>(sum1, ones, cat(XS[sm][0], XS[sm][1])); }
      if constexpr (sr >= 0) { ds_tr<sr * 8192>(XS[sr][0], xa[8]); ds_tr<sr * 8192 + 4096>(XS[sr][1], xa[8]); }
      if constexpr (t >= 96 && u == (t < 104 ? 4 : 0)) ds_b128<jj * 256>(xc, spa);
      if constexpr (t >= 96 && u == 7) {
        if constexpr (t < 104) wait_lgkm<6>();
        mfma_a<false>(acc[jj][0], WC[0], xc);
        mfma_a<false>(acc[jj][1], WC[1], xc);
        mfma_a<false>(acc[jj][2], WC[2], xc);
      }
    });
    asm volatile("s_nop 5");
    static_for<0, 15>([&](auto K) { eop(IC<7>{}, K); });
    eop(IC<7>{}, IC<15>{}); asm volatile("s_nop 0"); eop(IC<7>{}, IC<16>{}); asm volatile("s_nop 1"); eop(IC<7>{}, IC<17>{}); eop(IC<7>{}, IC<18>{});
    static_for<GS.first[160], GS.mid>(issue);
    if constexpr (T_C0 >= 160) { wait_vm<GS.mid - 1 - g_pos(10, 0)>(); check3(IC<0>{}); }
    static_for<GS.mid, GS.first2[0]>(issue);
    if constexpr (g_lastdv() >= 0) wait_vm<GS.first2[0] - 1 - g_lastdv()>();
    static_for<37, 60>([&](auto I) { if constexpr (g_pos(2, I.value) < GS.first[160]) to_acc(WD[I.value], WV[I.value - 37]); });
    f32x4 gbv[2], ac2[2][5], pt[8];
    auto pre = [&](auto J, auto K) {
      constexpr int j = J.value, k = K.value;
      if constexpr (k == 0) ds_hi16<j * 256>(sdv, sda);
      else if constexpr (k < 9) ds_hi16<(k - 1) * 128 + j * 32>(xr[k - 1], xad);
      else if constexpr (k == 9) rs2 = v_rcp(sdv);
      else if constexpr (k == 10) rs2 = v_mul(rs2, rs2);
      else G[k - 11] = v_mul(gbv[(k - 11) >> 2][(k - 11) & 3], rs2);
    };
    auto fin = [&](auto K) {
      constexpr int k = K.value, e = k & 7;
      if constexpr (k < 8) S[e] = v_add(pt[e >> 2][e & 3], pt[2 + (e >> 2)][e & 3]);
      else if constexpr (k < 24) S[e] = v_add(S[e], pt[2 * (k >> 3) + 2 + (e >> 2)][e & 3]);
      else if constexpr (k < 32) S[e] = v_fma(G[e], S[e], xr[e]);
      else ob[k - 32] = v_cvt(S[2 * (k - 32)], S[2 * (k - 32) + 1]);
    };
    auto prd = [&](auto J, auto K) { ds_f128<(J.value & 1) * 21760 + (K.value >> 1) * 5440 + (K.value & 1) * 256>(pt[K.value], pra); };
    static_for<0, 240>([&](auto GG) {
      constexpr int g = GG.value, o = g / 5, jj = o / 12, ks = o % 12, i = g % 5, q = g % 60;
      if constexpr (i == 0) { static_assert(g2_need(o) < GS.first2[g], "not issued yet"); wait_vm<GS.first2[g] - 1 - g2_need(o)>(); }
      if constexpr (g == G_F3) { wait_vm<GS.first2[g] - 1 - g_pos(7, 7)>(); bar(); }
      if constexpr (g == G_C1) { wait_vm<GS.first2[g] - 1 - g_pos(10, 1)>(); check3(IC<1>{}); }
      if constexpr (g == G_C2) { wait_vm<GS.first2[g] - 1 - g_pos(10, 2)>(); check3(IC<2>{}); }
      if constexpr (g == G_C3) { wait_vm<GS.first2[g] - 1 - g_pos(10, 3)>(); check3(IC<3>{}); }
      mfma_a<ks == 0>(ac2[jj & 1][i], WD[ks * 5 + i], H[o % G2_R]);
      if constexpr (i == 4) real();
      static_for<GS.first2[g], GS.first2[g + 1]>(issue);
      if constexpr (g == 30 || g == 32) ds_f128<(g - 30) * 8>(gbv[(g - 30) / 2], gba);
      if constexpr (jj >= 1) {
        if constexpr (q >= 5 && q < 15 && (q & 1)) ds_w128<((jj - 1) & 1) * 21760 + ((q - 5) / 2) * 1088>(pwa, ac2[(jj - 1) & 1][(q - 5) / 2]);
        if constexpr (q == 18) { wait_lgkm<0>(); bar(); }
        if constexpr (q >= 19 && q < 27) prd(IC<jj - 1>{}, IC<q - 19>{});
        if constexpr (q == 30) wait_lgkm<0>();
        if constexpr (q >= 31 && q < 31 + FINW) static_for<(36 * (q - 31) + FINW - 1) / FINW, (36 * (q - 30) + FINW - 1) / FINW>(fin);
      }
      if constexpr (jj >= 1 && q < 18 && !(q & 1)) pre(IC<jj - 1>{}, IC<q / 2>{});
      if constexpr (jj >= 1 && q >= 19 && q < 29) pre(IC<jj - 1>{}, IC<q - 10>{});
    });
    static_for<0, 9>([&](auto K) { pre(IC<3>{}, K); });
    static_for<0, 5>([&](auto I) { ds_w128<21760 + I.value * 1088>(pwa, ac2[1][I.value]); });
    wait_lgkm<5>();
    pre(IC<3>{}, IC<9>{}); asm volatile("s_nop 0");
    static_for<10, 19>([&](auto K) { pre(IC<3>{}, K); });
    wait_lgkm<0>(); bar();
    static_for<0, 8>([&](auto K) { prd(IC<3>{}, K); });
    wait_lgkm<4>();
    static_for<0, 8>(fin);
    wait_lgkm<0>();
    static_for<8, 36>(fin);
    __builtin_amdgcn_raw_buffer_store_b128(ob, ro, oo, 3 * (32 * D_ * 2), 0);
  }
  if (b == 0 && tid == 0) st_flag(ws, O_EPOCH, tag);
}

extern "C" void kernel_launch(void* const* d_in, const int* in_sizes, int n_in, void* d_out, int out_size,
                              void* d_ws, size_t ws_size, hipStream_t stream) {
  k_fused<<<256, 256, 0, stream>>>((const u16*)d_in[0], (const u16*)d_in[1], (const u16*)d_in[2], (const u16*)d_in[3],
                                   (const u16*)d_in[4], (const u16*)d_in[5], (const u16*)d_in[7], (const u16*)d_in[8],
                                   (const u16*)d_in[9], (const u16*)d_in[10], (const u16*)d_in[11], (char*)d_ws, (u16*)d_out);
}
```

```cpp
#include <hip/hip_runtime.h>
#include <stdint.h>

typedef unsigned short u16;
typedef unsigned int u32;
typedef __attribute__((ext_vector_type(2))) __bf16 bf16x2;
typedef __attribute__((ext_vector_type(4))) float f32x4;
typedef __attribute__((ext_vector_type(2))) float f32x2;
typedef __attribute__((ext_vector_type(4))) u32 u32x4;
typedef __attribute__((ext_vector_type(2))) u32 u32x2;

constexpr int S_ = 2048, D_ = 640, F_ = 1536;
constexpr int TP_ = S_ + 64;
constexpr int NMOD = 6 * D_;
constexpr int MODP = 5;
constexpr int CWG = 160;

__device__ __forceinline__ float bflo(u32 w) { return __uint_as_float(w << 16); }
__device__ __forceinline__ float bfhi(u32 w) { return __uint_as_float(w & 0xffff0000u); }
__device__ __forceinline__ u32 pack_bf16(float lo, float hi) {
  bf16x2 v; v[0] = (__bf16)lo; v[1] = (__bf16)hi; return __builtin_bit_cast(u32, v);
}
__device__ __forceinline__ float rbf(float x) { __bf16 b = (__bf16)x; return (float)b; }
#define SCHED() __builtin_amdgcn_sched_barrier(0)
template <int V> struct IC { static constexpr int value = V; };
template <int B, int E, class Fn> __device__ __forceinline__ void static_for(Fn&& f) {
  if constexpr (B < E) { f(IC<B>{}); static_for<B + 1, E>(f); }
}
__device__ __forceinline__ void mfma(f32x4& c, u32x4 a, u32x4 b) {
  asm volatile("v_mfma_f32_16x16x32_bf16 %0, %1, %2, %0" : "+v"(c) : "v"(a), "v"(b));
}
__device__ __forceinline__ void mfma0(f32x4& c, u32x4 a, u32x4 b) {
  asm volatile("v_mfma_f32_16x16x32_bf16 %0, %1, %2, 0" : "=&v"(c) : "v"(a), "v"(b));
}
template <bool FIRST> __device__ __forceinline__ void mfma_(f32x4& c, u32x4 a, u32x4 b) { if constexpr (FIRST) mfma0(c, a, b); else mfma(c, a, b); }
template <int OFF> __device__ __forceinline__ void ds_tr(u32x2& dst, unsigned addr) {
  asm volatile("ds_read_b64_tr_b16 %0, %1 offset:%2" : "=v"(dst) : "v"(addr), "i"(OFF) : "memory");
}
template <int OFF> __device__ __forceinline__ void ds_b32(u32& dst, unsigned addr) {
  asm volatile("ds_read_b32 %0, %1 offset:%2" : "=v"(dst) : "v"(addr), "i"(OFF) : "memory");
}
template <int OFF> __device__ __forceinline__ void ds_b64(u32x2& dst, unsigned addr) {
  asm volatile("ds_read_b64 %0, %1 offset:%2" : "=v"(dst) : "v"(addr), "i"(OFF) : "memory");
}
template <int OFF> __device__ __forceinline__ void ds_b128(u32x4& dst, unsigned addr) {
  asm volatile("ds_read_b128 %0, %1 offset:%2" : "=v"(dst) : "v"(addr), "i"(OFF) : "memory");
}
template <int OFF> __device__ __forceinline__ void ds_w128(unsigned addr, f32x4 v) {
  asm volatile("ds_write_b128 %0, %1 offset:%2" :: "v"(addr), "v"(v), "i"(OFF) : "memory");
}
template <int OFF> __device__ __forceinline__ void gld128(u32x4& dst, unsigned voff, const char* sb) {
  asm volatile("global_load_dwordx4 %0, %1, %2 offset:%3 sc1" : "=v"(dst) : "v"(voff), "s"(sb), "i"(OFF) : "memory");
}
template <int LOFF> __device__ __forceinline__ void dma16(unsigned ldsb, unsigned voff, const char* sb) {
  asm volatile("s_add_u32 m0, %0, %3\n\ts_nop 0\n\tglobal_load_lds_dwordx4 %1, %2 sc1" :: "s"(ldsb), "v"(voff), "s"(sb), "i"(LOFF) : "memory", "scc");
}
template <int LOFF> __device__ __forceinline__ void dma16x4(unsigned ldsb, unsigned v0, unsigned v1, const char* sb) {
  asm volatile("s_add_u32 m0, %0, %4\n\ts_nop 0\n\tglobal_load_lds_dwordx4 %1, %3 sc1\n\tglobal_load_lds_dwordx4 %2, %3 offset:1024 sc1\n\t"
               "global_load_lds_dwordx4 %1, %3 offset:2048 sc1\n\tglobal_load_lds_dwordx4 %2, %3 offset:3072 sc1"
               :: "s"(ldsb), "v"(v0), "v"(v1), "s"(sb), "i"(LOFF) : "memory", "scc");
}
template <int N> __device__ __forceinline__ void wait_vm() { asm volatile("s_waitcnt vmcnt(%0)" :: "i"(N) : "memory"); }
template <int N> __device__ __forceinline__ void wait_lgkm() { asm volatile("s_waitcnt lgkmcnt(%0)" :: "i"(N) : "memory"); }
__device__ __forceinline__ void real() { __builtin_amdgcn_s_setprio(0); }
__device__ __forceinline__ void bar() { asm volatile("s_barrier" ::: "memory"); }
template <class T> __device__ __forceinline__ void pin(T& a) { asm volatile("" : "+v"(a)); }
__device__ __forceinline__ u32x4 cat(u32x2 a, u32x2 b) { return u32x4{a[0], a[1], b[0], b[1]}; }

typedef __amdgpu_buffer_rsrc_t rsrc_t;
__device__ __forceinline__ rsrc_t mk_rsrc(const void* p, int bytes) { return __builtin_amdgcn_make_buffer_rsrc((void*)p, 0, bytes, 0x00020000); }
template <int CTRL> __device__ __forceinline__ float dpp_add(float x) {
  return x + __builtin_bit_cast(float, __builtin_amdgcn_mov_dpp(__builtin_bit_cast(int, x), CTRL, 0xf, 0xf, true));
}
constexpr int DPP_XOR1 = 0xB1, DPP_XOR2 = 0x4E, DPP_HMIRROR = 0x141, DPP_ROR8 = 0x128;
__device__ __forceinline__ float sum8(float x) { return dpp_add<DPP_HMIRROR>(dpp_add<DPP_XOR2>(dpp_add<DPP_XOR1>(x))); }
__device__ __forceinline__ float tred32(float a, float b) {
  auto r = __builtin_amdgcn_permlane32_swap(__float_as_uint(a), __float_as_uint(b), false, false);
  return __uint_as_float(r[0]) + __uint_as_float(r[1]);
}
__device__ __forceinline__ float tred16(float a, float b) {
  auto r = __builtin_amdgcn_permlane16_swap(__float_as_uint(a), __float_as_uint(b), false, false);
  return __uint_as_float(r[0]) + __uint_as_float(r[1]);
}

typedef unsigned long long u64;
#define GAS __attribute__((address_space(1)))
constexpr int SC1 = 16;
#define BS " sc1"
constexpr int A0_DELAY = 16;
constexpr int A3_DELAY = 16;
constexpr int A4_DELAY = 16;
constexpr int AM = 0;
constexpr int AU_DELAY = 16;
constexpr int PE = 4;
constexpr int NT = 0;
template <int OFF> __device__ __forceinline__ void gld128b(u32x4& dst, unsigned voff, const char* sb) {
  asm volatile("global_load_dwordx4 %0, %1, %2 offset:%3" BS : "=v"(dst) : "v"(voff), "s"(sb), "i"(OFF) : "memory");
}
template <int OFF> __device__ __forceinline__ void gld128a(u32x4& dst, unsigned voff, const char* sb) {
  asm volatile("global_load_dwordx4 %0, %1, %2 offset:%3" BS : "=a"(dst) : "v"(voff), "s"(sb), "i"(OFF) : "memory");
}
template <bool FIRST> __device__ __forceinline__ void mfma_a(f32x4& c, u32x4 a, u32x4 b) {
  if constexpr (FIRST) asm volatile("v_mfma_f32_16x16x32_bf16 %0, %1, %2, 0" : "=&v"(c) : "a"(a), "v"(b));
  else asm volatile("v_mfma_f32_16x16x32_bf16 %0, %1, %2, %0" : "+v"(c) : "a"(a), "v"(b));
}
typedef __attribute__((ext_vector_type(8))) __bf16 bf16x8;
__device__ __forceinline__ f32x4 mfma_b(u32x4 a, u32x4 b, f32x4 c) {
  return __builtin_amdgcn_mfma_f32_16x16x32_bf16(__builtin_bit_cast(bf16x8, a), __builtin_bit_cast(bf16x8, b), c, 0, 0, 0);
}
template <int LOFF> __device__ __forceinline__ void dma16p(unsigned ldsb, unsigned voff, const char* sb) {
  asm volatile("s_add_u32 m0, %0, %3\n\ts_nop 0\n\tglobal_load_lds_dwordx4 %1, %2" :: "s"(ldsb), "v"(voff), "s"(sb), "i"(LOFF) : "memory", "scc");
}
template <int LOFF> __device__ __forceinline__ void dma16x4p(unsigned ldsb, unsigned v0, const char* sb) {
  asm volatile("s_add_u32 m0, %0, %3\n\ts_nop 0\n\tglobal_load_lds_dwordx4 %1, %2\n\tglobal_load_lds_dwordx4 %1, %2 offset:1024\n\t"
               "global_load_lds_dwordx4 %1, %2 offset:2048\n\tglobal_load_lds_dwordx4 %1, %2 offset:3072"
               :: "s"(ldsb), "v"(v0), "s"(sb), "i"(LOFF) : "memory", "scc");
}

__device__ __forceinline__ void gld128p(u32x4& dst, unsigned voff, const char* sb) {
  asm volatile("global_load_dwordx4 %0, %1, %2" : "=v"(dst) : "v"(voff), "s"(sb) : "memory");
}
template <int OFF> __device__ __forceinline__ void gld32p(u32& dst, unsigned voff, const char* sb) {
  asm volatile("global_load_dword %0, %1, %2 offset:%3" : "=v"(dst) : "v"(voff), "s"(sb), "i"(OFF) : "memory");
}
template <int OFF> __device__ __forceinline__ void gld16p(u32& dst, unsigned voff, const char* sb) {
  asm volatile("global_load_ushort %0, %1, %2 offset:%3" : "=v"(dst) : "v"(voff), "s"(sb), "i"(OFF) : "memory");
}
constexpr int X_POS = 1;
constexpr int F_POS = 1;
template <int OFF> __device__ __forceinline__ void dma16n(unsigned voff, const char* sb) {
  asm volatile("global_load_lds_dwordx4 %0, %1 offset:%2 sc1" :: "v"(voff), "s"(sb), "i"(OFF) : "memory");
}
__device__ __forceinline__ u64 v_ne64(u32x2 v, u64 tag) { u64 m; asm volatile("v_cmp_ne_u64 %0, %1, %2" : "=s"(m) : "v"(v), "s"(tag)); return m; }
__device__ __forceinline__ float v_lo(u32 w) { float d; asm volatile("v_lshlrev_b32 %0, 16, %1" : "=v"(d) : "v"(w)); return d; }
__device__ __forceinline__ float v_hi(u32 w) { float d; asm volatile("v_and_b32 %0, 0xffff0000, %1" : "=v"(d) : "v"(w)); return d; }
__device__ __forceinline__ float v_fma(float a, float b, float c) { float d; asm volatile("v_fma_f32 %0, %1, %2, %3" : "=v"(d) : "v"(a), "v"(b), "v"(c)); return d; }
__device__ __forceinline__ float v_mul(float a, float b) { float d; asm volatile("v_mul_f32 %0, %1, %2" : "=v"(d) : "v"(a), "v"(b)); return d; }
__device__ __forceinline__ float v_add(float a, float b) { float d; asm volatile("v_add_f32 %0, %1, %2" : "=v"(d) : "v"(a), "v"(b)); return d; }
__device__ __forceinline__ float v_relu(float a) { float d; asm volatile("v_max_i32 %0, 0, %1" : "=v"(d) : "v"(a)); return d; }
__device__ __forceinline__ u32 v_cvt(float a, float b) { u32 d; asm volatile("v_cvt_pk_bf16_f32 %0, %1, %2" : "=v"(d) : "v"(a), "v"(b)); return d; }
__device__ __forceinline__ void v_swap32(u32& a, u32& b) { asm volatile("v_permlane32_swap_b32 %0, %1" : "+v"(a), "+v"(b)); }
__device__ __forceinline__ void v_swap16(u32& a, u32& b) { asm volatile("v_permlane16_swap_b32 %0, %1" : "+v"(a), "+v"(b)); }
template <int OFF> __device__ __forceinline__ void ds_f128(f32x4& dst, unsigned addr) {
  asm volatile("ds_read_b128 %0, %1 offset:%2" : "=v"(dst) : "v"(addr), "i"(OFF) : "memory");
}
__device__ __forceinline__ void asm_and3(u32& d, u32 a, u32 b, u32 c) { asm volatile("v_bitop3_b32 %0, %1, %2, %3 bitop3:0x80" : "=v"(d) : "v"(a), "v"(b), "v"(c)); }
__device__ __forceinline__ void asm_ne1(unsigned long long& m, u32 a) { asm volatile("v_cmp_ne_u32 %0, 1, %1" : "=s"(m) : "v"(a)); }
template <int OP> __device__ __forceinline__ void asm_op1(u32& d, u32 a) {
  if constexpr (OP == 0) asm volatile("v_mul_f32 %0, 0x3acccccd, %1" : "=v"(d) : "v"(a));
  else if constexpr (OP == 1) asm volatile("v_mov_b32 %0, %1" : "=v"(d) : "v"(a));
  else if constexpr (OP == 2) asm volatile("v_max_f32 %0, 0, %1" : "=v"(d) : "v"(a));
  else if constexpr (OP == 3) asm volatile("v_cvt_pk_bf16_f32 %0, 0, %1" : "=v"(d) : "v"(a));
  else if constexpr (OP == 4) asm volatile("v_add_f32 %0, 0x3727c5ac, %1" : "=v"(d) : "v"(a));
  else if constexpr (OP == 5) asm volatile("v_sqrt_f32 %0, %1" : "=v"(d) : "v"(a));
  else asm volatile("v_mov_b32 %0, 0" : "=v"(d));
}
template <int OP> __device__ __forceinline__ void asm_op2(u32& d, u32 a, u32 b) {
  if constexpr (OP == 0) asm volatile("v_add_f32 %0, %1, %2" : "=v"(d) : "v"(a), "v"(b));
  else if constexpr (OP == 1) asm volatile("v_fma_f32 %0, -%1, %1, %2" : "=v"(d) : "v"(a), "v"(b));
  else asm volatile("v_cvt_pk_bf16_f32 %0, -%1, %2" : "=v"(d) : "v"(a), "v"(b));
}
template <int LOFF> __device__ __forceinline__ void xdma0(unsigned ldsb, unsigned voff, u32x4 srd, unsigned& soff, unsigned inc) {
  asm volatile("s_add_u32 m0, %1, %5\n\ts_add_u32 %0, %0, %4\n\tbuffer_load_dwordx4 %2, %3, %0 offen" BS " lds"
               : "+s"(soff) : "s"(ldsb), "v"(voff), "s"(srd), "s"(inc), "i"(LOFF) : "memory", "scc");
}
__device__ __forceinline__ void xdma1(unsigned voff, u32x4 srd, unsigned soff) {
  asm volatile("buffer_load_dwordx4 %0, %1, %2 offen offset:1024" BS " lds" :: "v"(voff), "s"(srd), "s"(soff) : "memory");
}
template <int OFF> __device__ __forceinline__ void ds_w128u(unsigned addr, u32x4 v) {
  asm volatile("ds_write_b128 %0, %1 offset:%2" :: "v"(addr), "v"(v), "i"(OFF) : "memory");
}
__device__ __forceinline__ float v_rcp(float a) { float d; asm volatile("v_rcp_f32 %0, %1" : "=v"(d) : "v"(a)); return d; }
template <int OFF> __device__ __forceinline__ void ds_hi16(float& dst, unsigned addr) {
  asm volatile("ds_read_u16_d16_hi %0, %1 offset:%2" : "+v"(dst) : "v"(addr), "i"(OFF) : "memory");
}
__device__ __forceinline__ unsigned v_addc(unsigned a, unsigned c) { unsigned d; asm volatile("v_add_u32 %0, %2, %1" : "=v"(d) : "v"(a), "s"(c)); return d; }
__device__ __forceinline__ void to_acc(u32x4& d, u32x4 v) {
  asm volatile("" : "+v"(v));
  asm volatile("" : "=a"(d) : "0"(v));
}
__device__ __forceinline__ void bld32(u32& dst, unsigned voff, u32x4 srd) {
  asm volatile("buffer_load_dword %0, %1, %2, 0 offen sc1" : "=v"(dst) : "v"(voff), "s"(srd) : "memory");
}
template <int K> __device__ __forceinline__ void asm_dpp(u32& x) {
  if constexpr (K == 0) asm volatile("v_add_f32_dpp %0, %0, %0 quad_perm:[1,0,3,2] row_mask:0xf bank_mask:0xf" : "+v"(x));
  else if constexpr (K == 1) asm volatile("v_add_f32_dpp %0, %0, %0 quad_perm:[2,3,0,1] row_mask:0xf bank_mask:0xf" : "+v"(x));
  else asm volatile("v_add_f32_dpp %0, %0, %0 row_half_mirror row_mask:0xf bank_mask:0xf" : "+v"(x));
}
template <int L> __device__ __forceinline__ void asm_rdl(u32& d, u32 x) { asm volatile("v_readlane_b32 %0, %1, %2" : "=s"(d) : "v"(x), "i"(L)); }
template <int OP> __device__ __forceinline__ void asm_sop(float& d, u32 s, float a) {
  if constexpr (OP == 0) asm volatile("v_mov_b32 %0, %1" : "=v"(d) : "s"(s));
  else if constexpr (OP == 1) asm volatile("v_add_f32 %0, 1.0, %1" : "=v"(d) : "s"(s));
  else asm volatile("v_mul_f32 %0, %1, %2" : "=v"(d) : "s"(s), "v"(a));
}
constexpr int W1_STEPS = 21;
constexpr int O_EPOCH = 0;
constexpr int O_FK = 4096;
constexpr int O_FA0 = O_FK + 160 * 256;
constexpr int O_FW = O_FA0 + 8 * 12 * 128;
constexpr int O_FU = O_FW + 8 * 12 * 128;
constexpr int O_FX = O_FU + 8 * 14 * 128;
constexpr int O_F2 = O_FX + 8 * 32 * 128;
constexpr int O_F3 = O_F2 + 8 * 32 * 128;
constexpr int O_SG = O_F3 + 256 * 512;
constexpr int O_MODP = O_SG + 32 * 8192;
constexpr int O_ST1 = O_MODP + MODP * NMOD * 4;
constexpr int O_KVG = O_ST1 + 16384;
constexpr int O_XG = O_KVG + S_ * D_ * 2;
constexpr int O_X2T = O_XG + S_ * D_ * 2;
constexpr int O_W1F = O_X2T + TP_ * D_ * 2;
constexpr int O_W2F = O_W1F + 64 * W1_STEPS * 3 * 1024;
constexpr int O_H2F = O_W2F + 8 * 48 * 5 * 1024;
constexpr int WS_BYTES = O_H2F + S_ * F_ * 2;
static_assert(O_MODP % 256 == 0 && O_ST1 % 256 == 0 && O_KVG % 256 == 0 && O_X2T % 256 == 0 && O_W1F % 256 == 0, "alignment");

__device__ __forceinline__ void st128(u32x4 v, rsrc_t r, int vo, int so) {
  __builtin_amdgcn_raw_buffer_store_b128(v, r, vo, so, SC1);
  asm volatile("s_nop 1" :: "v"(v) : "memory");
}
constexpr int SPIN_CAP = 1 << 16;
constexpr unsigned CODE_BYTES = 42 * 1024;
__device__ __forceinline__ void st_flag(char* ws, int off, u64 tag) {
  __hip_atomic_store((GAS u64*)(ws + off), tag, __ATOMIC_RELAXED, __HIP_MEMORY_SCOPE_AGENT);
}
__device__ __forceinline__ bool tag_ok(u32x4 v, u64 tag) {
  const u32 lo = (u32)tag, hi = (u32)(tag >> 32);
  return ((v[0] ^ lo) | (v[1] ^ hi) | (v[2] ^ lo) | (v[3] ^ hi)) == 0;
}
template <int NL, int SLEEP> __device__ __forceinline__ void poll(rsrc_t rw, const int (&off)[NL], u64 tag) {
#pragma unroll 1
  for (int sp = 0; sp < SPIN_CAP; ++sp) {
    bool ok = true;
#pragma unroll
    for (int i = 0; i < NL; ++i) ok &= tag_ok(__builtin_amdgcn_raw_buffer_load_b128(rw, off[i], 0, SC1), tag);
    if (__all(ok)) break;
    if (SLEEP) __builtin_amdgcn_s_sleep(SLEEP);
    asm volatile("" ::: "memory");
  }
  asm volatile("" ::: "memory");
}
__device__ __forceinline__ u64 make_tag(u32x2 e) {
  return (((u64)(u32)__builtin_amdgcn_readfirstlane(e[1]) << 32) | (u32)__builtin_amdgcn_readfirstlane(e[0])) + 1;
}
__device__ __forceinline__ void drain_bar() { asm volatile("s_waitcnt vmcnt(0) lgkmcnt(0)\n\ts_barrier" ::: "memory"); }

constexpr int FC_BYTES = 2064 * 2;
constexpr int CH_BYTES = 4 * FC_BYTES;
constexpr int CKV_OFF = 66048;
constexpr int CX_OFF = CKV_OFF + 2048 + 16384, CS_OFF = CX_OFF + 16384;
struct CSched { int rd[290]; int op[290]; int seq[192]; };
constexpr int CBE = PE - 2;
constexpr int CW1 = 12, CW2 = CBE >= 2 ? 12 : (CBE == 1 ? 19 : 35);
constexpr CSched conv_make() {
  CSched S{};
  int rd = 0, op = 0, landed = 0, x = 0, fp = 0, fi = 0;
  for (int g = 0; g < 288; ++g) {
    const int e = 7 - g / 36, n = g % 36;
    S.rd[g] = rd; S.op[g] = op;
    const bool open = e < CBE || (e == CBE && n >= 24);
    bool ds = e > 0 && n < 24;
    if (open && !ds && rd < 24 && (e == 0 || n < 34)) { ++rd; ds = true; if (n >= 30 && n < 32 && rd < 24) ++rd; }
    if (e > 0 ? n == 35 : (n == CW1 || n == CW2)) landed = rd;
    for (int k = ds ? 1 : 2; k > 0; --k) {
      if (e == 0 && fp < 3 && x >= 32 * (fp + 1) && n >= (2 * fp + 2) * (2 * fp + 3) / 2 + 3 && (fi < 12 || k == 2)) {
        S.seq[op++] = 128 + 16 * fp + fi;
        if (++fi == 13) { fi = 0; ++fp; }
      } else if (x < (landed / 6) * 32) S.seq[op++] = x++;
    }
  }
  S.rd[288] = rd; S.op[288] = op;
  while (fp < 4) {
    if (x >= 32 * (fp + 1)) { S.seq[op++] = 128 + 16 * fp + fi; if (++fi == 13) { fi = 0; ++fp; } }
    else S.seq[op++] = x++;
  }
  S.op[289] = op;
  return S;
}
constexpr int conv_a(int e, int n) { if (e > 0) { int a = 0; while (n >= 8 - a) { n -= 8 - a; ++a; } return a; } int t = 0; while (n > t) { n -= t + 1; ++t; } return n; }
constexpr int conv_t(int e, int n) { if (e > 0) { int a = 0; while (n >= 8 - a) { n -= 8 - a; ++a; } return a + n; } int t = 0; while (n > t) { n -= t + 1; ++t; } return t; }
constexpr CSched CS = conv_make();
static_assert(CS.rd[288 - 36 + CW2] == 24, "reads must be out before the last wait");
constexpr int XSP = 2;
constexpr int XCH = 20;
constexpr int G_XCHK = 36 * (7 - PE) + XCH;
struct CVSched { int kind[290]; int idx[290]; };
constexpr CVSched cv_make() {
  CVSched S{};
  for (int i = 0; i < 3; ++i) { S.kind[G_XCHK - 44 + 4 * i] = 2; S.idx[G_XCHK - 44 + 4 * i] = i; }
  S.kind[G_XCHK + 4] = 5;
  for (int i = 0; i < 8; ++i) { S.kind[G_XCHK + 4 + XSP * (i + 1)] = 3 + i / 4; S.idx[G_XCHK + 4 + XSP * (i + 1)] = i & 3; }
  return S;
}
constexpr CVSched CV = cv_make();
constexpr int cv_younger(int g, int kind, int idx) {
  int n = -1;
  for (int i = 0; i <= g; ++i) { if (n >= 0 && CV.kind[i]) ++n; if (CV.kind[i] == kind && CV.idx[i] == idx) n = 0; }
  return n;
}
static_assert(G_XCHK > 44, "conv request schedule");
constexpr int G1_R = 6;
constexpr int G1_RS = 7;
constexpr int G1_LA = 4;
constexpr int G1_SP = 15 * 8192;
constexpr int G1_WPRE = 5;
constexpr int g1_ord(int g) { return (g >> 1) + 4 * (g & 1); }
constexpr int g1_ks(int t) { return t < 96 ? t >> 3 : 12 + (t & 7); }
constexpr int g1_j(int t) { return t < 96 ? t & 7 : g1_ord((t - 96) >> 3); }
constexpr int g1_slot(int ks) { return ks < 12 ? ks % G1_RS : G1_RS + ks - 12; }
struct GSched { int kind[320]; int idx[320]; int first[162]; int mid; int first2[242]; int n; };
constexpr int T_SPOLL = 66;
constexpr int WA = 24;
constexpr int PLATE = 16;
constexpr int PSL = 1;
constexpr int CR1 = 12;
constexpr int CR2 = 14;
constexpr int WDN1 = 60;
constexpr int T_DV = 112;
constexpr int T_DEND = 157;
constexpr int T_P0 = 147;
constexpr int DPRE = 0;
constexpr int G2PRE = 8;
constexpr int G_P1 = 2;
constexpr int G_C1 = 24;
constexpr int G_P2 = 30;
constexpr int G_C2 = 68;
constexpr int G_F3 = 12;
constexpr int G_P3 = 84;
constexpr int G_C3 = 124;
constexpr int FINW = 26;
constexpr int HRATE = 3;
constexpr int T_F2 = 159;
constexpr int T_F0 = 128;
constexpr int T_F1 = 144;
constexpr int T_C0 = 156;
constexpr int XPRE = 5, T_SST = 23, T_WPOLL = 46, T_WCHK = 69;
constexpr int G2R = 22;
constexpr int G2_R = G2R, G2_ST = 58;
constexpr GSched g_make() {
  GSched S{};
  int n = 0, x = 0, w = 3 * G1_WPRE, wa = WA, wd = 0, rs = 0, h = 0, credit = 0;
  auto put = [&](int k, int i) { S.kind[n] = k; S.idx[n] = i; ++n; };
  for (; x < 2 * XPRE; ++x) put(0, x);
  put(13, 0);
  for (int t = 0; t < 160; ++t) {
    S.first[t] = n;
    const int ks = t >> 3;
    credit += t < 96 ? CR1 : CR2;
    if (credit > 28) credit = 28;
    const bool xok = x < 40 && ((x >> 1) >= 12 || (x >> 1) <= ks + G1_RS - ((t & 7) >= 4 ? 1 : 2)), wok = w < 36 && w / 3 <= ks + G1_R - 1;
    const int n0 = n;
    if (t == T_WPOLL) put(4, 0);
    else if (t == T_SST) put(5, 0);
    else if (t == T_SPOLL || t == T_SPOLL + 1) put(6, t - T_SPOLL);
    else if (t >= 109 && (t & 7) == 5) put(7, (t - 109) >> 3);
    else if (t == T_F0 || t == T_F1 || t == T_F2) put(9, t == T_F0 ? 0 : t == T_F1 ? 1 : 2);
    else if (t == T_P0) put(10, 0);
    else if (t >= T_C0) put(11, h++);
    else if (credit >= 16) {
      if (xok && (!wok || (x >> 1) < w / 3)) put(0, x++);
      else if (wok) put(1, w++);
      else if (wa < 24) put(8, wa++);
      else if (rs < 4 && !(x & 1)) put(3, rs++);
      else if (t > T_WCHK && wd < (t < T_DV ? 37 : WDN1) && t < T_DEND) put(2, wd++);
    }
    if (n > n0) credit -= 16;
  }
  S.first[160] = n;
  put(7, 7);
  for (int i = 0; i < DPRE && wd < 60; ++i) put(2, wd++);
  S.mid = n;
  auto next = [&](int g) {
    const int hmax = g <= G_C1 ? 12 : (g <= G_C2 ? 24 : (g <= G_C3 ? 36 : 48));
    if (wd < 60 && (wd / 5 < h || h >= 12)) put(2, wd++);
    else if (h < hmax && h < g / 5 + G2_R - 1) put(11, h++);
  };
  for (int i = 0; i < G2PRE; ++i) next(0);
  for (int g = 0; g < 240; ++g) {
    S.first2[g] = n;
    if (g == G_F3) put(9, 3);
    else if (g == G_P1) put(10, 1);
    else if (g == G_P2) put(10, 2);
    else if (g == G_P3) put(10, 3);
    else if (g > 60 && g % 60 == G2_ST) put(12, g / 60 - 1);
    else if (wd < 60 ? (g & 1) : g % HRATE == 1) next(g);
  }
  S.first2[240] = n; S.n = n;
  return S;
}
constexpr GSched GS = g_make();
constexpr int g_pos(int kind, int idx) { for (int i = 0; i < GS.n; ++i) if (GS.kind[i] == kind && GS.idx[i] == idx) return i; return -1; }
constexpr int imax(int a, int b) { return a > b ? a : b; }
constexpr int g1_need(int t) {
  if (t == T_F0) return g_pos(7, 1);
  if (t == T_F1) return g_pos(7, 3);
  if (t == T_F2) return g_pos(7, 5);
  if (t == 92) return imax(g_pos(0, 31), g_pos(8, 23));
  if (t == 96) return g_pos(0, 39);
  if (t == 100) return 0;
  if (t > 92 || (t & 7) != 4) return -1;
  const int s = (t + 4) / 8;
  int p = imax(g_pos(0, 2 * s + 1), g_pos(1, 3 * s + 2));
  if (t == 12) p = imax(p, g_pos(0, 9));
  if (t == 84) p = imax(p, g_pos(6, 1));
  if (t == T_WCHK - 1) p = imax(p, g_pos(4, 0));
  return p;
}
constexpr int g_lastdv() { int p = -1; for (int i = 37; i < 60; ++i) if (g_pos(2, i) < GS.first[160]) p = g_pos(2, i); return p; }
constexpr int g2_need(int o) { return o < 12 ? imax(g_pos(11, o), g_pos(2, 5 * o + 4)) : g_pos(11, o); }
static_assert(g_pos(1, 35) >= 0 && g_pos(0, 39) >= 0 && g_pos(2, 59) >= 0 && g_pos(11, 47) >= 0 && g_pos(3, 3) >= 0, "schedule incomplete");
constexpr int XR_OFF = G1_SP + 2048;
constexpr int GB_OFF = XR_OFF + 10400;
constexpr int LDS_BYTES = 134 * 1024;
static_assert(GB_OFF + 320 <= LDS_BYTES, "LDS");
constexpr int AG_OFF = 0, AU_OFF = 20480, AD_OFF = 40960, AM_OFF = 61440;

__global__ __launch_bounds__(256, 1) void k_fused(const u16* __restrict__ xIn, const u16* __restrict__ kIn, const u16* __restrict__ vIn,
                                                  const u16* __restrict__ shk, const u16* __restrict__ ssmk, const u16* __restrict__ ssmD,
                                                  const u16* __restrict__ wg, const u16* __restrict__ wu, const u16* __restrict__ wd,
                                                  const u16* __restrict__ cIn, const u16* __restrict__ wmod,
                                                  char* __restrict__ ws, u16* __restrict__ out) {
  __shared__ __attribute__((aligned(16))) char smem[LDS_BYTES];
  u32 cpf = 0;
  if (blockIdx.x >= 240) {
    uint64_t pc; asm volatile("s_getpc_b64 %0" : "=s"(pc));
    const unsigned co = (((blockIdx.x >> 3) & 1) * 256 + threadIdx.x) * 128;
    asm volatile("global_load_dword %0, %1, %2" : "=v"(cpf) : "v"(co < CODE_BYTES ? co : 0u), "s"(pc & ~(uint64_t)127) : "memory");
  }
  u64 tag;
  const int tid = threadIdx.x, lane = tid & 63;
  const int wv = __builtin_amdgcn_readfirstlane(tid >> 6);
  const int b = blockIdx.x;
  const unsigned sbase = (unsigned)(size_t)smem;
  const unsigned l16 = lane * 16;
  int bs = blockIdx.x;
  asm volatile("" : "+s"(bs));
  const int xcd = bs & 7, idx = bs >> 3;
  int mt = (xcd & 1) * 8 + (idx & 7), nc = (xcd >> 1) * 4 + (idx >> 3);
  asm volatile("" : "+s"(mt), "+s"(nc));
  const int M = mt * 2 + ((nc >> 2) & 1), N = (nc >> 3) * 4 + (nc & 3);
  int kvo = (b >> 5) * 256 + lane * 4, kso[9];
  kso[0] = ((b & 31) * 64 + wv * 16) * 1280;
#pragma unroll
  for (int q = 1; q < 8; ++q) kso[q] = kso[0] + q * 2560;
  kso[8] = kso[0] ? kso[0] - 3 * 1280 : 0;
  asm volatile("" : "+v"(kvo), "+s"(kso[0]), "+s"(kso[1]), "+s"(kso[2]), "+s"(kso[3]), "+s"(kso[4]), "+s"(kso[5]), "+s"(kso[6]), "+s"(kso[7]), "+s"(kso[8]));
  asm volatile("" :: "s"(xIn), "s"(kIn), "s"(vIn), "s"(shk), "s"(ssmk), "s"(ssmD), "s"(wg), "s"(wu), "s"(wd), "s"(cIn), "s"(wmod), "s"(ws));
  const rsrc_t rw = mk_rsrc(ws, WS_BYTES);
  const u32x4 wsrd = {(u32)(uint64_t)ws, (u32)((uint64_t)ws >> 32) & 0xffffu, (u32)WS_BYTES, 0x00020000u};
  u32 tch = 0;
  auto touch = [&]() {
    constexpr int PER = (O_ST1 + 16384 - O_FK + 256 * 128 - 1) / (256 * 128);
    static_assert(PER <= 64, "one request per workgroup");
    tch = __builtin_amdgcn_raw_buffer_load_b32(rw, (wv == 3 && lane < PER) ? O_FK + (b * PER + lane) * 128 : WS_BYTES, 0, SC1);
  };
  u32x4 WD[60];
  const char* wdp = ws + O_W2F + (N * 48 + wv * 12) * 5120 + 4096;
  const unsigned l16b = l16 + 8192;
  auto issue_wd = [&](auto I) { constexpr int i = I.value; gld128a<((i & 7) - 4) * 1024>(WD[i], (i & 8) ? l16b : l16, wdp + (i >> 4) * 16384); };
  auto clear_slot = [&]() {
    const int so = O_SG + (mt * 2 + ((nc >> 1) & 1)) * 8192 + (((nc & 1) * 4 + wv) * 4 + (nc >> 2)) * 256;
    __builtin_amdgcn_raw_buffer_store_b32(0u, rw, lane * 4, so, SC1);
  };
  auto gemv = [&](int gcol, int gk, auto landed) {
    const int r8 = lane >> 3, cp = lane & 7;
    u32x4 wm[16]; u32 cv[2];
    static_for<0, 2>([&](auto I) { gld16p<I.value * 128>(cv[I.value], lane * 2, (const char*)(cIn + gk * 128)); });
    static_for<0, 16>([&](auto I) { gld128p(wm[I.value], r8 * (NMOD * 2) + cp * 16, (const char*)(wmod + (size_t)(gk * 128 + I.value * 8) * NMOD + gcol * 64)); });
    wait_vm<16>();
    float sl[2], sv[16];
#pragma unroll
    for (int i = 0; i < 2; ++i) { pin(cv[i]); const float x = bflo(cv[i]); sl[i] = rbf(x * __builtin_amdgcn_rcpf(1.f + __expf(-x))); }
#pragma unroll
    for (int it = 0; it < 16; ++it) { sv[it] = __shfl(sl[it >> 3], (it & 7) * 8 + r8); pin(sv[it]); }
    f32x2 acc[4];
    static_for<0, 4>([&](auto G) {
      constexpr int g = G.value;
      wait_vm<12 - 4 * g>();
#pragma unroll
      for (int it = 4 * g; it < 4 * g + 4; ++it) {
        pin(wm[it]);
        const f32x2 s2 = {sv[it], sv[it]};
#pragma unroll
        for (int q = 0; q < 4; ++q) {
          const f32x2 w2 = {bflo(wm[it][q]), bfhi(wm[it][q])};
          acc[q] = it ? __builtin_elementwise_fma(s2, w2, acc[q]) : s2 * w2;
        }
      }
#pragma unroll
      for (int q = 0; q < 4; ++q) pin(acc[q]);
    });
    landed();
    const float a0 = tred16(tred32(acc[0][0], acc[2][0]), tred32(acc[1][0], acc[3][0]));
    const float a1 = tred16(tred32(acc[0][1], acc[2][1]), tred32(acc[1][1], acc[3][1]));
    const float e0 = dpp_add<DPP_ROR8>(a0), e1 = dpp_add<DPP_ROR8>(a1);
    __builtin_amdgcn_raw_buffer_store_b32(__float_as_uint((lane & 8) ? e1 : e0), rw, O_MODP + (gk * NMOD + gcol * 64 + cp * 8 + r8) * 4, 0, SC1);
  };
  if (b >= CWG) {
    const int j = b - CWG;
    const int r8 = lane >> 3, cp = lane & 7;
    const int ks2 = j >> 1, hf = j & 1;
    const int ug = j % 24, kq = j / 24;
    float* LC = (float*)(smem + AM_OFF);
    u16* LS = (u16*)(smem + AM_OFF + 640);
    const int gt = wv * 96 + j;
    const bool gact = wv < 3, urgent = gt < 100;
    const int gq = gt / MODP, gk = gt - gq * MODP, gcol = urgent ? 30 + gq : (gq < 50 ? gq - 20 : gq);
    u32x2 ep = __builtin_amdgcn_raw_buffer_load_b64(rw, O_EPOCH, 0, SC1);
    touch();
    SCHED();
    if (!urgent) __builtin_amdgcn_s_sleep(A0_DELAY);
    if (gact) {
      gemv(gcol, gk, [&]() { pin(ep); pin(tch); });
      if (urgent) {
        wait_vm<0>();
        if (lane < 8) st_flag(ws, O_FU + (lane * 14 + (gt >> 3)) * 128 + (gt & 7) * 8, make_tag(ep));
        __builtin_amdgcn_s_sleep(AU_DELAY);
      }
    } else {
      pin(ep); pin(tch);
    }
    tag = make_tag(ep);
    {
      const unsigned vg = r8 * (F_ * 2) + ((cp ^ ((r8 >> 1) * 2)) * 16);
      auto share = [&](int w) {
        const char* gs = (const char*)(wg + (size_t)(kq * 160 + w * 40) * F_ + ug * 64);
        const char* us = (const char*)(wu + (size_t)(kq * 160 + w * 40) * F_ + ug * 64);
        static_for<0, 5>([&](auto I) {
          dma16p<AG_OFF + I.value * 1024>(sbase + w * 5120, vg, gs + I.value * (8 * F_ * 2));
          dma16p<AU_OFF + I.value * 1024>(sbase + w * 5120, vg ^ 16, us + I.value * (8 * F_ * 2));
        });
      };
      auto wdall = [&]() {
        const char* ds = (const char*)(wd + (size_t)(ks2 * 32) * D_ + hf * 320);
        static_for<0, 20>([&](auto I) { dma16p<AD_OFF + I.value * 1024>(sbase, r8 * (D_ * 2) + cp * 16, ds + (I.value & 3) * (8 * D_ * 2) + (I.value >> 2) * 128); });
      };
      if (wv == 3) {
        share(3);
        if (AM & 1) { __builtin_amdgcn_s_sleep(A3_DELAY); share(0); }
        if (AM & 2) { __builtin_amdgcn_s_sleep(A3_DELAY); share(1); share(2); }
        if (AM & 4) { __builtin_amdgcn_s_sleep(A4_DELAY); wdall(); }
      } else if (wv == 0 ? !(AM & 1) : !(AM & 2)) {
        share(wv);
        wait_vm<10>();
      } else {
        wait_vm<0>();
      }
    }
    bar();
    if (tid < 8) st_flag(ws, O_FA0 + (tid * 12 + (j >> 3)) * 128 + (j & 7) * 8, tag);
    const int kg = lane >> 4, q4 = (lane & 15) >> 2, p4 = lane & 3;
    if (wv == 0) {
      const int po[1] = {O_FU + ((b & 7) * 14 + (lane < 50 ? lane >> 2 : 0)) * 128 + (lane & 3) * 16};
      poll<1, 1>(rw, po, tag);
      f32x4 mm[2];
      const int lo = lane < 40 ? lane * 16 : 0;
#pragma unroll
      for (int i = 0; i < 2; ++i) {
        mm[i] = f32x4{0.f, 0.f, 0.f, 0.f};
#pragma unroll
        for (int p = 0; p < MODP; ++p)
          mm[i] += __builtin_bit_cast(f32x4, __builtin_amdgcn_raw_buffer_load_b128(rw, lo, O_MODP + (p * NMOD + (3 + i) * D_ + kq * 160) * 4, SC1));
      }
      if (lane < 40) {
        *(u32x2*)(LS + lane * 4) = u32x2{pack_bf16(mm[0][0], mm[0][1]), pack_bf16(mm[0][2], mm[0][3])};
        *(f32x4*)(LC + lane * 4) = f32x4{rbf(1.f + rbf(mm[1][0])), rbf(1.f + rbf(mm[1][1])), rbf(1.f + rbf(mm[1][2])), rbf(1.f + rbf(mm[1][3]))};
      }
    }
    drain_bar();
    asm volatile("" :: "v"(cpf));
    clear_slot();
    if (!(AM & 4)) {
      const char* ds = (const char*)(wd + (size_t)(ks2 * 32 + wv * 8) * D_ + hf * 320);
      static_for<0, 5>([&](auto I) { dma16p<AD_OFF + I.value * 4096>(sbase + wv * 1024, r8 * (D_ * 2) + cp * 16, ds + I.value * 128); });
    }
    {
      const int rr = 4 * kg + q4;
      u32x2 lo[10], hi[10], sbl[5], sbh[5]; u32x4 cl[5], ch[5];
      unsigned ad[2];
#pragma unroll
      for (int t = 0; t < 2; ++t) ad[t] = sbase + (p4 >> 1) * AU_OFF + rr * 128 + (((wv * 2 + t) ^ (((rr >> 1) & 3) * 2 + (p4 >> 1))) * 16) + (p4 & 1) * 8;
      const unsigned ac = sbase + AM_OFF + kg * 16, as = sbase + AM_OFF + 640 + kg * 8;
      auto rd = [&](auto I) {
        constexpr int i = I.value;
        ds_b128<i * 128>(cl[i], ac); ds_b128<i * 128 + 64>(ch[i], ac);
        ds_tr<i * 4096>(lo[2 * i], ad[0]); ds_tr<i * 4096 + 2048>(hi[2 * i], ad[0]); ds_tr<i * 4096>(lo[2 * i + 1], ad[1]); ds_tr<i * 4096 + 2048>(hi[2 * i + 1], ad[1]);
        ds_b64<i * 64>(sbl[i], as); ds_b64<i * 64 + 32>(sbh[i], as);
      };
      rd(IC<0>{});
      const int src = ((lane & 48) + ((lane & 1) * 8 + ((lane & 15) >> 1))) * 4;
      const u32x4 ones = {0x3f803f80u, 0x3f803f80u, 0x3f803f80u, 0x3f803f80u};
      f32x4 aU[2] = {}, aW[2] = {};
      int so[2];
#pragma unroll
      for (int t = 0; t < 2; ++t) { const int T = ug * 8 + wv * 2 + t, sw3 = T / 3; so[t] = O_W1F + ((sw3 * W1_STEPS + kq * 5) * 3 + T - sw3 * 3) * 1024; }
      static_for<0, 5>([&](auto KS) {
        constexpr int ks = KS.value;
        wait_lgkm<0>();
        pin(cl[ks]); pin(ch[ks]); pin(sbl[ks]); pin(sbh[ks]);
        if constexpr (ks < 4) rd(IC<ks + 1>{});
        u32x4 pm[2];
#pragma unroll
        for (int t = 0; t < 2; ++t) {
          pin(lo[2 * ks + t]); pin(hi[2 * ks + t]);
          const u32x4 raw = cat(lo[2 * ks + t], hi[2 * ks + t]);
          u32x4 sc;
#pragma unroll
          for (int q = 0; q < 2; ++q) {
            sc[q] = pack_bf16(bflo(raw[q]) * __uint_as_float(cl[ks][2 * q]), bfhi(raw[q]) * __uint_as_float(cl[ks][2 * q + 1]));
            sc[2 + q] = pack_bf16(bflo(raw[2 + q]) * __uint_as_float(ch[ks][2 * q]), bfhi(raw[2 + q]) * __uint_as_float(ch[ks][2 * q + 1]));
          }
#pragma unroll
          for (int q = 0; q < 4; ++q) pm[t][q] = (u32)__builtin_amdgcn_ds_bpermute(src, sc[q]);
          aW[t] = mfma_b(raw, cat(sbl[ks], sbh[ks]), aW[t]);
          aU[t] = mfma_b(sc, ones, aU[t]);
        }
#pragma unroll
        for (int t = 0; t < 2; ++t) st128(pm[t], rw, l16, so[t] + ks * 3072);
      });
      const f32x4 su = (lane & 4) ? aU[1] : aU[0], sw = (lane & 4) ? aW[1] : aW[0];
      const float U = p4 == 0 ? su[0] : p4 == 1 ? su[1] : p4 == 2 ? su[2] : su[3];
      const float W = p4 == 0 ? sw[0] : p4 == 1 ? sw[1] : p4 == 2 ? sw[2] : sw[3];
      const float u1 = rbf(U), u2 = rbf(U - u1), u3 = U - u1 - u2;
      const float w1 = rbf(W), w2 = rbf(W - w1), w3 = W - w1 - w2;
      const int crow = 2 * (4 * (kg & 1) + p4) + (kg >> 1);
      __builtin_amdgcn_raw_buffer_store_b128(u32x4{pack_bf16(u1, w1), pack_bf16(u2, w2), pack_bf16(u3, w3), 0u}, rw,
                                             (lane & 8) ? WS_BYTES : ((lane & 4) ? so[1] : so[0]) + (20 - kq * 5) * 3072 + (kq * 16 + crow) * 16, 0, SC1);
    }
  } else {
    const int d = (b >> 1) * 8 + (b & 1) + 2 * wv;
    const int row = lane & 15, kg = lane >> 4;
    const int tt = b & 31, ct = b >> 5;
    const int ov = ((lane >> 2) * 2048 + (lane & 3)) * 16, os = (ct * 16 * 2048 + (tt * 8 + wv * 2) * 4) * 16;
    const int stg = b * 2 + (wv - 2);
    const bool xact = wv >= 2 && b < 128;
    u32 xx[5][8];
    u32 draw; u32x2 ep;
    unsigned aAddr, bA[8];
    const unsigned fl = sbase + wv * CH_BYTES;
    {
      const rsrc_t rk = mk_rsrc(kIn, S_ * D_ * 2), rv = mk_rsrc(vIn, S_ * D_ * 2);
      u32 kk[19], vv[16];
#pragma unroll
      for (int q = 0; q < 3; ++q) kk[q] = __builtin_amdgcn_raw_buffer_load_b32(rk, kvo + q * 1280, kso[8], NT);
#pragma unroll
      for (int q = 0; q < 8; ++q) kk[3 + q] = __builtin_amdgcn_raw_buffer_load_b32(rk, kvo + (q & 1) * 1280, kso[q >> 1], NT);
      const u32x4 taps = __builtin_amdgcn_raw_buffer_load_b128(mk_rsrc(shk, D_ * 8), kvo * 4, 0, 0);
#pragma unroll
      for (int q = 0; q < 8; ++q) vv[q] = __builtin_amdgcn_raw_buffer_load_b32(rv, kvo + (q & 1) * 1280, kso[q >> 1], NT);
#pragma unroll
      for (int q = 8; q < 16; ++q) kk[3 + q] = __builtin_amdgcn_raw_buffer_load_b32(rk, kvo + (q & 1) * 1280, kso[q >> 1], NT);
#pragma unroll
      for (int q = 8; q < 16; ++q) vv[q] = __builtin_amdgcn_raw_buffer_load_b32(rv, kvo + (q & 1) * 1280, kso[q >> 1], NT);
      SCHED();
      SCHED();
      if (F_POS == 0) {
        const char* fs = (const char*)(ssmk + (size_t)d * S_);
        dma16x4p<FC_BYTES + 28>(fl, l16, fs);
        dma16x4p<3 * FC_BYTES + 24>(fl, l16, fs);
      }
      aAddr = fl + (row & 3) * FC_BYTES + 2 * (64 * kg + (row & ~3));
      pin(aAddr);
#pragma unroll
      for (int e = 0; e < 8; ++e) { const int g = 32 + 2 * row + 1 - 8 * kg - e; bA[e] = sbase + CKV_OFF + 16 * ((4 * g + wv) ^ ((g >> 2) & 7)); pin(bA[e]); }
      ep = __builtin_amdgcn_raw_buffer_load_b64(rw, O_EPOCH, 0, SC1);
      touch();
      draw = __builtin_amdgcn_raw_buffer_load_b32(mk_rsrc(ssmD, D_ * 2), (d >> 1) * 4, 0, 0);
      *(u32x2*)(smem + CKV_OFF + tid * 8) = u32x2{0, 0};
      if (lane < 13) *(u32*)(smem + wv * CH_BYTES + (lane < 7 ? FC_BYTES + lane * 4 : 3 * FC_BYTES + (lane - 7) * 4)) = 0;
      SCHED();
      if (kso[0] == 0) { kk[0] = 0; kk[1] = 0; kk[2] = 0; }
      const f32x2 w0 = {bflo(taps[0]), bflo(taps[2])}, w1 = {bfhi(taps[0]), bfhi(taps[2])};
      const f32x2 w2 = {bflo(taps[1]), bflo(taps[3])}, w3 = {bfhi(taps[1]), bfhi(taps[3])};
#pragma unroll
      for (int r = 0; r < 2; ++r) {
        f32x2 kv[8];
#pragma unroll
        for (int q = 0; q < 8; ++q) {
          const int i = 8 * r + q;
          f32x2 a = f32x2{bflo(kk[i]), bfhi(kk[i])} * w3;
          a = __builtin_elementwise_fma(f32x2{bflo(kk[i + 1]), bfhi(kk[i + 1])}, w2, a);
          a = __builtin_elementwise_fma(f32x2{bflo(kk[i + 2]), bfhi(kk[i + 2])}, w1, a);
          a = __builtin_elementwise_fma(f32x2{bflo(kk[i + 3]), bfhi(kk[i + 3])}, w0, a);
          kv[q] = a * f32x2{bflo(vv[i]), bfhi(vv[i])};
        }
        u32x4 o0, o1;
#pragma unroll
        for (int t = 0; t < 4; ++t) {
          o0[t] = pack_bf16(kv[7 - 2 * t][0], kv[6 - 2 * t][0]);
          o1[t] = pack_bf16(kv[7 - 2 * t][1], kv[6 - 2 * t][1]);
        }
        st128(o0, rw, ov + r * 64, O_KVG + os);
        st128(o1, rw, ov + r * 64, O_KVG + os + 16384);
      }
    }
    pin(ep); pin(draw); pin(tch);
    tag = make_tag(ep);
    auto fload = [&]() {
      const char* fs = (const char*)(ssmk + (size_t)d * S_);
      dma16x4p<FC_BYTES + 28>(fl, l16, fs);
      dma16x4p<3 * FC_BYTES + 24>(fl, l16, fs);
    };
    auto xload = [&]() {
      const char* xp = (const char*)xIn + stg * 8 * 1280;
      static_for<0, 40>([&](auto I) { constexpr int t = I.value / 5, c = I.value % 5; gld32p<c * 256 + (t & 1) * 1280>(xx[c][t], lane * 4, xp + (t & 6) * 1280); });
    };
    if (F_POS == 1) fload();
    if (xact && X_POS == 1) {
      xload();
      wait_vm<40 + (F_POS == 1 ? 8 : 0)>();
    } else {
      wait_vm<(F_POS == 1 ? 8 : 0)>();
    }
    asm volatile("s_waitcnt lgkmcnt(0)\n\ts_barrier" ::: "memory");
    if (tid < 32) st_flag(ws, O_FK + ((b & ~31) + tid) * 256 + (b & 31) * 8, tag);
    if (xact && X_POS == 2) xload();
    if (F_POS == 2) fload();
    int pa1[3] = {O_FX + ((b & 7) * 32 + (lane >> 2)) * 128 + (lane & 3) * 16, O_FX + ((b & 7) * 32 + 16 + (lane >> 2)) * 128 + (lane & 3) * 16,
                  O_FA0 + ((b & 7) * 12 + (lane < 48 ? lane >> 2 : 0)) * 128 + (lane & 3) * 16};
    const char* xgp = ws + O_XG + b * 16384 + wv * 4096;
    const char* stp = ws + O_ST1 + wv * 4096;
    asm volatile("" : "+s"(xgp), "+s"(stp));
    pin(pa1[0]); pin(pa1[1]); pin(pa1[2]);
    float Dv = (d & 1) ? bfhi(draw) : bflo(draw);
    unsigned ek = sbase + CKV_OFF + 16 * ((4 * (32 + 2 * row + (kg >> 1)) + wv) ^ ((row >> 1) & 7)) + ((kg & 1) ? 0 : 8);
    unsigned ex = (ek ^ 8) + (CX_OFF - CKV_OFF - 2048), es = sbase + CS_OFF + l16;
    unsigned mo = ((lane & 7) < MODP && lane < 24) ? O_MODP + ((lane & 7) * NMOD + (lane >> 3) * D_ + d) * 4 : WS_BYTES;
    pin(Dv); pin(ek); pin(ex); pin(es); pin(mo);
    unsigned ko[2] = {(unsigned)(lane ^ (lane >> 4)) * 16, ((unsigned)(lane ^ (lane >> 4)) * 16) ^ 64};
    pin(ko[0]); pin(ko[1]);
    auto fcopies = [&]() {
      u32x4 m[5]; u32 lo[5], hi[5];
      const unsigned src = fl + FC_BYTES + l16, src4 = fl + FC_BYTES + 4096 + (lane & 1) * 16;
      static_for<0, 4>([&](auto I) { constexpr int i = I.value; ds_b128<i * 1024>(m[i], src); ds_b32<i * 1024>(lo[i], src - 4); ds_b32<i * 1024 + 16>(hi[i], src); });
      ds_b128<0>(m[4], src4); ds_b32<0>(lo[4], src4 - 4); ds_b32<16>(hi[4], src4);
      wait_lgkm<0>();
#pragma unroll
      for (int i = 0; i < 5; ++i) {
        pin(m[i]); pin(lo[i]); pin(hi[i]);
        if (i == 0 && lane == 0) lo[0] = 0;
        const u32 s1 = __builtin_amdgcn_alignbit(m[i][1], m[i][0], 16), s2 = __builtin_amdgcn_alignbit(m[i][2], m[i][1], 16), s3 = __builtin_amdgcn_alignbit(m[i][3], m[i][2], 16);
        char* dst = smem + wv * CH_BYTES + (i < 4 ? i * 1024 + l16 : 4096 + (lane & 1) * 16);
        *(u32x4*)dst = u32x4{__builtin_amdgcn_alignbit(m[i][0], lo[i], 16), s1, s2, s3};
        *(u32x4*)(dst + 2 * FC_BYTES) = u32x4{s1, s2, s3, __builtin_amdgcn_alignbit(hi[i], m[i][3], 16)};
      }
    };
    const bool gx = wv == 2 && b >= 128 && b < 140;
    if (gx) gemv((b + 160) / MODP, (b + 160) % MODP, []() {});
    if (!xact) { if ((wv == 0 && F_POS != 2) || gx) wait_vm<1>(); else wait_vm<0>(); fcopies(); }
    if (xact) {
      wait_vm<(F_POS == 2 ? 8 : 0)>();
#pragma unroll
      for (int c = 0; c < 5; ++c)
#pragma unroll
        for (int t = 0; t < 8; ++t) pin(xx[c][t]);
#pragma unroll
      for (int c = 0; c < 5; ++c) {
        u32x4 p0, p1;
#pragma unroll
        for (int t = 0; t < 4; ++t) {
          p0[t] = __builtin_amdgcn_perm(xx[c][2 * t + 1], xx[c][2 * t], 0x05040100u);
          p1[t] = __builtin_amdgcn_perm(xx[c][2 * t + 1], xx[c][2 * t], 0x07060302u);
        }
        const int so = O_XG + (c * 16 * 2048 + stg * 4) * 16;
        st128(p0, rw, ov, so);
        st128(p1, rw, ov, so + 16384);
      }
      const bf16x2 ones = {(__bf16)1.0f, (__bf16)1.0f};
      float s1[8], s2[8];
#pragma unroll
      for (int t = 0; t < 8; ++t) {
        s1[t] = 0.f; s2[t] = 0.f;
#pragma unroll
        for (int c = 0; c < 5; ++c) {
          const bf16x2 v = __builtin_bit_cast(bf16x2, xx[c][t]);
          s1[t] = __builtin_amdgcn_fdot2_f32_bf16(v, ones, s1[t], false);
          s2[t] = __builtin_amdgcn_fdot2_f32_bf16(v, v, s2[t], false);
        }
      }
      float q1[2], q2[2];
#pragma unroll
      for (int e = 0; e < 2; ++e) {
        q1[e] = dpp_add<DPP_ROR8>(tred16(tred32(s1[e], s1[4 + e]), tred32(s1[2 + e], s1[6 + e])));
        q2[e] = dpp_add<DPP_ROR8>(tred16(tred32(s2[e], s2[4 + e]), tred32(s2[2 + e], s2[6 + e])));
      }
      const float mean = sum8((lane & 8) ? q1[1] : q1[0]) * (1.f / D_);
      const float var = fmaxf(sum8((lane & 8) ? q2[1] : q2[0]) * (1.f / D_) - mean * mean, 0.f);
      const float sd = rbf(__builtin_amdgcn_sqrtf(rbf(rbf(var) + 1e-5f)));
      const float rs = __builtin_amdgcn_rcpf(sd);
      const int s = stg * 8 + (lane >> 3);
      const int nt = s >> 8, col = (s >> 4) & 15, k4 = (s >> 2) & 3, rr = s & 3;
      if ((lane & 7) == 0) __builtin_amdgcn_raw_buffer_store_b64(u32x2{__float_as_uint(rs), __float_as_uint(-rbf(mean) * rs)}, rw,
                                                                 O_ST1 + (((nt * 2 + (rr >> 1)) * 64 + k4 * 16 + col) * 4 + (rr & 1) * 2) * 4, 0, SC1);
      if (F_POS == 2) wait_vm<11>();
      fcopies();
      wait_vm<0>();
      if (lane < 8) st_flag(ws, O_FX + (lane * 32 + (stg >> 3)) * 128 + (stg & 7) * 8, tag);
    } else if (wv < 2) {
      const int po[1] = {O_FK + b * 256 + wv * 128 + (lane & 7) * 16};
      poll<1, 0>(rw, po, tag);
      dma16x4<CKV_OFF + 2048>(sbase + wv * 8192, ko[0], ko[1], ws + O_KVG + b * 16384 + wv * 8192);
      dma16x4<CKV_OFF + 2048 + 4096>(sbase + wv * 8192, ko[0], ko[1], ws + O_KVG + b * 16384 + wv * 8192 + 4096);
    }
    f32x4 acc[8];
    u32x2 ra[2][8][2]; u32x4 rb[2][8];
    u32x4 pf[3]; u32 mraw; u64 nm[6];
    auto load_n = [&](auto E, auto N) {
      constexpr int e = E.value, n = N.value, bf = e & 1;
      if constexpr (n < 2) ds_b64<16 * e + 8 * n>(ra[bf][0][n], aAddr);
      else if constexpr (n < 10) ds_b128<2048 * (n - 2)>(rb[bf][n - 2], bA[e]);
      else ds_b64<512 * ((n - 8) >> 1) + 16 * e + 8 * (n & 1)>(ra[bf][(n - 8) >> 1][n & 1], aAddr);
    };
    u32x2 kvw[8], xw[8]; u32x4 sA[8], sB[8];
    float T[8][4], Hh[8][4], c0 = 0.f, c1 = 0.f;
    auto kvop = [&](auto K) {
      constexpr int k = K.value, nt = k >> 3, r = k & 3;
      if constexpr ((k & 4) == 0) T[nt][r] = (r & 1) ? v_lo(kvw[nt][1 - (r >> 1)]) : v_hi(kvw[nt][1 - (r >> 1)]);
      else T[nt][r] = v_mul(T[nt][r], Dv);
    };
    auto xrd = [&](auto K) {
      constexpr int k = K.value, nt = k / 3;
      if constexpr (k % 3 == 0) ds_b64<2048 * nt>(xw[nt], ex);
      else if constexpr (k % 3 == 1) ds_b128<2048 * nt>(sA[nt], es);
      else ds_b128<2048 * nt + 1024>(sB[nt], es);
    };
    float xf[8];
    auto xop = [&](auto K) {
      constexpr int k = K.value, nt = 2 * (k >> 5) + ((k >> 2) & 1), ph = (k >> 3) & 3, r = k & 3, i = k & 7;
      if constexpr (ph == 0) xf[i] = (r & 1) ? v_hi(xw[nt][r >> 1]) : v_lo(xw[nt][r >> 1]);
      else if constexpr (ph == 1) Hh[nt][r] = v_fma(xf[i], __uint_as_float((r < 2 ? sA[nt] : sB[nt])[2 * (r & 1)]), __uint_as_float((r < 2 ? sA[nt] : sB[nt])[2 * (r & 1) + 1]));
      else if constexpr (ph == 2) Hh[nt][r] = v_fma(Hh[nt][r], c1, c0);
      else T[nt][r] = v_fma(Hh[nt][r], T[nt][r], xf[i]);
    };
    u32x4 xo;
    auto sop = [&](auto K) {
      constexpr int id = CS.seq[K.value];
      if constexpr (id < 128) xop(IC<id>{});
      else {
        constexpr int p = (id - 128) >> 4, i = id & 15, nt = 2 * p + ((i >> 2) & 1);
        if constexpr (i < 8) T[nt][i & 3] = v_fma(Hh[nt][i & 3], acc[nt][i & 3], T[nt][i & 3]);
        else if constexpr (i < 12) xo[i - 8] = v_cvt(T[2 * p + ((i - 8) >> 1)][2 * (i & 1)], T[2 * p + ((i - 8) >> 1)][2 * (i & 1) + 1]);
        else st128(xo, rw, l16 + p * 1024, O_X2T + d * (TP_ * 2));
      }
    };
    u32 ms[3];
    auto mop = [&](auto K) {
      constexpr int k = K.value;
      if constexpr (k < 3) asm_dpp<k>(mraw);
      else if constexpr (k == 3) asm_op1<3>(mraw, mraw);
      else if constexpr (k < 7) asm_rdl<8 * (k - 4)>(ms[k - 4], mraw);
      else if constexpr (k == 7) asm_sop<0>(c0, ms[0], c0);
      else if constexpr (k == 8) asm_sop<1>(c1, ms[1], c1);
      else if constexpr (k == 9) asm_sop<2>(c0, ms[2], c0);
      else if constexpr (k == 10) { u32 t; asm_op1<3>(t, __float_as_uint(c1)); c1 = __uint_as_float(t); }
      else asm_sop<2>(c1, ms[2], c1);
    };
    clear_slot();
    wait_lgkm<0>();
    static_for<0, 2>([&](auto N) { load_n(IC<7>{}, N); });
    static_for<10, 24>([&](auto N) { load_n(IC<7>{}, N); });
    if (wv < 2) wait_vm<1>();
    bar();
    static_for<2, 10>([&](auto N) { load_n(IC<7>{}, N); });
    static_for<0, 8>([&](auto EE) {
      constexpr int e = 7 - EE.value, bf = e & 1;
      {
        static_for<0, 36>([&](auto NN) {
          constexpr int n = NN.value, a = conv_a(e, n), bb = conv_t(e, n) - a;
          if constexpr (e == 7 && n < 8) wait_lgkm<7>();
          mfma_<(e == 7 && a == 0)>(acc[a + bb], cat(ra[bf][a][0], ra[bf][a][1]), rb[bf][bb]);
          if constexpr (e > 0 && n < 24) load_n(IC<(e > 0 ? e - 1 : 0)>{}, IC<n>{});
          if constexpr (e == 7 && n >= 24 && n < 32) ds_b64<2048 * (n - 24)>(kvw[n - 24], ek);
          constexpr int g = 36 * (7 - e) + n;
          static_for<CS.rd[g], CS.rd[g + 1]>(xrd);
          if constexpr (e > 0 ? n == 35 : (n == CW1 || n == CW2)) wait_lgkm<0>();
          if constexpr (e == 0) real();
          static_for<CS.op[g], CS.op[g + 1]>(sop);
          if constexpr ((e == 6 || e == 5) && n < 32) kvop(IC<(6 - e) * 32 + n>{});
          if constexpr (CV.kind[g] == 2) gld128<0>(pf[CV.idx[g]], pa1[CV.idx[g]], ws);
          if constexpr (CV.kind[g] == 3) { if constexpr (CV.idx[g] == 0) dma16<CX_OFF>(sbase + wv * 4096, ko[0], xgp); else dma16n<CV.idx[g] * 1024>(ko[CV.idx[g] & 1], xgp); }
          if constexpr (CV.kind[g] == 4) { if constexpr (CV.idx[g] == 0) dma16<CS_OFF>(sbase + wv * 4096, l16, stp); else dma16n<CV.idx[g] * 1024>(l16, stp); }
          if constexpr (CV.kind[g] == 5) bld32(mraw, mo, wsrd);
          if constexpr (g == G_XCHK) { wait_vm<cv_younger(g, 2, 2)>(); }
          if constexpr (g >= G_XCHK && g < G_XCHK + 3) { constexpr int i = g - G_XCHK; nm[2 * i] = v_ne64(u32x2{pf[i][0], pf[i][1]}, tag); nm[2 * i + 1] = v_ne64(u32x2{pf[i][2], pf[i][3]}, tag); }
          if constexpr (g == G_XCHK + 3) {
            if (__builtin_expect(((nm[0] | nm[1]) | (nm[2] | nm[3]) | (nm[4] | nm[5])) != 0, 0)) poll<3, 1>(rw, pa1, tag);
          }
          if constexpr (e == CBE && n == 8) wait_vm<cv_younger(g, 5, 0)>();
          if constexpr (e == CBE && n >= 9 && n < 21) mop(IC<n - 9>{});
          if constexpr (e == CBE && n == 23) { wait_vm<cv_younger(g, 4, 3)>(); bar(); }
        });
      }
    });
    static_assert(CS.seq[CS.op[288] + 8] < 128 + 48, "the last pair's accumulators are read too early");
    static_for<CS.op[288], CS.op[289]>(sop);
  }
  const int kg = lane >> 4, c16 = lane & 15;
  const int kq0 = nc >> 2, r0 = (nc & 1) * 4 + wv, sgb = O_SG + (mt * 2 + ((nc >> 1) & 1)) * 8192;
  u32x2 tag2 = {(u32)tag, (u32)(tag >> 32)};
  u32x4 gbr;
  pin(tag2);
  {
    const int fb = O_F2 + (b & 7) * 32 * 128;
    const int jw = (lane & 3) * 24 + ((nc * 12 + wv * 3 + ((lane >> 2) & 1) * 2) >> 3);
    const int pa[1] = {fb + (20 + (jw >> 3)) * 128 + (jw & 6) * 8};
    u32x4 pfa;
    gld128<0>(pfa, pa[0], ws);
    const char* wsrc = ws + O_W1F + (nc * 4 + wv) * W1_STEPS * 3072;
    const char* wq[4];
    unsigned xinc[4];
#pragma unroll
    for (int i = 0; i < 4; ++i) {
      wq[i] = wsrc + ((kq0 + i) & 3) * (5 * 3072) + 4096;
      xinc[i] = (i && kq0 + i == 4) ? (unsigned)(-19 * 32 * TP_ * 2) : 32 * TP_ * 2;
      asm volatile("" : "+s"(wq[i]), "+s"(xinc[i]));
    }
    u32x4 W[G1_R][3], WA_[24], WC[3], pfs[2], pfw, gran;
    auto issue_w = [&](auto I) { constexpr int i = I.value; gld128b<(((i % 15) & 7) - 4) * 1024>(W[(i / 3) % G1_R][i % 3], ((i % 15) & 8) ? l16b : l16, wq[i / 15]); };
    int pc[2] = {fb + (lane >> 2) * 128 + (lane & 3) * 16, fb + (16 + (lane >> 2)) * 128 + (lane & 3) * 16};
    int f2o = lane < 8 ? O_F2 + (lane * 32 + (b >> 3)) * 128 + (b & 7) * 8 : WS_BYTES;
    pin(pc[0]); pin(pc[1]); pin(f2o);
    asm volatile("s_waitcnt vmcnt(1) lgkmcnt(0)\n\ts_barrier" ::: "memory");
    if (wv == 0) __builtin_amdgcn_raw_buffer_store_b64(tag2, rw, f2o, 0, SC1);
    if (bs >= CWG) {
      const int j = bs - CWG;
      u32x2 dl[5], dh[5];
      const unsigned add = sbase + AD_OFF + (8 * kg + (c16 >> 2)) * 128 + (lane & 3) * 8;
#pragma unroll
      for (int i = 0; i < 5; ++i) { const int tl = wv * 5 + i; const unsigned a2 = add + (tl >> 2) * 4096 + (tl & 3) * 32; ds_tr<0>(dl[i], a2); ds_tr<512>(dh[i], a2); }
      wait_lgkm<0>();
#pragma unroll
      for (int i = 0; i < 5; ++i) {
        pin(dl[i]); pin(dh[i]);
        __builtin_amdgcn_raw_buffer_store_b128(cat(dl[i], dh[i]), rw, l16, O_W2F + ((((j & 1) * 4 + wv) * 48 + (j >> 1)) * 5 + i) * 1024, SC1);
      }
      wait_vm<5>();
    } else {
      if (wv == 0) wait_vm<1>(); else wait_vm<0>();
    }
    pin(pfa);
    if (!__all(tag_ok(pfa, tag))) poll<1, 2>(rw, pa, tag);
    auto issue_a = [&](auto I) { constexpr int i = 36 + I.value; gld128a<(((i % 15) & 7) - 4) * 1024>(WA_[I.value], ((i % 15) & 8) ? l16b : l16, wq[i / 15]); };
    static_for<0, 3 * G1_WPRE>(issue_w);
    unsigned xoff[2];
#pragma unroll
    for (int p = 0; p < 2; ++p) {
      const int k = 4 * p + kg;
      xoff[p] = (k * TP_ + ((c16 ^ (2 * k)) * 8)) * 2 - p * 1024;
      pin(xoff[p]);
    }
    unsigned xso = O_X2T + ((kq0 * 160 - 32 + 8 * wv) * TP_ + mt * 128) * 2;
    const unsigned ldsw = sbase + wv * 2048;
    unsigned sgl = sgb + wv * 2048 + l16;
    int hvo = kg < 3 ? ((3 * wv + kg) >> 2) * 1024 + ((3 * wv + kg) & 3) * 256 + c16 * 16 : WS_BYTES;
    const int hso = O_H2F + (mt * 8 * 48 + 3 * nc) * 1024;
    unsigned spa = sbase + G1_SP + c16 * 16;
    int sgo = (c16 >> 2) == kg ? sgb + (r0 * 4 + kq0) * 256 + c16 * 16 : WS_BYTES;
    pin(sgl); pin(hvo); pin(sgo); pin(spa);
    int pw[1] = {O_FW + ((b & 7) * 12 + (lane < 48 ? lane >> 2 : 0)) * 128 + (lane & 3) * 16};
    unsigned xo = ((lane >> 3) * TP_ + (lane & 7) * 8) * 2;
    const char* xs = ws + O_X2T + (N * 80 * TP_ + M * 64) * 2;
    unsigned gbo = O_MODP + (((lane & 7) < MODP ? (lane & 7) : 0) * NMOD + 5 * D_ + N * 80 + wv * 20 + (lane < 40 ? lane >> 3 : 0) * 4) * 4;
    pin(pw[0]); pin(xo); pin(gbo);
    u32 hq[4]; u32x4 hs, H[G2_R], WV[23], pf3, ob;
    int po3 = O_F3 + (mt * 16 + nc) * 512 + wv * 32 + (lane & 1) * 16;
    int f3o = tid < 16 ? O_F3 + (mt * 16 + tid) * 512 + nc * 8 : WS_BYTES;
    int oo;
    int fwo = (bs >= CWG && tid < 8) ? O_FW + (tid * 12 + ((bs - CWG) >> 3)) * 128 + (bs & 7) * 8 : WS_BYTES;
    pin(po3); pin(f3o); pin(fwo);
    const char* hp = ws + O_H2F + (M * 4 * 48 + wv * 12) * 1024 + 4096;
    u16* outp;
    asm volatile("s_load_dwordx2 %0, %1, 0x60\n\ts_waitcnt lgkmcnt(0)" : "=s"(outp) : "s"(__builtin_amdgcn_kernarg_segment_ptr()));
    const rsrc_t ro = mk_rsrc(outp, S_ * D_ * 2);
    auto issue = [&](auto O) {
      constexpr int kind = GS.kind[O.value], i = GS.idx[O.value];
      if constexpr (kind == 0) {
        if constexpr (i & 1) xdma1(xoff[1], wsrd, xso);
        else xdma0<g1_slot(i >> 1) * 8192>(ldsw, xoff[0], wsrd, xso, xinc[(i % 10) ? 0 : i / 10]);
      }
      else if constexpr (kind == 1) issue_w(IC<i>{});
      else if constexpr (kind == 2) {
        if constexpr (i >= 37 && O.value < GS.first[160]) gld128b<((i & 7) - 4) * 1024>(WV[i - 37], (i & 8) ? l16b : l16, wdp + (i >> 4) * 16384);
        else issue_wd(IC<i>{});
      }
      else if constexpr (kind == 3) {
        if constexpr (i < 2) dma16<XR_OFF + i * 4160>(sbase + wv * 1040, xo, xs + (size_t)(wv + 4 * i) * 8 * TP_ * 2);
        else if constexpr (i == 2) dma16<XR_OFF + 8320>(sbase + (wv & 1) * 1040, xo, xs + (size_t)((wv & 1) + 8) * 8 * TP_ * 2);
        else gld128<0>(gbr, gbo, ws);
      }
      else if constexpr (kind == 4) gld128<0>(pfw, pw[0], ws);
      else if constexpr (kind == 5) __builtin_amdgcn_raw_buffer_store_b128(gran, rw, sgo, 0, SC1);
      else if constexpr (kind == 6) gld128<i * 1024>(pfs[i], sgl, ws);
      else if constexpr (kind == 7) { hs = u32x4{hq[0], hq[1], hq[2], hq[3]}; __builtin_amdgcn_raw_buffer_store_b128(hs, rw, hvo, hso + g1_ord(i) * (48 * 1024), SC1); }
      else if constexpr (kind == 8) issue_a(IC<i>{});
      else if constexpr (kind == 9) __builtin_amdgcn_raw_buffer_store_b64(tag2, rw, f3o + i * 128, 0, SC1);
      else if constexpr (kind == 10) gld128<i * 128>(pf3, po3, ws);
      else if constexpr (kind == 11) gld128b<(((i % 12) & 7) - 4) * 1024>(H[i % G2_R], ((i % 12) & 8) ? l16b : l16, hp + (i / 12) * (48 * 1024));
      else if constexpr (kind == 12) __builtin_amdgcn_raw_buffer_store_b128(ob, ro, oo, i * (32 * D_ * 2), 0);
      else __builtin_amdgcn_raw_buffer_store_b64(tag2, rw, fwo, 0, SC1);
    };
    auto check3 = [&](auto ST) {
      if (__builtin_expect((v_ne64(u32x2{pf3[0], pf3[1]}, tag) | v_ne64(u32x2{pf3[2], pf3[3]}, tag)) != 0, 0)) {
        const int po[1] = {po3 + ST.value * 128};
        poll<1, 0>(rw, po, tag);
      }
    };
    unsigned xa[9], xm[8];
#pragma unroll
    for (int jj = 0; jj < 9; ++jj) {
      const int rr = 4 * kg + (c16 >> 2);
      xa[jj] = sbase + rr * 256 + (((jj < 8 ? jj : r0) ^ (rr & 7)) * 32) + (lane & 3) * 8;
      pin(xa[jj]);
    }
    u32x4 ones = {0x3f803f80u, 0x3f803f80u, 0x3f803f80u, 0x3f803f80u};
    pin(ones);
    static_for<0, 3>([&](auto I) { gld128a<I.value * 1024>(WC[I.value], l16, wsrc + 20 * 3072); });
    static_for<0, WA - PLATE>(issue_a);
    if (wv == 0) { gld128<0>(pfs[0], pc[0], ws); gld128<0>(pfs[1], pc[1], ws); }
    static_for<WA - PLATE, WA>(issue_a);
    if (wv == 0) {
      wait_vm<PLATE>();
      pin(pfs[0]); pin(pfs[1]);
      if (!__all(tag_ok(pfs[0], tag) && tag_ok(pfs[1], tag))) poll<2, PSL>(rw, pc, tag);
    } else {
      wait_vm<3 * G1_WPRE + 3 + WA>();
    }
    bar();
    static_for<0, GS.first[0]>(issue);
    f32x4 acc[8][3];
    u32x2 X[G1_LA + 1][2], XS[5][2];
    auto xread = [&](auto T, auto HH) {
      constexpr int t = T.value, h = HH.value;
      if constexpr (t < 96) ds_tr<g1_slot(g1_ks(t)) * 8192 + h * 4096>(X[t % (G1_LA + 1)][h], xa[g1_j(t)]);
      else ds_tr<(g1_ks(t) - 12) * 8192 + h * 4096>(X[t % (G1_LA + 1)][h], xm[g1_j(t)]);
    };
    f32x4 gram, sum1;
    u32 fs[4], fa; u64 sbad;
    unsigned spw = sbase + G1_SP + (2 * wv + (kg >> 1)) * 256 + c16 * 16;
    pin(spw);
    auto fop = [&](auto K) {
      constexpr int k = K.value;
      if constexpr (k == 0) asm_and3(fa, pfs[0][1], pfs[0][3], pfs[1][1]);
      else if constexpr (k == 1) asm_and3(fa, fa, pfs[1][3], pfs[1][3]);
      else if constexpr (k == 2) asm_ne1(sbad, fa);
      else if constexpr (k == 4) v_swap32(fs[0], fs[1]);
      else if constexpr (k == 5) v_swap32(fs[2], fs[3]);
      else if constexpr (k == 6) asm_op2<0>(fs[0], fs[0], fs[1]);
      else if constexpr (k == 7) asm_op2<0>(fs[2], fs[2], fs[3]);
      else if constexpr (k == 8) v_swap16(fs[0], fs[2]);
      else if constexpr (k == 9) asm_op2<0>(fs[0], fs[0], fs[2]);
      else if constexpr (k == 10) asm_op1<0>(fs[0], fs[0]);
      else if constexpr (k == 11) asm_op1<1>(fs[1], fs[0]);
      else if constexpr (k == 12) v_swap16(fs[0], fs[1]);
      else if constexpr (k == 13) asm_op2<1>(fs[1], fs[0], fs[1]);
      else if constexpr (k == 14) asm_op1<2>(fs[1], fs[1]);
      else if constexpr (k == 15) asm_op1<3>(fs[1], fs[1]);
      else if constexpr (k == 16) asm_op1<4>(fs[1], fs[1]);
      else if constexpr (k == 17) asm_op1<3>(fs[1], fs[1]);
      else if constexpr (k == 18) asm_op1<5>(fs[1], fs[1]);
      else if constexpr (k == 19) asm_op2<2>(fs[0], fs[0], fs[1]);
      else if constexpr (k == 20) asm_op1<1>(fs[1], fs[0]);
      else if constexpr (k == 21) asm_op1<1>(fs[2], fs[0]);
      else if constexpr (k == 22) asm_op1<6>(fs[3], fs[0]);
      else if constexpr (k == 23) ds_w128u<0>(spw, u32x4{fs[0], fs[1], fs[2], fs[3]});
    };
    float eg[6]; u32x4 xc;
    auto eop = [&](auto J, auto K) {
      constexpr int j = J.value, k = K.value;
      if constexpr (k < 15) {
        if constexpr (k < 6) eg[k] = v_relu(acc[j][k >> 1][2 * (k & 1)]);
        else if constexpr (k < 12) eg[k - 6] = v_mul(eg[k - 6], acc[j][(k - 6) >> 1][2 * (k & 1) + 1]);
        else hq[k == 12 ? 1 : k == 13 ? 0 : 2] = v_cvt(eg[k == 12 ? 2 : k == 13 ? 0 : 4], eg[k == 12 ? 3 : k == 13 ? 1 : 5]);
      } else if constexpr (k == 15) v_swap32(hq[1], hq[3]);
      else if constexpr (k == 16) v_swap32(hq[0], hq[2]);
      else if constexpr (k == 17) v_swap16(hq[0], hq[1]);
      else v_swap16(hq[2], hq[3]);
    };
    unsigned pwa, pra, gba, xad, sda;
    float xr[2][8], sdv, G[2][8], S[8], rs2;
    hq[3] = 0;
    pin(hq[3]);
    wait_vm<GS.first[0] - 2>();
    bar();
    static_for<0, G1_LA>([&](auto T) { xread(T, IC<0>{}); xread(T, IC<1>{}); });
    static_for<0, 160>([&](auto T) {
      constexpr int t = T.value, ks = g1_ks(t), jj = g1_j(t), tn = t + G1_LA, u = t & 7;
      if constexpr (!(t & 1)) wait_lgkm<2 * ((tn <= 160 ? tn : 160) - t - 2)>();
      if constexpr (g1_need(t) >= 0) {
        static_assert(g1_need(t) < GS.first[t], "not issued yet");
        if constexpr (GS.first[t] - 1 - g1_need(t) < 63) wait_vm<GS.first[t] - 1 - g1_need(t)>();
        bar();
      }
      if constexpr (t == T_WCHK) {
        if (__builtin_expect((v_ne64(u32x2{pfw[0], pfw[1]}, tag) | v_ne64(u32x2{pfw[2], pfw[3]}, tag)) != 0, 0)) poll<1, 1>(rw, pw, tag);
      }
      if constexpr (t == T_C0) { wait_vm<GS.first[t] - 1 - g_pos(10, 0)>(); check3(IC<0>{}); }
      if constexpr (t == T_SST - 2) {
        pin(gram); pin(sum1);
        const int c3 = lane & 3;
        gran = u32x4{__float_as_uint(sum1[0]), 1u, __float_as_uint(c3 == 0 ? gram[0] : c3 == 1 ? gram[1] : c3 == 2 ? gram[2] : gram[3]), 1u};
        pin(gran);
      }
      if constexpr (t == 86) {
        if (__builtin_expect(sbad != 0, 0)) {
#pragma unroll 1
          for (int spn = 0; spn < SPIN_CAP; ++spn) {
            pfs[0] = __builtin_amdgcn_raw_buffer_load_b128(rw, sgl, 0, SC1);
            pfs[1] = __builtin_amdgcn_raw_buffer_load_b128(rw, sgl + 1024, 0, SC1);
            if (__all((pfs[0][1] & pfs[0][3] & pfs[1][1] & pfs[1][3]) == 1u)) break;
            asm volatile("" ::: "memory");
          }
        }
        fs[0] = pfs[0][0]; fs[1] = pfs[1][0]; fs[2] = pfs[0][2]; fs[3] = pfs[1][2];
      }
      auto ep = [&](auto A, auto B) { if constexpr (t >= 104) static_for<A.value, (B.value < 19 ? B.value : 19)>([&](auto K) { eop(IC<g1_j(t - 8)>{}, K); }); };
      constexpr int k0 = u == 0 ? 0 : 4 * u - 3;
      constexpr int sr = t == 0 ? 0 : t == 4 ? 1 : (t >= 12 && t < 15) ? t - 10 : -1, sm = t == 4 ? 0 : t == 8 ? 1 : (t >= 16 && t < 19) ? t - 14 : -1;
      const u32x4 xb = cat(X[t % (G1_LA + 1)][0], X[t % (G1_LA + 1)][1]);
      if constexpr (ks >= 12) mfma_a<false>(acc[jj][0], WA_[3 * (ks - 12) + 0], xb); else mfma_<ks == 0>(acc[jj][0], W[ks % G1_R][0], xb);
      if constexpr (tn < 160) xread(IC<tn>{}, IC<0>{});
      if constexpr (t >= 84 && t < 96) fop(IC<2 * (t - 84)>{});
      if constexpr (u > 0) ep(IC<k0>{}, IC<k0 + 1>{});
      if constexpr (ks >= 12) mfma_a<false>(acc[jj][1], WA_[3 * (ks - 12) + 1], xb); else mfma_<ks == 0>(acc[jj][1], W[ks % G1_R][1], xb);
      if constexpr (tn < 160) xread(IC<tn>{}, IC<1>{});
      if constexpr (t >= 84 && t < 96) fop(IC<2 * (t - 84) + 1>{});
      if constexpr (u > 0) ep(IC<k0 + 1>{}, IC<k0 + 2>{}); else ep(IC<0>{}, IC<1>{});
      if constexpr (ks >= 12) mfma_a<false>(acc[jj][2], WA_[3 * (ks - 12) + 2], xb); else mfma_<ks == 0>(acc[jj][2], W[ks % G1_R][2], xb);
      if constexpr (t >= 96) real();
      static_for<GS.first[t], GS.first[t + 1]>(issue);
      if constexpr (u > 0) ep(IC<k0 + 2>{}, IC<k0 + 4>{});
      if constexpr (t >= 84 && t < 92) xm[t - 84] = v_addc(xa[t - 84], G1_RS * 8192);
      if constexpr (sm >= 0) { mfma_<sm == 0>(gram, cat(XS[sm][0], XS[sm][1]), cat(XS[sm][0], XS[sm][1])); mfma_<sm == 0>(sum1, ones, cat(XS[sm][0], XS[sm][1])); }
      if constexpr (sr >= 0) { ds_tr<sr * 8192>(XS[sr][0], xa[8]); ds_tr<sr * 8192 + 4096>(XS[sr][1], xa[8]); }
      if constexpr (t >= 96 && u == (t < 104 ? 4 : 0)) ds_b128<jj * 256>(xc, spa);
      if constexpr (t >= 96 && u == 7) {
        if constexpr (t < 104) wait_lgkm<6>();
        mfma_a<false>(acc[jj][0], WC[0], xc);
        mfma_a<false>(acc[jj][1], WC[1], xc);
        mfma_a<false>(acc[jj][2], WC[2], xc);
      }
    });
    asm volatile("s_nop 5");
    static_for<0, 15>([&](auto K) { eop(IC<7>{}, K); });
    eop(IC<7>{}, IC<15>{}); asm volatile("s_nop 0"); eop(IC<7>{}, IC<16>{}); asm volatile("s_nop 1"); eop(IC<7>{}, IC<17>{}); eop(IC<7>{}, IC<18>{});
    static_for<GS.first[160], GS.mid>(issue);
    if constexpr (T_C0 >= 160) { wait_vm<GS.mid - 1 - g_pos(10, 0)>(); check3(IC<0>{}); }
    static_for<GS.mid, GS.first2[0]>(issue);
    {
      {
        pin(gbr);
        const float g = sum8((lane & 7) < MODP ? __uint_as_float(gbr[0]) : 0.f), g2 = sum8((lane & 7) < MODP ? __uint_as_float(gbr[1]) : 0.f);
        const float g3 = sum8((lane & 7) < MODP ? __uint_as_float(gbr[2]) : 0.f), g4 = sum8((lane & 7) < MODP ? __uint_as_float(gbr[3]) : 0.f);
        if ((lane & 7) == 0 && lane < 40) *(f32x4*)(smem + GB_OFF + wv * 80 + (lane >> 3) * 16) = f32x4{rbf(g), rbf(g2), rbf(g3), rbf(g4)};
      }
      const int lf = lane < 40 ? lane : 0, ft = 4 * wv + lf / 10, fc = lf % 10;
      pwa = sbase + wv * 5440 + l16 + (kg >> 1) * 32;
      pra = sbase + fc * 544 + ft * 16; gba = sbase + GB_OFF + fc * 32;
      xad = sbase + XR_OFF + fc * 1040 + ft * 2; sda = sbase + G1_SP + ((nc & 4) * 16 + ft) * 16 + 2;
      {
        const int pos = M * 64 + ft, l8 = (pos >> 3) & 63;
        const int tok = 256 * (2 * (pos >> 9) + ((pos >> 2) & 1)) + 16 * (l8 & 15) + 4 * (l8 >> 4) + (pos & 3);
        oo = lane < 40 ? (tok * D_ + N * 80 + 8 * fc) * 2 : S_ * D_ * 2;
      }
      sdv = 0.f;
#pragma unroll
      for (int e = 0; e < 8; ++e) { xr[0][e] = 0.f; pin(xr[0][e]); }
      pin(pwa); pin(pra); pin(gba); pin(xad); pin(sda); pin(oo); pin(sdv);
      wait_lgkm<0>();
    }
    static_assert(g_lastdv() < g_pos(11, 0) && g_pos(3, 3) < g_pos(7, 5), "landed behind earlier waits");
    f32x4 gbv[2], ac2[2][5], pt[8];
    auto pre = [&](auto J, auto K) {
      constexpr int j = J.value, k = K.value;
      if constexpr (k == 0) ds_hi16<j * 256>(sdv, sda);
      else if constexpr (k < 9) ds_hi16<(k - 1) * 128 + j * 32>(xr[j == 3][k - 1], xad);
      else if constexpr (k == 9) rs2 = v_rcp(sdv);
      else if constexpr (k == 10) rs2 = v_mul(rs2, rs2);
      else G[j == 3][k - 11] = v_mul(gbv[(k - 11) >> 2][(k - 11) & 3], rs2);
    };
    auto fin = [&](auto K) {
      constexpr int k = K.value & 63, e = k & 7, z = K.value >> 6;
      if constexpr (k < 8) S[e] = v_add(pt[e >> 2][e & 3], pt[2 + (e >> 2)][e & 3]);
      else if constexpr (k < 24) S[e] = v_add(S[e], pt[2 * (k >> 3) + 2 + (e >> 2)][e & 3]);
      else if constexpr (k < 32) S[e] = v_fma(G[z][e], S[e], xr[z][e]);
      else ob[k - 32] = v_cvt(S[2 * (k - 32)], S[2 * (k - 32) + 1]);
    };
    auto prd = [&](auto J, auto K) { ds_f128<(J.value & 1) * 21760 + (K.value >> 1) * 5440 + (K.value & 1) * 256>(pt[K.value], pra); };
    static_for<0, 240>([&](auto GG) {
      constexpr int g = GG.value, o = g < 225 ? g / 5 : 45 + (g - 225) % 3, jj = o / 12, ks = o % 12, i = g < 225 ? g % 5 : (g - 225) / 3, q = g % 60;
      if constexpr (i == 0) { static_assert(g2_need(o) < GS.first2[g], "not issued yet"); wait_vm<GS.first2[g] - 1 - g2_need(o)>(); }
      if constexpr (g == G_F3) { wait_vm<GS.first2[g] - 1 - g_pos(7, 7)>(); bar(); }
      if constexpr (g == G_C1) { wait_vm<GS.first2[g] - 1 - g_pos(10, 1)>(); check3(IC<1>{}); }
      if constexpr (g == G_C2) { wait_vm<GS.first2[g] - 1 - g_pos(10, 2)>(); check3(IC<2>{}); }
      if constexpr (g == G_C3) { wait_vm<GS.first2[g] - 1 - g_pos(10, 3)>(); check3(IC<3>{}); }
      mfma_a<ks == 0>(ac2[jj & 1][i], WD[ks * 5 + i], H[o % G2_R]);
      if constexpr (i == 4 || g >= 225) real();
      static_for<GS.first2[g], GS.first2[g + 1]>(issue);
      if constexpr (!(g & 1) && 37 + g / 2 < 60) { if constexpr (g_pos(2, 37 + g / 2) < GS.first[160]) to_acc(WD[37 + g / 2], WV[g / 2]); }
      if constexpr (g == 30 || g == 32) ds_f128<(g - 30) * 8>(gbv[(g - 30) / 2], gba);
      if constexpr (jj >= 1) {
        if constexpr (q >= 5 && q < 15 && (q & 1)) ds_w128<((jj - 1) & 1) * 21760 + ((q - 5) / 2) * 1088>(pwa, ac2[(jj - 1) & 1][(q - 5) / 2]);
        if constexpr (q == 18) { wait_lgkm<0>(); bar(); }
        if constexpr (q >= 19 && q < 27) prd(IC<jj - 1>{}, IC<q - 19>{});
        if constexpr (q == 30) wait_lgkm<0>();
        if constexpr (q >= 31 && q < 31 + FINW) static_for<(36 * (q - 31) + FINW - 1) / FINW, (36 * (q - 30) + FINW - 1) / FINW>(fin);
      }
      if constexpr (jj >= 1 && q < 18 && !(q & 1)) pre(IC<jj - 1>{}, IC<q / 2>{});
      if constexpr (jj >= 1 && q >= 19 && q < 29) pre(IC<jj - 1>{}, IC<q - 10>{});
      if constexpr (g >= 180 && g < 196 && (g & 1)) { u32 z; asm_op1<6>(z, 0u); xr[1][(g - 180) / 2] = __uint_as_float(z); }
      if constexpr (g >= 211 && g < 220) pre(IC<3>{}, IC<g - 211>{});
      if constexpr (g == 223) wait_lgkm<0>();
      if constexpr (g >= 224 && g < 234) pre(IC<3>{}, IC<g - 215>{});
      if constexpr (g == 231 || g == 234 || g == 237) ds_w128<21760 + ((g - 231) / 3) * 1088>(pwa, ac2[1][(g - 231) / 3]);
    });
    asm volatile("s_nop 1");
    ds_w128<21760 + 3 * 1088>(pwa, ac2[1][3]);
    asm volatile("s_nop 3");
    ds_w128<21760 + 4 * 1088>(pwa, ac2[1][4]);
    wait_lgkm<0>(); bar();
    static_for<0, 8>([&](auto K) { prd(IC<3>{}, K); });
    wait_lgkm<4>();
    static_for<64, 72>(fin);
    wait_lgkm<2>();
    static_for<72, 80>(fin);
    wait_lgkm<0>();
    static_for<80, 100>(fin);
    __builtin_amdgcn_raw_buffer_store_b128(ob, ro, oo, 3 * (32 * D_ * 2), 0);
  }
  if (b == 0 && tid == 0) st_flag(ws, O_EPOCH, tag);
}

extern "C" void kernel_launch(void* const* d_in, const int* in_sizes, int n_in, void* d_out, int out_size,
                              void* d_ws, size_t ws_size, hipStream_t stream) {
  k_fused<<<256, 256, 0, stream>>>((const u16*)d_in[0], (const u16*)d_in[1], (const u16*)d_in[2], (const u16*)d_in[3],
                                   (const u16*)d_in[4], (const u16*)d_in[5], (const u16*)d_in[7], (const u16*)d_in[8],
                                   (const u16*)d_in[9], (const u16*)d_in[10], (const u16*)d_in[11], (char*)d_ws, (u16*)d_out);
}
```
